```python
import torch

def _softmax(x, axis=-1):
    return torch.softmax(x, dim=axis)
def _gelu(x):
    return torch.nn.functional.gelu(x, approximate="tanh")
def _silu(x):
    return torch.nn.functional.silu(x)
def _sigmoid(x):
    return torch.sigmoid(x)
def _layernorm(x, axis=-1, eps=1e-5):
    m = x.mean(dim=axis, keepdim=True)
    v = x.var(dim=axis, keepdim=True, unbiased=False)
    return (x - m) / torch.sqrt(v + eps)
def _scatter_add(tgt, idx, src, axis=0):
    return tgt.clone().index_add_(axis, idx, src)
def _segment_sum(src, seg, n):
    out = torch.zeros((n,) + src.shape[1:], dtype=src.dtype)
    return out.index_add_(0, seg, src)
def _pad(x, pw):
    flat = tuple(v for pair in reversed(pw) for v in pair)
    return torch.nn.functional.pad(x, flat)

kernel_name = 'recurrent_scan__compressed_kv_cla__moe_arch_mod__prompt'

def setup_inputs():

    _w = (lambda a: (lambda t: t.to(torch.bfloat16) if t.is_floating_point() else t)(torch.as_tensor(a)))
    def _A_setup():
        B, H, Sq, Sk, D, M = 1, 32, 2048, 2048, 128, 1024
        Dc = 4 * D
        return {
            "x": _w((lambda *s: torch.randn(*s, dtype=torch.bfloat16) * 0.05)(B, Sq, M)),
            "c_kv": _w((lambda *s: torch.randn(*s, dtype=torch.bfloat16) * 0.2)(B, Sk, Dc)),
            "w_q": _w((lambda *s: torch.randn(*s, dtype=torch.bfloat16) * 3.2)(M, H, D) * (M ** -0.5)),
            "w_uk": _w((lambda *s: torch.randn(*s, dtype=torch.bfloat16) * 3.2)(Dc, H, D) * (Dc ** -0.5)),
            "w_uv": _w((lambda *s: torch.randn(*s, dtype=torch.bfloat16) * 0.2)(Dc, H, D) * (Dc ** -0.5)),
            "w_o": _w((lambda *s: torch.randn(*s, dtype=torch.bfloat16) * 0.2)(H, D, M) * ((H * D) ** -0.5)),
        }
    _A_in = _A_setup()

    def _B_setup():
        B, S, D, F, C = 1, 2048, 1024, 3584, 2
        return {
            "x": _w((lambda *s: torch.randn(*s, dtype=torch.bfloat16) * 0.05)(B, S, D)),
            "w_r": _w((lambda *s: torch.randn(*s, dtype=torch.bfloat16) * 0.2)(D)),
            "w_gate": _w((lambda *s: torch.randn(*s, dtype=torch.bfloat16) * 0.1)(D, F)),
            "w_up": _w((lambda *s: torch.randn(*s, dtype=torch.bfloat16) * 0.1)(D, F)),
            "w_down": _w((lambda *s: torch.randn(*s, dtype=torch.bfloat16) * 0.1)(F, D)),
        }
    _B_in = _B_setup()

    return {
        "A_x": _A_in["x"],
        "A_c_kv": _A_in["c_kv"],
        "A_w_q": _A_in["w_q"],
        "A_w_uk": _A_in["w_uk"],
        "A_w_uv": _A_in["w_uv"],
        "A_w_o": _A_in["w_o"],
        "B_x": _B_in["x"],
        "B_w_r": _B_in["w_r"],
        "B_w_gate": _B_in["w_gate"],
        "B_w_up": _B_in["w_up"],
        "B_w_down": _B_in["w_down"],
    }

def reference(A_x, A_c_kv, A_w_q, A_w_uk, A_w_uv, A_w_o, B_x, B_w_r, B_w_gate, B_w_up, B_w_down):
    def _A_ref(x, c_kv, w_q, w_uk, w_uv, w_o):
        B, Sq, M = x.shape[0], x.shape[1], x.shape[2]
        Sk = c_kv.shape[1]
        H, D = w_q.shape[1], w_q.shape[2]
        q = torch.einsum("bqm,mhd->bhqd", x, w_q)
        k = torch.einsum("bkc,chd->bhkd", c_kv, w_uk)
        v = torch.einsum("bkc,chd->bhkd", c_kv, w_uv)
        scores = torch.einsum("bhqd,bhkd->bhqk", q, k) * (D ** -0.5)
        off = Sk - Sq
        qi = torch.arange(Sq)[:, None]
        kj = torch.arange(Sk)[None, :]
        cmask = kj <= (qi + off)
        scores = torch.where(cmask[None, None], scores, -1e9)
        p = _softmax(scores, axis=-1)
        ctx = torch.einsum("bhqk,bhkd->bhqd", p, v)
        return torch.einsum("bhqd,hdm->bqm", ctx, w_o)

    def _B_ref(x, w_r, w_gate, w_up, w_down):
        B, S, D = x.shape
        C = 2
        r = x @ w_r
        idx = torch.argsort(-r, axis=-1)[:, :C]
        idx_e = (lambda x, n, axis: torch.repeat_interleave(x, n, dim=axis))(idx.reshape(B, C, 1), D, axis=2)
        x_sel = (lambda x, i, axis: torch.take_along_dim(x, i, dim=axis))(x, idx_e, axis=1)
        g = _silu(x_sel @ w_gate)
        u = x_sel @ w_up
        y = (g * u) @ w_down
        r_sel = _sigmoid((lambda x, i, axis: torch.take_along_dim(x, i, dim=axis))(r, idx, axis=1))
        y = y * r_sel.reshape(B, C, 1)
        b_off = (torch.arange(B) * S).reshape(B, 1)
        flat_idx = (idx + b_off).reshape(B * C)
        zero = (x * 0.0).reshape(B * S, D)
        upd = _scatter_add(zero, flat_idx, y.reshape(B * C, D), axis=0)
        return x + upd.reshape(B, S, D)

    x = A_x
    xn = _layernorm(x, axis=-1)
    s = _A_ref(xn, A_c_kv, A_w_q, A_w_uk, A_w_uv, A_w_o)
    g = _silu(_B_ref(xn, B_w_r, B_w_gate, B_w_up, B_w_down))
    return x + s * g
```

```cpp
#pragma clang diagnostic ignored "-Winline-asm"
#include <hip/hip_runtime.h>
#include <stdint.h>
#define SRD(n) const void* p##n, unsigned n##_bytes, unsigned n##_fmt
__global__ __launch_bounds__(256, 1) void k_all(SRD(x), SRD(ckv), SRD(wq), SRD(wuk), SRD(wuv), SRD(wo), SRD(wr), SRD(wg), SRD(wu), SRD(wd), SRD(ws), SRD(out)) {
    const int wave = __builtin_amdgcn_readfirstlane(threadIdx.x >> 6);
    asm volatile(
R"(
.macro Mvvv d,a,b
v_mfma_f32_16x16x32_bf16 v[\d:\d+3], v[\a:\a+3], v[\b:\b+3], v[\d:\d+3]
.endm
.macro Zvvv d,a,b
v_mfma_f32_16x16x32_bf16 v[\d:\d+3], v[\a:\a+3], v[\b:\b+3], 0
.endm
.macro Cvvv d,a,b,c
v_mfma_f32_16x16x32_bf16 v[\d:\d+3], v[\a:\a+3], v[\b:\b+3], v[\c:\c+3]
.endm
.macro Mvav d,a,b
v_mfma_f32_16x16x32_bf16 v[\d:\d+3], a[\a:\a+3], v[\b:\b+3], v[\d:\d+3]
.endm
.macro Zvav d,a,b
v_mfma_f32_16x16x32_bf16 v[\d:\d+3], a[\a:\a+3], v[\b:\b+3], 0
.endm
.macro Cvav d,a,b,c
v_mfma_f32_16x16x32_bf16 v[\d:\d+3], a[\a:\a+3], v[\b:\b+3], v[\c:\c+3]
.endm
.macro Mvaa d,a,b
v_mfma_f32_16x16x32_bf16 v[\d:\d+3], a[\a:\a+3], a[\b:\b+3], v[\d:\d+3]
.endm
.macro Zvaa d,a,b
v_mfma_f32_16x16x32_bf16 v[\d:\d+3], a[\a:\a+3], a[\b:\b+3], 0
.endm
.macro Mavv d,a,b
v_mfma_f32_16x16x32_bf16 a[\d:\d+3], v[\a:\a+3], v[\b:\b+3], a[\d:\d+3]
.endm
.macro Zavv d,a,b
v_mfma_f32_16x16x32_bf16 a[\d:\d+3], v[\a:\a+3], v[\b:\b+3], 0
.endm
.macro Maav d,a,b
v_mfma_f32_16x16x32_bf16 a[\d:\d+3], a[\a:\a+3], v[\b:\b+3], a[\d:\d+3]
.endm
.Lstart:
s_mov_b32 s56, %[b]
s_mov_b32 s57, %[w]
s_load_dwordx16 s[8:23], %[ka], 0x0
s_load_dwordx16 s[24:39], %[ka], 0x40
s_load_dwordx16 s[40:55], %[ka], 0x80
v_mbcnt_lo_u32_b32 v16, -1, 0
v_mbcnt_hi_u32_b32 v16, -1, v16
v_lshlrev_b32 v0, 4, v16
s_lshr_b32 s60, s56, 3
s_lshl2_add_u32 s58, s60, s57
s_lshl_b32 s58, s58, 10
s_mov_b32 s59, .Lhot_end-.Lstart
s_min_u32 s58, s58, s59
s_getpc_b64 s[62:63]
.Lcp1:
s_sub_u32 s62, s62, .Lcp1-.Lstart
s_subb_u32 s63, s63, 0
s_add_u32 s62, s62, s58
s_addc_u32 s63, s63, 0
s_and_b32 s62, s62, -16
global_load_dwordx4 v[28:31], v0, s[62:63]
s_and_b32 s61, s60, 7
s_and_b32 s65, s56, 7
s_lshl_b32 s65, s65, 2
s_lshr_b32 s65, 0x73625140, s65
s_and_b32 s65, s65, 7
s_lshr_b32 s58, s60, 3
s_lshl2_add_u32 s64, s65, s58
v_lshrrev_b32 v17, 3, v16
v_and_b32 v18, 7, v16
v_and_b32 v19, 15, v16
v_lshrrev_b32 v20, 4, v16
s_and_b32 s59, s57, 1
s_lshl_b32 s58, s59, 6
v_xor_b32 v21, v18, v20
v_lshlrev_b32 v21, 4, v21
v_lshl_add_u32 v1, v17, 10, v21
v_xor_b32 v1, s58, v1
s_lshl_b32 s58, s59, 5
v_bfe_u32 v22, v16, 2, 3
v_lshrrev_b32 v23, 5, v16
v_lshlrev_b32 v23, 6, v23
v_lshl_add_u32 v22, v22, 13, v23
v_and_b32 v23, 3, v16
v_lshl_add_u32 v3, v23, 4, v22
v_xor_b32 v3, s58, v3
s_lshr_b32 s58, s57, 1
s_lshl_b32 s66, s58, 14
s_lshl_b32 s58, s59, 10
s_add_u32 s66, s66, s58
s_lshl_b32 s67, s61, 18
s_lshl3_add_u32 s67, s66, s67
s_lshl_b32 s68, s57, 16
s_lshl_b32 s58, s64, 8
s_add_u32 s68, s68, s58
s_lshl_b32 s69, s57, 13
s_lshl_b32 s70, s57, 12
s_lshl_b32 s71, s57, 11
s_add_u32 m0, s70, 98304
s_add_u32 s72, s68, 0
s_waitcnt lgkmcnt(0)
buffer_load_dwordx4 v3, s[20:23], s72 offen lds
s_add_u32 m0, s70, 99200
s_add_u32 s72, s68, 0
buffer_load_dwordx4 v3, s[20:23], s72 offen offset:128 lds
s_add_u32 m0, s70, 100352
s_add_u32 s72, s68, 0
buffer_load_dwordx4 v3, s[24:27], s72 offen lds
s_add_u32 m0, s70, 101248
s_add_u32 s72, s68, 0
buffer_load_dwordx4 v3, s[24:27], s72 offen offset:128 lds
s_add_u32 m0, s66, 0
s_add_u32 s72, s67, 0
buffer_load_dwordx4 v1, s[12:15], s72 offen lds
s_add_u32 m0, s66, 2048
s_add_u32 s72, s67, 16384
buffer_load_dwordx4 v1, s[12:15], s72 offen lds
s_add_u32 m0, s66, 4096
s_add_u32 s72, s67, 32768
buffer_load_dwordx4 v1, s[12:15], s72 offen lds
s_add_u32 m0, s66, 6144
s_add_u32 s72, s67, 49152
buffer_load_dwordx4 v1, s[12:15], s72 offen lds
s_add_u32 m0, s66, 8192
s_add_u32 s72, s67, 65536
buffer_load_dwordx4 v1, s[12:15], s72 offen lds
s_add_u32 m0, s66, 10240
s_add_u32 s72, s67, 81920
buffer_load_dwordx4 v1, s[12:15], s72 offen lds
s_add_u32 m0, s66, 12288
s_add_u32 s72, s67, 98304
buffer_load_dwordx4 v1, s[12:15], s72 offen lds
s_add_u32 m0, s66, 14336
s_add_u32 s72, s67, 114688
buffer_load_dwordx4 v1, s[12:15], s72 offen lds
s_add_u32 m0, s70, 114688
s_add_u32 s72, s68, 262144
buffer_load_dwordx4 v3, s[20:23], s72 offen lds
s_add_u32 m0, s70, 115584
s_add_u32 s72, s68, 262144
buffer_load_dwordx4 v3, s[20:23], s72 offen offset:128 lds
s_add_u32 m0, s70, 116736
s_add_u32 s72, s68, 262144
buffer_load_dwordx4 v3, s[24:27], s72 offen lds
s_add_u32 m0, s70, 117632
s_add_u32 s72, s68, 262144
buffer_load_dwordx4 v3, s[24:27], s72 offen offset:128 lds
s_load_dwordx2 s[74:75], s[48:49], 0x0
s_add_u32 m0, s70, 131072
s_add_u32 s72, s68, 524288
buffer_load_dwordx4 v3, s[20:23], s72 offen lds
s_add_u32 m0, s70, 131968
s_add_u32 s72, s68, 524288
buffer_load_dwordx4 v3, s[20:23], s72 offen offset:128 lds
s_add_u32 m0, s70, 133120
s_add_u32 s72, s68, 524288
buffer_load_dwordx4 v3, s[24:27], s72 offen lds
s_add_u32 m0, s70, 134016
s_add_u32 s72, s68, 524288
buffer_load_dwordx4 v3, s[24:27], s72 offen offset:128 lds
s_add_u32 m0, s70, 147456
s_add_u32 s72, s68, 786432
buffer_load_dwordx4 v3, s[20:23], s72 offen lds
s_add_u32 m0, s70, 148352
s_add_u32 s72, s68, 786432
buffer_load_dwordx4 v3, s[20:23], s72 offen offset:128 lds
s_add_u32 m0, s70, 149504
s_add_u32 s72, s68, 786432
buffer_load_dwordx4 v3, s[24:27], s72 offen lds
s_add_u32 m0, s70, 150400
s_add_u32 s72, s68, 786432
buffer_load_dwordx4 v3, s[24:27], s72 offen offset:128 lds
v_lshl_add_u32 v21, v20, 3, v18
v_lshlrev_b32 v21, 4, v21
v_and_b32 v22, 1, v17
v_lshl_add_u32 v79, v22, 11, v21
v_mov_b32 v84, v21
s_lshl2_add_u32 s58, s56, s57
s_lshl_b32 s73, s58, 12
s_lshl2_add_u32 s76, s60, s57
s_lshl3_add_u32 s76, s76, s65
s_lshl_b32 s76, s76, 10
s_add_u32 s72, s76, 1048576
buffer_load_dwordx4 v[80:83], v0, s[20:23], s72 offen
s_add_u32 s72, s76, 1048576
buffer_load_dwordx4 v[80:83], v0, s[24:27], s72 offen
v_mul_u32_u24 v77, 2176, v22
v_add_u32 v77, v77, v21
s_mul_i32 s77, s58, 4352
s_add_u32 s77, s77, 4194304
s_lshl4_add_u32 s78, s58, 4096
s_lshl_b32 s58, s58, 1
s_sub_u32 s58, 0x7ff, s58
v_sub_u32 v78, s58, v22
s_add_u32 s79, s70, 4096
v_cmp_eq_u32 vcc, 0, v16
v_mov_b32 v23, 0x80000000
s_nop 0
v_cndmask_b32 v13, v23, 0, vcc
v_lshrrev_b32 v21, 1, v19
v_xor_b32 v21, v21, v20
v_lshlrev_b32 v21, 4, v21
v_lshl_add_u32 v21, v19, 7, v21
s_lshr_b32 s58, s57, 1
s_lshl_b32 s59, s58, 14
v_add_u32 v5, s59, v21
v_xor_b32 v6, 64, v5
v_add_u32 v7, 0x10000, v5
v_add_u32 v8, 0x10000, v6
s_add_u32 s59, s69, 0x10000
v_add_u32 v24, s59, v21
v_xor_b32 v25, 64, v24
v_lshrrev_b32 v21, 2, v19
v_lshlrev_b32 v21, 6, v21
v_and_b32 v22, 1, v20
v_lshl_add_u32 v21, v22, 5, v21
v_and_b32 v22, 3, v19
v_lshl_add_u32 v21, v22, 3, v21
v_add_u32 v21, 0x18000, v21
v_lshl_add_u32 v26, v20, 11, v21
v_xor_b32 v27, 32, v26
v_lshl_add_u32 v21, v20, 12, v21
s_and_b32 s59, s57, 1
s_lshl_b32 s72, s59, 10
v_add_u32 v9, s72, v21
v_xor_b32 v10, 32, v9
s_lshl_b32 s80, s64, 4
s_lshl1_add_u32 s80, s61, s80
s_add_u32 s80, s80, s58
s_mul_i32 s80, s80, 69632
s_lshl_b32 s81, s59, 13
s_add_u32 s81, s81, s80
s_add_u32 s81, s81, 16793600
s_lshl_b32 s59, s59, 11
s_add_u32 s80, s80, s59
s_add_u32 s80, s80, 16777216
s_lshl2_add_u32 s58, s61, s57
s_mul_i32 s82, s58, 69632
s_sub_u32 s83, 8581120, s82
s_add_u32 s82, s82, 4194304
v_xor_b32 v21, v18, v20
v_lshlrev_b32 v21, 4, v21
v_mul_u32_u24 v22, 2176, v17
v_add_u32 v11, v22, v21
v_xor_b32 v21, 64, v21
v_add_u32 v22, 17408, v22
v_add_u32 v12, v22, v21
s_lshl_b32 s84, s64, 5
s_add_u32 s58, s58, s84
s_lshl3_add_u32 s85, s58, 24576
s_lshl3_add_u32 s84, s84, 24576
s_waitcnt lgkmcnt(0)
s_add_u32 s74, s74, 0x7f4a7c15
s_addc_u32 s75, s75, 0x9e3779b9
s_waitcnt vmcnt(19)
s_barrier
ds_read_b128 v[96:99], v5 offset:0
ds_read_b64_tr_b16 v[128:129], v9 offset:0
ds_read_b64_tr_b16 v[130:131], v9 offset:256
ds_read_b64_tr_b16 v[136:137], v10 offset:0
ds_read_b64_tr_b16 v[138:139], v10 offset:256
ds_read_b64_tr_b16 v[144:145], v9 offset:512
ds_read_b64_tr_b16 v[146:147], v9 offset:768
ds_read_b64_tr_b16 v[152:153], v10 offset:512
ds_read_b64_tr_b16 v[154:155], v10 offset:768
ds_read_b64_tr_b16 v[160:161], v9 offset:2048
ds_read_b64_tr_b16 v[162:163], v9 offset:2304
s_waitcnt lgkmcnt(8)
Zavv 0,128,96
s_waitcnt lgkmcnt(6)
ds_read_b64_tr_b16 v[168:169], v10 offset:2048
ds_read_b64_tr_b16 v[170:171], v10 offset:2304
Zavv 4,136,96
s_waitcnt lgkmcnt(6)
ds_read_b64_tr_b16 v[176:177], v9 offset:2560
ds_read_b64_tr_b16 v[178:179], v9 offset:2816
Zavv 8,144,96
s_waitcnt lgkmcnt(6)
ds_read_b64_tr_b16 v[184:185], v10 offset:2560
ds_read_b64_tr_b16 v[186:187], v10 offset:2816
Zavv 12,152,96
s_waitcnt lgkmcnt(6)
ds_read_b128 v[104:107], v5 offset:2048
Zavv 16,96,160
s_waitcnt lgkmcnt(5)
ds_read_b128 v[112:115], v5 offset:4096
Zavv 20,96,168
s_waitcnt lgkmcnt(4)
s_add_u32 m0, s66, 32768
s_add_u32 s72, s67, 128
buffer_load_dwordx4 v1, s[12:15], s72 offen lds
Zavv 24,96,176
s_waitcnt lgkmcnt(2)
Zavv 28,96,184
s_waitcnt lgkmcnt(1)
s_add_u32 m0, s66, 34816
s_add_u32 s72, s67, 16512
buffer_load_dwordx4 v1, s[12:15], s72 offen lds
Zavv 32,128,104
Zavv 36,136,104
s_add_u32 m0, s66, 36864
s_add_u32 s72, s67, 32896
buffer_load_dwordx4 v1, s[12:15], s72 offen lds
Zavv 40,144,104
Zavv 44,152,104
s_add_u32 m0, s66, 38912
s_add_u32 s72, s67, 49280
buffer_load_dwordx4 v1, s[12:15], s72 offen lds
Zavv 48,104,160
s_waitcnt vmcnt(18)
Zavv 52,104,168
s_barrier
Zavv 56,104,176
s_add_u32 m0, s66, 40960
s_add_u32 s72, s67, 65664
ds_read_b128 v[120:123], v5 offset:6144
buffer_load_dwordx4 v1, s[12:15], s72 offen lds
Zavv 60,104,184
s_waitcnt lgkmcnt(1)
Zavv 64,128,112
s_add_u32 m0, s66, 43008
s_add_u32 s72, s67, 82048
ds_read_b128 v[96:99], v5 offset:8192
buffer_load_dwordx4 v1, s[12:15], s72 offen lds
Zavv 68,136,112
Zavv 72,144,112
s_add_u32 m0, s66, 45056
s_add_u32 s72, s67, 98432
buffer_load_dwordx4 v1, s[12:15], s72 offen lds
Zavv 76,152,112
Zavv 80,112,160
s_add_u32 m0, s66, 47104
s_add_u32 s72, s67, 114816
buffer_load_dwordx4 v1, s[12:15], s72 offen lds
Zavv 84,112,168
Zavv 88,112,176
buffer_load_dwordx4 v[32:35], v79, s[8:11], s73 offen
Zavv 92,112,184
s_waitcnt lgkmcnt(1)
Zavv 96,128,120
ds_read_b128 v[104:107], v5 offset:10240
buffer_load_dwordx4 v[36:39], v79, s[8:11], s73 offen offset:512
Zavv 100,136,120
s_waitcnt vmcnt(20)
Zavv 104,144,120
s_barrier
Zavv 108,152,120
ds_read_b64_tr_b16 v[132:133], v9 offset:16384
buffer_load_dwordx4 v[40:43], v79, s[8:11], s73 offen offset:1024
Zavv 112,120,160
ds_read_b64_tr_b16 v[134:135], v9 offset:16640
Zavv 116,120,168
ds_read_b64_tr_b16 v[140:141], v10 offset:16384
buffer_load_dwordx4 v[44:47], v79, s[8:11], s73 offen offset:1536
Zavv 120,120,176
s_waitcnt lgkmcnt(4)
ds_read_b64_tr_b16 v[142:143], v10 offset:16640
Zavv 124,120,184
ds_read_b64_tr_b16 v[148:149], v9 offset:16896
buffer_load_dwordx4 v[48:51], v84, s[32:35], 0 offen
Zavv 128,128,96
ds_read_b128 v[112:115], v5 offset:12288
Zavv 132,136,96
ds_read_b64_tr_b16 v[150:151], v9 offset:17152
buffer_load_dwordx4 v[52:55], v84, s[32:35], 0 offen offset:512
Zavv 136,144,96
ds_read_b64_tr_b16 v[156:157], v10 offset:16896
Zavv 140,152,96
ds_read_b64_tr_b16 v[158:159], v10 offset:17152
buffer_load_dwordx4 v[56:59], v84, s[32:35], 0 offen offset:1024
Zavv 144,96,160
ds_read_b64_tr_b16 v[164:165], v9 offset:18432
Zavv 148,96,168
ds_read_b64_tr_b16 v[166:167], v9 offset:18688
buffer_load_dwordx4 v[60:63], v84, s[32:35], 0 offen offset:1536
Zavv 152,96,176
ds_read_b64_tr_b16 v[172:173], v10 offset:18432
Zavv 156,96,184
s_waitcnt lgkmcnt(12)
ds_read_b64_tr_b16 v[174:175], v10 offset:18688
Zavv 160,128,104
ds_read_b128 v[120:123], v5 offset:14336
Zavv 164,136,104
ds_read_b64_tr_b16 v[180:181], v9 offset:18944
Zavv 168,144,104
s_waitcnt lgkmcnt(12)
ds_read_b128 v[100:103], v6 offset:0
Zavv 172,152,104
ds_read_b64_tr_b16 v[182:183], v9 offset:19200
Zavv 176,104,160
ds_read_b128 v[108:111], v6 offset:2048
Zavv 180,104,168
s_waitcnt lgkmcnt(12)
ds_read_b64_tr_b16 v[188:189], v10 offset:18944
Zavv 184,104,176
ds_read_b128 v[116:119], v6 offset:4096
Zavv 188,104,184
ds_read_b64_tr_b16 v[190:191], v10 offset:19200
Zavv 192,128,112
s_waitcnt lgkmcnt(12)
ds_read_b128 v[124:127], v6 offset:6144
Zavv 196,136,112
Zavv 200,144,112
Zavv 204,152,112
s_waitcnt vmcnt(18) lgkmcnt(0)
Zavv 208,112,160
s_barrier
Zavv 212,112,168
s_add_u32 m0, s70, 98304
s_add_u32 s72, s68, 1048576
ds_read_b64_tr_b16 v[192:193], v9 offset:32768
buffer_load_dwordx4 v3, s[20:23], s72 offen lds
Zavv 216,112,176
)"
R"(
ds_read_b64_tr_b16 v[194:195], v9 offset:33024
Zavv 220,112,184
s_add_u32 m0, s70, 99200
s_add_u32 s72, s68, 1048576
ds_read_b64_tr_b16 v[196:197], v9 offset:49152
buffer_load_dwordx4 v3, s[20:23], s72 offen offset:128 lds
Zavv 224,128,120
ds_read_b64_tr_b16 v[198:199], v9 offset:49408
Zavv 228,136,120
s_add_u32 m0, s70, 100352
s_add_u32 s72, s68, 1048576
ds_read_b64_tr_b16 v[200:201], v10 offset:32768
buffer_load_dwordx4 v3, s[24:27], s72 offen lds
Zavv 232,144,120
ds_read_b64_tr_b16 v[202:203], v10 offset:33024
Zavv 236,152,120
s_add_u32 m0, s70, 101248
s_add_u32 s72, s68, 1048576
ds_read_b64_tr_b16 v[204:205], v10 offset:49152
buffer_load_dwordx4 v3, s[24:27], s72 offen offset:128 lds
Zavv 240,120,160
ds_read_b64_tr_b16 v[206:207], v10 offset:49408
Zavv 244,120,168
s_add_u32 m0, s70, 114688
s_add_u32 s72, s68, 1310720
ds_read_b64_tr_b16 v[208:209], v9 offset:33280
buffer_load_dwordx4 v3, s[20:23], s72 offen lds
Zavv 248,120,176
ds_read_b64_tr_b16 v[210:211], v9 offset:33536
Zavv 252,120,184
s_add_u32 m0, s70, 115584
s_add_u32 s72, s68, 1310720
ds_read_b64_tr_b16 v[212:213], v9 offset:49664
buffer_load_dwordx4 v3, s[20:23], s72 offen offset:128 lds
Mavv 0,132,100
ds_read_b64_tr_b16 v[214:215], v9 offset:49920
Mavv 4,140,100
s_add_u32 m0, s70, 116736
s_add_u32 s72, s68, 1310720
ds_read_b64_tr_b16 v[216:217], v10 offset:33280
buffer_load_dwordx4 v3, s[24:27], s72 offen lds
Mavv 8,148,100
ds_read_b64_tr_b16 v[218:219], v10 offset:33536
Mavv 12,156,100
s_add_u32 m0, s70, 117632
s_add_u32 s72, s68, 1310720
ds_read_b64_tr_b16 v[220:221], v10 offset:49664
buffer_load_dwordx4 v3, s[24:27], s72 offen offset:128 lds
Mavv 16,100,164
s_waitcnt lgkmcnt(12)
ds_read_b64_tr_b16 v[222:223], v10 offset:49920
Mavv 20,100,172
s_add_u32 m0, s66, 65536
s_add_u32 s72, s67, 256
ds_read_b64_tr_b16 v[224:225], v9 offset:34816
buffer_load_dwordx4 v1, s[12:15], s72 offen lds
Mavv 24,100,180
s_waitcnt lgkmcnt(11)
ds_read_b64_tr_b16 v[226:227], v9 offset:35072
Mavv 28,100,188
s_add_u32 m0, s66, 67584
s_add_u32 s72, s67, 16640
ds_read_b64_tr_b16 v[228:229], v9 offset:51200
buffer_load_dwordx4 v1, s[12:15], s72 offen lds
Mavv 32,132,108
ds_read_b128 v[100:103], v6 offset:8192
Mavv 36,140,108
s_add_u32 m0, s66, 69632
s_add_u32 s72, s67, 33024
ds_read_b64_tr_b16 v[230:231], v9 offset:51456
buffer_load_dwordx4 v1, s[12:15], s72 offen lds
Mavv 40,148,108
s_waitcnt lgkmcnt(12)
ds_read_b64_tr_b16 v[232:233], v10 offset:34816
Mavv 44,156,108
s_add_u32 m0, s66, 71680
s_add_u32 s72, s67, 49408
ds_read_b64_tr_b16 v[234:235], v10 offset:35072
buffer_load_dwordx4 v1, s[12:15], s72 offen lds
Mavv 48,108,164
s_waitcnt lgkmcnt(11)
ds_read_b64_tr_b16 v[236:237], v10 offset:51200
Mavv 52,108,172
s_add_u32 m0, s66, 73728
s_add_u32 s72, s67, 65792
ds_read_b64_tr_b16 v[238:239], v10 offset:51456
buffer_load_dwordx4 v1, s[12:15], s72 offen lds
Mavv 56,108,180
ds_read_b64_tr_b16 v[240:241], v9 offset:35328
Mavv 60,108,188
s_add_u32 m0, s66, 75776
s_add_u32 s72, s67, 82176
ds_read_b64_tr_b16 v[242:243], v9 offset:35584
buffer_load_dwordx4 v1, s[12:15], s72 offen lds
Mavv 64,132,116
s_waitcnt lgkmcnt(12)
ds_read_b128 v[108:111], v6 offset:10240
Mavv 68,140,116
s_add_u32 m0, s66, 77824
s_add_u32 s72, s67, 98560
ds_read_b64_tr_b16 v[244:245], v9 offset:51712
buffer_load_dwordx4 v1, s[12:15], s72 offen lds
Mavv 72,148,116
s_waitcnt lgkmcnt(11)
ds_read_b64_tr_b16 v[246:247], v9 offset:51968
Mavv 76,156,116
s_add_u32 m0, s66, 79872
s_add_u32 s72, s67, 114944
ds_read_b64_tr_b16 v[248:249], v10 offset:35328
buffer_load_dwordx4 v1, s[12:15], s72 offen lds
Mavv 80,116,164
ds_read_b64_tr_b16 v[250:251], v10 offset:35584
Mavv 84,116,172
ds_read_b64_tr_b16 v[252:253], v10 offset:51712
Mavv 88,116,180
s_waitcnt lgkmcnt(12)
ds_read_b64_tr_b16 v[254:255], v10 offset:51968
Mavv 92,116,188
Mavv 96,132,124
ds_read_b128 v[116:119], v6 offset:12288
Mavv 100,140,124
Mavv 104,148,124
Mavv 108,156,124
Mavv 112,124,164
Mavv 116,124,172
Mavv 120,124,180
Mavv 124,124,188
Mavv 128,132,100
ds_read_b128 v[124:127], v6 offset:14336
Mavv 132,140,100
Mavv 136,148,100
Mavv 140,156,100
Mavv 144,100,164
Mavv 148,100,172
Mavv 152,100,180
s_waitcnt vmcnt(24) lgkmcnt(0)
Mavv 156,100,188
s_barrier
Mavv 160,132,108
Mavv 164,140,108
s_add_u32 s72, s76, 2097152
ds_read_b128 v[96:99], v5 offset:32768
Mavv 168,148,108
buffer_load_dwordx4 v[80:83], v0, s[20:23], s72 offen
Mavv 172,156,108
s_add_u32 s72, s76, 2097152
ds_read_b128 v[100:103], v6 offset:32768
Mavv 176,108,164
buffer_load_dwordx4 v[80:83], v0, s[24:27], s72 offen
Mavv 180,108,172
s_add_u32 m0, s70, 131072
s_add_u32 s72, s68, 1572864
Mavv 184,108,180
buffer_load_dwordx4 v3, s[20:23], s72 offen lds
Mavv 188,108,188
Mavv 192,132,116
Mavv 196,140,116
Mavv 200,148,116
s_add_u32 m0, s70, 131968
s_add_u32 s72, s68, 1572864
Mavv 204,156,116
buffer_load_dwordx4 v3, s[20:23], s72 offen offset:128 lds
Mavv 208,116,164
Mavv 212,116,172
ds_read_b128 v[104:107], v5 offset:34816
Mavv 216,116,180
Mavv 220,116,188
s_add_u32 m0, s70, 133120
s_add_u32 s72, s68, 1572864
ds_read_b128 v[108:111], v6 offset:34816
Mavv 224,132,124
buffer_load_dwordx4 v3, s[24:27], s72 offen lds
Mavv 228,140,124
Mavv 232,148,124
Mavv 236,156,124
Mavv 240,124,164
s_add_u32 m0, s70, 134016
s_add_u32 s72, s68, 1572864
Mavv 244,124,172
buffer_load_dwordx4 v3, s[24:27], s72 offen offset:128 lds
Mavv 248,124,180
Mavv 252,124,188
s_waitcnt lgkmcnt(3)
Mavv 0,192,96
s_waitcnt lgkmcnt(2)
ds_read_b128 v[112:115], v5 offset:36864
Mavv 0,196,100
Mavv 4,200,96
s_add_u32 m0, s70, 147456
s_add_u32 s72, s68, 1835008
ds_read_b128 v[116:119], v6 offset:36864
Mavv 4,204,100
buffer_load_dwordx4 v3, s[20:23], s72 offen lds
Mavv 8,208,96
Mavv 8,212,100
Mavv 12,216,96
Mavv 12,220,100
s_add_u32 m0, s70, 148352
s_add_u32 s72, s68, 1835008
Mavv 16,96,224
buffer_load_dwordx4 v3, s[20:23], s72 offen offset:128 lds
Mavv 16,100,228
Mavv 20,96,232
Mavv 20,100,236
Mavv 24,96,240
s_add_u32 m0, s70, 149504
s_add_u32 s72, s68, 1835008
Mavv 24,100,244
buffer_load_dwordx4 v3, s[24:27], s72 offen lds
Mavv 28,96,248
Mavv 28,100,252
s_waitcnt lgkmcnt(3)
Mavv 32,192,104
s_waitcnt lgkmcnt(2)
ds_read_b128 v[120:123], v5 offset:38912
Mavv 32,196,108
Mavv 36,200,104
s_add_u32 m0, s70, 150400
s_add_u32 s72, s68, 1835008
ds_read_b128 v[124:127], v6 offset:38912
Mavv 36,204,108
buffer_load_dwordx4 v3, s[24:27], s72 offen offset:128 lds
Mavv 40,208,104
Mavv 40,212,108
Mavv 44,216,104
Mavv 44,220,108
s_add_u32 m0, s66, 0
s_add_u32 s72, s67, 384
Mavv 48,104,224
buffer_load_dwordx4 v1, s[12:15], s72 offen lds
Mavv 48,108,228
Mavv 52,104,232
Mavv 52,108,236
Mavv 56,104,240
s_add_u32 m0, s66, 2048
s_add_u32 s72, s67, 16768
Mavv 56,108,244
buffer_load_dwordx4 v1, s[12:15], s72 offen lds
Mavv 60,104,248
Mavv 60,108,252
s_waitcnt lgkmcnt(3)
Mavv 64,192,112
s_waitcnt lgkmcnt(2)
ds_read_b128 v[96:99], v5 offset:40960
Mavv 64,196,116
Mavv 68,200,112
s_add_u32 m0, s66, 4096
s_add_u32 s72, s67, 33152
ds_read_b128 v[100:103], v6 offset:40960
Mavv 68,204,116
buffer_load_dwordx4 v1, s[12:15], s72 offen lds
Mavv 72,208,112
Mavv 72,212,116
Mavv 76,216,112
Mavv 76,220,116
s_add_u32 m0, s66, 6144
s_add_u32 s72, s67, 49536
Mavv 80,112,224
buffer_load_dwordx4 v1, s[12:15], s72 offen lds
Mavv 80,116,228
Mavv 84,112,232
Mavv 84,116,236
Mavv 88,112,240
s_add_u32 m0, s66, 8192
s_add_u32 s72, s67, 65920
Mavv 88,116,244
buffer_load_dwordx4 v1, s[12:15], s72 offen lds
Mavv 92,112,248
Mavv 92,116,252
s_waitcnt lgkmcnt(3)
Mavv 96,192,120
s_waitcnt lgkmcnt(2)
ds_read_b128 v[104:107], v5 offset:43008
Mavv 96,196,124
Mavv 100,200,120
s_add_u32 m0, s66, 10240
s_add_u32 s72, s67, 82304
ds_read_b128 v[108:111], v6 offset:43008
Mavv 100,204,124
s_waitcnt vmcnt(23)
buffer_load_dwordx4 v1, s[12:15], s72 offen lds
Mavv 104,208,120
s_barrier
Mavv 104,212,124
ds_read_b64_tr_b16 v[128:129], v9 offset:0
Mavv 108,216,120
ds_read_b64_tr_b16 v[130:131], v9 offset:256
Mavv 108,220,124
ds_read_b64_tr_b16 v[132:133], v9 offset:16384
Mavv 112,120,224
ds_read_b64_tr_b16 v[134:135], v9 offset:16640
Mavv 112,124,228
ds_read_b64_tr_b16 v[136:137], v10 offset:0
Mavv 116,120,232
ds_read_b64_tr_b16 v[138:139], v10 offset:256
Mavv 116,124,236
ds_read_b64_tr_b16 v[140:141], v10 offset:16384
Mavv 120,120,240
ds_read_b64_tr_b16 v[142:143], v10 offset:16640
Mavv 120,124,244
ds_read_b64_tr_b16 v[144:145], v9 offset:512
Mavv 124,120,248
ds_read_b64_tr_b16 v[146:147], v9 offset:768
v_mov_b32 v64, 0x3f803f80
Mavv 124,124,252
s_waitcnt lgkmcnt(13)
ds_read_b64_tr_b16 v[148:149], v9 offset:16896
v_mov_b32 v65, 0x3f803f80
Mavv 128,192,96
s_waitcnt lgkmcnt(13)
ds_read_b128 v[112:115], v5 offset:45056
v_mov_b32 v66, 0x3f803f80
Mavv 128,196,100
ds_read_b64_tr_b16 v[150:151], v9 offset:17152
v_mov_b32 v67, 0x3f803f80
Mavv 132,200,96
s_waitcnt lgkmcnt(12)
ds_read_b128 v[116:119], v6 offset:45056
Mavv 132,204,100
ds_read_b64_tr_b16 v[152:153], v10 offset:512
Zvvv 68,64,32
Mavv 136,208,96
ds_read_b64_tr_b16 v[154:155], v10 offset:768
Mvvv 68,64,36
Mavv 136,212,100
s_waitcnt lgkmcnt(12)
ds_read_b64_tr_b16 v[156:157], v10 offset:16896
Mvvv 68,64,40
Mavv 140,216,96
ds_read_b64_tr_b16 v[158:159], v10 offset:17152
Mvvv 68,64,44
Mavv 140,220,100
ds_read_b64_tr_b16 v[160:161], v9 offset:2048
Mavv 144,96,224
s_waitcnt lgkmcnt(12)
ds_read_b64_tr_b16 v[162:163], v9 offset:2304
Mavv 144,100,228
ds_read_b64_tr_b16 v[164:165], v9 offset:18432
Mavv 148,96,232
ds_read_b64_tr_b16 v[166:167], v9 offset:18688
Mavv 148,100,236
s_waitcnt lgkmcnt(12)
ds_read_b64_tr_b16 v[168:169], v10 offset:2048
Mavv 152,96,240
ds_read_b64_tr_b16 v[170:171], v10 offset:2304
Mavv 152,100,244
ds_read_b64_tr_b16 v[172:173], v10 offset:18432
Mavv 156,96,248
s_waitcnt lgkmcnt(12)
ds_read_b64_tr_b16 v[174:175], v10 offset:18688
Mavv 156,100,252
ds_read_b64_tr_b16 v[176:177], v9 offset:2560
v_mov_b32 v72, v68
Mavv 160,192,104
ds_read_b128 v[120:123], v5 offset:47104
Mavv 160,196,108
s_waitcnt lgkmcnt(12)
ds_read_b64_tr_b16 v[178:179], v9 offset:2816
v_add_f32_dpp v72, v72, v72 quad_perm:[1,0,3,2] row_mask:0xf bank_mask:0xf bound_ctrl:1
Mavv 164,200,104
ds_read_b128 v[124:127], v6 offset:47104
Mavv 164,204,108
ds_read_b64_tr_b16 v[180:181], v9 offset:18944
v_add_f32_dpp v72, v72, v72 quad_perm:[2,3,0,1] row_mask:0xf bank_mask:0xf bound_ctrl:1
Mavv 168,208,104
s_waitcnt lgkmcnt(12)
ds_read_b64_tr_b16 v[182:183], v9 offset:19200
Mavv 168,212,108
ds_read_b64_tr_b16 v[184:185], v10 offset:2560
v_add_f32_dpp v72, v72, v72 row_half_mirror row_mask:0xf bank_mask:0xf bound_ctrl:1
Mavv 172,216,104
ds_read_b64_tr_b16 v[186:187], v10 offset:2816
v_mul_f32 v72, 0x3a800000, v72
Mavv 172,220,108
s_waitcnt lgkmcnt(12)
ds_read_b64_tr_b16 v[188:189], v10 offset:18944
v_cvt_pk_bf16_f32 v73, 0, v72
Mavv 176,104,224
s_add_u32 m0, s66, 12288
s_add_u32 s72, s67, 98688
ds_read_b64_tr_b16 v[190:191], v10 offset:19200
Mavv 176,108,228
buffer_load_dwordx4 v1, s[12:15], s72 offen lds
Mavv 180,104,232
s_add_u32 s72, s76, 3145728
v_sub_f32 v72, v72, v73
v_mul_f32 v72, v72, v72
Mavv 180,108,236
buffer_load_dwordx4 v[80:83], v0, s[20:23], s72 offen
Mavv 184,104,240
v_mul_f32 v72, 0xc2000000, v72
v_lshlrev_b32 v75, 16, v32
Mavv 184,108,244
s_add_u32 m0, s66, 14336
s_add_u32 s72, s67, 115072
v_and_b32 v76, 0xffff0000, v32
Mavv 188,104,248
buffer_load_dwordx4 v1, s[12:15], s72 offen lds
Mavv 188,108,252
s_add_u32 s72, s76, 3145728
v_sub_f32 v75, v75, v73
v_sub_f32 v76, v76, v73
Mavv 192,192,112
buffer_load_dwordx4 v[80:83], v0, s[24:27], s72 offen
Mavv 192,196,116
s_add_u32 s72, s76, 0
v_fmac_f32 v72, v75, v75
v_cvt_pk_bf16_f32 v32, v75, v76
Mavv 196,200,112
buffer_load_dwordx4 v[80:83], v0, s[16:19], s72 offen
Mavv 196,204,116
s_waitcnt vmcnt(21) lgkmcnt(0)
v_fmac_f32 v72, v76, v76
v_lshlrev_b32 v75, 16, v33
Mavv 200,208,112
s_barrier
Mavv 200,212,116
v_and_b32 v76, 0xffff0000, v33
v_sub_f32 v75, v75, v73
Mavv 204,216,112
ds_read_b128 v[96:99], v7 offset:0
v_sub_f32 v76, v76, v73
Mavv 204,220,116
v_fmac_f32 v72, v75, v75
v_cvt_pk_bf16_f32 v33, v75, v76
Mavv 208,112,224
ds_read_b128 v[100:103], v8 offset:0
v_fmac_f32 v72, v76, v76
Mavv 208,116,228
s_add_u32 m0, s70, 98304
)"
R"(
s_add_u32 s72, s68, 2097152
v_lshlrev_b32 v75, 16, v34
Mavv 212,112,232
buffer_load_dwordx4 v3, s[20:23], s72 offen lds
Mavv 212,116,236
v_and_b32 v76, 0xffff0000, v34
v_sub_f32 v75, v75, v73
Mavv 216,112,240
v_sub_f32 v76, v76, v73
v_fmac_f32 v72, v75, v75
Mavv 216,116,244
v_cvt_pk_bf16_f32 v34, v75, v76
v_fmac_f32 v72, v76, v76
Mavv 220,112,248
s_add_u32 m0, s70, 99200
s_add_u32 s72, s68, 2097152
v_lshlrev_b32 v75, 16, v35
Mavv 220,116,252
buffer_load_dwordx4 v3, s[20:23], s72 offen offset:128 lds
Mavv 224,192,120
v_and_b32 v76, 0xffff0000, v35
v_sub_f32 v75, v75, v73
Mavv 224,196,124
v_sub_f32 v76, v76, v73
v_fmac_f32 v72, v75, v75
Mavv 228,200,120
ds_read_b128 v[104:107], v7 offset:2048
v_cvt_pk_bf16_f32 v35, v75, v76
Mavv 228,204,124
v_fmac_f32 v72, v76, v76
v_lshlrev_b32 v75, 16, v36
Mavv 232,208,120
s_add_u32 m0, s70, 100352
s_add_u32 s72, s68, 2097152
ds_read_b128 v[108:111], v8 offset:2048
Mavv 232,212,124
buffer_load_dwordx4 v3, s[24:27], s72 offen lds
Mavv 236,216,120
v_and_b32 v76, 0xffff0000, v36
v_sub_f32 v75, v75, v73
Mavv 236,220,124
v_sub_f32 v76, v76, v73
v_fmac_f32 v72, v75, v75
Mavv 240,120,224
v_cvt_pk_bf16_f32 v36, v75, v76
v_fmac_f32 v72, v76, v76
Mavv 240,124,228
s_add_u32 m0, s70, 101248
s_add_u32 s72, s68, 2097152
v_lshlrev_b32 v75, 16, v37
Mavv 244,120,232
buffer_load_dwordx4 v3, s[24:27], s72 offen offset:128 lds
Mavv 244,124,236
v_and_b32 v76, 0xffff0000, v37
v_sub_f32 v75, v75, v73
Mavv 248,120,240
v_sub_f32 v76, v76, v73
v_fmac_f32 v72, v75, v75
Mavv 248,124,244
v_cvt_pk_bf16_f32 v37, v75, v76
v_fmac_f32 v72, v76, v76
Mavv 252,120,248
s_add_u32 m0, s70, 114688
s_add_u32 s72, s68, 2359296
v_lshlrev_b32 v75, 16, v38
Mavv 252,124,252
s_waitcnt lgkmcnt(3)
buffer_load_dwordx4 v3, s[20:23], s72 offen lds
Mavv 0,128,96
s_waitcnt lgkmcnt(2)
ds_read_b128 v[112:115], v7 offset:4096
v_and_b32 v76, 0xffff0000, v38
Mavv 0,132,100
v_sub_f32 v75, v75, v73
v_sub_f32 v76, v76, v73
Mavv 4,136,96
ds_read_b128 v[116:119], v8 offset:4096
v_fmac_f32 v72, v75, v75
Mavv 4,140,100
s_add_u32 m0, s70, 115584
s_add_u32 s72, s68, 2359296
v_cvt_pk_bf16_f32 v38, v75, v76
Mavv 8,144,96
buffer_load_dwordx4 v3, s[20:23], s72 offen offset:128 lds
Mavv 8,148,100
v_fmac_f32 v72, v76, v76
v_lshlrev_b32 v75, 16, v39
Mavv 12,152,96
v_and_b32 v76, 0xffff0000, v39
v_sub_f32 v75, v75, v73
Mavv 12,156,100
v_sub_f32 v76, v76, v73
v_fmac_f32 v72, v75, v75
Mavv 16,96,160
s_add_u32 m0, s70, 116736
s_add_u32 s72, s68, 2359296
v_cvt_pk_bf16_f32 v39, v75, v76
Mavv 16,100,164
buffer_load_dwordx4 v3, s[24:27], s72 offen lds
Mavv 20,96,168
v_fmac_f32 v72, v76, v76
v_lshlrev_b32 v75, 16, v40
Mavv 20,100,172
v_and_b32 v76, 0xffff0000, v40
v_sub_f32 v75, v75, v73
Mavv 24,96,176
v_sub_f32 v76, v76, v73
v_fmac_f32 v72, v75, v75
Mavv 24,100,180
s_add_u32 m0, s70, 117632
s_add_u32 s72, s68, 2359296
v_cvt_pk_bf16_f32 v40, v75, v76
Mavv 28,96,184
buffer_load_dwordx4 v3, s[24:27], s72 offen offset:128 lds
Mavv 28,100,188
s_waitcnt lgkmcnt(3)
v_fmac_f32 v72, v76, v76
v_lshlrev_b32 v75, 16, v41
Mavv 32,128,104
s_waitcnt lgkmcnt(2)
ds_read_b128 v[120:123], v7 offset:6144
v_and_b32 v76, 0xffff0000, v41
Mavv 32,132,108
v_sub_f32 v75, v75, v73
v_sub_f32 v76, v76, v73
Mavv 36,136,104
s_add_u32 m0, s66, 32768
s_add_u32 s72, s67, 512
ds_read_b128 v[124:127], v8 offset:6144
Mavv 36,140,108
buffer_load_dwordx4 v1, s[12:15], s72 offen lds
Mavv 40,144,104
v_fmac_f32 v72, v75, v75
v_cvt_pk_bf16_f32 v41, v75, v76
Mavv 40,148,108
v_fmac_f32 v72, v76, v76
v_lshlrev_b32 v75, 16, v42
Mavv 44,152,104
v_and_b32 v76, 0xffff0000, v42
v_sub_f32 v75, v75, v73
Mavv 44,156,108
s_add_u32 m0, s66, 34816
s_add_u32 s72, s67, 16896
v_sub_f32 v76, v76, v73
Mavv 48,104,160
buffer_load_dwordx4 v1, s[12:15], s72 offen lds
Mavv 48,108,164
v_fmac_f32 v72, v75, v75
v_cvt_pk_bf16_f32 v42, v75, v76
Mavv 52,104,168
v_fmac_f32 v72, v76, v76
v_lshlrev_b32 v75, 16, v43
Mavv 52,108,172
v_and_b32 v76, 0xffff0000, v43
v_sub_f32 v75, v75, v73
Mavv 56,104,176
s_add_u32 m0, s66, 36864
s_add_u32 s72, s67, 33280
v_sub_f32 v76, v76, v73
Mavv 56,108,180
buffer_load_dwordx4 v1, s[12:15], s72 offen lds
Mavv 60,104,184
v_fmac_f32 v72, v75, v75
v_cvt_pk_bf16_f32 v43, v75, v76
Mavv 60,108,188
s_waitcnt lgkmcnt(3)
v_fmac_f32 v72, v76, v76
v_lshlrev_b32 v75, 16, v44
Mavv 64,128,112
s_waitcnt lgkmcnt(2)
ds_read_b128 v[96:99], v7 offset:8192
v_and_b32 v76, 0xffff0000, v44
Mavv 64,132,116
v_sub_f32 v75, v75, v73
v_sub_f32 v76, v76, v73
Mavv 68,136,112
s_add_u32 m0, s66, 38912
s_add_u32 s72, s67, 49664
ds_read_b128 v[100:103], v8 offset:8192
Mavv 68,140,116
buffer_load_dwordx4 v1, s[12:15], s72 offen lds
Mavv 72,144,112
v_fmac_f32 v72, v75, v75
v_cvt_pk_bf16_f32 v44, v75, v76
Mavv 72,148,116
v_fmac_f32 v72, v76, v76
v_lshlrev_b32 v75, 16, v45
Mavv 76,152,112
v_and_b32 v76, 0xffff0000, v45
v_sub_f32 v75, v75, v73
Mavv 76,156,116
s_add_u32 m0, s66, 40960
s_add_u32 s72, s67, 66048
v_sub_f32 v76, v76, v73
Mavv 80,112,160
buffer_load_dwordx4 v1, s[12:15], s72 offen lds
Mavv 80,116,164
v_fmac_f32 v72, v75, v75
v_cvt_pk_bf16_f32 v45, v75, v76
Mavv 84,112,168
v_fmac_f32 v72, v76, v76
v_lshlrev_b32 v75, 16, v46
Mavv 84,116,172
v_and_b32 v76, 0xffff0000, v46
v_sub_f32 v75, v75, v73
Mavv 88,112,176
s_add_u32 m0, s66, 43008
s_add_u32 s72, s67, 82432
v_sub_f32 v76, v76, v73
Mavv 88,116,180
buffer_load_dwordx4 v1, s[12:15], s72 offen lds
Mavv 92,112,184
v_fmac_f32 v72, v75, v75
v_cvt_pk_bf16_f32 v46, v75, v76
Mavv 92,116,188
s_waitcnt lgkmcnt(3)
v_fmac_f32 v72, v76, v76
v_lshlrev_b32 v75, 16, v47
Mavv 96,128,120
s_waitcnt lgkmcnt(2)
ds_read_b128 v[104:107], v7 offset:10240
v_and_b32 v76, 0xffff0000, v47
Mavv 96,132,124
v_sub_f32 v75, v75, v73
v_sub_f32 v76, v76, v73
Mavv 100,136,120
s_add_u32 m0, s66, 45056
s_add_u32 s72, s67, 98816
ds_read_b128 v[108:111], v8 offset:10240
Mavv 100,140,124
s_waitcnt vmcnt(25)
buffer_load_dwordx4 v1, s[12:15], s72 offen lds
Mavv 104,144,120
s_barrier
Mavv 104,148,124
ds_read_b64_tr_b16 v[192:193], v9 offset:32768
v_fmac_f32 v72, v75, v75
Mavv 108,152,120
ds_read_b64_tr_b16 v[194:195], v9 offset:33024
v_cvt_pk_bf16_f32 v47, v75, v76
Mavv 108,156,124
ds_read_b64_tr_b16 v[196:197], v9 offset:49152
v_fmac_f32 v72, v76, v76
Mavv 112,120,160
ds_read_b64_tr_b16 v[198:199], v9 offset:49408
Mavv 112,124,164
ds_read_b64_tr_b16 v[200:201], v10 offset:32768
v_add_f32_dpp v72, v72, v72 quad_perm:[1,0,3,2] row_mask:0xf bank_mask:0xf bound_ctrl:1
Mavv 116,120,168
ds_read_b64_tr_b16 v[202:203], v10 offset:33024
Mavv 116,124,172
ds_read_b64_tr_b16 v[204:205], v10 offset:49152
v_add_f32_dpp v72, v72, v72 quad_perm:[2,3,0,1] row_mask:0xf bank_mask:0xf bound_ctrl:1
Mavv 120,120,176
ds_read_b64_tr_b16 v[206:207], v10 offset:49408
Mavv 120,124,180
ds_read_b64_tr_b16 v[208:209], v9 offset:33280
v_add_f32_dpp v72, v72, v72 row_half_mirror row_mask:0xf bank_mask:0xf bound_ctrl:1
Mavv 124,120,184
ds_read_b64_tr_b16 v[210:211], v9 offset:33536
v_mov_b32 v75, v72
Mavv 124,124,188
s_waitcnt lgkmcnt(13)
ds_read_b64_tr_b16 v[212:213], v9 offset:49664
Mavv 128,128,96
s_waitcnt lgkmcnt(13)
ds_read_b128 v[112:115], v7 offset:12288
v_permlane16_swap_b32 v72, v75
Mavv 128,132,100
ds_read_b64_tr_b16 v[214:215], v9 offset:49920
v_add_f32 v72, v72, v75
Mavv 132,136,96
s_waitcnt lgkmcnt(12)
ds_read_b128 v[116:119], v8 offset:12288
v_mov_b32 v75, v72
Mavv 132,140,100
ds_read_b64_tr_b16 v[216:217], v10 offset:33280
Mavv 136,144,96
ds_read_b64_tr_b16 v[218:219], v10 offset:33536
v_permlane32_swap_b32 v72, v75
Mavv 136,148,100
s_waitcnt lgkmcnt(12)
ds_read_b64_tr_b16 v[220:221], v10 offset:49664
v_add_f32 v72, v72, v75
Mavv 140,152,96
ds_read_b64_tr_b16 v[222:223], v10 offset:49920
v_mul_f32 v72, 0x3a800000, v72
Mavv 140,156,100
ds_read_b64_tr_b16 v[224:225], v9 offset:34816
v_cvt_pk_bf16_f32 v72, 0, v72
Mavv 144,96,160
s_waitcnt lgkmcnt(12)
ds_read_b64_tr_b16 v[226:227], v9 offset:35072
v_add_f32 v72, 0x3727c5ac, v72
Mavv 144,100,164
ds_read_b64_tr_b16 v[228:229], v9 offset:51200
v_cvt_pk_bf16_f32 v72, 0, v72
Mavv 148,96,168
ds_read_b64_tr_b16 v[230:231], v9 offset:51456
Mavv 148,100,172
s_waitcnt lgkmcnt(12)
ds_read_b64_tr_b16 v[232:233], v10 offset:34816
Mavv 152,96,176
ds_read_b64_tr_b16 v[234:235], v10 offset:35072
Mavv 152,100,180
ds_read_b64_tr_b16 v[236:237], v10 offset:51200
Mavv 156,96,184
s_waitcnt lgkmcnt(12)
ds_read_b64_tr_b16 v[238:239], v10 offset:51456
Mavv 156,100,188
ds_read_b64_tr_b16 v[240:241], v9 offset:35328
Mavv 160,128,104
ds_read_b128 v[120:123], v7 offset:14336
Mavv 160,132,108
s_waitcnt lgkmcnt(12)
ds_read_b64_tr_b16 v[242:243], v9 offset:35584
Mavv 164,136,104
ds_read_b128 v[124:127], v8 offset:14336
Mavv 164,140,108
ds_read_b64_tr_b16 v[244:245], v9 offset:51712
Mavv 168,144,104
s_waitcnt lgkmcnt(12)
ds_read_b64_tr_b16 v[246:247], v9 offset:51968
Mavv 168,148,108
ds_read_b64_tr_b16 v[248:249], v10 offset:35328
Mavv 172,152,104
ds_read_b64_tr_b16 v[250:251], v10 offset:35584
Mavv 172,156,108
s_waitcnt lgkmcnt(12)
ds_read_b64_tr_b16 v[252:253], v10 offset:51712
Mavv 176,104,160
s_add_u32 m0, s66, 47104
s_add_u32 s72, s67, 115200
ds_read_b64_tr_b16 v[254:255], v10 offset:51968
Mavv 176,108,164
buffer_load_dwordx4 v1, s[12:15], s72 offen lds
Mavv 180,104,168
s_add_u32 s72, s76, 1048576
v_sqrt_f32 v74, v72
Mavv 180,108,172
buffer_load_dwordx4 v[80:83], v0, s[16:19], s72 offen
Mavv 184,104,176
s_add_u32 s72, s76, 2097152
v_cvt_pk_bf16_f32 v74, 0, v74
Mavv 184,108,180
buffer_load_dwordx4 v[80:83], v0, s[16:19], s72 offen
Mavv 188,104,184
s_add_u32 s72, s76, 3145728
v_rcp_f32 v74, v74
Mavv 188,108,188
buffer_load_dwordx4 v[80:83], v0, s[16:19], s72 offen
Mavv 192,128,112
v_lshlrev_b32 v75, 16, v32
v_and_b32 v76, 0xffff0000, v32
Mavv 192,132,116
v_mul_f32 v75, v75, v74
v_mul_f32 v76, v76, v74
Mavv 196,136,112
v_cvt_pk_bf16_f32 v32, v75, v76
v_lshlrev_b32 v75, 16, v33
Mavv 196,140,116
s_waitcnt vmcnt(21) lgkmcnt(0)
v_and_b32 v76, 0xffff0000, v33
v_mul_f32 v75, v75, v74
Mavv 200,144,112
s_barrier
Mavv 200,148,116
v_mul_f32 v76, v76, v74
v_cvt_pk_bf16_f32 v33, v75, v76
Mavv 204,152,112
ds_read_b128 v[96:99], v5 offset:0
v_lshlrev_b32 v75, 16, v34
Mavv 204,156,116
v_and_b32 v76, 0xffff0000, v34
v_mul_f32 v75, v75, v74
Mavv 208,112,160
ds_read_b128 v[100:103], v6 offset:0
v_mul_f32 v76, v76, v74
Mavv 208,116,164
s_add_u32 m0, s70, 131072
s_add_u32 s72, s68, 2621440
v_cvt_pk_bf16_f32 v34, v75, v76
Mavv 212,112,168
buffer_load_dwordx4 v3, s[20:23], s72 offen lds
Mavv 212,116,172
v_lshlrev_b32 v75, 16, v35
v_and_b32 v76, 0xffff0000, v35
Mavv 216,112,176
v_mul_f32 v75, v75, v74
v_mul_f32 v76, v76, v74
Mavv 216,116,180
v_cvt_pk_bf16_f32 v35, v75, v76
Mavv 220,112,184
s_add_u32 m0, s70, 131968
s_add_u32 s72, s68, 2621440
Mavv 220,116,188
buffer_load_dwordx4 v3, s[20:23], s72 offen offset:128 lds
Mavv 224,128,120
buffer_store_dwordx4 v[32:35], v77, s[48:51], s77 offen sc1
Mavv 224,132,124
v_lshlrev_b32 v75, 16, v36
v_and_b32 v76, 0xffff0000, v36
Mavv 228,136,120
ds_read_b128 v[104:107], v5 offset:2048
v_mul_f32 v75, v75, v74
Mavv 228,140,124
v_mul_f32 v76, v76, v74
v_cvt_pk_bf16_f32 v36, v75, v76
Mavv 232,144,120
s_add_u32 m0, s70, 133120
s_add_u32 s72, s68, 2621440
ds_read_b128 v[108:111], v6 offset:2048
Mavv 232,148,124
buffer_load_dwordx4 v3, s[24:27], s72 offen lds
Mavv 236,152,120
v_lshlrev_b32 v75, 16, v37
v_and_b32 v76, 0xffff0000, v37
Mavv 236,156,124
v_mul_f32 v75, v75, v74
v_mul_f32 v76, v76, v74
Mavv 240,120,160
v_cvt_pk_bf16_f32 v37, v75, v76
v_lshlrev_b32 v75, 16, v38
Mavv 240,124,164
s_add_u32 m0, s70, 134016
s_add_u32 s72, s68, 2621440
v_and_b32 v76, 0xffff0000, v38
Mavv 244,120,168
buffer_load_dwordx4 v3, s[24:27], s72 offen offset:128 lds
Mavv 244,124,172
v_mul_f32 v75, v75, v74
v_mul_f32 v76, v76, v74
Mavv 248,120,176
v_cvt_pk_bf16_f32 v38, v75, v76
v_lshlrev_b32 v75, 16, v39
Mavv 248,124,180
v_and_b32 v76, 0xffff0000, v39
v_mul_f32 v75, v75, v74
Mavv 252,120,184
s_add_u32 m0, s70, 147456
s_add_u32 s72, s68, 2883584
v_mul_f32 v76, v76, v74
Mavv 252,124,188
s_waitcnt lgkmcnt(3)
buffer_load_dwordx4 v3, s[20:23], s72 offen lds
Mavv 0,192,96
s_waitcnt lgkmcnt(2)
ds_read_b128 v[112:115], v5 offset:4096
)"
R"(
v_cvt_pk_bf16_f32 v39, v75, v76
Mavv 0,196,100
buffer_store_dwordx4 v[36:39], v77, s[48:51], s77 offen offset:512 sc1
Mavv 4,200,96
ds_read_b128 v[116:119], v6 offset:4096
v_lshlrev_b32 v75, 16, v40
Mavv 4,204,100
s_add_u32 m0, s70, 148352
s_add_u32 s72, s68, 2883584
v_and_b32 v76, 0xffff0000, v40
Mavv 8,208,96
buffer_load_dwordx4 v3, s[20:23], s72 offen offset:128 lds
Mavv 8,212,100
v_mul_f32 v75, v75, v74
v_mul_f32 v76, v76, v74
Mavv 12,216,96
v_cvt_pk_bf16_f32 v40, v75, v76
v_lshlrev_b32 v75, 16, v41
Mavv 12,220,100
v_and_b32 v76, 0xffff0000, v41
v_mul_f32 v75, v75, v74
Mavv 16,96,224
s_add_u32 m0, s70, 149504
s_add_u32 s72, s68, 2883584
v_mul_f32 v76, v76, v74
Mavv 16,100,228
buffer_load_dwordx4 v3, s[24:27], s72 offen lds
Mavv 20,96,232
v_cvt_pk_bf16_f32 v41, v75, v76
v_lshlrev_b32 v75, 16, v42
Mavv 20,100,236
v_and_b32 v76, 0xffff0000, v42
v_mul_f32 v75, v75, v74
Mavv 24,96,240
v_mul_f32 v76, v76, v74
v_cvt_pk_bf16_f32 v42, v75, v76
Mavv 24,100,244
s_add_u32 m0, s70, 150400
s_add_u32 s72, s68, 2883584
v_lshlrev_b32 v75, 16, v43
Mavv 28,96,248
buffer_load_dwordx4 v3, s[24:27], s72 offen offset:128 lds
Mavv 28,100,252
s_waitcnt lgkmcnt(3)
v_and_b32 v76, 0xffff0000, v43
v_mul_f32 v75, v75, v74
Mavv 32,192,104
s_waitcnt lgkmcnt(2)
ds_read_b128 v[120:123], v5 offset:6144
v_mul_f32 v76, v76, v74
Mavv 32,196,108
v_cvt_pk_bf16_f32 v43, v75, v76
Mavv 36,200,104
s_add_u32 m0, s66, 65536
s_add_u32 s72, s67, 640
ds_read_b128 v[124:127], v6 offset:6144
Mavv 36,204,108
buffer_load_dwordx4 v1, s[12:15], s72 offen lds
Mavv 40,208,104
buffer_store_dwordx4 v[40:43], v77, s[48:51], s77 offen offset:1024 sc1
Mavv 40,212,108
v_lshlrev_b32 v75, 16, v44
v_and_b32 v76, 0xffff0000, v44
Mavv 44,216,104
v_mul_f32 v75, v75, v74
v_mul_f32 v76, v76, v74
Mavv 44,220,108
s_add_u32 m0, s66, 67584
s_add_u32 s72, s67, 17024
v_cvt_pk_bf16_f32 v44, v75, v76
Mavv 48,104,224
buffer_load_dwordx4 v1, s[12:15], s72 offen lds
Mavv 48,108,228
v_lshlrev_b32 v75, 16, v45
v_and_b32 v76, 0xffff0000, v45
Mavv 52,104,232
v_mul_f32 v75, v75, v74
v_mul_f32 v76, v76, v74
Mavv 52,108,236
v_cvt_pk_bf16_f32 v45, v75, v76
v_lshlrev_b32 v75, 16, v46
Mavv 56,104,240
s_add_u32 m0, s66, 69632
s_add_u32 s72, s67, 33408
v_and_b32 v76, 0xffff0000, v46
Mavv 56,108,244
buffer_load_dwordx4 v1, s[12:15], s72 offen lds
Mavv 60,104,248
v_mul_f32 v75, v75, v74
v_mul_f32 v76, v76, v74
Mavv 60,108,252
s_waitcnt lgkmcnt(3)
v_cvt_pk_bf16_f32 v46, v75, v76
v_lshlrev_b32 v75, 16, v47
Mavv 64,192,112
s_waitcnt lgkmcnt(2)
ds_read_b128 v[96:99], v5 offset:8192
v_and_b32 v76, 0xffff0000, v47
Mavv 64,196,116
v_mul_f32 v75, v75, v74
v_mul_f32 v76, v76, v74
Mavv 68,200,112
s_add_u32 m0, s66, 71680
s_add_u32 s72, s67, 49792
ds_read_b128 v[100:103], v6 offset:8192
Mavv 68,204,116
buffer_load_dwordx4 v1, s[12:15], s72 offen lds
Mavv 72,208,112
v_cvt_pk_bf16_f32 v47, v75, v76
Mavv 72,212,116
buffer_store_dwordx4 v[44:47], v77, s[48:51], s77 offen offset:1536 sc1
Zvvv 68,48,32
Mavv 76,216,112
Mvvv 68,52,36
Mavv 76,220,116
s_add_u32 m0, s66, 73728
s_add_u32 s72, s67, 66176
Mvvv 68,56,40
Mavv 80,112,224
buffer_load_dwordx4 v1, s[12:15], s72 offen lds
Mvvv 68,60,44
Mavv 80,116,228
s_mov_b32 s86, 0x100001
s_mov_b32 s87, 0x10000100
Mavv 84,112,232
Mavv 84,116,236
Mavv 88,112,240
s_add_u32 m0, s66, 75776
s_add_u32 s72, s67, 82560
Mavv 88,116,244
buffer_load_dwordx4 v1, s[12:15], s72 offen lds
Mavv 92,112,248
Mavv 92,116,252
s_waitcnt lgkmcnt(3)
Mavv 96,192,120
s_waitcnt lgkmcnt(2)
ds_read_b128 v[104:107], v5 offset:10240
Mavv 96,196,124
v_cndmask_b32 v72, 0, v68, s[86:87]
Mavv 100,200,120
s_add_u32 m0, s66, 77824
s_add_u32 s72, s67, 98944
ds_read_b128 v[108:111], v6 offset:10240
Mavv 100,204,124
s_waitcnt vmcnt(29)
buffer_load_dwordx4 v1, s[12:15], s72 offen lds
Mavv 104,208,120
s_barrier
s_lshl_b64 s[86:87], s[86:87], 1
Mavv 104,212,124
ds_read_b64_tr_b16 v[128:129], v9 offset:0
v_cndmask_b32 v72, v72, v69, s[86:87]
Mavv 108,216,120
s_lshl_b64 s[86:87], s[86:87], 1
ds_read_b64_tr_b16 v[130:131], v9 offset:256
v_cndmask_b32 v72, v72, v70, s[86:87]
Mavv 108,220,124
s_lshl_b64 s[86:87], s[86:87], 1
ds_read_b64_tr_b16 v[132:133], v9 offset:16384
v_cndmask_b32 v72, v72, v71, s[86:87]
Mavv 112,120,224
ds_read_b64_tr_b16 v[134:135], v9 offset:16640
Mavv 112,124,228
ds_read_b64_tr_b16 v[136:137], v10 offset:0
v_add_f32_dpp v72, v72, v72 quad_perm:[1,0,3,2] row_mask:0xf bank_mask:0xf bound_ctrl:1
Mavv 116,120,232
ds_read_b64_tr_b16 v[138:139], v10 offset:256
Mavv 116,124,236
ds_read_b64_tr_b16 v[140:141], v10 offset:16384
v_add_f32_dpp v72, v72, v72 quad_perm:[2,3,0,1] row_mask:0xf bank_mask:0xf bound_ctrl:1
Mavv 120,120,240
ds_read_b64_tr_b16 v[142:143], v10 offset:16640
Mavv 120,124,244
ds_read_b64_tr_b16 v[144:145], v9 offset:512
v_add_f32_dpp v72, v72, v72 row_half_mirror row_mask:0xf bank_mask:0xf bound_ctrl:1
Mavv 124,120,248
ds_read_b64_tr_b16 v[146:147], v9 offset:768
v_mov_b32 v75, v72
Mavv 124,124,252
s_waitcnt lgkmcnt(13)
ds_read_b64_tr_b16 v[148:149], v9 offset:16896
Mavv 128,192,96
s_waitcnt lgkmcnt(13)
ds_read_b128 v[112:115], v5 offset:12288
v_permlane16_swap_b32 v72, v75
Mavv 128,196,100
ds_read_b64_tr_b16 v[150:151], v9 offset:17152
v_add_f32 v72, v72, v75
Mavv 132,200,96
s_waitcnt lgkmcnt(12)
ds_read_b128 v[116:119], v6 offset:12288
v_mov_b32 v75, v72
Mavv 132,204,100
ds_read_b64_tr_b16 v[152:153], v10 offset:512
Mavv 136,208,96
ds_read_b64_tr_b16 v[154:155], v10 offset:768
v_permlane32_swap_b32 v72, v75
Mavv 136,212,100
s_waitcnt lgkmcnt(12)
ds_read_b64_tr_b16 v[156:157], v10 offset:16896
v_add_f32 v72, v72, v75
Mavv 140,216,96
ds_read_b64_tr_b16 v[158:159], v10 offset:17152
v_cvt_pk_bf16_f32 v72, 0, v72
Mavv 140,220,100
ds_read_b64_tr_b16 v[160:161], v9 offset:2048
v_add_f32 v72, 0, v72
Mavv 144,96,224
s_waitcnt lgkmcnt(12)
ds_read_b64_tr_b16 v[162:163], v9 offset:2304
v_ashrrev_i32 v75, 31, v72
Mavv 144,100,228
ds_read_b64_tr_b16 v[164:165], v9 offset:18432
v_or_b32 v75, 0x80000000, v75
Mavv 148,96,232
ds_read_b64_tr_b16 v[166:167], v9 offset:18688
v_xor_b32 v75, v75, v72
Mavv 148,100,236
s_waitcnt lgkmcnt(12)
ds_read_b64_tr_b16 v[168:169], v10 offset:2048
v_and_b32 v75, 0xffff0000, v75
Mavv 152,96,240
ds_read_b64_tr_b16 v[170:171], v10 offset:2304
v_cmp_o_f32 vcc, v72, v72
Mavv 152,100,244
ds_read_b64_tr_b16 v[172:173], v10 offset:18432
Mavv 156,96,248
s_waitcnt lgkmcnt(12)
ds_read_b64_tr_b16 v[174:175], v10 offset:18688
v_cndmask_b32 v75, 0, v75, vcc
Mavv 156,100,252
ds_read_b64_tr_b16 v[176:177], v9 offset:2560
v_or_b32 v68, v75, v78
Mavv 160,192,104
ds_read_b128 v[120:123], v5 offset:14336
Mavv 160,196,108
s_waitcnt lgkmcnt(12)
ds_read_b64_tr_b16 v[178:179], v9 offset:2816
v_mov_b32_dpp v70, v68 row_ror:8 row_mask:0xf bank_mask:0xf bound_ctrl:1
Mavv 164,200,104
ds_read_b128 v[124:127], v6 offset:14336
v_mov_b32 v69, s74
Mavv 164,204,108
ds_read_b64_tr_b16 v[180:181], v9 offset:18944
v_mov_b32 v71, s75
Mavv 168,208,104
s_waitcnt lgkmcnt(12)
ds_read_b64_tr_b16 v[182:183], v9 offset:19200
Mavv 168,212,108
ds_read_b64_tr_b16 v[184:185], v10 offset:2560
Mavv 172,216,104
ds_read_b64_tr_b16 v[186:187], v10 offset:2816
Mavv 172,220,108
s_waitcnt lgkmcnt(12)
ds_read_b64_tr_b16 v[188:189], v10 offset:18944
Mavv 176,104,224
s_add_u32 m0, s66, 79872
s_add_u32 s72, s67, 115328
ds_read_b64_tr_b16 v[190:191], v10 offset:19200
Mavv 176,108,228
buffer_load_dwordx4 v1, s[12:15], s72 offen lds
Mavv 180,104,232
s_add_u32 s72, s76, 4194304
Mavv 180,108,236
buffer_load_dwordx4 v[80:83], v0, s[16:19], s72 offen
Mavv 184,104,240
s_add_u32 s72, s76, 5242880
Mavv 184,108,244
buffer_load_dwordx4 v[80:83], v0, s[16:19], s72 offen
Mavv 188,104,248
Mavv 188,108,252
Mavv 192,192,112
Mavv 192,196,116
Mavv 196,200,112
Mavv 196,204,116
s_waitcnt vmcnt(25) lgkmcnt(0)
Mavv 200,208,112
s_barrier
Mavv 200,212,116
Mavv 204,216,112
ds_read_b128 v[96:99], v5 offset:32768
Mavv 204,220,116
Mavv 208,112,224
ds_read_b128 v[100:103], v6 offset:32768
Mavv 208,116,228
s_add_u32 m0, s70, 98304
s_add_u32 s72, s68, 3145728
Mavv 212,112,232
buffer_load_dwordx4 v3, s[20:23], s72 offen lds
Mavv 212,116,236
Mavv 216,112,240
Mavv 216,116,244
Mavv 220,112,248
s_add_u32 m0, s70, 99200
s_add_u32 s72, s68, 3145728
Mavv 220,116,252
buffer_load_dwordx4 v3, s[20:23], s72 offen offset:128 lds
Mavv 224,192,120
Mavv 224,196,124
Mavv 228,200,120
ds_read_b128 v[104:107], v5 offset:34816
Mavv 228,204,124
Mavv 232,208,120
s_add_u32 m0, s70, 100352
s_add_u32 s72, s68, 3145728
ds_read_b128 v[108:111], v6 offset:34816
Mavv 232,212,124
buffer_load_dwordx4 v3, s[24:27], s72 offen lds
Mavv 236,216,120
Mavv 236,220,124
Mavv 240,120,224
Mavv 240,124,228
s_add_u32 m0, s70, 101248
s_add_u32 s72, s68, 3145728
Mavv 244,120,232
buffer_load_dwordx4 v3, s[24:27], s72 offen offset:128 lds
Mavv 244,124,236
Mavv 248,120,240
Mavv 248,124,244
Mavv 252,120,248
s_add_u32 m0, s70, 114688
s_add_u32 s72, s68, 3407872
Mavv 252,124,252
s_waitcnt lgkmcnt(3)
buffer_load_dwordx4 v3, s[20:23], s72 offen lds
Mavv 0,128,96
s_waitcnt lgkmcnt(2)
ds_read_b128 v[112:115], v5 offset:36864
Mavv 0,132,100
Mavv 4,136,96
ds_read_b128 v[116:119], v6 offset:36864
Mavv 4,140,100
s_add_u32 m0, s70, 115584
s_add_u32 s72, s68, 3407872
Mavv 8,144,96
buffer_load_dwordx4 v3, s[20:23], s72 offen offset:128 lds
Mavv 8,148,100
Mavv 12,152,96
Mavv 12,156,100
Mavv 16,96,160
s_add_u32 m0, s70, 116736
s_add_u32 s72, s68, 3407872
Mavv 16,100,164
buffer_load_dwordx4 v3, s[24:27], s72 offen lds
Mavv 20,96,168
Mavv 20,100,172
Mavv 24,96,176
Mavv 24,100,180
s_add_u32 m0, s70, 117632
s_add_u32 s72, s68, 3407872
Mavv 28,96,184
buffer_load_dwordx4 v3, s[24:27], s72 offen offset:128 lds
Mavv 28,100,188
s_waitcnt lgkmcnt(3)
Mavv 32,128,104
s_waitcnt lgkmcnt(2)
ds_read_b128 v[120:123], v5 offset:38912
Mavv 32,132,108
Mavv 36,136,104
s_add_u32 m0, s66, 0
s_add_u32 s72, s67, 768
ds_read_b128 v[124:127], v6 offset:38912
Mavv 36,140,108
buffer_load_dwordx4 v1, s[12:15], s72 offen lds
Mavv 40,144,104
Mavv 40,148,108
Mavv 44,152,104
Mavv 44,156,108
s_add_u32 m0, s66, 2048
s_add_u32 s72, s67, 17152
Mavv 48,104,160
buffer_load_dwordx4 v1, s[12:15], s72 offen lds
Mavv 48,108,164
Mavv 52,104,168
Mavv 52,108,172
Mavv 56,104,176
s_add_u32 m0, s66, 4096
s_add_u32 s72, s67, 33536
Mavv 56,108,180
buffer_load_dwordx4 v1, s[12:15], s72 offen lds
Mavv 60,104,184
Mavv 60,108,188
s_waitcnt lgkmcnt(3)
Mavv 64,128,112
s_waitcnt lgkmcnt(2)
ds_read_b128 v[96:99], v5 offset:40960
Mavv 64,132,116
Mavv 68,136,112
s_add_u32 m0, s66, 6144
s_add_u32 s72, s67, 49920
ds_read_b128 v[100:103], v6 offset:40960
Mavv 68,140,116
buffer_load_dwordx4 v1, s[12:15], s72 offen lds
Mavv 72,144,112
Mavv 72,148,116
Mavv 76,152,112
Mavv 76,156,116
s_add_u32 m0, s66, 8192
s_add_u32 s72, s67, 66304
Mavv 80,112,160
buffer_load_dwordx4 v1, s[12:15], s72 offen lds
Mavv 80,116,164
Mavv 84,112,168
Mavv 84,116,172
Mavv 88,112,176
s_add_u32 m0, s66, 10240
s_add_u32 s72, s67, 82688
Mavv 88,116,180
buffer_load_dwordx4 v1, s[12:15], s72 offen lds
Mavv 92,112,184
Mavv 92,116,188
s_waitcnt lgkmcnt(3)
Mavv 96,128,120
s_waitcnt lgkmcnt(2)
ds_read_b128 v[104:107], v5 offset:43008
Mavv 96,132,124
Mavv 100,136,120
s_add_u32 m0, s66, 12288
s_add_u32 s72, s67, 99072
ds_read_b128 v[108:111], v6 offset:43008
Mavv 100,140,124
s_waitcnt vmcnt(26)
buffer_load_dwordx4 v1, s[12:15], s72 offen lds
Mavv 104,144,120
s_barrier
Mavv 104,148,124
ds_read_b64_tr_b16 v[192:193], v9 offset:32768
Mavv 108,152,120
ds_read_b64_tr_b16 v[194:195], v9 offset:33024
Mavv 108,156,124
ds_read_b64_tr_b16 v[196:197], v9 offset:49152
Mavv 112,120,160
ds_read_b64_tr_b16 v[198:199], v9 offset:49408
Mavv 112,124,164
ds_read_b64_tr_b16 v[200:201], v10 offset:32768
Mavv 116,120,168
ds_read_b64_tr_b16 v[202:203], v10 offset:33024
Mavv 116,124,172
ds_read_b64_tr_b16 v[204:205], v10 offset:49152
Mavv 120,120,176
ds_read_b64_tr_b16 v[206:207], v10 offset:49408
Mavv 120,124,180
ds_read_b64_tr_b16 v[208:209], v9 offset:33280
Mavv 124,120,184
ds_read_b64_tr_b16 v[210:211], v9 offset:33536
Mavv 124,124,188
s_waitcnt lgkmcnt(13)
ds_read_b64_tr_b16 v[212:213], v9 offset:49664
Mavv 128,128,96
)"
R"(
s_waitcnt lgkmcnt(13)
ds_read_b128 v[112:115], v5 offset:45056
Mavv 128,132,100
s_waitcnt vmcnt(21)
ds_read_b64_tr_b16 v[214:215], v9 offset:49920
Mavv 132,136,96
s_waitcnt lgkmcnt(12)
ds_read_b128 v[116:119], v6 offset:45056
Mavv 132,140,100
ds_read_b64_tr_b16 v[216:217], v10 offset:33280
Mavv 136,144,96
ds_read_b64_tr_b16 v[218:219], v10 offset:33536
Mavv 136,148,100
s_waitcnt lgkmcnt(12)
ds_read_b64_tr_b16 v[220:221], v10 offset:49664
Mavv 140,152,96
ds_read_b64_tr_b16 v[222:223], v10 offset:49920
Mavv 140,156,100
ds_read_b64_tr_b16 v[224:225], v9 offset:34816
Mavv 144,96,160
s_waitcnt lgkmcnt(12)
ds_read_b64_tr_b16 v[226:227], v9 offset:35072
Mavv 144,100,164
ds_read_b64_tr_b16 v[228:229], v9 offset:51200
Mavv 148,96,168
ds_read_b64_tr_b16 v[230:231], v9 offset:51456
Mavv 148,100,172
s_waitcnt lgkmcnt(12)
ds_read_b64_tr_b16 v[232:233], v10 offset:34816
Mavv 152,96,176
ds_read_b64_tr_b16 v[234:235], v10 offset:35072
Mavv 152,100,180
ds_read_b64_tr_b16 v[236:237], v10 offset:51200
Mavv 156,96,184
s_waitcnt lgkmcnt(12)
ds_read_b64_tr_b16 v[238:239], v10 offset:51456
Mavv 156,100,188
ds_read_b64_tr_b16 v[240:241], v9 offset:35328
Mavv 160,128,104
ds_read_b128 v[120:123], v5 offset:47104
Mavv 160,132,108
s_waitcnt lgkmcnt(12)
ds_read_b64_tr_b16 v[242:243], v9 offset:35584
Mavv 164,136,104
ds_read_b128 v[124:127], v6 offset:47104
Mavv 164,140,108
ds_read_b64_tr_b16 v[244:245], v9 offset:51712
Mavv 168,144,104
s_waitcnt lgkmcnt(12)
ds_read_b64_tr_b16 v[246:247], v9 offset:51968
Mavv 168,148,108
ds_read_b64_tr_b16 v[248:249], v10 offset:35328
Mavv 172,152,104
ds_read_b64_tr_b16 v[250:251], v10 offset:35584
Mavv 172,156,108
s_waitcnt lgkmcnt(12)
ds_read_b64_tr_b16 v[252:253], v10 offset:51712
Mavv 176,104,160
s_add_u32 m0, s66, 14336
s_add_u32 s72, s67, 115456
ds_read_b64_tr_b16 v[254:255], v10 offset:51968
Mavv 176,108,164
buffer_load_dwordx4 v1, s[12:15], s72 offen lds
Mavv 180,104,168
s_add_u32 s72, s76, 6291456
buffer_store_dwordx4 v[68:71], v13, s[48:51], s78 offen sc1
Mavv 180,108,172
buffer_load_dwordx4 v[80:83], v0, s[16:19], s72 offen
Mavv 184,104,176
s_add_u32 s72, s76, 7340032
Mavv 184,108,180
buffer_load_dwordx4 v[80:83], v0, s[16:19], s72 offen
Mavv 188,104,184
Mavv 188,108,188
Mavv 192,128,112
Mavv 192,132,116
Mavv 196,136,112
Mavv 196,140,116
s_waitcnt vmcnt(21) lgkmcnt(0)
Mavv 200,144,112
s_barrier
Mavv 200,148,116
Mavv 204,152,112
ds_read_b128 v[96:99], v7 offset:0
Mavv 204,156,116
Mavv 208,112,160
ds_read_b128 v[100:103], v8 offset:0
Mavv 208,116,164
s_add_u32 m0, s70, 131072
s_add_u32 s72, s68, 3670016
Mavv 212,112,168
buffer_load_dwordx4 v3, s[20:23], s72 offen lds
Mavv 212,116,172
Mavv 216,112,176
Mavv 216,116,180
Mavv 220,112,184
s_add_u32 m0, s70, 131968
s_add_u32 s72, s68, 3670016
Mavv 220,116,188
buffer_load_dwordx4 v3, s[20:23], s72 offen offset:128 lds
Mavv 224,128,120
Mavv 224,132,124
Mavv 228,136,120
ds_read_b128 v[104:107], v7 offset:2048
Mavv 228,140,124
Mavv 232,144,120
s_add_u32 m0, s70, 133120
s_add_u32 s72, s68, 3670016
ds_read_b128 v[108:111], v8 offset:2048
Mavv 232,148,124
buffer_load_dwordx4 v3, s[24:27], s72 offen lds
Mavv 236,152,120
Mavv 236,156,124
Mavv 240,120,160
Mavv 240,124,164
s_add_u32 m0, s70, 134016
s_add_u32 s72, s68, 3670016
Mavv 244,120,168
buffer_load_dwordx4 v3, s[24:27], s72 offen offset:128 lds
Mavv 244,124,172
Mavv 248,120,176
Mavv 248,124,180
Mavv 252,120,184
s_add_u32 m0, s70, 147456
s_add_u32 s72, s68, 3932160
Mavv 252,124,188
s_waitcnt lgkmcnt(3)
buffer_load_dwordx4 v3, s[20:23], s72 offen lds
Mavv 0,192,96
s_waitcnt lgkmcnt(2)
ds_read_b128 v[112:115], v7 offset:4096
Mavv 0,196,100
Mavv 4,200,96
ds_read_b128 v[116:119], v8 offset:4096
Mavv 4,204,100
s_add_u32 m0, s70, 148352
s_add_u32 s72, s68, 3932160
Mavv 8,208,96
buffer_load_dwordx4 v3, s[20:23], s72 offen offset:128 lds
Mavv 8,212,100
Mavv 12,216,96
Mavv 12,220,100
Mavv 16,96,224
s_add_u32 m0, s70, 149504
s_add_u32 s72, s68, 3932160
Mavv 16,100,228
buffer_load_dwordx4 v3, s[24:27], s72 offen lds
Mavv 20,96,232
Mavv 20,100,236
Mavv 24,96,240
Mavv 24,100,244
s_add_u32 m0, s70, 150400
s_add_u32 s72, s68, 3932160
Mavv 28,96,248
buffer_load_dwordx4 v3, s[24:27], s72 offen offset:128 lds
Mavv 28,100,252
s_waitcnt lgkmcnt(3)
Mavv 32,192,104
s_waitcnt lgkmcnt(2)
ds_read_b128 v[120:123], v7 offset:6144
Mavv 32,196,108
Mavv 36,200,104
s_add_u32 m0, s66, 32768
s_add_u32 s72, s67, 896
ds_read_b128 v[124:127], v8 offset:6144
Mavv 36,204,108
buffer_load_dwordx4 v1, s[12:15], s72 offen lds
Mavv 40,208,104
Mavv 40,212,108
Mavv 44,216,104
Mavv 44,220,108
s_add_u32 m0, s66, 34816
s_add_u32 s72, s67, 17280
Mavv 48,104,224
buffer_load_dwordx4 v1, s[12:15], s72 offen lds
Mavv 48,108,228
Mavv 52,104,232
Mavv 52,108,236
Mavv 56,104,240
s_add_u32 m0, s66, 36864
s_add_u32 s72, s67, 33664
Mavv 56,108,244
buffer_load_dwordx4 v1, s[12:15], s72 offen lds
Mavv 60,104,248
Mavv 60,108,252
s_waitcnt lgkmcnt(3)
Mavv 64,192,112
s_waitcnt lgkmcnt(2)
ds_read_b128 v[96:99], v7 offset:8192
Mavv 64,196,116
Mavv 68,200,112
s_add_u32 m0, s66, 38912
s_add_u32 s72, s67, 50048
ds_read_b128 v[100:103], v8 offset:8192
Mavv 68,204,116
buffer_load_dwordx4 v1, s[12:15], s72 offen lds
Mavv 72,208,112
Mavv 72,212,116
Mavv 76,216,112
Mavv 76,220,116
s_add_u32 m0, s66, 40960
s_add_u32 s72, s67, 66432
Mavv 80,112,224
buffer_load_dwordx4 v1, s[12:15], s72 offen lds
Mavv 80,116,228
Mavv 84,112,232
Mavv 84,116,236
Mavv 88,112,240
s_add_u32 m0, s66, 43008
s_add_u32 s72, s67, 82816
Mavv 88,116,244
buffer_load_dwordx4 v1, s[12:15], s72 offen lds
Mavv 92,112,248
Mavv 92,116,252
s_waitcnt lgkmcnt(3)
Mavv 96,192,120
s_waitcnt lgkmcnt(2)
ds_read_b128 v[104:107], v7 offset:10240
Mavv 96,196,124
Mavv 100,200,120
s_add_u32 m0, s66, 45056
s_add_u32 s72, s67, 99200
ds_read_b128 v[108:111], v8 offset:10240
Mavv 100,204,124
s_waitcnt vmcnt(25)
buffer_load_dwordx4 v1, s[12:15], s72 offen lds
Mavv 104,208,120
s_barrier
Mavv 104,212,124
ds_read_b64_tr_b16 v[128:129], v9 offset:0
Mavv 108,216,120
ds_read_b64_tr_b16 v[130:131], v9 offset:256
Mavv 108,220,124
ds_read_b64_tr_b16 v[132:133], v9 offset:16384
Mavv 112,120,224
ds_read_b64_tr_b16 v[134:135], v9 offset:16640
Mavv 112,124,228
ds_read_b64_tr_b16 v[136:137], v10 offset:0
Mavv 116,120,232
ds_read_b64_tr_b16 v[138:139], v10 offset:256
Mavv 116,124,236
ds_read_b64_tr_b16 v[140:141], v10 offset:16384
Mavv 120,120,240
ds_read_b64_tr_b16 v[142:143], v10 offset:16640
Mavv 120,124,244
ds_read_b64_tr_b16 v[144:145], v9 offset:512
Mavv 124,120,248
ds_read_b64_tr_b16 v[146:147], v9 offset:768
Mavv 124,124,252
s_waitcnt lgkmcnt(13)
ds_read_b64_tr_b16 v[148:149], v9 offset:16896
Mavv 128,192,96
s_waitcnt lgkmcnt(13)
ds_read_b128 v[112:115], v7 offset:12288
Mavv 128,196,100
ds_read_b64_tr_b16 v[150:151], v9 offset:17152
Mavv 132,200,96
s_waitcnt lgkmcnt(12)
ds_read_b128 v[116:119], v8 offset:12288
Mavv 132,204,100
ds_read_b64_tr_b16 v[152:153], v10 offset:512
Mavv 136,208,96
ds_read_b64_tr_b16 v[154:155], v10 offset:768
Mavv 136,212,100
s_waitcnt lgkmcnt(12)
ds_read_b64_tr_b16 v[156:157], v10 offset:16896
Mavv 140,216,96
ds_read_b64_tr_b16 v[158:159], v10 offset:17152
Mavv 140,220,100
ds_read_b64_tr_b16 v[160:161], v9 offset:2048
Mavv 144,96,224
s_waitcnt lgkmcnt(12)
ds_read_b64_tr_b16 v[162:163], v9 offset:2304
Mavv 144,100,228
ds_read_b64_tr_b16 v[164:165], v9 offset:18432
Mavv 148,96,232
ds_read_b64_tr_b16 v[166:167], v9 offset:18688
Mavv 148,100,236
s_waitcnt lgkmcnt(12)
ds_read_b64_tr_b16 v[168:169], v10 offset:2048
Mavv 152,96,240
ds_read_b64_tr_b16 v[170:171], v10 offset:2304
Mavv 152,100,244
ds_read_b64_tr_b16 v[172:173], v10 offset:18432
Mavv 156,96,248
s_waitcnt lgkmcnt(12)
ds_read_b64_tr_b16 v[174:175], v10 offset:18688
Mavv 156,100,252
ds_read_b64_tr_b16 v[176:177], v9 offset:2560
Mavv 160,192,104
ds_read_b128 v[120:123], v7 offset:14336
Mavv 160,196,108
s_waitcnt lgkmcnt(12)
ds_read_b64_tr_b16 v[178:179], v9 offset:2816
Mavv 164,200,104
ds_read_b128 v[124:127], v8 offset:14336
Mavv 164,204,108
ds_read_b64_tr_b16 v[180:181], v9 offset:18944
Mavv 168,208,104
s_waitcnt lgkmcnt(12)
ds_read_b64_tr_b16 v[182:183], v9 offset:19200
Mavv 168,212,108
ds_read_b64_tr_b16 v[184:185], v10 offset:2560
Mavv 172,216,104
ds_read_b64_tr_b16 v[186:187], v10 offset:2816
Mavv 172,220,108
s_waitcnt lgkmcnt(12)
ds_read_b64_tr_b16 v[188:189], v10 offset:18944
Mavv 176,104,224
s_add_u32 m0, s66, 47104
s_add_u32 s72, s67, 115584
ds_read_b64_tr_b16 v[190:191], v10 offset:19200
Mavv 176,108,228
buffer_load_dwordx4 v1, s[12:15], s72 offen lds
Mavv 180,104,232
buffer_load_dwordx4 v[32:35], v0, s[48:51], s79 offen sc1
Mavv 180,108,236
Mavv 184,104,240
Mavv 184,108,244
Mavv 188,104,248
buffer_load_dwordx4 v[36:39], v0, s[48:51], s79 offen offset:1024 sc1
Mavv 188,108,252
Mavv 192,192,112
Mavv 192,196,116
Mavv 196,200,112
buffer_load_dwordx4 v[40:43], v0, s[48:51], s79 offen offset:2048 sc1
Mavv 196,204,116
s_waitcnt vmcnt(22) lgkmcnt(0)
Mavv 200,208,112
s_barrier
Mavv 200,212,116
Mavv 204,216,112
ds_read_b128 v[96:99], v5 offset:0
Mavv 204,220,116
buffer_load_dwordx4 v[44:47], v0, s[48:51], s79 offen offset:3072 sc1
Mavv 208,112,224
ds_read_b128 v[100:103], v6 offset:0
Mavv 208,116,228
s_add_u32 m0, s71, 98304
s_add_u32 s72, s68, 0
Mavv 212,112,232
buffer_load_dwordx4 v3, s[16:19], s72 offen lds
Mavv 212,116,236
Mavv 216,112,240
Mavv 216,116,244
Mavv 220,112,248
s_add_u32 m0, s71, 99200
s_add_u32 s72, s68, 0
Mavv 220,116,252
buffer_load_dwordx4 v3, s[16:19], s72 offen offset:128 lds
Mavv 224,192,120
Mavv 224,196,124
Mavv 228,200,120
ds_read_b128 v[104:107], v5 offset:2048
Mavv 228,204,124
Mavv 232,208,120
s_add_u32 m0, s71, 106496
s_add_u32 s72, s68, 262144
ds_read_b128 v[108:111], v6 offset:2048
Mavv 232,212,124
buffer_load_dwordx4 v3, s[16:19], s72 offen lds
Mavv 236,216,120
Mavv 236,220,124
Mavv 240,120,224
Mavv 240,124,228
s_add_u32 m0, s71, 107392
s_add_u32 s72, s68, 262144
Mavv 244,120,232
buffer_load_dwordx4 v3, s[16:19], s72 offen offset:128 lds
Mavv 244,124,236
Mavv 248,120,240
Mavv 248,124,244
Mavv 252,120,248
s_add_u32 m0, s71, 114688
s_add_u32 s72, s68, 524288
Mavv 252,124,252
s_waitcnt lgkmcnt(3)
buffer_load_dwordx4 v3, s[16:19], s72 offen lds
Mavv 0,128,96
s_waitcnt lgkmcnt(2)
ds_read_b128 v[112:115], v5 offset:4096
Mavv 0,132,100
Mavv 4,136,96
ds_read_b128 v[116:119], v6 offset:4096
Mavv 4,140,100
s_add_u32 m0, s71, 115584
s_add_u32 s72, s68, 524288
Mavv 8,144,96
buffer_load_dwordx4 v3, s[16:19], s72 offen offset:128 lds
Mavv 8,148,100
Mavv 12,152,96
Mavv 12,156,100
Mavv 16,96,160
s_add_u32 m0, s71, 122880
s_add_u32 s72, s68, 786432
Mavv 16,100,164
buffer_load_dwordx4 v3, s[16:19], s72 offen lds
Mavv 20,96,168
Mavv 20,100,172
Mavv 24,96,176
Mavv 24,100,180
s_add_u32 m0, s71, 123776
s_add_u32 s72, s68, 786432
Mavv 28,96,184
buffer_load_dwordx4 v3, s[16:19], s72 offen offset:128 lds
Mavv 28,100,188
s_waitcnt lgkmcnt(3)
Mavv 32,128,104
s_waitcnt lgkmcnt(2)
ds_read_b128 v[120:123], v5 offset:6144
Mavv 32,132,108
Mavv 36,136,104
ds_read_b128 v[124:127], v6 offset:6144
Mavv 36,140,108
Mavv 40,144,104
Mavv 40,148,108
Mavv 44,152,104
Mavv 44,156,108
Mavv 48,104,160
Mavv 48,108,164
Mavv 52,104,168
Mavv 52,108,172
Mavv 56,104,176
s_waitcnt vmcnt(11)
v_xor_b32 v50, s74, v33
v_bitop3_b32 v50, v35, s75, v50 bitop3:0xbe
Mavv 56,108,180
s_waitcnt vmcnt(10)
v_bitop3_b32 v50, v37, s74, v50 bitop3:0xbe
v_bitop3_b32 v50, v39, s75, v50 bitop3:0xbe
Mavv 60,104,184
s_waitcnt vmcnt(9)
v_bitop3_b32 v50, v41, s74, v50 bitop3:0xbe
v_bitop3_b32 v50, v43, s75, v50 bitop3:0xbe
Mavv 60,108,188
s_waitcnt vmcnt(8) lgkmcnt(3)
v_bitop3_b32 v50, v45, s74, v50 bitop3:0xbe
v_bitop3_b32 v50, v47, s75, v50 bitop3:0xbe
Mavv 64,128,112
s_waitcnt lgkmcnt(2)
ds_read_b128 v[96:99], v5 offset:8192
v_cmp_ne_u32 vcc, 0, v50
Mavv 64,132,116
s_cbranch_vccnz .Lscan
.Lscan_r:
Mavv 68,136,112
ds_read_b128 v[100:103], v6 offset:8192
v_max_u32 v48, v32, v34
Mavv 68,140,116
v_min_u32 v49, v32, v34
v_med3_u32 v49, v48, v49, v36
Mavv 72,144,112
v_max_u32 v48, v48, v36
v_med3_u32 v49, v48, v49, v38
Mavv 72,148,116
v_max_u32 v48, v48, v38
v_med3_u32 v49, v48, v49, v40
)"
R"(
Mavv 76,152,112
v_max_u32 v48, v48, v40
v_med3_u32 v49, v48, v49, v42
Mavv 76,156,116
v_max_u32 v48, v48, v42
v_med3_u32 v49, v48, v49, v44
Mavv 80,112,160
v_max_u32 v48, v48, v44
v_med3_u32 v49, v48, v49, v46
Mavv 80,116,164
v_max_u32 v48, v48, v46
v_mov_b32 v50, v48
Mavv 84,112,168
Mavv 84,116,172
v_max_u32_dpp v50, v50, v50 quad_perm:[1,0,3,2] row_mask:0xf bank_mask:0xf bound_ctrl:1
Mavv 88,112,176
Mavv 88,116,180
v_max_u32_dpp v50, v50, v50 quad_perm:[2,3,0,1] row_mask:0xf bank_mask:0xf bound_ctrl:1
Mavv 92,112,184
Mavv 92,116,188
s_waitcnt lgkmcnt(3)
v_max_u32_dpp v50, v50, v50 row_half_mirror row_mask:0xf bank_mask:0xf bound_ctrl:1
Mavv 96,128,120
s_waitcnt lgkmcnt(2)
ds_read_b128 v[104:107], v5 offset:10240
Mavv 96,132,124
v_max_u32_dpp v50, v50, v50 row_mirror row_mask:0xf bank_mask:0xf bound_ctrl:1
v_mov_b32 v51, v50
Mavv 100,136,120
ds_read_b128 v[108:111], v6 offset:10240
Mavv 100,140,124
v_permlane16_swap_b32 v50, v51
v_max_u32 v50, v50, v51
Mavv 104,144,120
s_barrier
Mavv 104,148,124
ds_read_b64_tr_b16 v[192:193], v9 offset:32768
v_mov_b32 v51, v50
Mavv 108,152,120
s_cmp_eq_u32 s78, 4096
s_cselect_b32 s73, 0, 0x80000000
ds_read_b64_tr_b16 v[194:195], v9 offset:33024
Mavv 108,156,124
ds_read_b64_tr_b16 v[196:197], v9 offset:49152
v_permlane32_swap_b32 v50, v51
Mavv 112,120,160
ds_read_b64_tr_b16 v[198:199], v9 offset:49408
v_max_u32 v50, v50, v51
Mavv 112,124,164
ds_read_b64_tr_b16 v[200:201], v10 offset:32768
v_cmp_eq_u32 vcc, v50, v48
Mavv 116,120,168
ds_read_b64_tr_b16 v[202:203], v10 offset:33024
v_mov_b32 v52, s74
Mavv 116,124,172
ds_read_b64_tr_b16 v[204:205], v10 offset:49152
v_cndmask_b32 v49, v48, v49, vcc
Mavv 120,120,176
ds_read_b64_tr_b16 v[206:207], v10 offset:49408
v_mov_b32 v53, s75
Mavv 120,124,180
ds_read_b64_tr_b16 v[208:209], v9 offset:33280
v_max_u32_dpp v49, v49, v49 quad_perm:[1,0,3,2] row_mask:0xf bank_mask:0xf bound_ctrl:1
Mavv 124,120,184
ds_read_b64_tr_b16 v[210:211], v9 offset:33536
Mavv 124,124,188
s_waitcnt lgkmcnt(13)
ds_read_b64_tr_b16 v[212:213], v9 offset:49664
v_max_u32_dpp v49, v49, v49 quad_perm:[2,3,0,1] row_mask:0xf bank_mask:0xf bound_ctrl:1
Mavv 128,128,96
s_waitcnt lgkmcnt(13)
ds_read_b128 v[112:115], v5 offset:12288
Mavv 128,132,100
ds_read_b64_tr_b16 v[214:215], v9 offset:49920
v_max_u32_dpp v49, v49, v49 row_half_mirror row_mask:0xf bank_mask:0xf bound_ctrl:1
Mavv 132,136,96
s_waitcnt lgkmcnt(12)
ds_read_b128 v[116:119], v6 offset:12288
Mavv 132,140,100
ds_read_b64_tr_b16 v[216:217], v10 offset:33280
v_max_u32_dpp v49, v49, v49 row_mirror row_mask:0xf bank_mask:0xf bound_ctrl:1
Mavv 136,144,96
ds_read_b64_tr_b16 v[218:219], v10 offset:33536
v_mov_b32 v51, v49
Mavv 136,148,100
s_waitcnt lgkmcnt(12)
ds_read_b64_tr_b16 v[220:221], v10 offset:49664
Mavv 140,152,96
ds_read_b64_tr_b16 v[222:223], v10 offset:49920
v_permlane16_swap_b32 v49, v51
Mavv 140,156,100
ds_read_b64_tr_b16 v[224:225], v9 offset:34816
v_max_u32 v49, v49, v51
Mavv 144,96,160
s_waitcnt lgkmcnt(12)
ds_read_b64_tr_b16 v[226:227], v9 offset:35072
v_mov_b32 v51, v49
Mavv 144,100,164
ds_read_b64_tr_b16 v[228:229], v9 offset:51200
Mavv 148,96,168
ds_read_b64_tr_b16 v[230:231], v9 offset:51456
v_permlane32_swap_b32 v49, v51
Mavv 148,100,172
s_waitcnt lgkmcnt(12)
ds_read_b64_tr_b16 v[232:233], v10 offset:34816
v_max_u32 v49, v49, v51
Mavv 152,96,176
ds_read_b64_tr_b16 v[234:235], v10 offset:35072
v_readfirstlane_b32 s89, v50
Mavv 152,100,180
ds_read_b64_tr_b16 v[236:237], v10 offset:51200
v_readfirstlane_b32 s90, v49
Mavv 156,96,184
s_waitcnt lgkmcnt(12)
ds_read_b64_tr_b16 v[238:239], v10 offset:51456
Mavv 156,100,188
ds_read_b64_tr_b16 v[240:241], v9 offset:35328
Mavv 160,128,104
ds_read_b128 v[120:123], v5 offset:14336
Mavv 160,132,108
s_waitcnt lgkmcnt(12)
ds_read_b64_tr_b16 v[242:243], v9 offset:35584
Mavv 164,136,104
ds_read_b128 v[124:127], v6 offset:14336
Mavv 164,140,108
ds_read_b64_tr_b16 v[244:245], v9 offset:51712
Mavv 168,144,104
s_waitcnt lgkmcnt(12)
ds_read_b64_tr_b16 v[246:247], v9 offset:51968
Mavv 168,148,108
ds_read_b64_tr_b16 v[248:249], v10 offset:35328
Mavv 172,152,104
ds_read_b64_tr_b16 v[250:251], v10 offset:35584
Mavv 172,156,108
s_waitcnt lgkmcnt(12)
ds_read_b64_tr_b16 v[252:253], v10 offset:51712
Mavv 176,104,160
s_add_u32 m0, s69, 65536
s_add_u32 s72, s83, 0
ds_read_b64_tr_b16 v[254:255], v10 offset:51968
Mavv 176,108,164
buffer_load_dwordx4 v11, s[48:51], s72 offen sc1 lds
Mavv 180,104,168
buffer_store_dwordx2 v[52:53], v13, s[48:51], s73 offen sc1
Mavv 180,108,172
s_add_u32 m0, s69, 66560
s_add_u32 s72, s83, 0
Mavv 184,104,176
buffer_load_dwordx4 v12, s[48:51], s72 offen sc1 lds
Mavv 184,108,180
global_load_dwordx4 v[80:83], v0, s[62:63]
Mavv 188,104,184
s_add_u32 m0, s69, 67584
s_add_u32 s72, s83, 34816
Mavv 188,108,188
buffer_load_dwordx4 v11, s[48:51], s72 offen sc1 lds
Mavv 192,128,112
Mavv 192,132,116
s_add_u32 m0, s69, 68608
s_add_u32 s72, s83, 34816
Mavv 196,136,112
buffer_load_dwordx4 v12, s[48:51], s72 offen sc1 lds
Mavv 196,140,116
s_waitcnt lgkmcnt(0)
Mavv 200,144,112
s_barrier
Mavv 200,148,116
Mavv 204,152,112
s_add_u32 m0, s69, 69632
s_add_u32 s72, s82, 0
ds_read_b128 v[96:99], v5 offset:32768
Mavv 204,156,116
buffer_load_dwordx4 v11, s[48:51], s72 offen sc1 lds
Mavv 208,112,160
ds_read_b128 v[100:103], v6 offset:32768
Mavv 208,116,164
s_add_u32 m0, s69, 70656
s_add_u32 s72, s82, 0
Mavv 212,112,168
buffer_load_dwordx4 v12, s[48:51], s72 offen sc1 lds
Mavv 212,116,172
Mavv 216,112,176
s_add_u32 m0, s69, 71680
s_add_u32 s72, s82, 34816
Mavv 216,116,180
buffer_load_dwordx4 v11, s[48:51], s72 offen sc1 lds
Mavv 220,112,184
Mavv 220,116,188
s_add_u32 m0, s69, 72704
s_add_u32 s72, s82, 34816
Mavv 224,128,120
buffer_load_dwordx4 v12, s[48:51], s72 offen sc1 lds
Mavv 224,132,124
Mavv 228,136,120
s_add_u32 m0, s71, 131072
s_add_u32 s72, s68, 1048576
ds_read_b128 v[104:107], v5 offset:34816
Mavv 228,140,124
buffer_load_dwordx4 v3, s[16:19], s72 offen lds
Mavv 232,144,120
ds_read_b128 v[108:111], v6 offset:34816
Mavv 232,148,124
Mavv 236,152,120
Mavv 236,156,124
s_add_u32 m0, s71, 131968
s_add_u32 s72, s68, 1048576
Mavv 240,120,160
buffer_load_dwordx4 v3, s[16:19], s72 offen offset:128 lds
Mavv 240,124,164
Mavv 244,120,168
Mavv 244,124,172
Mavv 248,120,176
s_add_u32 m0, s71, 139264
s_add_u32 s72, s68, 1310720
Mavv 248,124,180
buffer_load_dwordx4 v3, s[16:19], s72 offen lds
Mavv 252,120,184
Mavv 252,124,188
s_waitcnt lgkmcnt(3)
Mavv 0,192,96
s_waitcnt lgkmcnt(2)
ds_read_b128 v[112:115], v5 offset:36864
Mavv 0,196,100
Mavv 4,200,96
s_add_u32 m0, s71, 140160
s_add_u32 s72, s68, 1310720
ds_read_b128 v[116:119], v6 offset:36864
Mavv 4,204,100
buffer_load_dwordx4 v3, s[16:19], s72 offen offset:128 lds
Mavv 8,208,96
Mavv 8,212,100
Mavv 12,216,96
Mavv 12,220,100
s_add_u32 m0, s71, 147456
s_add_u32 s72, s68, 1572864
Mavv 16,96,224
buffer_load_dwordx4 v3, s[16:19], s72 offen lds
Mavv 16,100,228
Mavv 20,96,232
Mavv 20,100,236
Mavv 24,96,240
s_add_u32 m0, s71, 148352
s_add_u32 s72, s68, 1572864
Mavv 24,100,244
buffer_load_dwordx4 v3, s[16:19], s72 offen offset:128 lds
Mavv 28,96,248
Mavv 28,100,252
s_waitcnt lgkmcnt(3)
Mavv 32,192,104
s_waitcnt lgkmcnt(2)
ds_read_b128 v[120:123], v5 offset:38912
Mavv 32,196,108
Mavv 36,200,104
s_add_u32 m0, s71, 155648
s_add_u32 s72, s68, 1835008
ds_read_b128 v[124:127], v6 offset:38912
Mavv 36,204,108
buffer_load_dwordx4 v3, s[16:19], s72 offen lds
Mavv 40,208,104
Mavv 40,212,108
Mavv 44,216,104
Mavv 44,220,108
s_add_u32 m0, s71, 156544
s_add_u32 s72, s68, 1835008
Mavv 48,104,224
buffer_load_dwordx4 v3, s[16:19], s72 offen offset:128 lds
Mavv 48,108,228
v_accvgpr_read_b32 v30, a0
v_accvgpr_read_b32 v31, a1
Mavv 52,104,232
v_mul_f32 v30, 0x3e0293ee, v30
v_mul_f32 v31, 0x3e0293ee, v31
Mavv 52,108,236
v_cvt_pk_bf16_f32 v16, v30, v31
v_accvgpr_read_b32 v30, a2
Mavv 56,104,240
s_add_u32 m0, s69, 0
s_add_u32 s72, s83, 128
v_accvgpr_read_b32 v31, a3
Mavv 56,108,244
buffer_load_dwordx4 v11, s[48:51], s72 offen sc1 lds
Mavv 60,104,248
v_mul_f32 v30, 0x3e0293ee, v30
v_mul_f32 v31, 0x3e0293ee, v31
Mavv 60,108,252
s_waitcnt lgkmcnt(3)
v_cvt_pk_bf16_f32 v17, v30, v31
v_accvgpr_read_b32 v30, a4
Mavv 64,192,112
s_waitcnt lgkmcnt(2)
ds_read_b128 v[96:99], v5 offset:40960
v_accvgpr_read_b32 v31, a5
Mavv 64,196,116
v_mul_f32 v30, 0x3e0293ee, v30
v_mul_f32 v31, 0x3e0293ee, v31
Mavv 68,200,112
s_add_u32 m0, s69, 1024
s_add_u32 s72, s83, 128
ds_read_b128 v[100:103], v6 offset:40960
Mavv 68,204,116
buffer_load_dwordx4 v12, s[48:51], s72 offen sc1 lds
Mavv 72,208,112
v_cvt_pk_bf16_f32 v18, v30, v31
v_accvgpr_read_b32 v30, a6
Mavv 72,212,116
v_accvgpr_read_b32 v31, a7
v_mul_f32 v30, 0x3e0293ee, v30
Mavv 76,216,112
v_mul_f32 v31, 0x3e0293ee, v31
v_cvt_pk_bf16_f32 v19, v30, v31
Mavv 76,220,116
s_add_u32 m0, s69, 2048
s_add_u32 s72, s83, 34944
s_add_u32 s73, s80, 0
Mavv 80,112,224
buffer_load_dwordx4 v11, s[48:51], s72 offen sc1 lds
Mavv 80,116,228
buffer_store_dwordx4 v[16:19], v0, s[48:51], s73 offen sc1
Mavv 84,112,232
v_accvgpr_read_b32 v30, a8
v_accvgpr_read_b32 v31, a9
Mavv 84,116,236
v_mul_f32 v30, 0x3e0293ee, v30
v_mul_f32 v31, 0x3e0293ee, v31
Mavv 88,112,240
s_add_u32 m0, s69, 3072
s_add_u32 s72, s83, 34944
v_cvt_pk_bf16_f32 v20, v30, v31
Mavv 88,116,244
buffer_load_dwordx4 v12, s[48:51], s72 offen sc1 lds
Mavv 92,112,248
v_accvgpr_read_b32 v30, a10
v_accvgpr_read_b32 v31, a11
Mavv 92,116,252
s_waitcnt lgkmcnt(3)
v_mul_f32 v30, 0x3e0293ee, v30
v_mul_f32 v31, 0x3e0293ee, v31
Mavv 96,192,120
s_waitcnt lgkmcnt(2)
ds_read_b128 v[104:107], v5 offset:43008
v_cvt_pk_bf16_f32 v21, v30, v31
Mavv 96,196,124
v_accvgpr_read_b32 v30, a12
v_accvgpr_read_b32 v31, a13
Mavv 100,200,120
s_add_u32 m0, s69, 4096
s_add_u32 s72, s82, 128
ds_read_b128 v[108:111], v6 offset:43008
Mavv 100,204,124
s_waitcnt vmcnt(13)
buffer_load_dwordx4 v11, s[48:51], s72 offen sc1 lds
Mavv 104,208,120
v_mul_f32 v30, 0x3e0293ee, v30
v_mul_f32 v31, 0x3e0293ee, v31
Mavv 104,212,124
ds_read_b128 v[32:35], v24 offset:0
v_cvt_pk_bf16_f32 v22, v30, v31
Mavv 108,216,120
v_accvgpr_read_b32 v30, a14
v_accvgpr_read_b32 v31, a15
Mavv 108,220,124
s_add_u32 m0, s69, 5120
s_add_u32 s72, s82, 128
ds_read_b128 v[36:39], v25 offset:0
Mavv 112,120,224
buffer_load_dwordx4 v12, s[48:51], s72 offen sc1 lds
Mavv 112,124,228
ds_read_b128 v[40:43], v24 offset:2048
v_mul_f32 v30, 0x3e0293ee, v30
Mavv 116,120,232
v_mul_f32 v31, 0x3e0293ee, v31
v_cvt_pk_bf16_f32 v23, v30, v31
Mavv 116,124,236
s_add_u32 s73, s80, 1024
ds_read_b128 v[44:47], v25 offset:2048
Mavv 120,120,240
buffer_store_dwordx4 v[20:23], v0, s[48:51], s73 offen sc1
Mavv 120,124,244
s_add_u32 m0, s69, 6144
s_add_u32 s72, s82, 34944
ds_read_b128 v[48:51], v24 offset:4096
Mavv 124,120,248
buffer_load_dwordx4 v11, s[48:51], s72 offen sc1 lds
Mavv 124,124,252
s_waitcnt lgkmcnt(8)
ds_read_b128 v[52:55], v25 offset:4096
v_accvgpr_read_b32 v30, a32
Mavv 128,192,96
s_waitcnt lgkmcnt(8)
ds_read_b128 v[112:115], v5 offset:45056
v_accvgpr_read_b32 v31, a33
Mavv 128,196,100
ds_read_b128 v[56:59], v24 offset:6144
v_mul_f32 v30, 0x3e0293ee, v30
Mavv 132,200,96
ds_read_b128 v[116:119], v6 offset:45056
v_mul_f32 v31, 0x3e0293ee, v31
Mavv 132,204,100
s_add_u32 m0, s69, 7168
s_add_u32 s72, s82, 34944
ds_read_b128 v[60:63], v25 offset:6144
Mavv 136,208,96
buffer_load_dwordx4 v12, s[48:51], s72 offen sc1 lds
Mavv 136,212,100
v_cvt_pk_bf16_f32 v16, v30, v31
v_accvgpr_read_b32 v30, a34
Mavv 140,216,96
v_accvgpr_read_b32 v31, a35
v_mul_f32 v30, 0x3e0293ee, v30
Mavv 140,220,100
v_mul_f32 v31, 0x3e0293ee, v31
v_cvt_pk_bf16_f32 v17, v30, v31
Mavv 144,96,224
v_accvgpr_read_b32 v30, a36
v_accvgpr_read_b32 v31, a37
Mavv 144,100,228
v_mul_f32 v30, 0x3e0293ee, v30
v_mul_f32 v31, 0x3e0293ee, v31
Mavv 148,96,232
v_cvt_pk_bf16_f32 v18, v30, v31
v_accvgpr_read_b32 v30, a38
Mavv 148,100,236
v_accvgpr_read_b32 v31, a39
v_mul_f32 v30, 0x3e0293ee, v30
Mavv 152,96,240
v_mul_f32 v31, 0x3e0293ee, v31
v_cvt_pk_bf16_f32 v19, v30, v31
Mavv 152,100,244
s_add_u32 s73, s80, 4096
buffer_store_dwordx4 v[16:19], v0, s[48:51], s73 offen sc1
Mavv 156,96,248
v_accvgpr_read_b32 v30, a40
)"
R"(
v_accvgpr_read_b32 v31, a41
Mavv 156,100,252
s_waitcnt lgkmcnt(11)
v_mul_f32 v30, 0x3e0293ee, v30
v_mul_f32 v31, 0x3e0293ee, v31
Mavv 160,192,104
s_waitcnt lgkmcnt(10)
ds_read_b128 v[120:123], v5 offset:47104
v_cvt_pk_bf16_f32 v20, v30, v31
Mavv 160,196,108
v_accvgpr_read_b32 v30, a42
v_accvgpr_read_b32 v31, a43
Mavv 164,200,104
ds_read_b128 v[124:127], v6 offset:47104
v_mul_f32 v30, 0x3e0293ee, v30
Mavv 164,204,108
v_mul_f32 v31, 0x3e0293ee, v31
v_cvt_pk_bf16_f32 v21, v30, v31
Mavv 168,208,104
v_accvgpr_read_b32 v30, a44
v_accvgpr_read_b32 v31, a45
Mavv 168,212,108
v_mul_f32 v30, 0x3e0293ee, v30
v_mul_f32 v31, 0x3e0293ee, v31
Mavv 172,216,104
v_cvt_pk_bf16_f32 v22, v30, v31
v_accvgpr_read_b32 v30, a46
Mavv 172,220,108
v_accvgpr_read_b32 v31, a47
v_mul_f32 v30, 0x3e0293ee, v30
Mavv 176,104,224
v_subrev_u32 v5, 0x10000, v24
v_subrev_u32 v6, 0x10000, v25
Mavv 176,108,228
v_mov_b32 v7, v24
v_mov_b32 v8, v25
Mavv 180,104,232
v_mul_f32 v31, 0x3e0293ee, v31
v_cvt_pk_bf16_f32 v23, v30, v31
Mavv 180,108,236
s_add_u32 s73, s80, 5120
buffer_store_dwordx4 v[20:23], v0, s[48:51], s73 offen sc1
Mavv 184,104,240
v_accvgpr_read_b32 v30, a16
v_accvgpr_read_b32 v31, a17
Mavv 184,108,244
v_cvt_pk_bf16_f32 v16, v30, v31
v_accvgpr_read_b32 v30, a18
Mavv 188,104,248
v_accvgpr_read_b32 v31, a19
v_cvt_pk_bf16_f32 v17, v30, v31
Mavv 188,108,252
s_waitcnt lgkmcnt(5)
v_accvgpr_read_b32 v30, a48
v_accvgpr_read_b32 v31, a49
Mavv 192,192,112
s_waitcnt lgkmcnt(3)
v_cvt_pk_bf16_f32 v18, v30, v31
v_accvgpr_read_b32 v30, a50
Mavv 192,196,116
v_accvgpr_read_b32 v31, a51
v_cvt_pk_bf16_f32 v19, v30, v31
Mavv 196,200,112
s_add_u32 s73, s81, 0
buffer_store_dwordx4 v[16:19], v0, s[48:51], s73 offen sc1
Mavv 196,204,116
s_waitcnt lgkmcnt(0)
v_accvgpr_read_b32 v30, a20
v_accvgpr_read_b32 v31, a21
Mavv 200,208,112
s_barrier
Mavv 200,212,116
v_cvt_pk_bf16_f32 v20, v30, v31
v_accvgpr_read_b32 v30, a22
Mavv 204,216,112
ds_read_b64_tr_b16 v[96:97], v26 offset:0
v_accvgpr_read_b32 v31, a23
Mavv 204,220,116
v_cvt_pk_bf16_f32 v21, v30, v31
v_accvgpr_read_b32 v30, a52
Mavv 208,112,224
ds_read_b64_tr_b16 v[98:99], v26 offset:256
v_accvgpr_read_b32 v31, a53
Mavv 208,116,228
v_cvt_pk_bf16_f32 v22, v30, v31
v_accvgpr_read_b32 v30, a54
Mavv 212,112,232
s_add_u32 m0, s69, 32768
s_add_u32 s72, s83, 256
ds_read_b64_tr_b16 v[100:101], v26 offset:8192
Mavv 212,116,236
buffer_load_dwordx4 v11, s[48:51], s72 offen sc1 lds
Mavv 216,112,240
ds_read_b64_tr_b16 v[102:103], v26 offset:8448
v_accvgpr_read_b32 v31, a55
Mavv 216,116,244
s_add_u32 m0, s69, 33792
s_add_u32 s72, s83, 256
v_cvt_pk_bf16_f32 v23, v30, v31
Mavv 220,112,248
s_add_u32 s73, s81, 2048
buffer_load_dwordx4 v12, s[48:51], s72 offen sc1 lds
Mavv 220,116,252
buffer_store_dwordx4 v[20:23], v0, s[48:51], s73 offen sc1
Mavv 224,192,120
s_add_u32 m0, s69, 34816
s_add_u32 s72, s83, 35072
v_accvgpr_read_b32 v30, a24
Mavv 224,196,124
buffer_load_dwordx4 v11, s[48:51], s72 offen sc1 lds
Mavv 228,200,120
ds_read_b64_tr_b16 v[104:105], v27 offset:0
v_accvgpr_read_b32 v31, a25
Mavv 228,204,124
v_cvt_pk_bf16_f32 v16, v30, v31
v_accvgpr_read_b32 v30, a26
Mavv 232,208,120
s_add_u32 m0, s69, 35840
s_add_u32 s72, s83, 35072
ds_read_b64_tr_b16 v[106:107], v27 offset:256
Mavv 232,212,124
buffer_load_dwordx4 v12, s[48:51], s72 offen sc1 lds
Mavv 236,216,120
ds_read_b64_tr_b16 v[108:109], v27 offset:8192
v_accvgpr_read_b32 v31, a27
Mavv 236,220,124
v_cvt_pk_bf16_f32 v17, v30, v31
v_accvgpr_read_b32 v30, a56
Mavv 240,120,224
s_add_u32 m0, s69, 36864
s_add_u32 s72, s82, 256
ds_read_b64_tr_b16 v[110:111], v27 offset:8448
Mavv 240,124,228
buffer_load_dwordx4 v11, s[48:51], s72 offen sc1 lds
Mavv 244,120,232
v_accvgpr_read_b32 v31, a57
v_cvt_pk_bf16_f32 v18, v30, v31
Mavv 244,124,236
s_add_u32 m0, s69, 37888
s_add_u32 s72, s82, 256
v_accvgpr_read_b32 v30, a58
Mavv 248,120,240
buffer_load_dwordx4 v12, s[48:51], s72 offen sc1 lds
Mavv 248,124,244
v_accvgpr_read_b32 v31, a59
v_cvt_pk_bf16_f32 v19, v30, v31
Mavv 252,120,248
s_add_u32 m0, s69, 38912
s_add_u32 s72, s82, 35072
s_add_u32 s73, s81, 4096
Mavv 252,124,252
s_waitcnt lgkmcnt(6)
buffer_load_dwordx4 v11, s[48:51], s72 offen sc1 lds
Zvvv 128,96,32
s_waitcnt lgkmcnt(4)
ds_read_b64_tr_b16 v[112:113], v26 offset:512
Mvvv 128,100,36
s_add_u32 m0, s69, 39936
s_add_u32 s72, s82, 35072
Zvvv 160,96,40
buffer_load_dwordx4 v12, s[48:51], s72 offen sc1 lds
ds_read_b64_tr_b16 v[114:115], v26 offset:768
Mvvv 160,100,44
buffer_store_dwordx4 v[16:19], v0, s[48:51], s73 offen sc1
Zvvv 192,96,48
ds_read_b64_tr_b16 v[116:117], v26 offset:8704
v_accvgpr_read_b32 v30, a28
Mvvv 192,100,52
v_accvgpr_read_b32 v31, a29
v_cvt_pk_bf16_f32 v20, v30, v31
Zvvv 224,96,56
s_waitcnt vmcnt(13)
ds_read_b64_tr_b16 v[118:119], v26 offset:8960
v_accvgpr_read_b32 v30, a30
Mvvv 224,100,60
s_waitcnt lgkmcnt(6)
ds_read_b128 v[64:67], v5 offset:0
v_accvgpr_read_b32 v31, a31
Zvvv 132,104,32
s_waitcnt lgkmcnt(5)
ds_read_b64_tr_b16 v[120:121], v27 offset:512
v_cvt_pk_bf16_f32 v21, v30, v31
Mvvv 132,108,36
ds_read_b128 v[68:71], v6 offset:0
v_accvgpr_read_b32 v30, a60
Zvvv 164,104,40
ds_read_b64_tr_b16 v[122:123], v27 offset:768
v_accvgpr_read_b32 v31, a61
Mvvv 164,108,44
ds_read_b128 v[72:75], v5 offset:2048
v_cvt_pk_bf16_f32 v22, v30, v31
Zvvv 196,104,48
ds_read_b64_tr_b16 v[124:125], v27 offset:8704
v_accvgpr_read_b32 v30, a62
Mvvv 196,108,52
ds_read_b128 v[76:79], v6 offset:2048
v_accvgpr_read_b32 v31, a63
Zvvv 228,104,56
ds_read_b64_tr_b16 v[126:127], v27 offset:8960
v_cvt_pk_bf16_f32 v23, v30, v31
Mvvv 228,108,60
s_waitcnt lgkmcnt(10)
s_add_u32 s73, s81, 6144
ds_read_b128 v[80:83], v5 offset:4096
Zvvv 136,112,32
s_waitcnt lgkmcnt(9)
ds_read_b64_tr_b16 v[96:97], v26 offset:1024
Mvvv 136,116,36
ds_read_b128 v[84:87], v6 offset:4096
Zvvv 168,112,40
ds_read_b64_tr_b16 v[98:99], v26 offset:1280
Mvvv 168,116,44
ds_read_b128 v[88:91], v5 offset:6144
Zvvv 200,112,48
s_lshl_b32 s63, s61, 1
s_mul_i32 s62, s64, 1114112
ds_read_b64_tr_b16 v[100:101], v26 offset:9216
Mvvv 200,116,52
s_add_u32 s62, s62, s70
s_add_u32 s62, s62, 16777216
ds_read_b128 v[92:95], v6 offset:6144
Zvvv 232,112,56
s_waitcnt lgkmcnt(12)
ds_read_b64_tr_b16 v[102:103], v26 offset:9472
v_lshrrev_b32 v1, 4, v0
Mvvv 232,116,60
s_waitcnt lgkmcnt(8)
buffer_store_dwordx4 v[20:23], v0, s[48:51], s73 offen sc1
Zvvv 140,120,32
s_add_u32 m0, s69, 65536
s_add_u32 s72, s83, 384
ds_read_b64_tr_b16 v[104:105], v27 offset:1024
Mvvv 140,124,36
buffer_load_dwordx4 v11, s[48:51], s72 offen sc1 lds
Zvvv 172,120,40
ds_read_b64_tr_b16 v[106:107], v27 offset:1280
v_accvgpr_read_b32 v30, a64
Mvvv 172,124,44
v_accvgpr_read_b32 v31, a65
v_mul_f32 v30, 0x3e0293ee, v30
Zvvv 204,120,48
s_add_u32 m0, s69, 66560
s_add_u32 s72, s83, 384
ds_read_b64_tr_b16 v[108:109], v27 offset:9216
Mvvv 204,124,52
buffer_load_dwordx4 v12, s[48:51], s72 offen sc1 lds
Zvvv 236,120,56
ds_read_b64_tr_b16 v[110:111], v27 offset:9472
v_mul_f32 v31, 0x3e0293ee, v31
Mvvv 236,124,60
s_waitcnt lgkmcnt(4)
v_cvt_pk_bf16_f32 v16, v30, v31
v_accvgpr_read_b32 v30, a66
Zvvv 144,96,32
s_add_u32 m0, s69, 67584
s_add_u32 s72, s83, 35200
ds_read_b64_tr_b16 v[112:113], v26 offset:1536
Mvvv 144,100,36
buffer_load_dwordx4 v11, s[48:51], s72 offen sc1 lds
Zvvv 176,96,40
ds_read_b64_tr_b16 v[114:115], v26 offset:1792
v_accvgpr_read_b32 v31, a67
Mvvv 176,100,44
v_mul_f32 v30, 0x3e0293ee, v30
v_mul_f32 v31, 0x3e0293ee, v31
Zvvv 208,96,48
s_add_u32 m0, s69, 68608
s_add_u32 s72, s83, 35200
ds_read_b64_tr_b16 v[116:117], v26 offset:9728
Mvvv 208,100,52
buffer_load_dwordx4 v12, s[48:51], s72 offen sc1 lds
Zvvv 240,96,56
ds_read_b64_tr_b16 v[118:119], v26 offset:9984
v_cvt_pk_bf16_f32 v17, v30, v31
Mvvv 240,100,60
s_waitcnt lgkmcnt(4)
v_accvgpr_read_b32 v30, a68
v_accvgpr_read_b32 v31, a69
Zvvv 148,104,32
s_add_u32 m0, s69, 69632
s_add_u32 s72, s82, 384
ds_read_b64_tr_b16 v[120:121], v27 offset:1536
Mvvv 148,108,36
buffer_load_dwordx4 v11, s[48:51], s72 offen sc1 lds
Zvvv 180,104,40
ds_read_b64_tr_b16 v[122:123], v27 offset:1792
v_mul_f32 v30, 0x3e0293ee, v30
Mvvv 180,108,44
v_mul_f32 v31, 0x3e0293ee, v31
v_cvt_pk_bf16_f32 v18, v30, v31
Zvvv 212,104,48
s_add_u32 m0, s69, 70656
s_add_u32 s72, s82, 384
ds_read_b64_tr_b16 v[124:125], v27 offset:9728
Mvvv 212,108,52
buffer_load_dwordx4 v12, s[48:51], s72 offen sc1 lds
Zvvv 244,104,56
s_waitcnt lgkmcnt(5)
ds_read_b64_tr_b16 v[126:127], v27 offset:9984
v_accvgpr_read_b32 v30, a70
Mvvv 244,108,60
s_add_u32 m0, s69, 71680
s_add_u32 s72, s82, 35200
v_accvgpr_read_b32 v31, a71
Zvvv 152,112,32
s_waitcnt lgkmcnt(4)
buffer_load_dwordx4 v11, s[48:51], s72 offen sc1 lds
Mvvv 152,116,36
v_mul_f32 v30, 0x3e0293ee, v30
v_mul_f32 v31, 0x3e0293ee, v31
Zvvv 184,112,40
s_add_u32 m0, s69, 72704
s_add_u32 s72, s82, 35200
v_cvt_pk_bf16_f32 v19, v30, v31
Mvvv 184,116,44
s_add_u32 s73, s80, 8192
buffer_load_dwordx4 v12, s[48:51], s72 offen sc1 lds
Zvvv 216,112,48
buffer_store_dwordx4 v[16:19], v0, s[48:51], s73 offen sc1
Mvvv 216,116,52
s_waitcnt lgkmcnt(0)
v_accvgpr_read_b32 v30, a72
v_accvgpr_read_b32 v31, a73
Zvvv 248,112,56
s_barrier
Mvvv 248,116,60
ds_read_b64_tr_b16 v[96:97], v26 offset:16384
v_mul_f32 v30, 0x3e0293ee, v30
Zvvv 156,120,32
ds_read_b64_tr_b16 v[98:99], v26 offset:16640
v_mul_f32 v31, 0x3e0293ee, v31
Mvvv 156,124,36
ds_read_b64_tr_b16 v[100:101], v26 offset:24576
v_cvt_pk_bf16_f32 v20, v30, v31
Zvvv 188,120,40
ds_read_b64_tr_b16 v[102:103], v26 offset:24832
v_accvgpr_read_b32 v30, a74
Mvvv 188,124,44
v_accvgpr_read_b32 v31, a75
v_mul_f32 v30, 0x3e0293ee, v30
Zvvv 220,120,48
ds_read_b64_tr_b16 v[104:105], v27 offset:16384
v_mul_f32 v31, 0x3e0293ee, v31
Mvvv 220,124,52
ds_read_b64_tr_b16 v[106:107], v27 offset:16640
v_cvt_pk_bf16_f32 v21, v30, v31
Zvvv 252,120,56
ds_read_b64_tr_b16 v[108:109], v27 offset:24576
v_accvgpr_read_b32 v30, a76
Mvvv 252,124,60
s_waitcnt lgkmcnt(5)
ds_read_b64_tr_b16 v[110:111], v27 offset:24832
v_accvgpr_read_b32 v31, a77
Mvvv 128,96,64
s_waitcnt lgkmcnt(4)
s_add_u32 m0, s69, 0
s_add_u32 s72, s83, 512
ds_read_b64_tr_b16 v[112:113], v26 offset:16896
Mvvv 128,100,68
buffer_load_dwordx4 v11, s[48:51], s72 offen sc1 lds
Mvvv 160,96,72
ds_read_b64_tr_b16 v[114:115], v26 offset:17152
v_mul_f32 v30, 0x3e0293ee, v30
Mvvv 160,100,76
v_mul_f32 v31, 0x3e0293ee, v31
v_cvt_pk_bf16_f32 v22, v30, v31
Mvvv 192,96,80
s_add_u32 m0, s69, 1024
s_add_u32 s72, s83, 512
ds_read_b64_tr_b16 v[116:117], v26 offset:25088
Mvvv 192,100,84
buffer_load_dwordx4 v12, s[48:51], s72 offen sc1 lds
Mvvv 224,96,88
s_waitcnt vmcnt(13)
ds_read_b64_tr_b16 v[118:119], v26 offset:25344
v_accvgpr_read_b32 v30, a78
Mvvv 224,100,92
s_waitcnt lgkmcnt(6)
ds_read_b128 v[32:35], v5 offset:32768
v_accvgpr_read_b32 v31, a79
Mvvv 132,104,64
s_waitcnt lgkmcnt(5)
ds_read_b64_tr_b16 v[120:121], v27 offset:16896
v_mul_f32 v30, 0x3e0293ee, v30
Mvvv 132,108,68
ds_read_b128 v[36:39], v6 offset:32768
v_mul_f32 v31, 0x3e0293ee, v31
Mvvv 164,104,72
ds_read_b64_tr_b16 v[122:123], v27 offset:17152
v_cvt_pk_bf16_f32 v23, v30, v31
Mvvv 164,108,76
s_add_u32 s73, s80, 9216
ds_read_b128 v[40:43], v5 offset:34816
v_add_u32 v1, 4, v1
Mvvv 196,104,80
ds_read_b64_tr_b16 v[124:125], v27 offset:25088
v_and_b32 v1, 63, v1
Mvvv 196,108,84
ds_read_b128 v[44:47], v6 offset:34816
v_subrev_u32 v2, s63, v1
Mvvv 228,104,88
ds_read_b64_tr_b16 v[126:127], v27 offset:25344
v_add_u32 v4, -2, v1
Mvvv 228,108,92
s_waitcnt lgkmcnt(10)
ds_read_b128 v[48:51], v5 offset:36864
v_sub_u32 v9, 33, v1
Mvvv 136,112,64
s_waitcnt lgkmcnt(9)
ds_read_b64_tr_b16 v[96:97], v26 offset:17408
v_cmp_eq_u32 vcc, 3, v2
Mvvv 136,116,68
ds_read_b128 v[52:55], v6 offset:36864
Mvvv 168,112,72
ds_read_b64_tr_b16 v[98:99], v26 offset:17664
v_cndmask_b32 v4, v4, v9, vcc
Mvvv 168,116,76
ds_read_b128 v[56:59], v5 offset:38912
v_add_u32 v9, -1, v1
Mvvv 200,112,80
ds_read_b64_tr_b16 v[100:101], v26 offset:25600
v_cmp_eq_u32 vcc, 2, v2
Mvvv 200,116,84
s_waitcnt lgkmcnt(11)
ds_read_b128 v[60:63], v6 offset:38912
Mvvv 232,112,88
s_add_u32 m0, s69, 2048
s_add_u32 s72, s83, 35328
ds_read_b64_tr_b16 v[102:103], v26 offset:25856
Mvvv 232,116,92
s_waitcnt lgkmcnt(12)
)"
R"(
buffer_load_dwordx4 v11, s[48:51], s72 offen sc1 lds
Mvvv 140,120,64
s_waitcnt lgkmcnt(8)
ds_read_b64_tr_b16 v[104:105], v27 offset:17408
v_cndmask_b32 v4, v4, v9, vcc
Mvvv 140,124,68
buffer_store_dwordx4 v[20:23], v0, s[48:51], s73 offen sc1
Mvvv 172,120,72
s_add_u32 m0, s69, 3072
s_add_u32 s72, s83, 35328
ds_read_b64_tr_b16 v[106:107], v27 offset:17664
Mvvv 172,124,76
buffer_load_dwordx4 v12, s[48:51], s72 offen sc1 lds
Mvvv 204,120,80
ds_read_b64_tr_b16 v[108:109], v27 offset:25600
v_accvgpr_read_b32 v30, a96
Mvvv 204,124,84
v_accvgpr_read_b32 v31, a97
v_mul_f32 v30, 0x3e0293ee, v30
Mvvv 236,120,88
s_add_u32 m0, s69, 4096
s_add_u32 s72, s82, 512
ds_read_b64_tr_b16 v[110:111], v27 offset:25856
Mvvv 236,124,92
s_waitcnt lgkmcnt(4)
buffer_load_dwordx4 v11, s[48:51], s72 offen sc1 lds
Mvvv 144,96,64
s_add_u32 m0, s71, 98304
s_add_u32 s72, s68, 2097152
ds_read_b64_tr_b16 v[112:113], v26 offset:17920
Mvvv 144,100,68
buffer_load_dwordx4 v3, s[16:19], s72 offen lds
Mvvv 176,96,72
s_add_u32 m0, s69, 5120
s_add_u32 s72, s82, 512
ds_read_b64_tr_b16 v[114:115], v26 offset:18176
Mvvv 176,100,76
buffer_load_dwordx4 v12, s[48:51], s72 offen sc1 lds
Mvvv 208,96,80
s_add_u32 m0, s71, 99200
s_add_u32 s72, s68, 2097152
ds_read_b64_tr_b16 v[116:117], v26 offset:26112
Mvvv 208,100,84
buffer_load_dwordx4 v3, s[16:19], s72 offen offset:128 lds
Mvvv 240,96,88
s_add_u32 m0, s69, 6144
s_add_u32 s72, s82, 35328
ds_read_b64_tr_b16 v[118:119], v26 offset:26368
Mvvv 240,100,92
s_waitcnt lgkmcnt(4)
buffer_load_dwordx4 v11, s[48:51], s72 offen sc1 lds
Mvvv 148,104,64
s_add_u32 m0, s71, 106496
s_add_u32 s72, s68, 2359296
ds_read_b64_tr_b16 v[120:121], v27 offset:17920
Mvvv 148,108,68
buffer_load_dwordx4 v3, s[16:19], s72 offen lds
Mvvv 180,104,72
s_add_u32 m0, s69, 7168
s_add_u32 s72, s82, 35328
ds_read_b64_tr_b16 v[122:123], v27 offset:18176
Mvvv 180,108,76
buffer_load_dwordx4 v12, s[48:51], s72 offen sc1 lds
Mvvv 212,104,80
s_add_u32 m0, s71, 107392
s_add_u32 s72, s68, 2359296
ds_read_b64_tr_b16 v[124:125], v27 offset:26112
Mvvv 212,108,84
buffer_load_dwordx4 v3, s[16:19], s72 offen offset:128 lds
Mvvv 244,104,88
ds_read_b64_tr_b16 v[126:127], v27 offset:26368
v_mul_f32 v31, 0x3e0293ee, v31
Mvvv 244,108,92
s_waitcnt lgkmcnt(6)
v_cvt_pk_bf16_f32 v16, v30, v31
v_accvgpr_read_b32 v30, a98
Mvvv 152,112,64
s_waitcnt lgkmcnt(4)
v_accvgpr_read_b32 v31, a99
v_mul_f32 v30, 0x3e0293ee, v30
Mvvv 152,116,68
v_mul_f32 v31, 0x3e0293ee, v31
v_cvt_pk_bf16_f32 v17, v30, v31
Mvvv 184,112,72
v_accvgpr_read_b32 v30, a100
v_accvgpr_read_b32 v31, a101
Mvvv 184,116,76
v_mul_f32 v30, 0x3e0293ee, v30
v_mul_f32 v31, 0x3e0293ee, v31
Mvvv 216,112,80
v_cvt_pk_bf16_f32 v18, v30, v31
v_accvgpr_read_b32 v30, a102
Mvvv 216,116,84
s_waitcnt lgkmcnt(0)
v_accvgpr_read_b32 v31, a103
v_mul_f32 v30, 0x3e0293ee, v30
Mvvv 248,112,88
s_barrier
Mvvv 248,116,92
ds_read_b64_tr_b16 v[96:97], v26 offset:32768
v_mul_f32 v31, 0x3e0293ee, v31
Mvvv 156,120,64
ds_read_b64_tr_b16 v[98:99], v26 offset:33024
v_cvt_pk_bf16_f32 v19, v30, v31
Mvvv 156,124,68
s_add_u32 s73, s80, 12288
ds_read_b64_tr_b16 v[100:101], v26 offset:40960
v_sub_u32 v9, 32, v1
Mvvv 188,120,72
ds_read_b64_tr_b16 v[102:103], v26 offset:41216
v_cmp_eq_u32 vcc, 1, v2
Mvvv 188,124,76
buffer_store_dwordx4 v[16:19], v0, s[48:51], s73 offen sc1
Mvvv 220,120,80
ds_read_b64_tr_b16 v[104:105], v27 offset:32768
v_accvgpr_read_b32 v30, a104
Mvvv 220,124,84
ds_read_b64_tr_b16 v[106:107], v27 offset:33024
v_accvgpr_read_b32 v31, a105
Mvvv 252,120,88
ds_read_b64_tr_b16 v[108:109], v27 offset:40960
v_mul_f32 v30, 0x3e0293ee, v30
Mvvv 252,124,92
s_waitcnt lgkmcnt(5)
ds_read_b64_tr_b16 v[110:111], v27 offset:41216
v_mul_f32 v31, 0x3e0293ee, v31
Mvvv 128,96,32
s_waitcnt lgkmcnt(4)
s_add_u32 m0, s69, 32768
s_add_u32 s72, s83, 640
ds_read_b64_tr_b16 v[112:113], v26 offset:33280
Mvvv 128,100,36
buffer_load_dwordx4 v11, s[48:51], s72 offen sc1 lds
Mvvv 160,96,40
ds_read_b64_tr_b16 v[114:115], v26 offset:33536
v_cvt_pk_bf16_f32 v20, v30, v31
Mvvv 160,100,44
v_accvgpr_read_b32 v30, a106
v_accvgpr_read_b32 v31, a107
Mvvv 192,96,48
s_add_u32 m0, s69, 33792
s_add_u32 s72, s83, 640
ds_read_b64_tr_b16 v[116:117], v26 offset:41472
Mvvv 192,100,52
buffer_load_dwordx4 v12, s[48:51], s72 offen sc1 lds
Mvvv 224,96,56
s_waitcnt vmcnt(17)
ds_read_b64_tr_b16 v[118:119], v26 offset:41728
v_mul_f32 v30, 0x3e0293ee, v30
Mvvv 224,100,60
s_waitcnt lgkmcnt(6)
ds_read_b128 v[64:67], v7 offset:0
v_mul_f32 v31, 0x3e0293ee, v31
Mvvv 132,104,32
s_waitcnt lgkmcnt(5)
ds_read_b64_tr_b16 v[120:121], v27 offset:33280
v_cvt_pk_bf16_f32 v21, v30, v31
Mvvv 132,108,36
ds_read_b128 v[68:71], v8 offset:0
v_accvgpr_read_b32 v30, a108
Mvvv 164,104,40
ds_read_b64_tr_b16 v[122:123], v27 offset:33536
v_accvgpr_read_b32 v31, a109
Mvvv 164,108,44
ds_read_b128 v[72:75], v7 offset:2048
v_mul_f32 v30, 0x3e0293ee, v30
Mvvv 196,104,48
ds_read_b64_tr_b16 v[124:125], v27 offset:41472
v_mul_f32 v31, 0x3e0293ee, v31
Mvvv 196,108,52
ds_read_b128 v[76:79], v8 offset:2048
v_cvt_pk_bf16_f32 v22, v30, v31
Mvvv 228,104,56
ds_read_b64_tr_b16 v[126:127], v27 offset:41728
v_accvgpr_read_b32 v30, a110
Mvvv 228,108,60
s_waitcnt lgkmcnt(10)
ds_read_b128 v[80:83], v7 offset:4096
v_accvgpr_read_b32 v31, a111
Mvvv 136,112,32
s_waitcnt lgkmcnt(9)
ds_read_b64_tr_b16 v[96:97], v26 offset:33792
v_mul_f32 v30, 0x3e0293ee, v30
Mvvv 136,116,36
ds_read_b128 v[84:87], v8 offset:4096
v_mul_f32 v31, 0x3e0293ee, v31
Mvvv 168,112,40
ds_read_b64_tr_b16 v[98:99], v26 offset:34048
v_cvt_pk_bf16_f32 v23, v30, v31
Mvvv 168,116,44
s_add_u32 s73, s80, 13312
ds_read_b128 v[88:91], v7 offset:6144
v_cndmask_b32 v4, v4, v9, vcc
Mvvv 200,112,48
ds_read_b64_tr_b16 v[100:101], v26 offset:41984
v_cmp_gt_i32 vcc, 1, v2
Mvvv 200,116,52
s_waitcnt lgkmcnt(11)
ds_read_b128 v[92:95], v8 offset:6144
Mvvv 232,112,56
s_add_u32 m0, s69, 34816
s_add_u32 s72, s83, 35456
ds_read_b64_tr_b16 v[102:103], v26 offset:42240
Mvvv 232,116,60
s_waitcnt lgkmcnt(12)
buffer_load_dwordx4 v11, s[48:51], s72 offen sc1 lds
Mvvv 140,120,32
s_waitcnt lgkmcnt(8)
ds_read_b64_tr_b16 v[104:105], v27 offset:33792
v_cndmask_b32 v4, v4, v1, vcc
Mvvv 140,124,36
buffer_store_dwordx4 v[20:23], v0, s[48:51], s73 offen sc1
Mvvv 172,120,40
s_add_u32 m0, s69, 35840
s_add_u32 s72, s83, 35456
ds_read_b64_tr_b16 v[106:107], v27 offset:34048
Mvvv 172,124,44
buffer_load_dwordx4 v12, s[48:51], s72 offen sc1 lds
Mvvv 204,120,48
ds_read_b64_tr_b16 v[108:109], v27 offset:41984
v_accvgpr_read_b32 v30, a80
Mvvv 204,124,52
v_accvgpr_read_b32 v31, a81
v_cvt_pk_bf16_f32 v16, v30, v31
Mvvv 236,120,56
s_add_u32 m0, s69, 36864
s_add_u32 s72, s82, 640
ds_read_b64_tr_b16 v[110:111], v27 offset:42240
Mvvv 236,124,60
s_waitcnt lgkmcnt(4)
buffer_load_dwordx4 v11, s[48:51], s72 offen sc1 lds
Mvvv 144,96,32
s_add_u32 m0, s71, 114688
s_add_u32 s72, s68, 2621440
ds_read_b64_tr_b16 v[112:113], v26 offset:34304
Mvvv 144,100,36
buffer_load_dwordx4 v3, s[16:19], s72 offen lds
Mvvv 176,96,40
s_add_u32 m0, s69, 37888
s_add_u32 s72, s82, 640
ds_read_b64_tr_b16 v[114:115], v26 offset:34560
Mvvv 176,100,44
buffer_load_dwordx4 v12, s[48:51], s72 offen sc1 lds
Mvvv 208,96,48
s_add_u32 m0, s71, 115584
s_add_u32 s72, s68, 2621440
ds_read_b64_tr_b16 v[116:117], v26 offset:42496
Mvvv 208,100,52
buffer_load_dwordx4 v3, s[16:19], s72 offen offset:128 lds
Mvvv 240,96,56
s_add_u32 m0, s69, 38912
s_add_u32 s72, s82, 35456
ds_read_b64_tr_b16 v[118:119], v26 offset:42752
Mvvv 240,100,60
s_waitcnt lgkmcnt(4)
buffer_load_dwordx4 v11, s[48:51], s72 offen sc1 lds
Mvvv 148,104,32
s_add_u32 m0, s71, 122880
s_add_u32 s72, s68, 2883584
ds_read_b64_tr_b16 v[120:121], v27 offset:34304
Mvvv 148,108,36
buffer_load_dwordx4 v3, s[16:19], s72 offen lds
Mvvv 180,104,40
s_add_u32 m0, s69, 39936
s_add_u32 s72, s82, 35456
ds_read_b64_tr_b16 v[122:123], v27 offset:34560
Mvvv 180,108,44
buffer_load_dwordx4 v12, s[48:51], s72 offen sc1 lds
Mvvv 212,104,48
s_add_u32 m0, s71, 123776
s_add_u32 s72, s68, 2883584
ds_read_b64_tr_b16 v[124:125], v27 offset:42496
Mvvv 212,108,52
buffer_load_dwordx4 v3, s[16:19], s72 offen offset:128 lds
Mvvv 244,104,56
ds_read_b64_tr_b16 v[126:127], v27 offset:42752
v_accvgpr_read_b32 v30, a82
Mvvv 244,108,60
s_waitcnt lgkmcnt(6)
v_accvgpr_read_b32 v31, a83
v_cvt_pk_bf16_f32 v17, v30, v31
Mvvv 152,112,32
s_waitcnt lgkmcnt(4)
v_accvgpr_read_b32 v30, a112
v_accvgpr_read_b32 v31, a113
Mvvv 152,116,36
v_cvt_pk_bf16_f32 v18, v30, v31
v_accvgpr_read_b32 v30, a114
Mvvv 184,112,40
v_accvgpr_read_b32 v31, a115
v_cvt_pk_bf16_f32 v19, v30, v31
Mvvv 184,116,44
s_add_u32 s73, s81, 1024
buffer_store_dwordx4 v[16:19], v0, s[48:51], s73 offen sc1
Mvvv 216,112,48
v_accvgpr_read_b32 v30, a84
v_accvgpr_read_b32 v31, a85
Mvvv 216,116,52
s_waitcnt lgkmcnt(0)
v_cvt_pk_bf16_f32 v20, v30, v31
v_accvgpr_read_b32 v30, a86
Mvvv 248,112,56
s_barrier
Mvvv 248,116,60
ds_read_b64_tr_b16 v[96:97], v26 offset:49152
v_accvgpr_read_b32 v31, a87
Mvvv 156,120,32
ds_read_b64_tr_b16 v[98:99], v26 offset:49408
v_cvt_pk_bf16_f32 v21, v30, v31
Mvvv 156,124,36
ds_read_b64_tr_b16 v[100:101], v26 offset:57344
v_accvgpr_read_b32 v30, a116
Mvvv 188,120,40
ds_read_b64_tr_b16 v[102:103], v26 offset:57600
v_accvgpr_read_b32 v31, a117
Mvvv 188,124,44
v_cvt_pk_bf16_f32 v22, v30, v31
v_accvgpr_read_b32 v30, a118
Mvvv 220,120,48
ds_read_b64_tr_b16 v[104:105], v27 offset:49152
v_accvgpr_read_b32 v31, a119
Mvvv 220,124,52
ds_read_b64_tr_b16 v[106:107], v27 offset:49408
v_cvt_pk_bf16_f32 v23, v30, v31
Mvvv 252,120,56
s_add_u32 s73, s81, 3072
ds_read_b64_tr_b16 v[108:109], v27 offset:57344
v_mul_u32_u24 v4, 34816, v4
Mvvv 252,124,60
s_waitcnt lgkmcnt(5)
s_add_u32 s58, s62, 0
ds_read_b64_tr_b16 v[110:111], v27 offset:57600
v_add_u32 v4, s58, v4
Mvvv 128,96,64
s_waitcnt lgkmcnt(4)
s_add_u32 m0, s69, 65536
s_add_u32 s72, s83, 768
ds_read_b64_tr_b16 v[112:113], v26 offset:49664
Mvvv 128,100,68
s_and_b32 s58, s65, 6
buffer_load_dwordx4 v11, s[48:51], s72 offen sc1 lds
Mvvv 160,96,72
s_lshl_b32 s58, s58, 2
s_mul_i32 s66, s58, 36864
ds_read_b64_tr_b16 v[114:115], v26 offset:49920
Mvvv 160,100,76
s_lshl_b32 s67, s58, 18
buffer_store_dwordx4 v[20:23], v0, s[48:51], s73 offen sc1
Mvvv 192,96,80
s_add_u32 m0, s69, 66560
s_add_u32 s72, s83, 768
ds_read_b64_tr_b16 v[116:117], v26 offset:57856
Mvvv 192,100,84
s_lshl_b32 s58, s61, 8
buffer_load_dwordx4 v12, s[48:51], s72 offen sc1 lds
Mvvv 224,96,88
s_waitcnt vmcnt(19) lgkmcnt(3)
ds_read_b64_tr_b16 v[118:119], v26 offset:58112
v_accvgpr_read_b32 v30, a88
Mvvv 224,100,92
s_add_u32 s67, s67, s58
ds_read_b128 v[32:35], v5 offset:0
v_accvgpr_read_b32 v31, a89
Mvvv 132,104,64
s_lshl3_add_u32 s67, s70, s67
ds_read_b64_tr_b16 v[120:121], v27 offset:49664
v_cvt_pk_bf16_f32 v16, v30, v31
Mvvv 132,108,68
s_and_b32 s86, s64, 7
ds_read_b128 v[36:39], v6 offset:0
v_accvgpr_read_b32 v30, a90
Mvvv 164,104,72
s_lshl_b32 s58, s86, 12
ds_read_b64_tr_b16 v[122:123], v27 offset:49920
v_accvgpr_read_b32 v31, a91
Mvvv 164,108,76
s_add_u32 s66, s66, s58
ds_read_b128 v[40:43], v5 offset:2048
v_cvt_pk_bf16_f32 v17, v30, v31
Mvvv 196,104,80
s_add_u32 s66, s66, 54525952
ds_read_b64_tr_b16 v[124:125], v27 offset:57856
v_accvgpr_read_b32 v30, a120
Mvvv 196,108,84
s_lshr_b32 s79, s65, 1
ds_read_b128 v[44:47], v6 offset:2048
v_accvgpr_read_b32 v31, a121
Mvvv 228,104,88
ds_read_b64_tr_b16 v[126:127], v27 offset:58112
v_cvt_pk_bf16_f32 v18, v30, v31
Mvvv 228,108,92
s_waitcnt lgkmcnt(10)
ds_read_b128 v[48:51], v5 offset:4096
v_accvgpr_read_b32 v30, a122
Mvvv 136,112,64
s_waitcnt lgkmcnt(9)
ds_read_b64_tr_b16 v[96:97], v26 offset:50176
v_accvgpr_read_b32 v31, a123
Mvvv 136,116,68
ds_read_b128 v[52:55], v6 offset:4096
v_cvt_pk_bf16_f32 v19, v30, v31
Mvvv 168,112,72
s_waitcnt lgkmcnt(8)
s_add_u32 s73, s81, 5120
ds_read_b64_tr_b16 v[98:99], v26 offset:50432
Mvvv 168,116,76
s_cmp_le_u32 s79, 0
s_cselect_b32 s76, 1, 0
ds_read_b128 v[56:59], v5 offset:6144
Mvvv 200,112,80
s_cmp_le_u32 s79, 1
s_cselect_b32 s77, 2, 1
ds_read_b64_tr_b16 v[100:101], v26 offset:58368
Mvvv 200,116,84
s_cmp_le_u32 s79, 2
s_cselect_b32 s78, 3, 2
ds_read_b128 v[60:63], v6 offset:6144
)"
R"(
Mvvv 232,112,88
s_add_u32 m0, s69, 67584
s_add_u32 s72, s83, 35584
ds_read_b64_tr_b16 v[102:103], v26 offset:58624
Mvvv 232,116,92
s_waitcnt lgkmcnt(8)
buffer_load_dwordx4 v11, s[48:51], s72 offen sc1 lds
Mvvv 140,120,64
s_cmp_le_u32 s79, s57
s_addc_u32 s58, s57, 0
ds_read_b64_tr_b16 v[104:105], v27 offset:50176
Mvvv 140,124,68
buffer_store_dwordx4 v[16:19], v0, s[48:51], s73 offen sc1
Mvvv 172,120,72
s_add_u32 m0, s69, 68608
s_add_u32 s72, s83, 35584
ds_read_b64_tr_b16 v[106:107], v27 offset:50432
Mvvv 172,124,76
buffer_load_dwordx4 v12, s[48:51], s72 offen sc1 lds
Mvvv 204,120,80
ds_read_b64_tr_b16 v[108:109], v27 offset:58368
v_accvgpr_read_b32 v30, a92
Mvvv 204,124,84
v_accvgpr_read_b32 v31, a93
v_cvt_pk_bf16_f32 v20, v30, v31
Mvvv 236,120,88
s_add_u32 m0, s69, 69632
s_add_u32 s72, s82, 768
ds_read_b64_tr_b16 v[110:111], v27 offset:58624
Mvvv 236,124,92
s_waitcnt lgkmcnt(4)
buffer_load_dwordx4 v11, s[48:51], s72 offen sc1 lds
Mvvv 144,96,64
s_add_u32 m0, s71, 131072
s_add_u32 s72, s68, 3145728
ds_read_b64_tr_b16 v[112:113], v26 offset:50688
Mvvv 144,100,68
buffer_load_dwordx4 v3, s[16:19], s72 offen lds
Mvvv 176,96,72
s_add_u32 m0, s69, 70656
s_add_u32 s72, s82, 768
ds_read_b64_tr_b16 v[114:115], v26 offset:50944
Mvvv 176,100,76
buffer_load_dwordx4 v12, s[48:51], s72 offen sc1 lds
Mvvv 208,96,80
s_add_u32 m0, s71, 131968
s_add_u32 s72, s68, 3145728
ds_read_b64_tr_b16 v[116:117], v26 offset:58880
Mvvv 208,100,84
buffer_load_dwordx4 v3, s[16:19], s72 offen offset:128 lds
Mvvv 240,96,88
s_add_u32 m0, s69, 71680
s_add_u32 s72, s82, 35584
ds_read_b64_tr_b16 v[118:119], v26 offset:59136
Mvvv 240,100,92
s_waitcnt lgkmcnt(4)
buffer_load_dwordx4 v11, s[48:51], s72 offen sc1 lds
Mvvv 148,104,64
s_add_u32 m0, s71, 139264
s_add_u32 s72, s68, 3407872
ds_read_b64_tr_b16 v[120:121], v27 offset:50688
Mvvv 148,108,68
buffer_load_dwordx4 v3, s[16:19], s72 offen lds
Mvvv 180,104,72
s_add_u32 m0, s69, 72704
s_add_u32 s72, s82, 35584
ds_read_b64_tr_b16 v[122:123], v27 offset:50944
Mvvv 180,108,76
buffer_load_dwordx4 v12, s[48:51], s72 offen sc1 lds
Mvvv 212,104,80
s_add_u32 m0, s71, 140160
s_add_u32 s72, s68, 3407872
ds_read_b64_tr_b16 v[124:125], v27 offset:58880
Mvvv 212,108,84
buffer_load_dwordx4 v3, s[16:19], s72 offen offset:128 lds
Mvvv 244,104,88
ds_read_b64_tr_b16 v[126:127], v27 offset:59136
v_accvgpr_read_b32 v30, a94
Mvvv 244,108,92
s_waitcnt lgkmcnt(6)
v_accvgpr_read_b32 v31, a95
v_cvt_pk_bf16_f32 v21, v30, v31
Mvvv 152,112,64
s_waitcnt lgkmcnt(4)
v_accvgpr_read_b32 v30, a124
v_accvgpr_read_b32 v31, a125
Mvvv 152,116,68
v_cvt_pk_bf16_f32 v22, v30, v31
v_accvgpr_read_b32 v30, a126
Mvvv 184,112,72
v_accvgpr_read_b32 v31, a127
v_cvt_pk_bf16_f32 v23, v30, v31
Mvvv 184,116,76
s_add_u32 s73, s81, 7168
buffer_store_dwordx4 v[20:23], v0, s[48:51], s73 offen sc1
Mvvv 216,112,80
v_accvgpr_read_b32 v30, a128
v_accvgpr_read_b32 v31, a129
Mvvv 216,116,84
s_waitcnt vmcnt(30) lgkmcnt(0)
v_mul_f32 v30, 0x3e0293ee, v30
v_mul_f32 v31, 0x3e0293ee, v31
Mvvv 248,112,88
s_barrier
Mvvv 248,116,92
ds_read_b64_tr_b16 v[96:97], v26 offset:0
v_cvt_pk_bf16_f32 v16, v30, v31
Mvvv 156,120,64
ds_read_b64_tr_b16 v[98:99], v26 offset:256
v_accvgpr_read_b32 v30, a130
Mvvv 156,124,68
ds_read_b64_tr_b16 v[100:101], v26 offset:8192
v_accvgpr_read_b32 v31, a131
Mvvv 188,120,72
ds_read_b64_tr_b16 v[102:103], v26 offset:8448
v_mul_f32 v30, 0x3e0293ee, v30
Mvvv 188,124,76
v_mul_f32 v31, 0x3e0293ee, v31
v_cvt_pk_bf16_f32 v17, v30, v31
Mvvv 220,120,80
ds_read_b64_tr_b16 v[104:105], v27 offset:0
v_accvgpr_read_b32 v30, a132
Mvvv 220,124,84
ds_read_b64_tr_b16 v[106:107], v27 offset:256
v_accvgpr_read_b32 v31, a133
Mvvv 252,120,88
ds_read_b64_tr_b16 v[108:109], v27 offset:8192
v_mul_f32 v30, 0x3e0293ee, v30
Mvvv 252,124,92
s_waitcnt lgkmcnt(5)
ds_read_b64_tr_b16 v[110:111], v27 offset:8448
v_mul_f32 v31, 0x3e0293ee, v31
Mvvv 128,96,32
s_waitcnt lgkmcnt(4)
s_add_u32 m0, s69, 0
s_add_u32 s72, s83, 896
ds_read_b64_tr_b16 v[112:113], v26 offset:512
Mvvv 128,100,36
buffer_load_dwordx4 v11, s[48:51], s72 offen sc1 lds
Mvvv 160,96,40
ds_read_b64_tr_b16 v[114:115], v26 offset:768
v_cvt_pk_bf16_f32 v18, v30, v31
Mvvv 160,100,44
v_accvgpr_read_b32 v30, a134
v_accvgpr_read_b32 v31, a135
Mvvv 192,96,48
s_add_u32 m0, s69, 1024
s_add_u32 s72, s83, 896
ds_read_b64_tr_b16 v[116:117], v26 offset:8704
Mvvv 192,100,52
buffer_load_dwordx4 v12, s[48:51], s72 offen sc1 lds
Mvvv 224,96,56
s_waitcnt vmcnt(19)
ds_read_b64_tr_b16 v[118:119], v26 offset:8960
v_mul_f32 v30, 0x3e0293ee, v30
Mvvv 224,100,60
s_waitcnt lgkmcnt(6)
ds_read_b128 v[64:67], v5 offset:32768
v_mul_f32 v31, 0x3e0293ee, v31
Mvvv 132,104,32
s_waitcnt lgkmcnt(5)
ds_read_b64_tr_b16 v[120:121], v27 offset:512
v_cvt_pk_bf16_f32 v19, v30, v31
Mvvv 132,108,36
s_add_u32 s73, s80, 34816
ds_read_b128 v[68:71], v6 offset:32768
Mvvv 164,104,40
s_cmp_eq_u32 s57, 3
s_cselect_b32 s58, s79, s58
ds_read_b64_tr_b16 v[122:123], v27 offset:768
Mvvv 164,108,44
s_mul_i32 s58, s58, 1179648
s_add_u32 s87, s66, s58
ds_read_b128 v[72:75], v5 offset:34816
Mvvv 196,104,48
s_lshl3_add_u32 s91, s61, s65
s_sub_u32 s91, s91, 8
ds_read_b64_tr_b16 v[124:125], v27 offset:8704
Mvvv 196,108,52
ds_read_b128 v[76:79], v6 offset:34816
v_add_u32 v2, s63, v1
Mvvv 228,104,56
ds_read_b64_tr_b16 v[126:127], v27 offset:8960
v_add_u32 v9, -32, v2
Mvvv 228,108,60
s_waitcnt lgkmcnt(10)
ds_read_b128 v[80:83], v5 offset:36864
v_mul_u32_u24 v9, 36864, v9
Mvvv 136,112,32
s_waitcnt lgkmcnt(9)
ds_read_b64_tr_b16 v[96:97], v26 offset:1024
v_add_u32 v9, s87, v9
Mvvv 136,116,36
ds_read_b128 v[84:87], v6 offset:36864
v_cmp_lt_u32 vcc, 31, v2
Mvvv 168,112,40
ds_read_b64_tr_b16 v[98:99], v26 offset:1280
Mvvv 168,116,44
ds_read_b128 v[88:91], v5 offset:38912
v_cndmask_b32 v4, v4, v9, vcc
Mvvv 200,112,48
ds_read_b64_tr_b16 v[100:101], v26 offset:9216
v_lshrrev_b32 v1, 4, v0
Mvvv 200,116,52
s_waitcnt lgkmcnt(11)
ds_read_b128 v[92:95], v6 offset:38912
v_add_u32 v1, 2, v1
Mvvv 232,112,56
s_add_u32 m0, s69, 2048
s_add_u32 s72, s83, 35712
ds_read_b64_tr_b16 v[102:103], v26 offset:9472
Mvvv 232,116,60
s_waitcnt lgkmcnt(12)
buffer_load_dwordx4 v11, s[48:51], s72 offen sc1 lds
Mvvv 140,120,32
s_waitcnt lgkmcnt(8)
ds_read_b64_tr_b16 v[104:105], v27 offset:1024
v_and_b32 v1, 63, v1
Mvvv 140,124,36
buffer_store_dwordx4 v[16:19], v0, s[48:51], s73 offen sc1
Mvvv 172,120,40
s_add_u32 m0, s69, 3072
s_add_u32 s72, s83, 35712
ds_read_b64_tr_b16 v[106:107], v27 offset:1280
Mvvv 172,124,44
buffer_load_dwordx4 v12, s[48:51], s72 offen sc1 lds
Mvvv 204,120,48
ds_read_b64_tr_b16 v[108:109], v27 offset:9216
v_accvgpr_read_b32 v30, a136
Mvvv 204,124,52
v_accvgpr_read_b32 v31, a137
v_mul_f32 v30, 0x3e0293ee, v30
Mvvv 236,120,56
s_add_u32 m0, s69, 4096
s_add_u32 s72, s82, 896
ds_read_b64_tr_b16 v[110:111], v27 offset:9472
Mvvv 236,124,60
s_waitcnt lgkmcnt(4)
buffer_load_dwordx4 v11, s[48:51], s72 offen sc1 lds
Mvvv 144,96,32
s_add_u32 m0, s71, 147456
s_add_u32 s72, s68, 3670016
ds_read_b64_tr_b16 v[112:113], v26 offset:1536
Mvvv 144,100,36
buffer_load_dwordx4 v3, s[16:19], s72 offen lds
Mvvv 176,96,40
s_add_u32 m0, s69, 5120
s_add_u32 s72, s82, 896
ds_read_b64_tr_b16 v[114:115], v26 offset:1792
Mvvv 176,100,44
buffer_load_dwordx4 v12, s[48:51], s72 offen sc1 lds
Mvvv 208,96,48
s_add_u32 m0, s71, 148352
s_add_u32 s72, s68, 3670016
ds_read_b64_tr_b16 v[116:117], v26 offset:9728
Mvvv 208,100,52
buffer_load_dwordx4 v3, s[16:19], s72 offen offset:128 lds
Mvvv 240,96,56
s_add_u32 m0, s69, 6144
s_add_u32 s72, s82, 35712
ds_read_b64_tr_b16 v[118:119], v26 offset:9984
Mvvv 240,100,60
s_waitcnt lgkmcnt(4)
buffer_load_dwordx4 v11, s[48:51], s72 offen sc1 lds
Mvvv 148,104,32
s_add_u32 m0, s71, 155648
s_add_u32 s72, s68, 3932160
ds_read_b64_tr_b16 v[120:121], v27 offset:1536
Mvvv 148,108,36
buffer_load_dwordx4 v3, s[16:19], s72 offen lds
Mvvv 180,104,40
s_add_u32 m0, s69, 7168
s_add_u32 s72, s82, 35712
ds_read_b64_tr_b16 v[122:123], v27 offset:1792
Mvvv 180,108,44
buffer_load_dwordx4 v12, s[48:51], s72 offen sc1 lds
Mvvv 212,104,48
s_add_u32 m0, s71, 156544
s_add_u32 s72, s68, 3932160
ds_read_b64_tr_b16 v[124:125], v27 offset:9728
Mvvv 212,108,52
buffer_load_dwordx4 v3, s[16:19], s72 offen offset:128 lds
Mvvv 244,104,56
ds_read_b64_tr_b16 v[126:127], v27 offset:9984
v_mul_f32 v31, 0x3e0293ee, v31
Mvvv 244,108,60
s_waitcnt lgkmcnt(6)
v_cvt_pk_bf16_f32 v20, v30, v31
v_accvgpr_read_b32 v30, a138
Mvvv 152,112,32
s_waitcnt lgkmcnt(4)
v_accvgpr_read_b32 v31, a139
v_mul_f32 v30, 0x3e0293ee, v30
Mvvv 152,116,36
v_mul_f32 v31, 0x3e0293ee, v31
v_cvt_pk_bf16_f32 v21, v30, v31
Mvvv 184,112,40
v_accvgpr_read_b32 v30, a140
v_accvgpr_read_b32 v31, a141
Mvvv 184,116,44
v_mul_f32 v30, 0x3e0293ee, v30
v_mul_f32 v31, 0x3e0293ee, v31
Mvvv 216,112,48
v_cvt_pk_bf16_f32 v22, v30, v31
v_accvgpr_read_b32 v30, a142
Mvvv 216,116,52
s_waitcnt vmcnt(29) lgkmcnt(0)
v_accvgpr_read_b32 v31, a143
v_mul_f32 v30, 0x3e0293ee, v30
Mvvv 248,112,56
s_barrier
Mvvv 248,116,60
ds_read_b64_tr_b16 v[96:97], v26 offset:16384
v_mul_f32 v31, 0x3e0293ee, v31
Mvvv 156,120,32
ds_read_b64_tr_b16 v[98:99], v26 offset:16640
v_cvt_pk_bf16_f32 v23, v30, v31
Mvvv 156,124,36
s_add_u32 s73, s80, 35840
ds_read_b64_tr_b16 v[100:101], v26 offset:24576
v_subrev_u32 v2, s63, v1
Mvvv 188,120,40
ds_read_b64_tr_b16 v[102:103], v26 offset:24832
v_add_u32 v14, -2, v1
Mvvv 188,124,44
buffer_store_dwordx4 v[20:23], v0, s[48:51], s73 offen sc1
Mvvv 220,120,48
ds_read_b64_tr_b16 v[104:105], v27 offset:16384
v_accvgpr_read_b32 v30, a160
Mvvv 220,124,52
ds_read_b64_tr_b16 v[106:107], v27 offset:16640
v_accvgpr_read_b32 v31, a161
Mvvv 252,120,56
ds_read_b64_tr_b16 v[108:109], v27 offset:24576
v_mul_f32 v30, 0x3e0293ee, v30
Mvvv 252,124,60
s_waitcnt lgkmcnt(5)
ds_read_b64_tr_b16 v[110:111], v27 offset:24832
v_mul_f32 v31, 0x3e0293ee, v31
Mvvv 128,96,64
s_waitcnt lgkmcnt(4)
s_add_u32 m0, s69, 32768
s_add_u32 s72, s83, 1024
ds_read_b64_tr_b16 v[112:113], v26 offset:16896
Mvvv 128,100,68
buffer_load_dwordx4 v11, s[48:51], s72 offen sc1 lds
Mvvv 160,96,72
ds_read_b64_tr_b16 v[114:115], v26 offset:17152
v_cvt_pk_bf16_f32 v16, v30, v31
Mvvv 160,100,76
v_accvgpr_read_b32 v30, a162
v_accvgpr_read_b32 v31, a163
Mvvv 192,96,80
s_add_u32 m0, s69, 33792
s_add_u32 s72, s83, 1024
ds_read_b64_tr_b16 v[116:117], v26 offset:25088
Mvvv 192,100,84
buffer_load_dwordx4 v12, s[48:51], s72 offen sc1 lds
Mvvv 224,96,88
s_waitcnt vmcnt(18)
ds_read_b64_tr_b16 v[118:119], v26 offset:25344
v_mul_f32 v30, 0x3e0293ee, v30
Mvvv 224,100,92
s_waitcnt lgkmcnt(6)
ds_read_b128 v[32:35], v7 offset:0
v_mul_f32 v31, 0x3e0293ee, v31
Mvvv 132,104,64
s_waitcnt lgkmcnt(5)
ds_read_b64_tr_b16 v[120:121], v27 offset:16896
v_cvt_pk_bf16_f32 v17, v30, v31
Mvvv 132,108,68
ds_read_b128 v[36:39], v8 offset:0
v_accvgpr_read_b32 v30, a164
Mvvv 164,104,72
ds_read_b64_tr_b16 v[122:123], v27 offset:17152
v_accvgpr_read_b32 v31, a165
Mvvv 164,108,76
ds_read_b128 v[40:43], v7 offset:2048
v_mul_f32 v30, 0x3e0293ee, v30
Mvvv 196,104,80
ds_read_b64_tr_b16 v[124:125], v27 offset:25088
v_mul_f32 v31, 0x3e0293ee, v31
Mvvv 196,108,84
ds_read_b128 v[44:47], v8 offset:2048
v_cvt_pk_bf16_f32 v18, v30, v31
Mvvv 228,104,88
ds_read_b64_tr_b16 v[126:127], v27 offset:25344
v_accvgpr_read_b32 v30, a166
Mvvv 228,108,92
s_waitcnt lgkmcnt(10)
ds_read_b128 v[48:51], v7 offset:4096
v_accvgpr_read_b32 v31, a167
Mvvv 136,112,64
s_waitcnt lgkmcnt(9)
ds_read_b64_tr_b16 v[96:97], v26 offset:17408
v_mul_f32 v30, 0x3e0293ee, v30
Mvvv 136,116,68
ds_read_b128 v[52:55], v8 offset:4096
v_mul_f32 v31, 0x3e0293ee, v31
Mvvv 168,112,72
ds_read_b64_tr_b16 v[98:99], v26 offset:17664
v_cvt_pk_bf16_f32 v19, v30, v31
Mvvv 168,116,76
s_add_u32 s73, s80, 38912
ds_read_b128 v[56:59], v7 offset:6144
v_sub_u32 v9, 33, v1
Mvvv 200,112,80
ds_read_b64_tr_b16 v[100:101], v26 offset:25600
v_cmp_eq_u32 vcc, 3, v2
Mvvv 200,116,84
s_waitcnt lgkmcnt(11)
ds_read_b128 v[60:63], v8 offset:6144
Mvvv 232,112,88
s_add_u32 m0, s69, 34816
s_add_u32 s72, s83, 35840
ds_read_b64_tr_b16 v[102:103], v26 offset:25856
Mvvv 232,116,92
s_waitcnt lgkmcnt(12)
buffer_load_dwordx4 v11, s[48:51], s72 offen sc1 lds
)"
R"(
Mvvv 140,120,64
s_waitcnt lgkmcnt(8)
ds_read_b64_tr_b16 v[104:105], v27 offset:17408
v_cndmask_b32 v14, v14, v9, vcc
Mvvv 140,124,68
buffer_store_dwordx4 v[16:19], v0, s[48:51], s73 offen sc1
Mvvv 172,120,72
s_add_u32 m0, s69, 35840
s_add_u32 s72, s83, 35840
ds_read_b64_tr_b16 v[106:107], v27 offset:17664
Mvvv 172,124,76
buffer_load_dwordx4 v12, s[48:51], s72 offen sc1 lds
Mvvv 204,120,80
ds_read_b64_tr_b16 v[108:109], v27 offset:25600
v_accvgpr_read_b32 v30, a168
Mvvv 204,124,84
v_accvgpr_read_b32 v31, a169
v_mul_f32 v30, 0x3e0293ee, v30
Mvvv 236,120,88
s_add_u32 m0, s69, 36864
s_add_u32 s72, s82, 1024
ds_read_b64_tr_b16 v[110:111], v27 offset:25856
Mvvv 236,124,92
s_waitcnt lgkmcnt(4)
buffer_load_dwordx4 v11, s[48:51], s72 offen sc1 lds
Mvvv 144,96,64
s_add_u32 m0, s71, 98304
s_add_u32 s72, s68, 4194304
ds_read_b64_tr_b16 v[112:113], v26 offset:17920
Mvvv 144,100,68
buffer_load_dwordx4 v3, s[16:19], s72 offen lds
Mvvv 176,96,72
s_add_u32 m0, s69, 37888
s_add_u32 s72, s82, 1024
ds_read_b64_tr_b16 v[114:115], v26 offset:18176
Mvvv 176,100,76
buffer_load_dwordx4 v12, s[48:51], s72 offen sc1 lds
Mvvv 208,96,80
s_add_u32 m0, s71, 99200
s_add_u32 s72, s68, 4194304
ds_read_b64_tr_b16 v[116:117], v26 offset:26112
Mvvv 208,100,84
buffer_load_dwordx4 v3, s[16:19], s72 offen offset:128 lds
Mvvv 240,96,88
s_add_u32 m0, s69, 38912
s_add_u32 s72, s82, 35840
ds_read_b64_tr_b16 v[118:119], v26 offset:26368
Mvvv 240,100,92
s_waitcnt lgkmcnt(4)
buffer_load_dwordx4 v11, s[48:51], s72 offen sc1 lds
Mvvv 148,104,64
s_add_u32 m0, s71, 106496
s_add_u32 s72, s68, 4456448
ds_read_b64_tr_b16 v[120:121], v27 offset:17920
Mvvv 148,108,68
buffer_load_dwordx4 v3, s[16:19], s72 offen lds
Mvvv 180,104,72
s_add_u32 m0, s69, 39936
s_add_u32 s72, s82, 35840
ds_read_b64_tr_b16 v[122:123], v27 offset:18176
Mvvv 180,108,76
buffer_load_dwordx4 v12, s[48:51], s72 offen sc1 lds
Mvvv 212,104,80
s_add_u32 m0, s71, 107392
s_add_u32 s72, s68, 4456448
ds_read_b64_tr_b16 v[124:125], v27 offset:26112
Mvvv 212,108,84
buffer_load_dwordx4 v3, s[16:19], s72 offen offset:128 lds
Mvvv 244,104,88
ds_read_b64_tr_b16 v[126:127], v27 offset:26368
v_mul_f32 v31, 0x3e0293ee, v31
Mvvv 244,108,92
s_waitcnt lgkmcnt(6)
v_cvt_pk_bf16_f32 v20, v30, v31
v_accvgpr_read_b32 v30, a170
Mvvv 152,112,64
s_waitcnt lgkmcnt(4)
v_accvgpr_read_b32 v31, a171
v_mul_f32 v30, 0x3e0293ee, v30
Mvvv 152,116,68
v_mul_f32 v31, 0x3e0293ee, v31
v_cvt_pk_bf16_f32 v21, v30, v31
Mvvv 184,112,72
v_accvgpr_read_b32 v30, a172
v_accvgpr_read_b32 v31, a173
Mvvv 184,116,76
v_mul_f32 v30, 0x3e0293ee, v30
v_mul_f32 v31, 0x3e0293ee, v31
Mvvv 216,112,80
v_cvt_pk_bf16_f32 v22, v30, v31
v_accvgpr_read_b32 v30, a174
Mvvv 216,116,84
s_waitcnt vmcnt(28) lgkmcnt(0)
v_accvgpr_read_b32 v31, a175
v_mul_f32 v30, 0x3e0293ee, v30
Mvvv 248,112,88
s_barrier
Mvvv 248,116,92
ds_read_b64_tr_b16 v[96:97], v26 offset:32768
v_mul_f32 v31, 0x3e0293ee, v31
Mvvv 156,120,64
ds_read_b64_tr_b16 v[98:99], v26 offset:33024
v_cvt_pk_bf16_f32 v23, v30, v31
Mvvv 156,124,68
s_add_u32 s73, s80, 39936
ds_read_b64_tr_b16 v[100:101], v26 offset:40960
v_add_u32 v9, -1, v1
Mvvv 188,120,72
ds_read_b64_tr_b16 v[102:103], v26 offset:41216
v_cmp_eq_u32 vcc, 2, v2
Mvvv 188,124,76
buffer_store_dwordx4 v[20:23], v0, s[48:51], s73 offen sc1
Mvvv 220,120,80
ds_read_b64_tr_b16 v[104:105], v27 offset:32768
v_accvgpr_read_b32 v30, a144
Mvvv 220,124,84
ds_read_b64_tr_b16 v[106:107], v27 offset:33024
v_accvgpr_read_b32 v31, a145
Mvvv 252,120,88
ds_read_b64_tr_b16 v[108:109], v27 offset:40960
v_cvt_pk_bf16_f32 v16, v30, v31
Mvvv 252,124,92
s_waitcnt lgkmcnt(5)
ds_read_b64_tr_b16 v[110:111], v27 offset:41216
v_accvgpr_read_b32 v30, a146
Mvvv 128,96,32
s_waitcnt lgkmcnt(4)
s_add_u32 m0, s69, 65536
s_add_u32 s72, s83, 1152
ds_read_b64_tr_b16 v[112:113], v26 offset:33280
Mvvv 128,100,36
buffer_load_dwordx4 v11, s[48:51], s72 offen sc1 lds
Mvvv 160,96,40
ds_read_b64_tr_b16 v[114:115], v26 offset:33536
v_accvgpr_read_b32 v31, a147
Mvvv 160,100,44
v_cvt_pk_bf16_f32 v17, v30, v31
v_accvgpr_read_b32 v30, a176
Mvvv 192,96,48
s_add_u32 m0, s69, 66560
s_add_u32 s72, s83, 1152
ds_read_b64_tr_b16 v[116:117], v26 offset:41472
Mvvv 192,100,52
buffer_load_dwordx4 v12, s[48:51], s72 offen sc1 lds
Mvvv 224,96,56
s_waitcnt vmcnt(18)
ds_read_b64_tr_b16 v[118:119], v26 offset:41728
v_accvgpr_read_b32 v31, a177
Mvvv 224,100,60
s_waitcnt lgkmcnt(6)
ds_read_b128 v[64:67], v5 offset:0
v_cvt_pk_bf16_f32 v18, v30, v31
Mvvv 132,104,32
s_waitcnt lgkmcnt(5)
ds_read_b64_tr_b16 v[120:121], v27 offset:33280
v_accvgpr_read_b32 v30, a178
Mvvv 132,108,36
ds_read_b128 v[68:71], v6 offset:0
v_accvgpr_read_b32 v31, a179
Mvvv 164,104,40
ds_read_b64_tr_b16 v[122:123], v27 offset:33536
v_cvt_pk_bf16_f32 v19, v30, v31
Mvvv 164,108,44
s_add_u32 s73, s81, 34816
ds_read_b128 v[72:75], v5 offset:2048
v_cndmask_b32 v14, v14, v9, vcc
Mvvv 196,104,48
ds_read_b64_tr_b16 v[124:125], v27 offset:41472
v_sub_u32 v9, 32, v1
Mvvv 196,108,52
ds_read_b128 v[76:79], v6 offset:2048
v_cmp_eq_u32 vcc, 1, v2
Mvvv 228,104,56
ds_read_b64_tr_b16 v[126:127], v27 offset:41728
Mvvv 228,108,60
s_waitcnt lgkmcnt(10)
ds_read_b128 v[80:83], v5 offset:4096
v_cndmask_b32 v14, v14, v9, vcc
Mvvv 136,112,32
s_waitcnt lgkmcnt(9)
ds_read_b64_tr_b16 v[96:97], v26 offset:33792
v_cmp_gt_i32 vcc, 1, v2
Mvvv 136,116,36
ds_read_b128 v[84:87], v6 offset:4096
Mvvv 168,112,40
ds_read_b64_tr_b16 v[98:99], v26 offset:34048
v_cndmask_b32 v14, v14, v1, vcc
Mvvv 168,116,44
ds_read_b128 v[88:91], v5 offset:6144
v_mul_u32_u24 v14, 34816, v14
Mvvv 200,112,48
s_add_u32 s58, s62, 16384
ds_read_b64_tr_b16 v[100:101], v26 offset:41984
v_add_u32 v14, s58, v14
Mvvv 200,116,52
s_waitcnt lgkmcnt(11)
ds_read_b128 v[92:95], v6 offset:6144
v_add_u32 v2, s63, v1
Mvvv 232,112,56
s_add_u32 m0, s69, 67584
s_add_u32 s72, s83, 35968
ds_read_b64_tr_b16 v[102:103], v26 offset:42240
Mvvv 232,116,60
s_waitcnt lgkmcnt(12)
buffer_load_dwordx4 v11, s[48:51], s72 offen sc1 lds
Mvvv 140,120,32
s_waitcnt lgkmcnt(8)
ds_read_b64_tr_b16 v[104:105], v27 offset:33792
v_add_u32 v9, -32, v2
Mvvv 140,124,36
buffer_store_dwordx4 v[16:19], v0, s[48:51], s73 offen sc1
Mvvv 172,120,40
s_add_u32 m0, s69, 68608
s_add_u32 s72, s83, 35968
ds_read_b64_tr_b16 v[106:107], v27 offset:34048
Mvvv 172,124,44
buffer_load_dwordx4 v12, s[48:51], s72 offen sc1 lds
Mvvv 204,120,48
ds_read_b64_tr_b16 v[108:109], v27 offset:41984
v_accvgpr_read_b32 v30, a148
Mvvv 204,124,52
v_accvgpr_read_b32 v31, a149
v_cvt_pk_bf16_f32 v20, v30, v31
Mvvv 236,120,56
s_add_u32 m0, s69, 69632
s_add_u32 s72, s82, 1152
ds_read_b64_tr_b16 v[110:111], v27 offset:42240
Mvvv 236,124,60
s_waitcnt lgkmcnt(4)
buffer_load_dwordx4 v11, s[48:51], s72 offen sc1 lds
Mvvv 144,96,32
s_add_u32 m0, s71, 114688
s_add_u32 s72, s68, 4718592
ds_read_b64_tr_b16 v[112:113], v26 offset:34304
Mvvv 144,100,36
buffer_load_dwordx4 v3, s[16:19], s72 offen lds
Mvvv 176,96,40
s_add_u32 m0, s69, 70656
s_add_u32 s72, s82, 1152
ds_read_b64_tr_b16 v[114:115], v26 offset:34560
Mvvv 176,100,44
buffer_load_dwordx4 v12, s[48:51], s72 offen sc1 lds
Mvvv 208,96,48
s_add_u32 m0, s71, 115584
s_add_u32 s72, s68, 4718592
ds_read_b64_tr_b16 v[116:117], v26 offset:42496
Mvvv 208,100,52
buffer_load_dwordx4 v3, s[16:19], s72 offen offset:128 lds
Mvvv 240,96,56
s_add_u32 m0, s69, 71680
s_add_u32 s72, s82, 35968
ds_read_b64_tr_b16 v[118:119], v26 offset:42752
Mvvv 240,100,60
s_waitcnt lgkmcnt(4)
buffer_load_dwordx4 v11, s[48:51], s72 offen sc1 lds
Mvvv 148,104,32
s_add_u32 m0, s71, 122880
s_add_u32 s72, s68, 4980736
ds_read_b64_tr_b16 v[120:121], v27 offset:34304
Mvvv 148,108,36
buffer_load_dwordx4 v3, s[16:19], s72 offen lds
Mvvv 180,104,40
s_add_u32 m0, s69, 72704
s_add_u32 s72, s82, 35968
ds_read_b64_tr_b16 v[122:123], v27 offset:34560
Mvvv 180,108,44
buffer_load_dwordx4 v12, s[48:51], s72 offen sc1 lds
Mvvv 212,104,48
s_add_u32 m0, s71, 123776
s_add_u32 s72, s68, 4980736
ds_read_b64_tr_b16 v[124:125], v27 offset:42496
Mvvv 212,108,52
buffer_load_dwordx4 v3, s[16:19], s72 offen offset:128 lds
Mvvv 244,104,56
ds_read_b64_tr_b16 v[126:127], v27 offset:42752
v_accvgpr_read_b32 v30, a150
Mvvv 244,108,60
s_waitcnt lgkmcnt(6)
v_accvgpr_read_b32 v31, a151
v_cvt_pk_bf16_f32 v21, v30, v31
Mvvv 152,112,32
s_waitcnt lgkmcnt(4)
v_accvgpr_read_b32 v30, a180
v_accvgpr_read_b32 v31, a181
Mvvv 152,116,36
v_cvt_pk_bf16_f32 v22, v30, v31
v_accvgpr_read_b32 v30, a182
Mvvv 184,112,40
v_accvgpr_read_b32 v31, a183
v_cvt_pk_bf16_f32 v23, v30, v31
Mvvv 184,116,44
s_add_u32 s73, s81, 36864
buffer_store_dwordx4 v[20:23], v0, s[48:51], s73 offen sc1
Mvvv 216,112,48
v_accvgpr_read_b32 v30, a152
v_accvgpr_read_b32 v31, a153
Mvvv 216,116,52
s_waitcnt vmcnt(29) lgkmcnt(0)
v_cvt_pk_bf16_f32 v16, v30, v31
v_accvgpr_read_b32 v30, a154
Mvvv 248,112,56
s_barrier
Mvvv 248,116,60
ds_read_b64_tr_b16 v[96:97], v26 offset:49152
v_accvgpr_read_b32 v31, a155
Mvvv 156,120,32
ds_read_b64_tr_b16 v[98:99], v26 offset:49408
v_cvt_pk_bf16_f32 v17, v30, v31
Mvvv 156,124,36
ds_read_b64_tr_b16 v[100:101], v26 offset:57344
v_accvgpr_read_b32 v30, a184
Mvvv 188,120,40
ds_read_b64_tr_b16 v[102:103], v26 offset:57600
v_accvgpr_read_b32 v31, a185
Mvvv 188,124,44
v_cvt_pk_bf16_f32 v18, v30, v31
v_accvgpr_read_b32 v30, a186
Mvvv 220,120,48
ds_read_b64_tr_b16 v[104:105], v27 offset:49152
v_accvgpr_read_b32 v31, a187
Mvvv 220,124,52
ds_read_b64_tr_b16 v[106:107], v27 offset:49408
v_cvt_pk_bf16_f32 v19, v30, v31
Mvvv 252,120,56
s_add_u32 s73, s81, 38912
ds_read_b64_tr_b16 v[108:109], v27 offset:57344
v_mul_u32_u24 v9, 131072, v9
Mvvv 252,124,60
s_waitcnt lgkmcnt(5)
s_add_u32 s58, s67, 262144
ds_read_b64_tr_b16 v[110:111], v27 offset:57600
v_add_u32 v9, s58, v9
Mvvv 128,96,64
s_waitcnt lgkmcnt(4)
s_add_u32 m0, s69, 0
s_add_u32 s72, s83, 1280
ds_read_b64_tr_b16 v[112:113], v26 offset:49664
Mvvv 128,100,68
buffer_load_dwordx4 v11, s[48:51], s72 offen sc1 lds
Mvvv 160,96,72
ds_read_b64_tr_b16 v[114:115], v26 offset:49920
v_cmp_lt_u32 vcc, 31, v2
Mvvv 160,100,76
buffer_store_dwordx4 v[16:19], v0, s[48:51], s73 offen sc1
Mvvv 192,96,80
s_add_u32 m0, s69, 1024
s_add_u32 s72, s83, 1280
ds_read_b64_tr_b16 v[116:117], v26 offset:57856
Mvvv 192,100,84
buffer_load_dwordx4 v12, s[48:51], s72 offen sc1 lds
Mvvv 224,96,88
s_waitcnt vmcnt(19)
ds_read_b64_tr_b16 v[118:119], v26 offset:58112
v_accvgpr_read_b32 v30, a156
Mvvv 224,100,92
s_waitcnt lgkmcnt(6)
ds_read_b128 v[32:35], v5 offset:32768
v_accvgpr_read_b32 v31, a157
Mvvv 132,104,64
s_waitcnt lgkmcnt(5)
ds_read_b64_tr_b16 v[120:121], v27 offset:49664
v_cvt_pk_bf16_f32 v20, v30, v31
Mvvv 132,108,68
ds_read_b128 v[36:39], v6 offset:32768
v_accvgpr_read_b32 v30, a158
Mvvv 164,104,72
ds_read_b64_tr_b16 v[122:123], v27 offset:49920
v_accvgpr_read_b32 v31, a159
Mvvv 164,108,76
ds_read_b128 v[40:43], v5 offset:34816
v_cvt_pk_bf16_f32 v21, v30, v31
Mvvv 196,104,80
ds_read_b64_tr_b16 v[124:125], v27 offset:57856
v_accvgpr_read_b32 v30, a188
Mvvv 196,108,84
ds_read_b128 v[44:47], v6 offset:34816
v_accvgpr_read_b32 v31, a189
Mvvv 228,104,88
ds_read_b64_tr_b16 v[126:127], v27 offset:58112
v_cvt_pk_bf16_f32 v22, v30, v31
Mvvv 228,108,92
s_waitcnt lgkmcnt(10)
ds_read_b128 v[48:51], v5 offset:36864
v_accvgpr_read_b32 v30, a190
Mvvv 136,112,64
s_waitcnt lgkmcnt(9)
ds_read_b64_tr_b16 v[96:97], v26 offset:50176
v_accvgpr_read_b32 v31, a191
Mvvv 136,116,68
s_waitcnt lgkmcnt(6)
ds_read_b128 v[52:55], v6 offset:36864
v_cvt_pk_bf16_f32 v23, v30, v31
Mvvv 168,112,72
s_add_u32 s73, s81, 40960
ds_read_b64_tr_b16 v[98:99], v26 offset:50432
v_cndmask_b32 v14, v14, v9, vcc
Mvvv 168,116,76
s_and_b32 s92, s63, 2
s_lshl_b32 s92, s92, 14
ds_read_b128 v[56:59], v5 offset:38912
Mvvv 200,112,80
s_mov_b32 s93, 0
s_mov_b32 s94, 60
ds_read_b64_tr_b16 v[100:101], v26 offset:58368
Mvvv 200,116,84
s_waitcnt lgkmcnt(6)
s_mov_b64 s[12:13], s[48:49]
s_mov_b64 s[14:15], s[50:51]
ds_read_b128 v[60:63], v6 offset:38912
Mvvv 232,112,88
s_add_u32 m0, s69, 2048
s_add_u32 s72, s83, 36096
ds_read_b64_tr_b16 v[102:103], v26 offset:58624
Mvvv 232,116,92
s_mov_b32 s24, 0
buffer_load_dwordx4 v11, s[48:51], s72 offen sc1 lds
)"
R"(
Mvvv 140,120,64
s_mov_b32 s25, 0
s_mov_b32 s26, 0
ds_read_b64_tr_b16 v[104:105], v27 offset:50176
Mvvv 140,124,68
s_waitcnt lgkmcnt(5)
s_mov_b64 s[20:21], s[28:29]
buffer_store_dwordx4 v[20:23], v0, s[48:51], s73 offen sc1
Mvvv 172,120,72
s_add_u32 m0, s69, 3072
s_add_u32 s72, s83, 36096
ds_read_b64_tr_b16 v[106:107], v27 offset:50432
Mvvv 172,124,76
s_mov_b64 s[22:23], s[30:31]
buffer_load_dwordx4 v12, s[48:51], s72 offen sc1 lds
Mvvv 204,120,80
s_and_b32 s58, s61, 3
ds_read_b64_tr_b16 v[108:109], v27 offset:58368
v_accvgpr_read_b32 v30, a192
Mvvv 204,124,84
s_waitcnt lgkmcnt(3)
s_lshl2_add_u32 s58, s64, s58
v_accvgpr_read_b32 v31, a193
v_mul_f32 v30, 0x3e0293ee, v30
Mvvv 236,120,88
s_add_u32 m0, s69, 4096
s_add_u32 s72, s82, 1280
ds_read_b64_tr_b16 v[110:111], v27 offset:58624
Mvvv 236,124,92
s_lshl_b32 s58, s58, 14
buffer_load_dwordx4 v11, s[48:51], s72 offen sc1 lds
Mvvv 144,96,64
s_add_u32 m0, s71, 131072
s_add_u32 s72, s68, 5242880
ds_read_b64_tr_b16 v[112:113], v26 offset:50688
Mvvv 144,100,68
s_add_u32 s58, s58, s70
buffer_load_dwordx4 v3, s[16:19], s72 offen lds
Mvvv 176,96,72
s_add_u32 m0, s69, 5120
s_add_u32 s72, s82, 1280
ds_read_b64_tr_b16 v[114:115], v26 offset:50944
Mvvv 176,100,76
s_sub_u32 s58, s58, 0x200000
buffer_load_dwordx4 v12, s[48:51], s72 offen sc1 lds
Mvvv 208,96,80
s_add_u32 m0, s71, 131968
s_add_u32 s72, s68, 5242880
ds_read_b64_tr_b16 v[116:117], v26 offset:58880
Mvvv 208,100,84
buffer_load_dwordx4 v3, s[16:19], s72 offen offset:128 lds
Mvvv 240,96,88
s_add_u32 m0, s69, 6144
s_add_u32 s72, s82, 36096
ds_read_b64_tr_b16 v[118:119], v26 offset:59136
Mvvv 240,100,92
s_waitcnt lgkmcnt(4)
buffer_load_dwordx4 v11, s[48:51], s72 offen sc1 lds
Mvvv 148,104,64
s_add_u32 m0, s71, 139264
s_add_u32 s72, s68, 5505024
ds_read_b64_tr_b16 v[120:121], v27 offset:50688
Mvvv 148,108,68
buffer_load_dwordx4 v3, s[16:19], s72 offen lds
Mvvv 180,104,72
s_add_u32 m0, s69, 7168
s_add_u32 s72, s82, 36096
ds_read_b64_tr_b16 v[122:123], v27 offset:50944
Mvvv 180,108,76
buffer_load_dwordx4 v12, s[48:51], s72 offen sc1 lds
Mvvv 212,104,80
s_add_u32 m0, s71, 140160
s_add_u32 s72, s68, 5505024
ds_read_b64_tr_b16 v[124:125], v27 offset:58880
Mvvv 212,108,84
buffer_load_dwordx4 v3, s[16:19], s72 offen offset:128 lds
Mvvv 244,104,88
ds_read_b64_tr_b16 v[126:127], v27 offset:59136
v_mul_f32 v31, 0x3e0293ee, v31
Mvvv 244,108,92
s_waitcnt lgkmcnt(6)
v_cvt_pk_bf16_f32 v16, v30, v31
v_accvgpr_read_b32 v30, a194
Mvvv 152,112,64
s_waitcnt lgkmcnt(4)
v_accvgpr_read_b32 v31, a195
v_mul_f32 v30, 0x3e0293ee, v30
Mvvv 152,116,68
v_mul_f32 v31, 0x3e0293ee, v31
v_cvt_pk_bf16_f32 v17, v30, v31
Mvvv 184,112,72
v_accvgpr_read_b32 v30, a196
v_accvgpr_read_b32 v31, a197
Mvvv 184,116,76
v_mul_f32 v30, 0x3e0293ee, v30
v_mul_f32 v31, 0x3e0293ee, v31
Mvvv 216,112,80
v_cvt_pk_bf16_f32 v18, v30, v31
v_accvgpr_read_b32 v30, a198
Mvvv 216,116,84
s_waitcnt vmcnt(29) lgkmcnt(0)
v_accvgpr_read_b32 v31, a199
v_mul_f32 v30, 0x3e0293ee, v30
Mvvv 248,112,88
s_barrier
Mvvv 248,116,92
ds_read_b64_tr_b16 v[96:97], v26 offset:0
v_mul_f32 v31, 0x3e0293ee, v31
Mvvv 156,120,64
ds_read_b64_tr_b16 v[98:99], v26 offset:256
v_cvt_pk_bf16_f32 v19, v30, v31
Mvvv 156,124,68
s_add_u32 s73, s80, 43008
ds_read_b64_tr_b16 v[100:101], v26 offset:8192
Mvvv 188,120,72
s_cmp_ge_u32 s61, 4
s_cselect_b32 s95, s58, 0x80800000
ds_read_b64_tr_b16 v[102:103], v26 offset:8448
Mvvv 188,124,76
s_lshl_b32 s58, s61, 5
buffer_store_dwordx4 v[16:19], v0, s[48:51], s73 offen sc1
Mvvv 220,120,80
s_add_u32 s58, s58, s64
ds_read_b64_tr_b16 v[104:105], v27 offset:0
v_accvgpr_read_b32 v30, a200
Mvvv 220,124,84
s_mul_i32 s96, s58, 36864
ds_read_b64_tr_b16 v[106:107], v27 offset:256
v_accvgpr_read_b32 v31, a201
Mvvv 252,120,88
s_add_u32 s96, s96, s69
ds_read_b64_tr_b16 v[108:109], v27 offset:8192
v_mul_f32 v30, 0x3e0293ee, v30
Mvvv 252,124,92
s_waitcnt lgkmcnt(5)
s_add_u32 s96, s96, 54525952
ds_read_b64_tr_b16 v[110:111], v27 offset:8448
v_mul_f32 v31, 0x3e0293ee, v31
Mvvv 128,96,32
s_waitcnt lgkmcnt(4)
s_add_u32 m0, s69, 32768
s_add_u32 s72, s83, 1408
ds_read_b64_tr_b16 v[112:113], v26 offset:512
Mvvv 128,100,36
s_xor_b32 s58, s58, 0x1e0
buffer_load_dwordx4 v11, s[48:51], s72 offen sc1 lds
Mvvv 160,96,40
s_mul_i32 s97, s58, 36864
ds_read_b64_tr_b16 v[114:115], v26 offset:768
v_cvt_pk_bf16_f32 v20, v30, v31
Mvvv 160,100,44
s_sub_u32 s97, s97, s69
v_accvgpr_read_b32 v30, a202
v_accvgpr_read_b32 v31, a203
Mvvv 192,96,48
s_add_u32 m0, s69, 33792
s_add_u32 s72, s83, 1408
ds_read_b64_tr_b16 v[116:117], v26 offset:8704
Mvvv 192,100,52
s_add_u32 s97, s97, 54550528
buffer_load_dwordx4 v12, s[48:51], s72 offen sc1 lds
Mvvv 224,96,56
s_waitcnt vmcnt(19) lgkmcnt(3)
ds_read_b64_tr_b16 v[118:119], v26 offset:8960
v_mul_f32 v30, 0x3e0293ee, v30
Mvvv 224,100,60
s_lshl2_add_u32 s58, s57, s79
ds_read_b128 v[64:67], v7 offset:0
v_mul_f32 v31, 0x3e0293ee, v31
Mvvv 132,104,32
s_lshl_b32 s58, s58, 10
ds_read_b64_tr_b16 v[120:121], v27 offset:512
v_cvt_pk_bf16_f32 v21, v30, v31
Mvvv 132,108,36
s_waitcnt lgkmcnt(4)
s_lshr_b32 s59, s86, 1
ds_read_b128 v[68:71], v8 offset:0
v_accvgpr_read_b32 v30, a204
Mvvv 164,104,40
s_lshl_b32 s59, s59, 8
ds_read_b64_tr_b16 v[122:123], v27 offset:768
v_accvgpr_read_b32 v31, a205
Mvvv 164,108,44
s_add_u32 s58, s58, s59
ds_read_b128 v[72:75], v7 offset:2048
v_mul_f32 v30, 0x3e0293ee, v30
Mvvv 196,104,48
s_and_b32 s59, s86, 1
ds_read_b64_tr_b16 v[124:125], v27 offset:8704
v_mul_f32 v31, 0x3e0293ee, v31
Mvvv 196,108,52
s_waitcnt lgkmcnt(6)
s_lshl3_add_u32 s58, s59, s58
ds_read_b128 v[76:79], v8 offset:2048
v_cvt_pk_bf16_f32 v22, v30, v31
Mvvv 228,104,56
s_lshl4_add_u32 s58, s61, s58
ds_read_b64_tr_b16 v[126:127], v27 offset:8960
v_accvgpr_read_b32 v30, a206
Mvvv 228,108,60
s_add_u32 s98, s58, 32768
ds_read_b128 v[80:83], v7 offset:4096
v_accvgpr_read_b32 v31, a207
Mvvv 136,112,32
s_and_b32 s58, s57, 2
ds_read_b64_tr_b16 v[96:97], v26 offset:1024
v_mul_f32 v30, 0x3e0293ee, v30
Mvvv 136,116,36
s_lshl_b32 s99, s58, 13
ds_read_b128 v[84:87], v8 offset:4096
v_mul_f32 v31, 0x3e0293ee, v31
Mvvv 168,112,40
s_lshl2_add_u32 s4, s61, s58
ds_read_b64_tr_b16 v[98:99], v26 offset:1280
v_cvt_pk_bf16_f32 v23, v30, v31
Mvvv 168,116,44
s_add_u32 s73, s80, 44032
ds_read_b128 v[88:91], v7 offset:6144
Mvvv 200,112,48
s_bitcmp1_b32 s57, 0
s_cselect_b64 s[6:7], -1, 0
ds_read_b64_tr_b16 v[100:101], v26 offset:9216
Mvvv 200,116,52
s_waitcnt lgkmcnt(11)
s_getpc_b64 s[32:33]
.Lgb:
ds_read_b128 v[92:95], v8 offset:6144
Mvvv 232,112,56
s_add_u32 m0, s69, 34816
s_add_u32 s72, s83, 36224
ds_read_b64_tr_b16 v[102:103], v26 offset:9472
Mvvv 232,116,60
s_waitcnt lgkmcnt(8)
buffer_load_dwordx4 v11, s[48:51], s72 offen sc1 lds
Mvvv 140,120,32
s_add_u32 s32, s32, .Lsb-.Lgb
s_addc_u32 s33, s33, 0
ds_read_b64_tr_b16 v[104:105], v27 offset:1024
Mvvv 140,124,36
s_mov_b32 s34, 0x91000100
buffer_store_dwordx4 v[20:23], v0, s[48:51], s73 offen sc1
Mvvv 172,120,40
s_add_u32 m0, s69, 35840
s_add_u32 s72, s83, 36224
ds_read_b64_tr_b16 v[106:107], v27 offset:1280
Mvvv 172,124,44
s_mov_b32 s35, 0
buffer_load_dwordx4 v12, s[48:51], s72 offen sc1 lds
Mvvv 204,120,48
s_lshl2_add_u32 s58, s61, 5
ds_read_b64_tr_b16 v[108:109], v27 offset:9216
v_accvgpr_read_b32 v30, a224
Mvvv 204,124,52
s_bitset1_b64 s[34:35], s58
v_accvgpr_read_b32 v31, a225
v_mul_f32 v30, 0x3e0293ee, v30
Mvvv 236,120,56
s_add_u32 m0, s69, 36864
s_add_u32 s72, s82, 1408
ds_read_b64_tr_b16 v[110:111], v27 offset:9472
Mvvv 236,124,60
s_waitcnt lgkmcnt(4)
buffer_load_dwordx4 v11, s[48:51], s72 offen sc1 lds
Mvvv 144,96,32
s_add_u32 m0, s71, 147456
s_add_u32 s72, s68, 5767168
ds_read_b64_tr_b16 v[112:113], v26 offset:1536
Mvvv 144,100,36
buffer_load_dwordx4 v3, s[16:19], s72 offen lds
Mvvv 176,96,40
s_add_u32 m0, s69, 37888
s_add_u32 s72, s82, 1408
ds_read_b64_tr_b16 v[114:115], v26 offset:1792
Mvvv 176,100,44
buffer_load_dwordx4 v12, s[48:51], s72 offen sc1 lds
Mvvv 208,96,48
s_add_u32 m0, s71, 148352
s_add_u32 s72, s68, 5767168
ds_read_b64_tr_b16 v[116:117], v26 offset:9728
Mvvv 208,100,52
buffer_load_dwordx4 v3, s[16:19], s72 offen offset:128 lds
Mvvv 240,96,56
s_add_u32 m0, s69, 38912
s_add_u32 s72, s82, 36224
ds_read_b64_tr_b16 v[118:119], v26 offset:9984
Mvvv 240,100,60
s_waitcnt lgkmcnt(4)
buffer_load_dwordx4 v11, s[48:51], s72 offen sc1 lds
Mvvv 148,104,32
s_add_u32 m0, s71, 155648
s_add_u32 s72, s68, 6029312
ds_read_b64_tr_b16 v[120:121], v27 offset:1536
Mvvv 148,108,36
buffer_load_dwordx4 v3, s[16:19], s72 offen lds
Mvvv 180,104,40
s_add_u32 m0, s69, 39936
s_add_u32 s72, s82, 36224
ds_read_b64_tr_b16 v[122:123], v27 offset:1792
Mvvv 180,108,44
buffer_load_dwordx4 v12, s[48:51], s72 offen sc1 lds
Mvvv 212,104,48
s_add_u32 m0, s71, 156544
s_add_u32 s72, s68, 6029312
ds_read_b64_tr_b16 v[124:125], v27 offset:9728
Mvvv 212,108,52
buffer_load_dwordx4 v3, s[16:19], s72 offen offset:128 lds
Mvvv 244,104,56
ds_read_b64_tr_b16 v[126:127], v27 offset:9984
v_mul_f32 v31, 0x3e0293ee, v31
Mvvv 244,108,60
s_waitcnt lgkmcnt(6)
v_cvt_pk_bf16_f32 v16, v30, v31
v_accvgpr_read_b32 v30, a226
Mvvv 152,112,32
s_waitcnt lgkmcnt(4)
v_accvgpr_read_b32 v31, a227
v_mul_f32 v30, 0x3e0293ee, v30
Mvvv 152,116,36
v_mul_f32 v31, 0x3e0293ee, v31
v_cvt_pk_bf16_f32 v17, v30, v31
Mvvv 184,112,40
v_accvgpr_read_b32 v30, a228
v_accvgpr_read_b32 v31, a229
Mvvv 184,116,44
v_mul_f32 v30, 0x3e0293ee, v30
v_mul_f32 v31, 0x3e0293ee, v31
Mvvv 216,112,48
v_cvt_pk_bf16_f32 v18, v30, v31
v_accvgpr_read_b32 v30, a230
Mvvv 216,116,52
s_waitcnt vmcnt(29) lgkmcnt(0)
v_accvgpr_read_b32 v31, a231
v_mul_f32 v30, 0x3e0293ee, v30
Mvvv 248,112,56
s_barrier
Mvvv 248,116,60
ds_read_b64_tr_b16 v[96:97], v26 offset:16384
v_mul_f32 v31, 0x3e0293ee, v31
Mvvv 156,120,32
ds_read_b64_tr_b16 v[98:99], v26 offset:16640
v_cvt_pk_bf16_f32 v19, v30, v31
Mvvv 156,124,36
s_add_u32 s73, s80, 47104
ds_read_b64_tr_b16 v[100:101], v26 offset:24576
v_lshrrev_b32 v1, 1, v0
Mvvv 188,120,40
s_sub_u32 s58, 15, s61
s_lshr_b32 s58, s58, 1
ds_read_b64_tr_b16 v[102:103], v26 offset:24832
Mvvv 188,124,44
s_lshl_b32 s58, s58, 5
buffer_store_dwordx4 v[16:19], v0, s[48:51], s73 offen sc1
Mvvv 220,120,48
s_add_u32 s58, s58, 24
ds_read_b64_tr_b16 v[104:105], v27 offset:16384
v_accvgpr_read_b32 v30, a232
Mvvv 220,124,52
ds_read_b64_tr_b16 v[106:107], v27 offset:16640
v_accvgpr_read_b32 v31, a233
Mvvv 252,120,56
ds_read_b64_tr_b16 v[108:109], v27 offset:24576
v_mul_f32 v30, 0x3e0293ee, v30
Mvvv 252,124,60
s_waitcnt lgkmcnt(5)
ds_read_b64_tr_b16 v[110:111], v27 offset:24832
v_mul_f32 v31, 0x3e0293ee, v31
Mvvv 128,96,64
s_waitcnt lgkmcnt(4)
s_add_u32 m0, s69, 65536
s_add_u32 s72, s83, 1536
ds_read_b64_tr_b16 v[112:113], v26 offset:16896
Mvvv 128,100,68
buffer_load_dwordx4 v11, s[48:51], s72 offen sc1 lds
Mvvv 160,96,72
ds_read_b64_tr_b16 v[114:115], v26 offset:17152
v_cvt_pk_bf16_f32 v20, v30, v31
Mvvv 160,100,76
v_accvgpr_read_b32 v30, a234
v_accvgpr_read_b32 v31, a235
Mvvv 192,96,80
s_add_u32 m0, s69, 66560
s_add_u32 s72, s83, 1536
ds_read_b64_tr_b16 v[116:117], v26 offset:25088
Mvvv 192,100,84
buffer_load_dwordx4 v12, s[48:51], s72 offen sc1 lds
Mvvv 224,96,88
s_waitcnt vmcnt(18)
ds_read_b64_tr_b16 v[118:119], v26 offset:25344
v_mul_f32 v30, 0x3e0293ee, v30
Mvvv 224,100,92
s_waitcnt lgkmcnt(6)
ds_read_b128 v[32:35], v5 offset:0
v_mul_f32 v31, 0x3e0293ee, v31
Mvvv 132,104,64
s_waitcnt lgkmcnt(5)
ds_read_b64_tr_b16 v[120:121], v27 offset:16896
v_cvt_pk_bf16_f32 v21, v30, v31
Mvvv 132,108,68
ds_read_b128 v[36:39], v6 offset:0
v_accvgpr_read_b32 v30, a236
Mvvv 164,104,72
ds_read_b64_tr_b16 v[122:123], v27 offset:17152
v_accvgpr_read_b32 v31, a237
Mvvv 164,108,76
ds_read_b128 v[40:43], v5 offset:2048
v_mul_f32 v30, 0x3e0293ee, v30
Mvvv 196,104,80
ds_read_b64_tr_b16 v[124:125], v27 offset:25088
v_mul_f32 v31, 0x3e0293ee, v31
Mvvv 196,108,84
ds_read_b128 v[44:47], v6 offset:2048
v_cvt_pk_bf16_f32 v22, v30, v31
Mvvv 228,104,88
ds_read_b64_tr_b16 v[126:127], v27 offset:25344
v_accvgpr_read_b32 v30, a238
Mvvv 228,108,92
s_waitcnt lgkmcnt(10)
ds_read_b128 v[48:51], v5 offset:4096
v_accvgpr_read_b32 v31, a239
Mvvv 136,112,64
s_waitcnt lgkmcnt(9)
)"
R"(
ds_read_b64_tr_b16 v[96:97], v26 offset:17408
v_mul_f32 v30, 0x3e0293ee, v30
Mvvv 136,116,68
ds_read_b128 v[52:55], v6 offset:4096
v_mul_f32 v31, 0x3e0293ee, v31
Mvvv 168,112,72
ds_read_b64_tr_b16 v[98:99], v26 offset:17664
v_cvt_pk_bf16_f32 v23, v30, v31
Mvvv 168,116,76
s_add_u32 s73, s80, 48128
ds_read_b128 v[56:59], v5 offset:6144
v_min_u32 v1, s58, v1
Mvvv 200,112,80
ds_read_b64_tr_b16 v[100:101], v26 offset:25600
Mvvv 200,116,84
s_waitcnt lgkmcnt(11)
ds_read_b128 v[60:63], v6 offset:6144
Mvvv 232,112,88
s_add_u32 m0, s69, 67584
s_add_u32 s72, s83, 36352
ds_read_b64_tr_b16 v[102:103], v26 offset:25856
Mvvv 232,116,92
s_waitcnt lgkmcnt(12)
buffer_load_dwordx4 v11, s[48:51], s72 offen sc1 lds
Mvvv 140,120,64
s_waitcnt lgkmcnt(8)
ds_read_b64_tr_b16 v[104:105], v27 offset:17408
Mvvv 140,124,68
buffer_store_dwordx4 v[20:23], v0, s[48:51], s73 offen sc1
Mvvv 172,120,72
s_add_u32 m0, s69, 68608
s_add_u32 s72, s83, 36352
ds_read_b64_tr_b16 v[106:107], v27 offset:17664
Mvvv 172,124,76
buffer_load_dwordx4 v12, s[48:51], s72 offen sc1 lds
Mvvv 204,120,80
ds_read_b64_tr_b16 v[108:109], v27 offset:25600
v_accvgpr_read_b32 v30, a208
Mvvv 204,124,84
v_accvgpr_read_b32 v31, a209
v_cvt_pk_bf16_f32 v16, v30, v31
Mvvv 236,120,88
s_add_u32 m0, s69, 69632
s_add_u32 s72, s82, 1536
ds_read_b64_tr_b16 v[110:111], v27 offset:25856
Mvvv 236,124,92
s_waitcnt lgkmcnt(4)
buffer_load_dwordx4 v11, s[48:51], s72 offen sc1 lds
Mvvv 144,96,64
s_add_u32 m0, s71, 98304
s_add_u32 s72, s68, 6291456
ds_read_b64_tr_b16 v[112:113], v26 offset:17920
Mvvv 144,100,68
buffer_load_dwordx4 v3, s[16:19], s72 offen lds
Mvvv 176,96,72
s_add_u32 m0, s69, 70656
s_add_u32 s72, s82, 1536
ds_read_b64_tr_b16 v[114:115], v26 offset:18176
Mvvv 176,100,76
buffer_load_dwordx4 v12, s[48:51], s72 offen sc1 lds
Mvvv 208,96,80
s_add_u32 m0, s71, 99200
s_add_u32 s72, s68, 6291456
ds_read_b64_tr_b16 v[116:117], v26 offset:26112
Mvvv 208,100,84
buffer_load_dwordx4 v3, s[16:19], s72 offen offset:128 lds
Mvvv 240,96,88
s_add_u32 m0, s69, 71680
s_add_u32 s72, s82, 36352
ds_read_b64_tr_b16 v[118:119], v26 offset:26368
Mvvv 240,100,92
s_waitcnt lgkmcnt(4)
buffer_load_dwordx4 v11, s[48:51], s72 offen sc1 lds
Mvvv 148,104,64
s_add_u32 m0, s71, 106496
s_add_u32 s72, s68, 6553600
ds_read_b64_tr_b16 v[120:121], v27 offset:17920
Mvvv 148,108,68
buffer_load_dwordx4 v3, s[16:19], s72 offen lds
Mvvv 180,104,72
s_add_u32 m0, s69, 72704
s_add_u32 s72, s82, 36352
ds_read_b64_tr_b16 v[122:123], v27 offset:18176
Mvvv 180,108,76
buffer_load_dwordx4 v12, s[48:51], s72 offen sc1 lds
Mvvv 212,104,80
s_add_u32 m0, s71, 107392
s_add_u32 s72, s68, 6553600
ds_read_b64_tr_b16 v[124:125], v27 offset:26112
Mvvv 212,108,84
buffer_load_dwordx4 v3, s[16:19], s72 offen offset:128 lds
Mvvv 244,104,88
ds_read_b64_tr_b16 v[126:127], v27 offset:26368
v_accvgpr_read_b32 v30, a210
Mvvv 244,108,92
s_waitcnt lgkmcnt(6)
v_accvgpr_read_b32 v31, a211
v_cvt_pk_bf16_f32 v17, v30, v31
Mvvv 152,112,64
s_waitcnt lgkmcnt(4)
v_accvgpr_read_b32 v30, a240
v_accvgpr_read_b32 v31, a241
Mvvv 152,116,68
v_cvt_pk_bf16_f32 v18, v30, v31
v_accvgpr_read_b32 v30, a242
Mvvv 184,112,72
v_accvgpr_read_b32 v31, a243
v_cvt_pk_bf16_f32 v19, v30, v31
Mvvv 184,116,76
s_add_u32 s73, s81, 35840
buffer_store_dwordx4 v[16:19], v0, s[48:51], s73 offen sc1
Mvvv 216,112,80
v_accvgpr_read_b32 v30, a212
v_accvgpr_read_b32 v31, a213
Mvvv 216,116,84
s_waitcnt vmcnt(29) lgkmcnt(0)
v_cvt_pk_bf16_f32 v20, v30, v31
v_accvgpr_read_b32 v30, a214
Mvvv 248,112,88
s_barrier
Mvvv 248,116,92
ds_read_b64_tr_b16 v[96:97], v26 offset:32768
v_accvgpr_read_b32 v31, a215
Mvvv 156,120,64
ds_read_b64_tr_b16 v[98:99], v26 offset:33024
v_cvt_pk_bf16_f32 v21, v30, v31
Mvvv 156,124,68
ds_read_b64_tr_b16 v[100:101], v26 offset:40960
v_accvgpr_read_b32 v30, a244
Mvvv 188,120,72
ds_read_b64_tr_b16 v[102:103], v26 offset:41216
v_accvgpr_read_b32 v31, a245
Mvvv 188,124,76
v_cvt_pk_bf16_f32 v22, v30, v31
v_accvgpr_read_b32 v30, a246
Mvvv 220,120,80
ds_read_b64_tr_b16 v[104:105], v27 offset:32768
v_accvgpr_read_b32 v31, a247
Mvvv 220,124,84
ds_read_b64_tr_b16 v[106:107], v27 offset:33024
v_cvt_pk_bf16_f32 v23, v30, v31
Mvvv 252,120,88
s_add_u32 s73, s81, 37888
ds_read_b64_tr_b16 v[108:109], v27 offset:40960
Mvvv 252,124,92
s_waitcnt lgkmcnt(5)
ds_read_b64_tr_b16 v[110:111], v27 offset:41216
Mvvv 128,96,32
s_waitcnt lgkmcnt(4)
s_add_u32 m0, s69, 0
s_add_u32 s72, s83, 1664
ds_read_b64_tr_b16 v[112:113], v26 offset:33280
Mvvv 128,100,36
buffer_load_dwordx4 v11, s[48:51], s72 offen sc1 lds
Mvvv 160,96,40
ds_read_b64_tr_b16 v[114:115], v26 offset:33536
Mvvv 160,100,44
buffer_store_dwordx4 v[20:23], v0, s[48:51], s73 offen sc1
Mvvv 192,96,48
s_add_u32 m0, s69, 1024
s_add_u32 s72, s83, 1664
ds_read_b64_tr_b16 v[116:117], v26 offset:41472
Mvvv 192,100,52
buffer_load_dwordx4 v12, s[48:51], s72 offen sc1 lds
Mvvv 224,96,56
s_waitcnt vmcnt(19)
ds_read_b64_tr_b16 v[118:119], v26 offset:41728
v_accvgpr_read_b32 v30, a216
Mvvv 224,100,60
s_waitcnt lgkmcnt(6)
ds_read_b128 v[64:67], v5 offset:32768
v_accvgpr_read_b32 v31, a217
Mvvv 132,104,32
s_waitcnt lgkmcnt(5)
ds_read_b64_tr_b16 v[120:121], v27 offset:33280
v_cvt_pk_bf16_f32 v16, v30, v31
Mvvv 132,108,36
ds_read_b128 v[68:71], v6 offset:32768
v_accvgpr_read_b32 v30, a218
Mvvv 164,104,40
ds_read_b64_tr_b16 v[122:123], v27 offset:33536
v_accvgpr_read_b32 v31, a219
Mvvv 164,108,44
ds_read_b128 v[72:75], v5 offset:34816
v_cvt_pk_bf16_f32 v17, v30, v31
Mvvv 196,104,48
ds_read_b64_tr_b16 v[124:125], v27 offset:41472
v_accvgpr_read_b32 v30, a248
Mvvv 196,108,52
ds_read_b128 v[76:79], v6 offset:34816
v_accvgpr_read_b32 v31, a249
Mvvv 228,104,56
ds_read_b64_tr_b16 v[126:127], v27 offset:41728
v_cvt_pk_bf16_f32 v18, v30, v31
Mvvv 228,108,60
s_waitcnt lgkmcnt(10)
ds_read_b128 v[80:83], v5 offset:36864
v_accvgpr_read_b32 v30, a250
Mvvv 136,112,32
s_waitcnt lgkmcnt(9)
ds_read_b64_tr_b16 v[96:97], v26 offset:33792
v_accvgpr_read_b32 v31, a251
Mvvv 136,116,36
ds_read_b128 v[84:87], v6 offset:36864
v_cvt_pk_bf16_f32 v19, v30, v31
Mvvv 168,112,40
s_add_u32 s73, s81, 39936
ds_read_b64_tr_b16 v[98:99], v26 offset:34048
Mvvv 168,116,44
ds_read_b128 v[88:91], v5 offset:38912
Mvvv 200,112,48
ds_read_b64_tr_b16 v[100:101], v26 offset:41984
Mvvv 200,116,52
s_waitcnt lgkmcnt(11)
ds_read_b128 v[92:95], v6 offset:38912
Mvvv 232,112,56
s_add_u32 m0, s69, 2048
s_add_u32 s72, s83, 36480
ds_read_b64_tr_b16 v[102:103], v26 offset:42240
Mvvv 232,116,60
s_waitcnt lgkmcnt(12)
buffer_load_dwordx4 v11, s[48:51], s72 offen sc1 lds
Mvvv 140,120,32
s_waitcnt lgkmcnt(8)
ds_read_b64_tr_b16 v[104:105], v27 offset:33792
Mvvv 140,124,36
buffer_store_dwordx4 v[16:19], v0, s[48:51], s73 offen sc1
Mvvv 172,120,40
s_add_u32 m0, s69, 3072
s_add_u32 s72, s83, 36480
ds_read_b64_tr_b16 v[106:107], v27 offset:34048
Mvvv 172,124,44
buffer_load_dwordx4 v12, s[48:51], s72 offen sc1 lds
Mvvv 204,120,48
ds_read_b64_tr_b16 v[108:109], v27 offset:41984
v_accvgpr_read_b32 v30, a220
Mvvv 204,124,52
v_accvgpr_read_b32 v31, a221
v_cvt_pk_bf16_f32 v20, v30, v31
Mvvv 236,120,56
s_add_u32 m0, s69, 4096
s_add_u32 s72, s82, 1664
ds_read_b64_tr_b16 v[110:111], v27 offset:42240
Mvvv 236,124,60
s_waitcnt lgkmcnt(4)
buffer_load_dwordx4 v11, s[48:51], s72 offen sc1 lds
Mvvv 144,96,32
s_add_u32 m0, s71, 114688
s_add_u32 s72, s68, 6815744
ds_read_b64_tr_b16 v[112:113], v26 offset:34304
Mvvv 144,100,36
buffer_load_dwordx4 v3, s[16:19], s72 offen lds
Mvvv 176,96,40
s_add_u32 m0, s69, 5120
s_add_u32 s72, s82, 1664
ds_read_b64_tr_b16 v[114:115], v26 offset:34560
Mvvv 176,100,44
buffer_load_dwordx4 v12, s[48:51], s72 offen sc1 lds
Mvvv 208,96,48
s_add_u32 m0, s71, 115584
s_add_u32 s72, s68, 6815744
ds_read_b64_tr_b16 v[116:117], v26 offset:42496
Mvvv 208,100,52
buffer_load_dwordx4 v3, s[16:19], s72 offen offset:128 lds
Mvvv 240,96,56
s_add_u32 m0, s69, 6144
s_add_u32 s72, s82, 36480
ds_read_b64_tr_b16 v[118:119], v26 offset:42752
Mvvv 240,100,60
s_waitcnt lgkmcnt(4)
buffer_load_dwordx4 v11, s[48:51], s72 offen sc1 lds
Mvvv 148,104,32
s_add_u32 m0, s71, 122880
s_add_u32 s72, s68, 7077888
ds_read_b64_tr_b16 v[120:121], v27 offset:34304
Mvvv 148,108,36
buffer_load_dwordx4 v3, s[16:19], s72 offen lds
Mvvv 180,104,40
s_add_u32 m0, s69, 7168
s_add_u32 s72, s82, 36480
ds_read_b64_tr_b16 v[122:123], v27 offset:34560
Mvvv 180,108,44
buffer_load_dwordx4 v12, s[48:51], s72 offen sc1 lds
Mvvv 212,104,48
s_add_u32 m0, s71, 123776
s_add_u32 s72, s68, 7077888
ds_read_b64_tr_b16 v[124:125], v27 offset:42496
Mvvv 212,108,52
buffer_load_dwordx4 v3, s[16:19], s72 offen offset:128 lds
Mvvv 244,104,56
ds_read_b64_tr_b16 v[126:127], v27 offset:42752
v_accvgpr_read_b32 v30, a222
Mvvv 244,108,60
s_waitcnt lgkmcnt(6)
v_accvgpr_read_b32 v31, a223
v_cvt_pk_bf16_f32 v21, v30, v31
Mvvv 152,112,32
s_waitcnt lgkmcnt(4)
v_accvgpr_read_b32 v30, a252
v_accvgpr_read_b32 v31, a253
Mvvv 152,116,36
v_cvt_pk_bf16_f32 v22, v30, v31
v_accvgpr_read_b32 v30, a254
Mvvv 184,112,40
v_accvgpr_read_b32 v31, a255
v_cvt_pk_bf16_f32 v23, v30, v31
Mvvv 184,116,44
s_add_u32 s73, s81, 41984
buffer_store_dwordx4 v[20:23], v0, s[48:51], s73 offen sc1
Mvvv 216,112,48
v_accvgpr_write_b32 a0, 0
v_accvgpr_write_b32 a1, 0
Mvvv 216,116,52
s_waitcnt vmcnt(30) lgkmcnt(0)
v_accvgpr_write_b32 a2, 0
v_accvgpr_write_b32 a3, 0
Mvvv 248,112,56
s_barrier
Mvvv 248,116,60
ds_read_b64_tr_b16 v[96:97], v26 offset:49152
v_mov_b32 v16, s74
Mvvv 156,120,32
ds_read_b64_tr_b16 v[98:99], v26 offset:49408
v_mov_b32 v17, s75
Mvvv 156,124,36
ds_read_b64_tr_b16 v[100:101], v26 offset:57344
v_accvgpr_write_b32 a4, 0
Mvvv 188,120,40
ds_read_b64_tr_b16 v[102:103], v26 offset:57600
v_accvgpr_write_b32 a5, 0
Mvvv 188,124,44
v_accvgpr_write_b32 a6, 0
v_accvgpr_write_b32 a7, 0
Mvvv 220,120,48
ds_read_b64_tr_b16 v[104:105], v27 offset:49152
v_accvgpr_write_b32 a8, 0
Mvvv 220,124,52
ds_read_b64_tr_b16 v[106:107], v27 offset:49408
v_accvgpr_write_b32 a9, 0
Mvvv 252,120,56
ds_read_b64_tr_b16 v[108:109], v27 offset:57344
v_accvgpr_write_b32 a10, 0
Mvvv 252,124,60
s_waitcnt lgkmcnt(5)
ds_read_b64_tr_b16 v[110:111], v27 offset:57600
v_accvgpr_write_b32 a11, 0
Mvvv 128,96,64
s_waitcnt lgkmcnt(4)
s_add_u32 m0, s69, 32768
s_add_u32 s72, s83, 1792
ds_read_b64_tr_b16 v[112:113], v26 offset:49664
Mvvv 128,100,68
buffer_load_dwordx4 v11, s[48:51], s72 offen sc1 lds
Mvvv 160,96,72
ds_read_b64_tr_b16 v[114:115], v26 offset:49920
v_accvgpr_write_b32 a12, 0
Mvvv 160,100,76
v_accvgpr_write_b32 a13, 0
v_accvgpr_write_b32 a14, 0
Mvvv 192,96,80
s_add_u32 m0, s69, 33792
s_add_u32 s72, s83, 1792
ds_read_b64_tr_b16 v[116:117], v26 offset:57856
Mvvv 192,100,84
buffer_load_dwordx4 v12, s[48:51], s72 offen sc1 lds
Mvvv 224,96,88
s_waitcnt vmcnt(19)
ds_read_b64_tr_b16 v[118:119], v26 offset:58112
v_accvgpr_write_b32 a15, 0
Mvvv 224,100,92
s_waitcnt lgkmcnt(6)
ds_read_b128 v[32:35], v7 offset:0
v_accvgpr_write_b32 a16, 0
Mvvv 132,104,64
s_waitcnt lgkmcnt(5)
ds_read_b64_tr_b16 v[120:121], v27 offset:49664
v_accvgpr_write_b32 a17, 0
Mvvv 132,108,68
ds_read_b128 v[36:39], v8 offset:0
v_accvgpr_write_b32 a18, 0
Mvvv 164,104,72
ds_read_b64_tr_b16 v[122:123], v27 offset:49920
v_accvgpr_write_b32 a19, 0
Mvvv 164,108,76
ds_read_b128 v[40:43], v7 offset:2048
v_accvgpr_write_b32 a20, 0
Mvvv 196,104,80
ds_read_b64_tr_b16 v[124:125], v27 offset:57856
v_accvgpr_write_b32 a21, 0
Mvvv 196,108,84
ds_read_b128 v[44:47], v8 offset:2048
v_accvgpr_write_b32 a22, 0
Mvvv 228,104,88
ds_read_b64_tr_b16 v[126:127], v27 offset:58112
v_accvgpr_write_b32 a23, 0
Mvvv 228,108,92
s_waitcnt lgkmcnt(10)
ds_read_b128 v[48:51], v7 offset:4096
v_accvgpr_write_b32 a24, 0
Mvvv 136,112,64
s_waitcnt lgkmcnt(9)
ds_read_b64_tr_b16 v[96:97], v26 offset:50176
v_accvgpr_write_b32 a25, 0
Mvvv 136,116,68
ds_read_b128 v[52:55], v8 offset:4096
v_accvgpr_write_b32 a26, 0
Mvvv 168,112,72
ds_read_b64_tr_b16 v[98:99], v26 offset:50432
v_accvgpr_write_b32 a27, 0
Mvvv 168,116,76
ds_read_b128 v[56:59], v7 offset:6144
v_accvgpr_write_b32 a28, 0
Mvvv 200,112,80
ds_read_b64_tr_b16 v[100:101], v26 offset:58368
v_accvgpr_write_b32 a29, 0
Mvvv 200,116,84
s_waitcnt lgkmcnt(11)
)"
R"(
ds_read_b128 v[60:63], v8 offset:6144
v_accvgpr_write_b32 a30, 0
Mvvv 232,112,88
s_add_u32 m0, s69, 34816
s_add_u32 s72, s83, 36608
ds_read_b64_tr_b16 v[102:103], v26 offset:58624
Mvvv 232,116,92
s_waitcnt lgkmcnt(12)
buffer_load_dwordx4 v11, s[48:51], s72 offen sc1 lds
Mvvv 140,120,64
s_waitcnt lgkmcnt(8)
ds_read_b64_tr_b16 v[104:105], v27 offset:50176
v_accvgpr_write_b32 a31, 0
Mvvv 140,124,68
v_accvgpr_write_b32 a32, 0
v_accvgpr_write_b32 a33, 0
Mvvv 172,120,72
s_add_u32 m0, s69, 35840
s_add_u32 s72, s83, 36608
ds_read_b64_tr_b16 v[106:107], v27 offset:50432
Mvvv 172,124,76
buffer_load_dwordx4 v12, s[48:51], s72 offen sc1 lds
Mvvv 204,120,80
ds_read_b64_tr_b16 v[108:109], v27 offset:58368
v_accvgpr_write_b32 a34, 0
Mvvv 204,124,84
v_accvgpr_write_b32 a35, 0
v_accvgpr_write_b32 a36, 0
Mvvv 236,120,88
s_add_u32 m0, s69, 36864
s_add_u32 s72, s82, 1792
ds_read_b64_tr_b16 v[110:111], v27 offset:58624
Mvvv 236,124,92
s_waitcnt lgkmcnt(4)
buffer_load_dwordx4 v11, s[48:51], s72 offen sc1 lds
Mvvv 144,96,64
s_add_u32 m0, s71, 131072
s_add_u32 s72, s68, 7340032
ds_read_b64_tr_b16 v[112:113], v26 offset:50688
Mvvv 144,100,68
buffer_load_dwordx4 v3, s[16:19], s72 offen lds
Mvvv 176,96,72
s_add_u32 m0, s69, 37888
s_add_u32 s72, s82, 1792
ds_read_b64_tr_b16 v[114:115], v26 offset:50944
Mvvv 176,100,76
buffer_load_dwordx4 v12, s[48:51], s72 offen sc1 lds
Mvvv 208,96,80
s_add_u32 m0, s71, 131968
s_add_u32 s72, s68, 7340032
ds_read_b64_tr_b16 v[116:117], v26 offset:58880
Mvvv 208,100,84
buffer_load_dwordx4 v3, s[16:19], s72 offen offset:128 lds
Mvvv 240,96,88
s_add_u32 m0, s69, 38912
s_add_u32 s72, s82, 36608
ds_read_b64_tr_b16 v[118:119], v26 offset:59136
Mvvv 240,100,92
s_waitcnt lgkmcnt(4)
buffer_load_dwordx4 v11, s[48:51], s72 offen sc1 lds
Mvvv 148,104,64
s_add_u32 m0, s71, 139264
s_add_u32 s72, s68, 7602176
ds_read_b64_tr_b16 v[120:121], v27 offset:50688
Mvvv 148,108,68
buffer_load_dwordx4 v3, s[16:19], s72 offen lds
Mvvv 180,104,72
s_add_u32 m0, s69, 39936
s_add_u32 s72, s82, 36608
ds_read_b64_tr_b16 v[122:123], v27 offset:50944
Mvvv 180,108,76
buffer_load_dwordx4 v12, s[48:51], s72 offen sc1 lds
Mvvv 212,104,80
s_add_u32 m0, s71, 140160
s_add_u32 s72, s68, 7602176
ds_read_b64_tr_b16 v[124:125], v27 offset:58880
Mvvv 212,108,84
buffer_load_dwordx4 v3, s[16:19], s72 offen offset:128 lds
Mvvv 244,104,88
ds_read_b64_tr_b16 v[126:127], v27 offset:59136
v_accvgpr_write_b32 a37, 0
Mvvv 244,108,92
s_waitcnt lgkmcnt(6)
v_accvgpr_write_b32 a38, 0
v_accvgpr_write_b32 a39, 0
Mvvv 152,112,64
s_waitcnt lgkmcnt(4)
v_accvgpr_write_b32 a40, 0
v_accvgpr_write_b32 a41, 0
Mvvv 152,116,68
v_accvgpr_write_b32 a42, 0
v_accvgpr_write_b32 a43, 0
Mvvv 184,112,72
v_accvgpr_write_b32 a44, 0
v_accvgpr_write_b32 a45, 0
Mvvv 184,116,76
v_accvgpr_write_b32 a46, 0
v_accvgpr_write_b32 a47, 0
Mvvv 216,112,80
v_accvgpr_write_b32 a48, 0
v_accvgpr_write_b32 a49, 0
Mvvv 216,116,84
s_waitcnt vmcnt(28) lgkmcnt(0)
v_accvgpr_write_b32 a50, 0
v_accvgpr_write_b32 a51, 0
Mvvv 248,112,88
s_barrier
Mvvv 248,116,92
ds_read_b64_tr_b16 v[96:97], v26 offset:0
v_accvgpr_write_b32 a52, 0
Mvvv 156,120,64
ds_read_b64_tr_b16 v[98:99], v26 offset:256
v_accvgpr_write_b32 a53, 0
Mvvv 156,124,68
ds_read_b64_tr_b16 v[100:101], v26 offset:8192
v_accvgpr_write_b32 a54, 0
Mvvv 188,120,72
ds_read_b64_tr_b16 v[102:103], v26 offset:8448
v_accvgpr_write_b32 a55, 0
Mvvv 188,124,76
v_accvgpr_write_b32 a56, 0
v_accvgpr_write_b32 a57, 0
Mvvv 220,120,80
ds_read_b64_tr_b16 v[104:105], v27 offset:0
v_accvgpr_write_b32 a58, 0
Mvvv 220,124,84
ds_read_b64_tr_b16 v[106:107], v27 offset:256
v_accvgpr_write_b32 a59, 0
Mvvv 252,120,88
ds_read_b64_tr_b16 v[108:109], v27 offset:8192
v_accvgpr_write_b32 a60, 0
Mvvv 252,124,92
s_waitcnt lgkmcnt(5)
ds_read_b64_tr_b16 v[110:111], v27 offset:8448
v_accvgpr_write_b32 a61, 0
Mvvv 128,96,32
s_waitcnt lgkmcnt(4)
s_add_u32 m0, s69, 65536
s_add_u32 s72, s83, 1920
ds_read_b64_tr_b16 v[112:113], v26 offset:512
Mvvv 128,100,36
buffer_load_dwordx4 v11, s[48:51], s72 offen sc1 lds
Mvvv 160,96,40
ds_read_b64_tr_b16 v[114:115], v26 offset:768
v_accvgpr_write_b32 a62, 0
Mvvv 160,100,44
v_accvgpr_write_b32 a63, 0
v_accvgpr_write_b32 a64, 0
Mvvv 192,96,48
s_add_u32 m0, s69, 66560
s_add_u32 s72, s83, 1920
ds_read_b64_tr_b16 v[116:117], v26 offset:8704
Mvvv 192,100,52
buffer_load_dwordx4 v12, s[48:51], s72 offen sc1 lds
Mvvv 224,96,56
s_waitcnt vmcnt(16)
ds_read_b64_tr_b16 v[118:119], v26 offset:8960
v_accvgpr_write_b32 a65, 0
Mvvv 224,100,60
s_waitcnt lgkmcnt(6)
ds_read_b128 v[64:67], v5 offset:0
v_accvgpr_write_b32 a66, 0
Mvvv 132,104,32
s_waitcnt lgkmcnt(5)
ds_read_b64_tr_b16 v[120:121], v27 offset:512
v_accvgpr_write_b32 a67, 0
Mvvv 132,108,36
ds_read_b128 v[68:71], v6 offset:0
v_accvgpr_write_b32 a68, 0
Mvvv 164,104,40
ds_read_b64_tr_b16 v[122:123], v27 offset:768
v_accvgpr_write_b32 a69, 0
Mvvv 164,108,44
ds_read_b128 v[72:75], v5 offset:2048
v_accvgpr_write_b32 a70, 0
Mvvv 196,104,48
ds_read_b64_tr_b16 v[124:125], v27 offset:8704
v_accvgpr_write_b32 a71, 0
Mvvv 196,108,52
ds_read_b128 v[76:79], v6 offset:2048
v_accvgpr_write_b32 a72, 0
Mvvv 228,104,56
ds_read_b64_tr_b16 v[126:127], v27 offset:8960
v_accvgpr_write_b32 a73, 0
Mvvv 228,108,60
s_waitcnt lgkmcnt(10)
ds_read_b128 v[80:83], v5 offset:4096
v_accvgpr_write_b32 a74, 0
Mvvv 136,112,32
s_waitcnt lgkmcnt(9)
ds_read_b64_tr_b16 v[96:97], v26 offset:1024
v_accvgpr_write_b32 a75, 0
Mvvv 136,116,36
ds_read_b128 v[84:87], v6 offset:4096
v_accvgpr_write_b32 a76, 0
Mvvv 168,112,40
ds_read_b64_tr_b16 v[98:99], v26 offset:1280
v_accvgpr_write_b32 a77, 0
Mvvv 168,116,44
ds_read_b128 v[88:91], v5 offset:6144
v_accvgpr_write_b32 a78, 0
Mvvv 200,112,48
ds_read_b64_tr_b16 v[100:101], v26 offset:9216
v_accvgpr_write_b32 a79, 0
Mvvv 200,116,52
s_waitcnt lgkmcnt(11)
ds_read_b128 v[92:95], v6 offset:6144
v_accvgpr_write_b32 a80, 0
Mvvv 232,112,56
s_add_u32 m0, s69, 67584
s_add_u32 s72, s83, 36736
ds_read_b64_tr_b16 v[102:103], v26 offset:9472
Mvvv 232,116,60
s_waitcnt lgkmcnt(12)
buffer_load_dwordx4 v11, s[48:51], s72 offen sc1 lds
Mvvv 140,120,32
s_waitcnt lgkmcnt(8)
ds_read_b64_tr_b16 v[104:105], v27 offset:1024
v_accvgpr_write_b32 a81, 0
Mvvv 140,124,36
v_accvgpr_write_b32 a82, 0
v_accvgpr_write_b32 a83, 0
Mvvv 172,120,40
s_add_u32 m0, s69, 68608
s_add_u32 s72, s83, 36736
ds_read_b64_tr_b16 v[106:107], v27 offset:1280
Mvvv 172,124,44
buffer_load_dwordx4 v12, s[48:51], s72 offen sc1 lds
Mvvv 204,120,48
s_waitcnt vmcnt(16)
ds_read_b64_tr_b16 v[108:109], v27 offset:9216
v_accvgpr_write_b32 a84, 0
Mvvv 204,124,52
buffer_store_dwordx2 v[16:17], v13, s[48:51], s85 offen sc1
Mvvv 236,120,56
s_add_u32 m0, s69, 69632
s_add_u32 s72, s82, 1920
ds_read_b64_tr_b16 v[110:111], v27 offset:9472
Mvvv 236,124,60
s_waitcnt lgkmcnt(4)
buffer_load_dwordx4 v11, s[48:51], s72 offen sc1 lds
Mvvv 144,96,32
s_add_u32 m0, s71, 147456
s_add_u32 s72, s68, 7864320
ds_read_b64_tr_b16 v[112:113], v26 offset:1536
Mvvv 144,100,36
buffer_load_dwordx4 v3, s[16:19], s72 offen lds
Mvvv 176,96,40
s_add_u32 m0, s69, 70656
s_add_u32 s72, s82, 1920
ds_read_b64_tr_b16 v[114:115], v26 offset:1792
Mvvv 176,100,44
buffer_load_dwordx4 v12, s[48:51], s72 offen sc1 lds
Mvvv 208,96,48
s_add_u32 m0, s71, 148352
s_add_u32 s72, s68, 7864320
ds_read_b64_tr_b16 v[116:117], v26 offset:9728
Mvvv 208,100,52
buffer_load_dwordx4 v3, s[16:19], s72 offen offset:128 lds
Mvvv 240,96,56
s_add_u32 m0, s69, 71680
s_add_u32 s72, s82, 36736
ds_read_b64_tr_b16 v[118:119], v26 offset:9984
Mvvv 240,100,60
s_waitcnt lgkmcnt(4)
buffer_load_dwordx4 v11, s[48:51], s72 offen sc1 lds
Mvvv 148,104,32
s_add_u32 m0, s71, 155648
s_add_u32 s72, s68, 8126464
ds_read_b64_tr_b16 v[120:121], v27 offset:1536
Mvvv 148,108,36
buffer_load_dwordx4 v3, s[16:19], s72 offen lds
Mvvv 180,104,40
s_add_u32 m0, s69, 72704
s_add_u32 s72, s82, 36736
ds_read_b64_tr_b16 v[122:123], v27 offset:1792
Mvvv 180,108,44
buffer_load_dwordx4 v12, s[48:51], s72 offen sc1 lds
Mvvv 212,104,48
s_add_u32 m0, s71, 156544
s_add_u32 s72, s68, 8126464
ds_read_b64_tr_b16 v[124:125], v27 offset:9728
Mvvv 212,108,52
buffer_load_dwordx4 v3, s[16:19], s72 offen offset:128 lds
Mvvv 244,104,56
ds_read_b64_tr_b16 v[126:127], v27 offset:9984
v_accvgpr_write_b32 a85, 0
Mvvv 244,108,60
s_waitcnt lgkmcnt(6)
v_accvgpr_write_b32 a86, 0
v_accvgpr_write_b32 a87, 0
Mvvv 152,112,32
s_waitcnt lgkmcnt(4)
v_accvgpr_write_b32 a88, 0
v_accvgpr_write_b32 a89, 0
Mvvv 152,116,36
v_accvgpr_write_b32 a90, 0
v_accvgpr_write_b32 a91, 0
Mvvv 184,112,40
v_accvgpr_write_b32 a92, 0
v_accvgpr_write_b32 a93, 0
Mvvv 184,116,44
v_accvgpr_write_b32 a94, 0
v_accvgpr_write_b32 a95, 0
Mvvv 216,112,48
v_accvgpr_write_b32 a96, 0
v_accvgpr_write_b32 a97, 0
Mvvv 216,116,52
s_waitcnt lgkmcnt(0)
v_accvgpr_write_b32 a98, 0
v_accvgpr_write_b32 a99, 0
Mvvv 248,112,56
s_barrier
Mvvv 248,116,60
ds_read_b64_tr_b16 v[96:97], v26 offset:16384
v_accvgpr_write_b32 a100, 0
Mvvv 156,120,32
ds_read_b64_tr_b16 v[98:99], v26 offset:16640
v_accvgpr_write_b32 a101, 0
Mvvv 156,124,36
ds_read_b64_tr_b16 v[100:101], v26 offset:24576
v_accvgpr_write_b32 a102, 0
Mvvv 188,120,40
ds_read_b64_tr_b16 v[102:103], v26 offset:24832
v_accvgpr_write_b32 a103, 0
Mvvv 188,124,44
v_accvgpr_write_b32 a104, 0
v_accvgpr_write_b32 a105, 0
Mvvv 220,120,48
ds_read_b64_tr_b16 v[104:105], v27 offset:16384
v_accvgpr_write_b32 a106, 0
Mvvv 220,124,52
ds_read_b64_tr_b16 v[106:107], v27 offset:16640
v_accvgpr_write_b32 a107, 0
Mvvv 252,120,56
ds_read_b64_tr_b16 v[108:109], v27 offset:24576
v_accvgpr_write_b32 a108, 0
Mvvv 252,124,60
s_waitcnt lgkmcnt(5)
ds_read_b64_tr_b16 v[110:111], v27 offset:24832
v_accvgpr_write_b32 a109, 0
Mvvv 128,96,64
s_waitcnt lgkmcnt(4)
ds_read_b64_tr_b16 v[112:113], v26 offset:16896
v_accvgpr_write_b32 a110, 0
Mvvv 128,100,68
v_accvgpr_write_b32 a111, 0
v_accvgpr_write_b32 a112, 0
Mvvv 160,96,72
ds_read_b64_tr_b16 v[114:115], v26 offset:17152
v_accvgpr_write_b32 a113, 0
Mvvv 160,100,76
v_accvgpr_write_b32 a114, 0
v_accvgpr_write_b32 a115, 0
Mvvv 192,96,80
s_lshl2_add_u32 s82, s60, s57
ds_read_b64_tr_b16 v[116:117], v26 offset:25088
v_accvgpr_write_b32 a116, 0
Mvvv 192,100,84
s_lshl_b32 s82, s82, 10
v_accvgpr_write_b32 a117, 0
v_accvgpr_write_b32 a118, 0
Mvvv 224,96,88
s_waitcnt vmcnt(14) lgkmcnt(3)
ds_read_b64_tr_b16 v[118:119], v26 offset:25344
v_accvgpr_write_b32 a119, 0
Mvvv 224,100,92
s_mov_b32 s83, .Lhot_end-.Lstart
ds_read_b128 v[32:35], v5 offset:32768
v_accvgpr_write_b32 a120, 0
Mvvv 132,104,64
s_min_u32 s82, s82, s83
ds_read_b64_tr_b16 v[120:121], v27 offset:16896
v_accvgpr_write_b32 a121, 0
Mvvv 132,108,68
s_getpc_b64 s[16:17]
.Lcp3:
ds_read_b128 v[36:39], v6 offset:32768
v_accvgpr_write_b32 a122, 0
Mvvv 164,104,72
ds_read_b64_tr_b16 v[122:123], v27 offset:17152
v_accvgpr_write_b32 a123, 0
Mvvv 164,108,76
ds_read_b128 v[40:43], v5 offset:34816
v_accvgpr_write_b32 a124, 0
Mvvv 196,104,80
ds_read_b64_tr_b16 v[124:125], v27 offset:25088
v_accvgpr_write_b32 a125, 0
Mvvv 196,108,84
ds_read_b128 v[44:47], v6 offset:34816
v_accvgpr_write_b32 a126, 0
Mvvv 228,104,88
s_waitcnt lgkmcnt(7)
ds_read_b64_tr_b16 v[126:127], v27 offset:25344
v_accvgpr_write_b32 a127, 0
Mvvv 228,108,92
s_sub_u32 s16, s16, .Lcp3-.Lstart
s_subb_u32 s17, s17, 0
ds_read_b128 v[48:51], v5 offset:36864
Mvvv 136,112,64
s_add_u32 s16, s16, s82
s_addc_u32 s17, s17, 0
ds_read_b64_tr_b16 v[96:97], v26 offset:17408
Mvvv 136,116,68
s_and_b32 s16, s16, -16
ds_read_b128 v[52:55], v6 offset:36864
Mvvv 168,112,72
ds_read_b64_tr_b16 v[98:99], v26 offset:17664
Mvvv 168,116,76
ds_read_b128 v[56:59], v5 offset:38912
Mvvv 200,112,80
ds_read_b64_tr_b16 v[100:101], v26 offset:25600
Mvvv 200,116,84
ds_read_b128 v[60:63], v6 offset:38912
Mvvv 232,112,88
s_waitcnt lgkmcnt(12)
ds_read_b64_tr_b16 v[102:103], v26 offset:25856
Mvvv 232,116,92
s_waitcnt lgkmcnt(12)
global_load_dwordx4 v[28:31], v0, s[16:17]
Mvvv 140,120,64
s_waitcnt lgkmcnt(8)
ds_read_b64_tr_b16 v[104:105], v27 offset:17408
Mvvv 140,124,68
Mvvv 172,120,72
ds_read_b64_tr_b16 v[106:107], v27 offset:17664
Mvvv 172,124,76
Mvvv 204,120,80
ds_read_b64_tr_b16 v[108:109], v27 offset:25600
)"
R"(
Mvvv 204,124,84
Mvvv 236,120,88
s_lshl3_add_u32 s88, s57, 0x18000
ds_read_b64_tr_b16 v[110:111], v27 offset:25856
v_mov_b32 v9, s88
Mvvv 236,124,92
s_waitcnt lgkmcnt(8)
v_mov_b32 v22, s89
v_mov_b32 v23, s90
Mvvv 144,96,64
s_waitcnt lgkmcnt(4)
ds_read_b64_tr_b16 v[112:113], v26 offset:17920
Mvvv 144,100,68
ds_write_b64 v9, v[22:23]
Mvvv 176,96,72
ds_read_b64_tr_b16 v[114:115], v26 offset:18176
v_bfe_u32 v9, v0, 4, 3
Mvvv 176,100,76
v_lshlrev_b32 v9, 2, v9
v_add_u32 v9, 0x18000, v9
Mvvv 208,96,80
ds_read_b64_tr_b16 v[116:117], v26 offset:26112
Mvvv 208,100,84
Mvvv 240,96,88
ds_read_b64_tr_b16 v[118:119], v26 offset:26368
Mvvv 240,100,92
s_waitcnt lgkmcnt(7)
Mvvv 148,104,64
s_waitcnt lgkmcnt(5)
ds_read_b64_tr_b16 v[120:121], v27 offset:17920
Mvvv 148,108,68
Mvvv 180,104,72
ds_read_b64_tr_b16 v[122:123], v27 offset:18176
Mvvv 180,108,76
Mvvv 212,104,80
ds_read_b64_tr_b16 v[124:125], v27 offset:26112
Mvvv 212,108,84
Mvvv 244,104,88
ds_read_b64_tr_b16 v[126:127], v27 offset:26368
Mvvv 244,108,92
s_waitcnt lgkmcnt(6)
Mvvv 152,112,64
s_waitcnt lgkmcnt(4)
Mvvv 152,116,68
Mvvv 184,112,72
Mvvv 184,116,76
Mvvv 216,112,80
Mvvv 216,116,84
s_waitcnt vmcnt(14) lgkmcnt(0)
Mvvv 248,112,88
s_barrier
Mvvv 248,116,92
ds_read_b64_tr_b16 v[96:97], v26 offset:32768
Mvvv 156,120,64
ds_read_b64_tr_b16 v[98:99], v26 offset:33024
Mvvv 156,124,68
ds_read_b64_tr_b16 v[100:101], v26 offset:40960
Mvvv 188,120,72
ds_read_b64_tr_b16 v[102:103], v26 offset:41216
Mvvv 188,124,76
ds_read_b32 v22, v9
Mvvv 220,120,80
ds_read_b64_tr_b16 v[104:105], v27 offset:32768
Mvvv 220,124,84
ds_read_b64_tr_b16 v[106:107], v27 offset:33024
Mvvv 252,120,88
ds_read_b64_tr_b16 v[108:109], v27 offset:40960
Mvvv 252,124,92
s_waitcnt lgkmcnt(6)
ds_read_b64_tr_b16 v[110:111], v27 offset:41216
Mvvv 128,96,32
s_waitcnt lgkmcnt(5)
ds_read_b64_tr_b16 v[112:113], v26 offset:33280
Mvvv 128,100,36
buffer_load_dwordx2 v[20:21], v1, s[48:51], s84 offen sc1
Mvvv 160,96,40
ds_read_b64_tr_b16 v[114:115], v26 offset:33536
Mvvv 160,100,44
s_waitcnt lgkmcnt(6)
v_mov_b32 v10, v22
Mvvv 192,96,48
ds_read_b64_tr_b16 v[116:117], v26 offset:41472
Mvvv 192,100,52
v_max_u32_dpp v10, v10, v10 quad_perm:[1,0,3,2] row_mask:0xf bank_mask:0xf bound_ctrl:1
Mvvv 224,96,56
s_waitcnt vmcnt(3)
ds_read_b64_tr_b16 v[118:119], v26 offset:41728
Mvvv 224,100,60
s_waitcnt lgkmcnt(6)
ds_read_b128 v[64:67], v7 offset:0
v_max_u32_dpp v10, v10, v10 quad_perm:[2,3,0,1] row_mask:0xf bank_mask:0xf bound_ctrl:1
Mvvv 132,104,32
s_waitcnt lgkmcnt(5)
ds_read_b64_tr_b16 v[120:121], v27 offset:33280
Mvvv 132,108,36
ds_read_b128 v[68:71], v8 offset:0
v_max_u32_dpp v10, v10, v10 row_half_mirror row_mask:0xf bank_mask:0xf bound_ctrl:1
Mvvv 164,104,40
ds_read_b64_tr_b16 v[122:123], v27 offset:33536
v_cmp_eq_u32 vcc, v10, v22
Mvvv 164,108,44
ds_read_b128 v[72:75], v7 offset:2048
Mvvv 196,104,48
ds_read_b64_tr_b16 v[124:125], v27 offset:41472
v_cndmask_b32 v22, v22, 0, vcc
Mvvv 196,108,52
ds_read_b128 v[76:79], v8 offset:2048
Mvvv 228,104,56
ds_read_b64_tr_b16 v[126:127], v27 offset:41728
v_max_u32_dpp v22, v22, v22 quad_perm:[1,0,3,2] row_mask:0xf bank_mask:0xf bound_ctrl:1
Mvvv 228,108,60
s_waitcnt lgkmcnt(10)
ds_read_b128 v[80:83], v7 offset:4096
Mvvv 136,112,32
s_waitcnt lgkmcnt(9)
ds_read_b64_tr_b16 v[96:97], v26 offset:33792
v_max_u32_dpp v22, v22, v22 quad_perm:[2,3,0,1] row_mask:0xf bank_mask:0xf bound_ctrl:1
Mvvv 136,116,36
ds_read_b128 v[84:87], v8 offset:4096
Mvvv 168,112,40
ds_read_b64_tr_b16 v[98:99], v26 offset:34048
v_max_u32_dpp v22, v22, v22 row_half_mirror row_mask:0xf bank_mask:0xf bound_ctrl:1
Mvvv 168,116,44
ds_read_b128 v[88:91], v7 offset:6144
v_readfirstlane_b32 s89, v10
Mvvv 200,112,48
ds_read_b64_tr_b16 v[100:101], v26 offset:41984
v_readfirstlane_b32 s90, v22
Mvvv 200,116,52
ds_read_b128 v[92:95], v8 offset:6144
Mvvv 232,112,56
s_waitcnt lgkmcnt(12)
ds_read_b64_tr_b16 v[102:103], v26 offset:42240
Mvvv 232,116,60
s_waitcnt lgkmcnt(12)
Mvvv 140,120,32
s_waitcnt lgkmcnt(8)
ds_read_b64_tr_b16 v[104:105], v27 offset:33792
Mvvv 140,124,36
Mvvv 172,120,40
ds_read_b64_tr_b16 v[106:107], v27 offset:34048
Mvvv 172,124,44
Mvvv 204,120,48
ds_read_b64_tr_b16 v[108:109], v27 offset:41984
Mvvv 204,124,52
Mvvv 236,120,56
ds_read_b64_tr_b16 v[110:111], v27 offset:42240
Mvvv 236,124,60
s_waitcnt lgkmcnt(8)
Mvvv 144,96,32
s_waitcnt lgkmcnt(4)
ds_read_b64_tr_b16 v[112:113], v26 offset:34304
Mvvv 144,100,36
Mvvv 176,96,40
ds_read_b64_tr_b16 v[114:115], v26 offset:34560
Mvvv 176,100,44
Mvvv 208,96,48
ds_read_b64_tr_b16 v[116:117], v26 offset:42496
Mvvv 208,100,52
Mvvv 240,96,56
ds_read_b64_tr_b16 v[118:119], v26 offset:42752
Mvvv 240,100,60
s_waitcnt lgkmcnt(6)
Mvvv 148,104,32
s_waitcnt lgkmcnt(4)
ds_read_b64_tr_b16 v[120:121], v27 offset:34304
Mvvv 148,108,36
Mvvv 180,104,40
ds_read_b64_tr_b16 v[122:123], v27 offset:34560
Mvvv 180,108,44
Mvvv 212,104,48
ds_read_b64_tr_b16 v[124:125], v27 offset:42496
Mvvv 212,108,52
Mvvv 244,104,56
ds_read_b64_tr_b16 v[126:127], v27 offset:42752
Mvvv 244,108,60
s_waitcnt lgkmcnt(6)
Mvvv 152,112,32
s_waitcnt vmcnt(0) lgkmcnt(4)
v_xor_b32 v20, s74, v20
v_bitop3_b32 v20, v21, s75, v20 bitop3:0xbe
Mvvv 152,116,36
v_cmp_ne_u32 vcc, 0, v20
Mvvv 184,112,40
s_cbranch_vccnz .Lkvf
.Lkvf_r:
Mvvv 184,116,44
Mvvv 216,112,48
Mvvv 216,116,52
s_waitcnt lgkmcnt(0)
Mvvv 248,112,56
s_barrier
Mvvv 248,116,60
ds_read_b64_tr_b16 v[96:97], v26 offset:49152
Mvvv 156,120,32
ds_read_b64_tr_b16 v[98:99], v26 offset:49408
v_readlane_b32 s5, v4, 60
Mvvv 156,124,36
s_add_u32 s58, s92, 0
s_and_b32 s58, s58, 0xc000
ds_read_b64_tr_b16 v[100:101], v26 offset:57344
Mvvv 188,120,40
s_add_u32 s58, s58, s70
s_add_u32 m0, s58, 0
ds_read_b64_tr_b16 v[102:103], v26 offset:57600
Mvvv 188,124,44
Mvvv 220,120,48
ds_read_b64_tr_b16 v[104:105], v27 offset:49152
Mvvv 220,124,52
ds_read_b64_tr_b16 v[106:107], v27 offset:49408
Mvvv 252,120,56
ds_read_b64_tr_b16 v[108:109], v27 offset:57344
Mvvv 252,124,60
s_waitcnt lgkmcnt(5)
ds_read_b64_tr_b16 v[110:111], v27 offset:57600
Mvvv 128,96,64
s_waitcnt lgkmcnt(4)
ds_read_b64_tr_b16 v[112:113], v26 offset:49664
Mvvv 128,100,68
buffer_load_dwordx4 v0, s[48:51], s5 offen sc1 lds
Mvvv 160,96,72
ds_read_b64_tr_b16 v[114:115], v26 offset:49920
Mvvv 160,100,76
buffer_load_dwordx4 v0, s[48:51], s5 offen offset:1024 sc1 lds
Mvvv 192,96,80
ds_read_b64_tr_b16 v[116:117], v26 offset:57856
Mvvv 192,100,84
buffer_load_dwordx4 v0, s[48:51], s5 offen offset:2048 sc1 lds
Mvvv 224,96,88
ds_read_b64_tr_b16 v[118:119], v26 offset:58112
Mvvv 224,100,92
s_waitcnt lgkmcnt(6)
buffer_load_dwordx4 v0, s[48:51], s5 offen offset:3072 sc1 lds
Mvvv 132,104,64
s_waitcnt lgkmcnt(4)
ds_read_b64_tr_b16 v[120:121], v27 offset:49664
v_readlane_b32 s5, v4, 61
Mvvv 132,108,68
s_add_u32 s58, s92, 16384
s_and_b32 s58, s58, 0xc000
s_add_u32 s58, s58, s70
Mvvv 164,104,72
s_add_u32 m0, s58, 0
ds_read_b64_tr_b16 v[122:123], v27 offset:49920
Mvvv 164,108,76
Mvvv 196,104,80
ds_read_b64_tr_b16 v[124:125], v27 offset:57856
Mvvv 196,108,84
buffer_load_dwordx4 v0, s[48:51], s5 offen sc1 lds
Mvvv 228,104,88
ds_read_b64_tr_b16 v[126:127], v27 offset:58112
Mvvv 228,108,92
s_waitcnt lgkmcnt(6)
buffer_load_dwordx4 v0, s[48:51], s5 offen offset:1024 sc1 lds
Mvvv 136,112,64
s_waitcnt lgkmcnt(4)
ds_read_b64_tr_b16 v[96:97], v26 offset:50176
Mvvv 136,116,68
buffer_load_dwordx4 v0, s[48:51], s5 offen offset:2048 sc1 lds
Mvvv 168,112,72
ds_read_b64_tr_b16 v[98:99], v26 offset:50432
v_cvt_pk_bf16_f32 v128, v128, v129
Mvvv 168,116,76
v_cvt_pk_bf16_f32 v129, v130, v131
v_cvt_pk_bf16_f32 v130, v132, v133
Mvvv 200,112,80
ds_read_b64_tr_b16 v[100:101], v26 offset:58368
v_cvt_pk_bf16_f32 v131, v134, v135
Mvvv 200,116,84
v_cvt_pk_bf16_f32 v160, v160, v161
v_cvt_pk_bf16_f32 v161, v162, v163
Mvvv 232,112,88
ds_read_b64_tr_b16 v[102:103], v26 offset:58624
v_cvt_pk_bf16_f32 v162, v164, v165
Mvvv 232,116,92
s_waitcnt lgkmcnt(6)
v_cvt_pk_bf16_f32 v163, v166, v167
v_cvt_pk_bf16_f32 v192, v192, v193
Mvvv 140,120,64
s_waitcnt lgkmcnt(4)
ds_read_b64_tr_b16 v[104:105], v27 offset:50176
v_cvt_pk_bf16_f32 v193, v194, v195
Mvvv 140,124,68
v_cvt_pk_bf16_f32 v194, v196, v197
v_cvt_pk_bf16_f32 v195, v198, v199
Mvvv 172,120,72
ds_read_b64_tr_b16 v[106:107], v27 offset:50432
v_cvt_pk_bf16_f32 v224, v224, v225
Mvvv 172,124,76
v_cvt_pk_bf16_f32 v225, v226, v227
v_cvt_pk_bf16_f32 v226, v228, v229
Mvvv 204,120,80
ds_read_b64_tr_b16 v[108:109], v27 offset:58368
v_cvt_pk_bf16_f32 v227, v230, v231
Mvvv 204,124,84
buffer_load_dwordx4 v0, s[48:51], s5 offen offset:3072 sc1 lds
Mvvv 236,120,88
s_waitcnt lgkmcnt(5)
ds_read_b64_tr_b16 v[110:111], v27 offset:58624
v_readlane_b32 s5, v4, 62
Mvvv 236,124,92
s_add_u32 s58, s92, 32768
s_and_b32 s58, s58, 0xc000
s_add_u32 s58, s58, s70
Mvvv 144,96,64
s_waitcnt lgkmcnt(4)
s_add_u32 m0, s58, 0
ds_read_b64_tr_b16 v[112:113], v26 offset:50688
Mvvv 144,100,68
Mvvv 176,96,72
ds_read_b64_tr_b16 v[114:115], v26 offset:50944
v_cvt_pk_bf16_f32 v136, v136, v137
Mvvv 176,100,76
v_cvt_pk_bf16_f32 v137, v138, v139
v_cvt_pk_bf16_f32 v138, v140, v141
Mvvv 208,96,80
ds_read_b64_tr_b16 v[116:117], v26 offset:58880
v_cvt_pk_bf16_f32 v139, v142, v143
Mvvv 208,100,84
v_cvt_pk_bf16_f32 v168, v168, v169
v_cvt_pk_bf16_f32 v169, v170, v171
Mvvv 240,96,88
ds_read_b64_tr_b16 v[118:119], v26 offset:59136
v_cvt_pk_bf16_f32 v170, v172, v173
Mvvv 240,100,92
s_waitcnt lgkmcnt(6)
v_cvt_pk_bf16_f32 v171, v174, v175
v_cvt_pk_bf16_f32 v200, v200, v201
Mvvv 148,104,64
s_waitcnt lgkmcnt(4)
ds_read_b64_tr_b16 v[120:121], v27 offset:50688
v_cvt_pk_bf16_f32 v201, v202, v203
Mvvv 148,108,68
v_cvt_pk_bf16_f32 v202, v204, v205
v_cvt_pk_bf16_f32 v203, v206, v207
Mvvv 180,104,72
ds_read_b64_tr_b16 v[122:123], v27 offset:50944
v_cvt_pk_bf16_f32 v232, v232, v233
Mvvv 180,108,76
v_cvt_pk_bf16_f32 v233, v234, v235
v_cvt_pk_bf16_f32 v234, v236, v237
Mvvv 212,104,80
ds_read_b64_tr_b16 v[124:125], v27 offset:58880
v_cvt_pk_bf16_f32 v235, v238, v239
Mvvv 212,108,84
buffer_load_dwordx4 v0, s[48:51], s5 offen sc1 lds
Mvvv 244,104,88
ds_read_b64_tr_b16 v[126:127], v27 offset:59136
Mvvv 244,108,92
s_waitcnt lgkmcnt(6)
buffer_load_dwordx4 v0, s[48:51], s5 offen offset:1024 sc1 lds
Mvvv 152,112,64
s_waitcnt lgkmcnt(4)
buffer_load_dwordx4 v0, s[48:51], s5 offen offset:2048 sc1 lds
Mvvv 152,116,68
buffer_load_dwordx4 v0, s[48:51], s5 offen offset:3072 sc1 lds
Mvvv 184,112,72
v_cvt_pk_bf16_f32 v144, v144, v145
v_cvt_pk_bf16_f32 v145, v146, v147
Mvvv 184,116,76
v_cvt_pk_bf16_f32 v146, v148, v149
v_cvt_pk_bf16_f32 v147, v150, v151
Mvvv 216,112,80
v_cvt_pk_bf16_f32 v176, v176, v177
v_cvt_pk_bf16_f32 v177, v178, v179
Mvvv 216,116,84
s_waitcnt lgkmcnt(0)
v_cvt_pk_bf16_f32 v178, v180, v181
v_cvt_pk_bf16_f32 v179, v182, v183
Mvvv 248,112,88
s_barrier
v_cvt_pk_bf16_f32 v208, v208, v209
v_cvt_pk_bf16_f32 v209, v210, v211
Mvvv 248,116,92
v_cvt_pk_bf16_f32 v210, v212, v213
v_cvt_pk_bf16_f32 v211, v214, v215
Mvvv 156,120,64
v_cvt_pk_bf16_f32 v240, v240, v241
v_cvt_pk_bf16_f32 v241, v242, v243
Mvvv 156,124,68
v_cvt_pk_bf16_f32 v242, v244, v245
v_cvt_pk_bf16_f32 v243, v246, v247
Mvvv 188,120,72
Mvvv 188,124,76
Mvvv 220,120,80
Mvvv 220,124,84
Mvvv 252,120,88
Mvvv 252,124,92
v_cvt_pk_bf16_f32 v152, v152, v153
v_cvt_pk_bf16_f32 v153, v154, v155
v_cvt_pk_bf16_f32 v154, v156, v157
v_cvt_pk_bf16_f32 v155, v158, v159
v_cvt_pk_bf16_f32 v184, v184, v185
v_cvt_pk_bf16_f32 v185, v186, v187
v_cvt_pk_bf16_f32 v186, v188, v189
v_cvt_pk_bf16_f32 v187, v190, v191
v_cvt_pk_bf16_f32 v216, v216, v217
v_cvt_pk_bf16_f32 v217, v218, v219
v_cvt_pk_bf16_f32 v218, v220, v221
v_cvt_pk_bf16_f32 v219, v222, v223
v_cvt_pk_bf16_f32 v248, v248, v249
v_cvt_pk_bf16_f32 v249, v250, v251
v_cvt_pk_bf16_f32 v250, v252, v253
v_cvt_pk_bf16_f32 v251, v254, v255
v_readlane_b32 s5, v14, 62
s_add_u32 s58, s92, 0
s_and_b32 s58, s58, 0xc000
s_add_u32 s58, s58, s70
s_add_u32 m0, s58, 65536
s_nop 0
buffer_load_dwordx4 v0, s[48:51], s5 offen sc1 lds
buffer_load_dwordx4 v0, s[48:51], s5 offen offset:1024 sc1 lds
buffer_load_dwordx4 v0, s[48:51], s5 offen offset:2048 sc1 lds
buffer_load_dwordx4 v0, s[48:51], s5 offen offset:3072 sc1 lds
)"
R"(
v_mov_b32 v12, v4
v_mov_b32 v3, v0
v_add_u32 v1, s92, v0
s_not_b64 s[18:19], s[6:7]
s_cmp_eq_u32 s4, 0
s_cselect_b64 s[16:17], s[18:19], 0
v_bfe_u32 v120, v0, 4, 4
v_lshrrev_b32 v121, 8, v0
v_lshlrev_b32 v121, 2, v121
v_sub_u32 v121, v120, v121
v_cndmask_b32 v120, 64, v121, s[16:17]
v_cndmask_b32 v121, v121, -16, s[16:17]
v_mov_b32 v122, 0xff800000
v_cmp_gt_i32 vcc, 0, v120
v_cndmask_b32 v156, 0, v122, s[16:17]
s_nop 0
v_cndmask_b32 v148, 0, v122, vcc
v_cndmask_b32 v164, 0, v122, vcc
v_cmp_gt_i32 vcc, 1, v120
v_cndmask_b32 v157, 0, v122, s[16:17]
s_nop 0
v_cndmask_b32 v149, 0, v122, vcc
v_cndmask_b32 v165, 0, v122, vcc
v_cmp_gt_i32 vcc, 2, v120
v_cndmask_b32 v158, 0, v122, s[16:17]
s_nop 0
v_cndmask_b32 v150, 0, v122, vcc
v_cndmask_b32 v166, 0, v122, vcc
v_cmp_gt_i32 vcc, 3, v120
v_cndmask_b32 v159, 0, v122, s[16:17]
s_nop 0
v_cndmask_b32 v151, 0, v122, vcc
v_cndmask_b32 v167, 0, v122, vcc
s_waitcnt vmcnt(12)
s_barrier
ds_read_b128 a[128:131], v1 offset:0
ds_read_b128 a[132:135], v1 offset:1024
ds_read_b128 a[136:139], v1 offset:2048
ds_read_b128 a[140:143], v1 offset:3072
ds_read_b128 a[144:147], v1 offset:4096
ds_read_b128 a[148:151], v1 offset:5120
ds_read_b128 a[152:155], v1 offset:6144
ds_read_b128 a[156:159], v1 offset:7168
s_waitcnt lgkmcnt(7)
Zvav 32,128,128
s_waitcnt lgkmcnt(6)
ds_read_b128 a[160:163], v1 offset:8192
v_and_b32 v26, 48, v0
Mvav 32,132,136
s_waitcnt lgkmcnt(6)
ds_read_b128 a[164:167], v1 offset:9216
v_mov_b32 v27, 0x3f803f80
Mvav 32,136,144
s_waitcnt lgkmcnt(6)
ds_read_b128 a[168:171], v1 offset:10240
v_cmp_eq_u32 vcc, 0, v26
Mvav 32,140,152
s_waitcnt lgkmcnt(6)
ds_read_b128 a[172:175], v1 offset:11264
Zvav 48,144,128
s_waitcnt lgkmcnt(6)
ds_read_b128 a[176:179], v1 offset:12288
v_cndmask_b32 v96, 0, v27, vcc
Mvav 48,148,136
s_waitcnt lgkmcnt(6)
ds_read_b128 a[180:183], v1 offset:13312
v_cndmask_b32 v97, 0, v27, vcc
Mvav 48,152,144
s_waitcnt lgkmcnt(6)
ds_read_b128 a[184:187], v1 offset:14336
v_cndmask_b32 v98, 0, v27, vcc
Mvav 48,156,152
ds_read_b128 a[188:191], v1 offset:15360
v_cndmask_b32 v99, 0, v27, vcc
Zvav 36,128,160
v_cmp_eq_u32 vcc, 16, v26
Mvav 36,132,168
Mvav 36,136,176
v_cndmask_b32 v100, 0, v27, vcc
v_cndmask_b32 v101, 0, v27, vcc
Mvav 36,140,184
v_cndmask_b32 v102, 0, v27, vcc
v_cndmask_b32 v103, 0, v27, vcc
Zvav 52,144,160
v_cmp_eq_u32 vcc, 32, v26
Mvav 52,148,168
Mvav 52,152,176
s_waitcnt lgkmcnt(0)
v_cndmask_b32 v104, 0, v27, vcc
v_cndmask_b32 v105, 0, v27, vcc
Mvav 52,156,184
s_barrier
Cvav 40,128,192,148
v_cndmask_b32 v106, 0, v27, vcc
v_cndmask_b32 v107, 0, v27, vcc
Mvav 40,132,200
v_cmp_eq_u32 vcc, 48, v26
v_max3_f32 v4, v32, v33, v34
Mvav 40,136,208
v_max3_f32 v4, v4, v35, v48
v_max3_f32 v4, v4, v49, v50
Mvav 40,140,216
v_cndmask_b32 v108, 0, v27, vcc
v_cndmask_b32 v109, 0, v27, vcc
Cvav 56,144,192,156
v_cndmask_b32 v110, 0, v27, vcc
v_cndmask_b32 v111, 0, v27, vcc
Mvav 56,148,200
v_max_f32 v4, v4, v51
v_mov_b32 v26, v4
Mvav 56,152,208
Mvav 56,156,216
v_permlane16_swap_b32 v4, v26
v_max_f32 v4, v4, v26
Zvav 44,128,224
v_mov_b32 v26, v4
Mvav 44,132,232
Mvav 44,136,240
v_permlane32_swap_b32 v4, v26
v_max_f32 v4, v4, v26
Mvav 44,140,248
v_sub_f32 v7, 0, v4
v_sub_f32 v6, 0, v4
Cvav 60,144,224,164
v_sub_f32 v5, 0, v4
v_sub_f32 v4, 0, v4
Mvav 60,148,232
v_add_f32 v32, v32, v4
v_add_f32 v33, v33, v4
Mvav 60,152,240
v_add_f32 v34, v34, v4
Mvav 60,156,248
v_add_f32 v35, v35, v4
v_exp_f32 v120, v32
v_exp_f32 v121, v33
v_exp_f32 v122, v34
v_readlane_b32 s5, v14, 63
s_add_u32 s58, s92, 16384
s_and_b32 s58, s58, 0xc000
s_add_u32 s58, s58, s70
s_add_u32 m0, s58, 65536
s_nop 0
buffer_load_dwordx4 v0, s[48:51], s5 offen sc1 lds
v_exp_f32 v123, v35
v_cvt_pk_bf16_f32 v196, v120, v121
v_cvt_pk_bf16_f32 v197, v122, v123
v_add_f32 v48, v48, v4
buffer_load_dwordx4 v0, s[48:51], s5 offen offset:1024 sc1 lds
v_add_f32 v49, v49, v4
v_add_f32 v50, v50, v4
v_add_f32 v51, v51, v4
v_exp_f32 v120, v48
buffer_load_dwordx4 v0, s[48:51], s5 offen offset:2048 sc1 lds
v_exp_f32 v121, v49
v_exp_f32 v122, v50
v_exp_f32 v123, v51
v_cvt_pk_bf16_f32 v198, v120, v121
buffer_load_dwordx4 v0, s[48:51], s5 offen offset:3072 sc1 lds
v_cvt_pk_bf16_f32 v199, v122, v123
v_max3_f32 v8, v36, v37, v38
v_max3_f32 v8, v8, v39, v52
v_max3_f32 v8, v8, v53, v54
v_readlane_b32 s5, v12, 63
s_add_u32 s58, s92, 49152
s_and_b32 s58, s58, 0xc000
s_add_u32 s58, s58, s70
s_add_u32 m0, s58, 0
s_nop 0
buffer_load_dwordx4 v0, s[48:51], s5 offen sc1 lds
v_max_f32 v8, v8, v55
v_mov_b32 v26, v8
s_nop 1
v_permlane16_swap_b32 v8, v26
v_max_f32 v8, v8, v26
buffer_load_dwordx4 v0, s[48:51], s5 offen offset:1024 sc1 lds
v_mov_b32 v26, v8
s_nop 1
v_permlane32_swap_b32 v8, v26
v_max_f32 v8, v8, v26
v_sub_f32 v11, 0, v8
buffer_load_dwordx4 v0, s[48:51], s5 offen offset:2048 sc1 lds
v_sub_f32 v10, 0, v8
v_sub_f32 v9, 0, v8
v_sub_f32 v8, 0, v8
v_add_f32 v36, v36, v8
buffer_load_dwordx4 v0, s[48:51], s5 offen offset:3072 sc1 lds
v_add_f32 v37, v37, v8
v_add_f32 v38, v38, v8
v_add_f32 v39, v39, v8
v_exp_f32 v120, v36
s_mov_b32 s94, 0
v_readlane_b32 s5, v12, s94
v_readlane_b32 s82, v14, s94
s_or_b32 m0, s70, s92
s_nop 2
buffer_load_dwordx4 v0, s[48:51], s5 offen sc1 lds
v_exp_f32 v121, v37
v_exp_f32 v122, v38
v_exp_f32 v123, v39
v_cvt_pk_bf16_f32 v204, v120, v121
buffer_load_dwordx4 v0, s[48:51], s5 offen offset:1024 sc1 lds
v_cvt_pk_bf16_f32 v205, v122, v123
v_add_f32 v52, v52, v8
v_add_f32 v53, v53, v8
v_add_f32 v54, v54, v8
buffer_load_dwordx4 v0, s[48:51], s5 offen offset:2048 sc1 lds
v_add_f32 v55, v55, v8
v_exp_f32 v120, v52
v_exp_f32 v121, v53
v_exp_f32 v122, v54
buffer_load_dwordx4 v0, s[48:51], s5 offen offset:3072 sc1 lds
v_exp_f32 v123, v55
v_cvt_pk_bf16_f32 v206, v120, v121
v_cvt_pk_bf16_f32 v207, v122, v123
v_max3_f32 v132, v40, v41, v42
s_xor_b32 s58, s92, 0x18000
s_or_b32 m0, s70, s58
s_nop 0
buffer_load_dwordx4 v3, s[12:15], s82 offen sc1 lds
v_max3_f32 v132, v132, v43, v56
v_max3_f32 v132, v132, v57, v58
v_max_f32 v132, v132, v59
v_mov_b32 v26, v132
s_add_u32 s72, s82, s24
buffer_load_dwordx4 v3, s[12:15], s72 offen offset:1024 sc1 lds
v_permlane16_swap_b32 v132, v26
v_max_f32 v132, v132, v26
v_mov_b32 v26, v132
s_nop 1
v_permlane32_swap_b32 v132, v26
s_add_u32 s72, s82, s25
buffer_load_dwordx4 v3, s[12:15], s72 offen offset:2048 sc1 lds
v_max_f32 v132, v132, v26
v_sub_f32 v135, 0, v132
v_sub_f32 v134, 0, v132
v_sub_f32 v133, 0, v132
s_add_u32 s72, s82, s26
buffer_load_dwordx4 v3, s[12:15], s72 offen offset:3072 sc1 lds
v_sub_f32 v132, 0, v132
v_add_f32 v40, v40, v132
v_add_f32 v41, v41, v132
v_add_f32 v42, v42, v132
s_add_u32 s95, s95, 0x200000
buffer_load_dwordx4 v[28:31], v0, s[20:23], s95 offen
v_add_f32 v43, v43, v132
v_exp_f32 v120, v40
v_exp_f32 v121, v41
v_exp_f32 v122, v42
buffer_load_dwordx4 v[28:31], v0, s[20:23], s95 offen offset:1024
v_exp_f32 v123, v43
v_cvt_pk_bf16_f32 v212, v120, v121
v_cvt_pk_bf16_f32 v213, v122, v123
v_add_f32 v56, v56, v132
buffer_load_dwordx4 v[28:31], v0, s[20:23], s95 offen offset:2048
v_add_f32 v57, v57, v132
v_add_f32 v58, v58, v132
v_add_f32 v59, v59, v132
v_exp_f32 v120, v56
buffer_load_dwordx4 v[28:31], v0, s[20:23], s95 offen offset:3072
v_exp_f32 v121, v57
v_exp_f32 v122, v58
v_exp_f32 v123, v59
v_cvt_pk_bf16_f32 v214, v120, v121
s_mov_b32 vcc_lo, 0xaaaaaaaa
s_mov_b32 vcc_hi, 0xaaaaaaaa
v_mov_b32 v24, s89
v_mov_b32 v25, s90
v_cndmask_b32 v24, v24, v25, vcc
v_not_b32 v24, v24
v_and_b32 v24, 0x7ff, v24
v_mul_u32_u24 v24, 2176, v24
v_lshrrev_b32 v25, 8, v0
v_lshl_add_u32 v24, v25, 4, v24
s_lshr_b32 s59, s60, 3
s_lshl2_add_u32 s59, s59, s57
s_lshl_b32 s59, s59, 7
s_add_u32 s59, s59, 4194304
buffer_load_dwordx4 v[16:19], v24, s[48:51], s59 offen sc1
v_cvt_pk_bf16_f32 v215, v122, v123
v_max3_f32 v140, v44, v45, v46
v_max3_f32 v140, v140, v47, v60
v_max3_f32 v140, v140, v61, v62
buffer_load_dwordx4 v[20:23], v24, s[48:51], s59 offen offset:64 sc1
v_max_f32 v140, v140, v63
v_mov_b32 v26, v140
s_nop 1
v_permlane16_swap_b32 v140, v26
v_max_f32 v140, v140, v26
s_mov_b32 s93, 1
s_lshr_b32 s59, s60, 3
s_lshl2_add_u32 s59, s59, s57
s_mul_i32 s59, s59, 458752
s_lshl_b32 s68, s91, 7
s_add_u32 s68, s68, s59
s_add_u32 s71, s69, 131072
s_add_u32 s73, s93, 4
s_mov_b32 s62, 57344
s_mov_b32 s63, 114688
s_mov_b32 s64, 2048
s_cmp_eq_u32 s61, 0
s_cselect_b32 s73, 0, s73
v_bfe_u32 v25, v0, 6, 3
v_mul_u32_u24 v25, 7168, v25
v_lshrrev_b32 v24, 9, v0
v_lshl_add_u32 v25, v24, 6, v25
v_and_b32 v24, 48, v0
v_add_u32 v25, v25, v24
v_mov_b32 v26, v140
s_nop 1
v_permlane32_swap_b32 v140, v26
v_max_f32 v140, v140, v26
v_sub_f32 v143, 0, v140
v_sub_f32 v142, 0, v140
v_sub_f32 v141, 0, v140
v_sub_f32 v140, 0, v140
v_add_f32 v44, v44, v140
v_add_f32 v45, v45, v140
v_add_f32 v46, v46, v140
v_add_f32 v47, v47, v140
v_exp_f32 v120, v44
v_exp_f32 v121, v45
v_exp_f32 v122, v46
v_exp_f32 v123, v47
v_cvt_pk_bf16_f32 v220, v120, v121
v_cvt_pk_bf16_f32 v221, v122, v123
v_add_f32 v60, v60, v140
v_add_f32 v61, v61, v140
v_add_f32 v62, v62, v140
v_add_f32 v63, v63, v140
v_exp_f32 v120, v60
v_exp_f32 v121, v61
v_exp_f32 v122, v62
v_exp_f32 v123, v63
v_cvt_pk_bf16_f32 v222, v120, v121
v_cvt_pk_bf16_f32 v223, v122, v123
v_bfe_u32 v120, v0, 4, 4
v_lshrrev_b32 v121, 8, v0
v_lshlrev_b32 v121, 2, v121
v_sub_u32 v121, v120, v121
v_cndmask_b32 v120, 64, v121, s[18:19]
v_cndmask_b32 v121, v121, -16, s[18:19]
v_mov_b32 v122, 0xff800000
v_cmp_gt_i32 vcc, 0, v120
v_cndmask_b32 v156, v132, v122, s[18:19]
s_nop 0
v_cndmask_b32 v148, v132, v122, vcc
v_cndmask_b32 v164, v140, v122, vcc
v_cmp_gt_i32 vcc, 1, v120
v_cndmask_b32 v157, v132, v122, s[18:19]
s_nop 0
v_cndmask_b32 v149, v132, v122, vcc
v_cndmask_b32 v165, v140, v122, vcc
v_cmp_gt_i32 vcc, 2, v120
v_cndmask_b32 v158, v132, v122, s[18:19]
s_nop 0
v_cndmask_b32 v150, v132, v122, vcc
v_cndmask_b32 v166, v140, v122, vcc
v_cmp_gt_i32 vcc, 3, v120
v_cndmask_b32 v159, v132, v122, s[18:19]
s_nop 0
v_cndmask_b32 v151, v132, v122, vcc
v_cndmask_b32 v167, v140, v122, vcc
v_cmp_gt_i32 vcc, 0, v121
v_cndmask_b32 v180, v140, v122, s[18:19]
s_nop 0
v_cndmask_b32 v172, v132, v122, vcc
v_cndmask_b32 v188, v140, v122, vcc
v_cmp_gt_i32 vcc, 1, v121
v_cndmask_b32 v181, v140, v122, s[18:19]
s_nop 0
v_cndmask_b32 v173, v132, v122, vcc
v_cndmask_b32 v189, v140, v122, vcc
v_cmp_gt_i32 vcc, 2, v121
v_cndmask_b32 v182, v140, v122, s[18:19]
s_nop 0
v_cndmask_b32 v174, v132, v122, vcc
v_cndmask_b32 v190, v140, v122, vcc
v_cmp_gt_i32 vcc, 3, v121
v_cndmask_b32 v183, v140, v122, s[18:19]
s_nop 0
v_cndmask_b32 v175, v132, v122, vcc
v_cndmask_b32 v191, v140, v122, vcc
v_mov_b32 v112, 0
v_mov_b32 v113, 0
v_mov_b32 v114, 0
v_mov_b32 v115, 0
s_mov_b32 s93, 1
s_or_b32 s81, s92, 0x10000
v_add_u32 v2, s81, v0
s_cmp_eq_u32 s99, 0
s_cselect_b32 s85, 0x30000, s70
s_cmp_eq_u32 s4, 2
s_cselect_b32 s59, s99, 0
s_add_u32 s80, s92, 0x4000
s_add_u32 s80, s80, s59
s_and_b32 s80, s80, 0xc000
s_waitcnt lgkmcnt(0)
s_waitcnt vmcnt(22)
s_cmp_eq_u32 s4, 0
s_cbranch_scc1 .Ldiag1
s_branch .Lloop1
.p2align 6
.Lloop0:
Cvav 32,128,128,4
s_waitcnt vmcnt(30)
s_barrier
Mvav 32,132,136
v_readlane_b32 s5, v12, s94
v_readlane_b32 s82, v14, s94
Mvav 32,136,144
Mvav 32,140,152
Cvvv 112,96,228,116
Mvvv 112,100,236
s_or_b32 m0, s70, s92
Mvvv 112,104,244
buffer_load_dwordx4 v0, s[48:51], s5 offen sc1 lds
Mvvv 112,108,252
buffer_load_dwordx4 v0, s[48:51], s5 offen offset:1024 sc1 lds
Cvav 36,128,160,8
Mvav 36,132,168
buffer_load_dwordx4 v0, s[48:51], s5 offen offset:2048 sc1 lds
Mvav 36,136,176
v_exp_f32 v120, v32
Mvav 36,140,184
Cvav 40,128,192,132
v_exp_f32 v121, v33
Mvav 40,132,200
v_add_f32 v26, v112, v113
v_add_f32 v27, v114, v115
Mvav 40,136,208
v_exp_f32 v122, v34
Mvav 40,140,216
v_add_f32 v26, v26, v27
v_cmp_nge_f32 vcc, 0x53800000, v26
Cvav 44,128,224,140
v_exp_f32 v123, v35
Mvav 44,132,232
buffer_load_dwordx4 v0, s[48:51], s5 offen offset:3072 sc1 lds
Mvav 44,136,240
v_exp_f32 v124, v36
Mvav 44,140,248
s_xor_b32 s58, s92, 0x18000
v_cvt_pk_bf16_f32 v196, v120, v121
v_cvt_pk_bf16_f32 v197, v122, v123
Cvav 48,144,128,4
s_or_b32 m0, s70, s58
v_exp_f32 v125, v37
Mvav 48,148,136
s_or_b32 s81, s92, 0x10000
buffer_load_dwordx4 v3, s[12:15], s82 offen sc1 lds
)"
R"(
Mvav 48,152,144
s_add_u32 s72, s82, s24
v_exp_f32 v126, v38
Mvav 48,156,152
buffer_load_dwordx4 v3, s[12:15], s72 offen offset:1024 sc1 lds
Cvav 52,144,160,8
v_exp_f32 v127, v39
Mvav 52,148,168
v_cvt_pk_bf16_f32 v204, v124, v125
v_cvt_pk_bf16_f32 v205, v126, v127
Mvav 52,152,176
s_add_u32 s72, s82, s25
v_exp_f32 v120, v40
Mvav 52,156,184
buffer_load_dwordx4 v3, s[12:15], s72 offen offset:2048 sc1 lds
Cvav 56,144,192,132
v_exp_f32 v121, v41
Mvav 56,148,200
v_cvt_pk_bf16_f32 v212, v120, v121
v_add_u32 v2, s81, v0
Mvav 56,152,208
s_add_u32 s72, s82, s26
v_exp_f32 v122, v42
Mvav 56,156,216
buffer_load_dwordx4 v3, s[12:15], s72 offen offset:3072 sc1 lds
Cvav 60,144,224,140
v_exp_f32 v123, v43
Mvav 60,148,232
s_cmp_lt_u32 s93, s73
s_cselect_b32 m0, s71, 0x30000
v_cvt_pk_bf16_f32 v213, v122, v123
Mvav 60,152,240
v_exp_f32 v124, v44
Mvav 60,156,248
s_cbranch_vccnz .Lab0
.Lab0_r:
Maav 0,196,228
v_exp_f32 v125, v45
Maav 4,196,236
buffer_load_dwordx4 v25, s[36:39], s68 offen lds
Maav 64,196,244
v_exp_f32 v126, v46
Maav 68,196,252
s_add_u32 m0, m0, 1024
v_cvt_pk_bf16_f32 v220, v124, v125
Maav 8,204,228
s_add_u32 s72, s68, s62
v_exp_f32 v127, v47
Maav 12,204,236
buffer_load_dwordx4 v25, s[36:39], s72 offen lds
Maav 72,204,244
s_add_u32 s95, s95, 0x200000
v_exp_f32 v120, v48
Maav 76,204,252
buffer_load_dwordx4 v[28:31], v0, s[20:23], s95 offen
Maav 16,212,228
v_exp_f32 v121, v49
Maav 20,212,236
v_cvt_pk_bf16_f32 v221, v126, v127
v_cvt_pk_bf16_f32 v198, v120, v121
Maav 80,212,244
v_exp_f32 v122, v50
Maav 84,212,252
buffer_load_dwordx4 v[28:31], v0, s[20:23], s95 offen offset:1024
Maav 24,220,228
v_exp_f32 v123, v51
Maav 28,220,236
v_cvt_pk_bf16_f32 v199, v122, v123
Maav 88,220,244
s_add_u32 s59, s93, 2
v_exp_f32 v124, v52
Maav 92,220,252
buffer_load_dwordx4 v[28:31], v0, s[20:23], s95 offen offset:2048
Maav 32,228,228
v_exp_f32 v125, v53
Maav 36,228,236
s_cmp_eq_u32 s59, s4
s_cselect_b32 s59, s99, 0
v_cvt_pk_bf16_f32 v206, v124, v125
Maav 96,228,244
s_add_u32 s80, s92, 0x4000
v_exp_f32 v126, v54
Maav 100,228,252
s_add_u32 s80, s80, s59
buffer_load_dwordx4 v[28:31], v0, s[20:23], s95 offen offset:3072
Maav 40,236,228
s_and_b32 s80, s80, 0xc000
v_exp_f32 v127, v55
Maav 44,236,236
s_add_u32 s93, s93, 1
s_add_u32 s68, s68, s63
v_cvt_pk_bf16_f32 v207, v126, v127
Maav 104,236,244
s_add_u32 s71, s71, s64
v_exp_f32 v120, v56
Maav 108,236,252
v_exp_f32 v121, v57
Maav 48,244,228
v_exp_f32 v122, v58
Maav 52,244,236
v_exp_f32 v123, v59
Maav 112,244,244
v_exp_f32 v124, v60
Maav 116,244,252
v_exp_f32 v125, v61
Maav 56,252,228
v_exp_f32 v126, v62
Maav 60,252,236
v_exp_f32 v127, v63
Maav 120,252,244
s_waitcnt lgkmcnt(0)
v_cvt_pk_bf16_f32 v214, v120, v121
v_cvt_pk_bf16_f32 v215, v122, v123
Maav 124,252,252
v_cvt_pk_bf16_f32 v222, v124, v125
v_cvt_pk_bf16_f32 v223, v126, v127
.Lloop1:
Cvav 64,160,128,4
s_waitcnt vmcnt(30)
s_barrier
Mvav 64,164,136
ds_read_b128 a[192:195], v2 offset:0
v_add_u32 v1, s80, v0
Mvav 64,168,144
ds_read_b128 a[200:203], v2 offset:2048
Mvav 64,172,152
ds_read_b128 a[208:211], v2 offset:4096
Cvvv 116,96,196,112
ds_read_b128 a[216:219], v2 offset:6144
Mvvv 116,100,204
ds_read_b128 a[224:227], v2 offset:8192
Mvvv 116,104,212
ds_read_b128 a[232:235], v2 offset:10240
Mvvv 116,108,220
ds_read_b128 a[240:243], v2 offset:12288
Cvav 68,160,160,8
v_exp_f32 v120, v64
Mvav 68,164,168
ds_read_b128 a[248:251], v2 offset:14336
Mvav 68,168,176
v_exp_f32 v121, v65
Mvav 68,172,184
ds_read_b128 a[196:199], v2 offset:1024
v_cvt_pk_bf16_f32 v228, v120, v121
Cvav 72,160,192,132
v_exp_f32 v122, v66
Mvav 72,164,200
ds_read_b128 a[204:207], v2 offset:3072
v_add_f32 v26, v116, v117
Mvav 72,168,208
v_exp_f32 v123, v67
Mvav 72,172,216
ds_read_b128 a[212:215], v2 offset:5120
v_add_f32 v27, v118, v119
Cvav 76,160,224,140
v_exp_f32 v124, v68
Mvav 76,164,232
ds_read_b128 a[220:223], v2 offset:7168
v_add_f32 v26, v26, v27
Mvav 76,168,240
v_exp_f32 v125, v69
Mvav 76,172,248
ds_read_b128 a[228:231], v2 offset:9216
v_cmp_nge_f32 vcc, 0x53800000, v26
Cvav 80,176,128,4
s_or_b32 s81, s92, 0x10000
v_exp_f32 v126, v70
Mvav 80,180,136
ds_read_b128 a[236:239], v2 offset:11264
v_cvt_pk_bf16_f32 v229, v122, v123
Mvav 80,184,144
v_exp_f32 v127, v71
Mvav 80,188,152
s_cmp_lt_u32 s93, s73
s_cselect_b32 m0, s71, 0x30000
ds_read_b128 a[244:247], v2 offset:13312
Cvav 84,176,160,8
s_waitcnt lgkmcnt(12)
v_exp_f32 v120, v72
Mvav 84,180,168
ds_read_b128 a[252:255], v2 offset:15360
buffer_load_dwordx4 v25, s[36:39], s68 offen lds
Mvav 84,184,176
v_exp_f32 v121, v73
Mvav 84,188,184
ds_read_b128 a[128:131], v1 offset:0
v_cvt_pk_bf16_f32 v236, v124, v125
Cvav 88,176,192,132
s_waitcnt lgkmcnt(11)
v_exp_f32 v122, v74
Mvav 88,180,200
s_add_u32 m0, m0, 1024
ds_read_b128 a[132:135], v1 offset:1024
v_cvt_pk_bf16_f32 v237, v126, v127
Mvav 88,184,208
s_add_u32 s72, s68, s62
v_exp_f32 v123, v75
Mvav 88,188,216
ds_read_b128 a[136:139], v1 offset:2048
buffer_load_dwordx4 v25, s[36:39], s72 offen lds
Cvav 92,176,224,140
v_exp_f32 v124, v76
Mvav 92,180,232
ds_read_b128 a[140:143], v1 offset:3072
v_cvt_pk_bf16_f32 v244, v120, v121
Mvav 92,184,240
v_exp_f32 v125, v77
Mvav 92,188,248
s_cbranch_vccnz .Lab1
.Lab1_r:
Maav 0,192,196
v_exp_f32 v126, v78
Maav 4,192,204
ds_read_b128 a[144:147], v1 offset:4096
v_cvt_pk_bf16_f32 v245, v122, v123
Maav 64,192,212
v_exp_f32 v127, v79
Maav 68,192,220
s_waitcnt lgkmcnt(12)
ds_read_b128 a[148:151], v1 offset:5120
v_cvt_pk_bf16_f32 v252, v124, v125
Maav 8,200,196
v_exp_f32 v120, v80
Maav 12,200,204
ds_read_b128 a[152:155], v1 offset:6144
v_cvt_pk_bf16_f32 v253, v126, v127
Maav 72,200,212
v_exp_f32 v121, v81
Maav 76,200,220
ds_read_b128 a[156:159], v1 offset:7168
v_cvt_pk_bf16_f32 v230, v120, v121
Maav 16,208,196
v_exp_f32 v122, v82
Maav 20,208,204
s_waitcnt lgkmcnt(12)
ds_read_b128 a[160:163], v1 offset:8192
v_add_u32 v2, s81, v0
Maav 80,208,212
v_exp_f32 v123, v83
Maav 84,208,220
ds_read_b128 a[164:167], v1 offset:9216
v_cvt_pk_bf16_f32 v231, v122, v123
Maav 24,216,196
v_exp_f32 v124, v84
Maav 28,216,204
s_waitcnt lgkmcnt(11)
ds_read_b128 a[168:171], v1 offset:10240
Maav 88,216,212
s_add_u32 s94, s94, 1
v_exp_f32 v125, v85
Maav 92,216,220
s_add_u32 s92, s92, 0x4000
ds_read_b128 a[172:175], v1 offset:11264
v_cvt_pk_bf16_f32 v238, v124, v125
Maav 32,224,196
s_and_b32 s92, s92, 0xc000
v_exp_f32 v126, v86
Maav 36,224,204
s_add_u32 s80, s92, 0x4000
s_and_b32 s80, s80, 0xc000
ds_read_b128 a[176:179], v1 offset:12288
Maav 96,224,212
s_add_u32 s93, s93, 1
v_exp_f32 v127, v87
Maav 100,224,220
s_add_u32 s68, s68, s63
ds_read_b128 a[180:183], v1 offset:13312
v_cvt_pk_bf16_f32 v239, v126, v127
Maav 40,232,196
s_add_u32 s71, s71, s64
v_exp_f32 v120, v88
Maav 44,232,204
s_waitcnt lgkmcnt(12)
ds_read_b128 a[184:187], v1 offset:14336
Maav 104,232,212
ds_read_b128 a[188:191], v1 offset:15360
Maav 108,232,220
v_exp_f32 v121, v89
Maav 48,240,196
v_exp_f32 v122, v90
Maav 52,240,204
v_exp_f32 v123, v91
Maav 112,240,212
v_exp_f32 v124, v92
Maav 116,240,220
v_exp_f32 v125, v93
Maav 56,248,196
v_exp_f32 v126, v94
Maav 60,248,204
v_exp_f32 v127, v95
Maav 120,248,212
s_waitcnt lgkmcnt(0)
v_cvt_pk_bf16_f32 v246, v120, v121
v_cvt_pk_bf16_f32 v247, v122, v123
Maav 124,248,220
v_cvt_pk_bf16_f32 v254, v124, v125
v_cvt_pk_bf16_f32 v255, v126, v127
s_bitcmp1_b64 s[34:35], s93
s_cbranch_scc1 .Lhab1
.Lhab1_r:
s_cmp_lt_u32 s93, s4
s_cbranch_scc1 .Lloop0
Cvav 32,128,128,4
s_waitcnt vmcnt(20)
s_barrier
Mvav 32,132,136
v_readlane_b32 s5, v12, s94
v_readlane_b32 s82, v14, s94
Mvav 32,136,144
Mvav 32,140,152
Cvvv 112,96,228,116
Mvvv 112,100,236
s_or_b32 m0, s70, s92
Mvvv 112,104,244
buffer_load_dwordx4 v0, s[48:51], s5 offen sc1 lds
Mvvv 112,108,252
Cvav 36,128,160,8
buffer_load_dwordx4 v0, s[48:51], s5 offen offset:1024 sc1 lds
Mvav 36,132,168
Mvav 36,136,176
v_exp_f32 v120, v32
Mvav 36,140,184
buffer_load_dwordx4 v0, s[48:51], s5 offen offset:2048 sc1 lds
Cvav 40,128,192,148
v_exp_f32 v121, v33
Mvav 40,132,200
v_add_f32 v26, v112, v113
v_add_f32 v27, v114, v115
Mvav 40,136,208
v_exp_f32 v122, v34
Mvav 40,140,216
v_add_f32 v26, v26, v27
v_cmp_nge_f32 vcc, 0x53800000, v26
Cvav 44,128,224,140
v_exp_f32 v123, v35
Mvav 44,132,232
v_cvt_pk_bf16_f32 v196, v120, v121
v_cvt_pk_bf16_f32 v197, v122, v123
Mvav 44,136,240
v_exp_f32 v124, v36
Mvav 44,140,248
buffer_load_dwordx4 v0, s[48:51], s5 offen offset:3072 sc1 lds
Cvav 48,144,128,4
s_add_u32 s81, s92, s99
v_exp_f32 v125, v37
Mvav 48,148,136
s_and_b32 s81, s81, 0xc000
s_or_b32 s81, s81, 0x10000
v_cvt_pk_bf16_f32 v204, v124, v125
Mvav 48,152,144
v_exp_f32 v126, v38
Mvav 48,156,152
s_xor_b32 s58, s92, 0x18000
Cvav 52,144,160,8
s_or_b32 m0, s70, s58
v_exp_f32 v127, v39
Mvav 52,148,168
buffer_load_dwordx4 v3, s[12:15], s82 offen sc1 lds
Mvav 52,152,176
v_exp_f32 v120, v40
Mvav 52,156,184
v_cvt_pk_bf16_f32 v205, v126, v127
Cvav 56,144,192,156
s_add_u32 s72, s82, s24
v_exp_f32 v121, v41
Mvav 56,148,200
buffer_load_dwordx4 v3, s[12:15], s72 offen offset:1024 sc1 lds
Mvav 56,152,208
v_exp_f32 v122, v42
Mvav 56,156,216
v_cvt_pk_bf16_f32 v212, v120, v121
v_add_u32 v2, s81, v0
Cvav 60,144,224,164
s_add_u32 s72, s82, s25
v_exp_f32 v123, v43
Mvav 60,148,232
buffer_load_dwordx4 v3, s[12:15], s72 offen offset:2048 sc1 lds
Mvav 60,152,240
v_exp_f32 v124, v44
Mvav 60,156,248
s_cbranch_vccnz .Ld0
.Ld0_r:
Maav 0,196,228
v_exp_f32 v125, v45
Maav 4,196,236
v_cvt_pk_bf16_f32 v213, v122, v123
v_cvt_pk_bf16_f32 v220, v124, v125
Maav 64,196,244
v_exp_f32 v126, v46
Maav 68,196,252
Maav 8,204,228
s_add_u32 s72, s82, s26
v_exp_f32 v127, v47
Maav 12,204,236
buffer_load_dwordx4 v3, s[12:15], s72 offen offset:3072 sc1 lds
Maav 72,204,244
v_exp_f32 v120, v48
Maav 76,204,252
s_cmp_lt_u32 s93, s73
s_cselect_b32 m0, s71, 0x30000
v_cvt_pk_bf16_f32 v221, v126, v127
Maav 16,212,228
v_exp_f32 v121, v49
Maav 20,212,236
buffer_load_dwordx4 v25, s[36:39], s68 offen lds
Maav 80,212,244
v_exp_f32 v122, v50
Maav 84,212,252
s_add_u32 m0, m0, 1024
v_cvt_pk_bf16_f32 v198, v120, v121
Maav 24,220,228
s_add_u32 s72, s68, s62
v_exp_f32 v123, v51
Maav 28,220,236
buffer_load_dwordx4 v25, s[36:39], s72 offen lds
Maav 88,220,244
s_add_u32 s80, s92, 0x4000
v_exp_f32 v124, v52
Maav 92,220,252
s_add_u32 s80, s80, s99
s_and_b32 s80, s80, 0xc000
v_cvt_pk_bf16_f32 v199, v122, v123
Maav 32,228,228
s_add_u32 s93, s93, 1
v_exp_f32 v125, v53
Maav 36,228,236
s_add_u32 s68, s68, s63
s_add_u32 s71, s71, s64
v_cvt_pk_bf16_f32 v206, v124, v125
Maav 96,228,244
v_exp_f32 v126, v54
Maav 100,228,252
Maav 40,236,228
v_exp_f32 v127, v55
Maav 44,236,236
v_cvt_pk_bf16_f32 v207, v126, v127
Maav 104,236,244
v_exp_f32 v120, v56
Maav 108,236,252
v_exp_f32 v121, v57
Maav 48,244,228
v_exp_f32 v122, v58
Maav 52,244,236
v_exp_f32 v123, v59
Maav 112,244,244
v_exp_f32 v124, v60
Maav 116,244,252
v_exp_f32 v125, v61
Maav 56,252,228
v_exp_f32 v126, v62
Maav 60,252,236
v_exp_f32 v127, v63
Maav 120,252,244
s_waitcnt lgkmcnt(0)
v_cvt_pk_bf16_f32 v214, v120, v121
v_cvt_pk_bf16_f32 v215, v122, v123
Maav 124,252,252
v_cvt_pk_bf16_f32 v222, v124, v125
v_cvt_pk_bf16_f32 v223, v126, v127
s_bitcmp1_b64 s[34:35], s93
s_cbranch_scc1 .Lhd0
.Lhd0_r:
.Ldiag1:
Cvav 64,160,128,4
s_waitcnt vmcnt(16)
s_barrier
Mvav 64,164,136
s_lshr_b32 s58, s99, 14
ds_read_b128 a[192:195], v2 offset:0
v_add_u32 v1, s80, v0
Mvav 64,168,144
s_add_u32 s94, s94, s58
s_add_u32 s92, s92, s99
ds_read_b128 a[200:203], v2 offset:2048
v_mov_b32 v88, 0xff800000
v_mov_b32 v89, 0xff800000
Mvav 64,172,152
s_and_b32 s92, s92, 0xc000
ds_read_b128 a[208:211], v2 offset:4096
v_mov_b32 v90, 0xff800000
v_mov_b32 v91, 0xff800000
Cvvv 116,96,196,112
ds_read_b128 a[216:219], v2 offset:6144
v_readlane_b32 s5, v12, s94
v_mov_b32 v246, 0
v_mov_b32 v247, 0
Mvvv 116,100,204
ds_read_b128 a[224:227], v2 offset:8192
v_readlane_b32 s82, v14, s94
Mvvv 116,104,212
ds_read_b128 a[232:235], v2 offset:10240
Mvvv 116,108,220
ds_read_b128 a[240:243], v2 offset:12288
Cvav 68,160,160,8
v_exp_f32 v120, v64
Mvav 68,164,168
ds_read_b128 a[248:251], v2 offset:14336
Mvav 68,168,176
s_or_b32 m0, s85, s92
v_exp_f32 v121, v65
Mvav 68,172,184
ds_read_b128 a[196:199], v2 offset:1024
buffer_load_dwordx4 v0, s[48:51], s5 offen sc1 lds
)"
R"(
Cvav 72,160,192,172
v_exp_f32 v122, v66
Mvav 72,164,200
ds_read_b128 a[204:207], v2 offset:3072
v_add_f32 v26, v116, v117
Mvav 72,168,208
v_exp_f32 v123, v67
Mvav 72,172,216
ds_read_b128 a[212:215], v2 offset:5120
v_add_f32 v27, v118, v119
v_cvt_pk_bf16_f32 v229, v122, v123
Cvav 76,160,224,180
v_exp_f32 v124, v68
Mvav 76,164,232
ds_read_b128 a[220:223], v2 offset:7168
v_add_f32 v26, v26, v27
Mvav 76,168,240
v_exp_f32 v125, v69
Mvav 76,172,248
ds_read_b128 a[228:231], v2 offset:9216
v_cmp_nge_f32 vcc, 0x53800000, v26
Cvav 80,176,128,4
s_or_b32 s81, s92, 0x10000
v_exp_f32 v126, v70
Mvav 80,180,136
ds_read_b128 a[236:239], v2 offset:11264
buffer_load_dwordx4 v0, s[48:51], s5 offen offset:1024 sc1 lds
Mvav 80,184,144
v_exp_f32 v127, v71
Mvav 80,188,152
ds_read_b128 a[244:247], v2 offset:13312
v_cvt_pk_bf16_f32 v228, v120, v121
v_cvt_pk_bf16_f32 v237, v126, v127
Cvav 84,176,160,8
s_waitcnt lgkmcnt(12)
v_exp_f32 v120, v72
Mvav 84,180,168
ds_read_b128 a[252:255], v2 offset:15360
buffer_load_dwordx4 v0, s[48:51], s5 offen offset:2048 sc1 lds
Mvav 84,184,176
v_exp_f32 v121, v73
Mvav 84,188,184
ds_read_b128 a[128:131], v1 offset:0
buffer_load_dwordx4 v0, s[48:51], s5 offen offset:3072 sc1 lds
Cvav 92,176,224,188
v_exp_f32 v122, v74
Mvav 92,180,232
ds_read_b128 a[132:135], v1 offset:1024
v_cvt_pk_bf16_f32 v236, v124, v125
Mvav 92,184,240
s_waitcnt lgkmcnt(3)
v_exp_f32 v123, v75
Mvav 92,188,248
s_xor_b32 s58, s92, 0x18000
s_cbranch_vccnz .Ld1
.Ld1_r:
Maav 0,192,196
s_or_b32 m0, s85, s58
v_exp_f32 v124, v76
v_bfe_u32 v32, v0, 4, 4
Maav 4,192,204
ds_read_b128 a[136:139], v1 offset:2048
buffer_load_dwordx4 v3, s[12:15], s82 offen sc1 lds
Maav 64,192,212
s_add_u32 s72, s82, s24
v_exp_f32 v125, v77
v_lshrrev_b32 v33, 8, v0
v_lshlrev_b32 v33, 2, v33
Maav 68,192,220
ds_read_b128 a[140:143], v1 offset:3072
buffer_load_dwordx4 v3, s[12:15], s72 offen offset:1024 sc1 lds
Maav 8,200,196
s_waitcnt lgkmcnt(2)
v_exp_f32 v126, v78
v_cvt_pk_bf16_f32 v252, v124, v125
v_sub_u32 v33, v32, v33
v_cndmask_b32 v32, 64, v33, s[6:7]
Maav 12,200,204
ds_read_b128 a[144:147], v1 offset:4096
v_cvt_pk_bf16_f32 v244, v120, v121
v_cndmask_b32 v33, v33, -16, s[6:7]
v_mov_b32 v34, 0xff800000
Maav 72,200,212
s_add_u32 s72, s82, s25
v_exp_f32 v127, v79
v_cmp_gt_i32 vcc, 0, v32
v_cndmask_b32 v156, v4, v34, s[6:7]
Maav 76,200,220
ds_read_b128 a[148:151], v1 offset:5120
buffer_load_dwordx4 v3, s[12:15], s72 offen offset:2048 sc1 lds
Maav 16,208,196
s_add_u32 s72, s82, s26
v_exp_f32 v120, v80
v_cndmask_b32 v148, v4, v34, vcc
v_cndmask_b32 v164, v8, v34, vcc
Maav 20,208,204
ds_read_b128 a[152:155], v1 offset:6144
buffer_load_dwordx4 v3, s[12:15], s72 offen offset:3072 sc1 lds
Maav 80,208,212
v_exp_f32 v121, v81
v_cmp_gt_i32 vcc, 1, v32
v_cndmask_b32 v157, v4, v34, s[6:7]
Maav 84,208,220
s_cmp_lt_u32 s93, s73
s_cselect_b32 m0, s71, 0x30000
ds_read_b128 a[156:159], v1 offset:7168
v_cvt_pk_bf16_f32 v245, v122, v123
v_cndmask_b32 v149, v4, v34, vcc
v_cndmask_b32 v165, v8, v34, vcc
Maav 24,216,196
v_exp_f32 v122, v82
v_cmp_gt_i32 vcc, 2, v32
v_cndmask_b32 v158, v4, v34, s[6:7]
Maav 28,216,204
ds_read_b128 a[160:163], v1 offset:8192
buffer_load_dwordx4 v25, s[36:39], s68 offen lds
Maav 88,216,212
s_add_u32 m0, m0, 1024
s_add_u32 s72, s68, s62
v_exp_f32 v123, v83
v_cndmask_b32 v150, v4, v34, vcc
v_cndmask_b32 v166, v8, v34, vcc
Maav 92,216,220
ds_read_b128 a[164:167], v1 offset:9216
buffer_load_dwordx4 v25, s[36:39], s72 offen lds
Maav 32,224,196
v_exp_f32 v124, v84
v_cmp_gt_i32 vcc, 3, v32
v_cndmask_b32 v159, v4, v34, s[6:7]
Maav 36,224,204
ds_read_b128 a[168:171], v1 offset:10240
v_cvt_pk_bf16_f32 v253, v126, v127
v_cndmask_b32 v151, v4, v34, vcc
v_cndmask_b32 v167, v8, v34, vcc
Maav 96,224,212
v_exp_f32 v125, v85
v_cmp_gt_i32 vcc, 0, v33
v_cndmask_b32 v180, v8, v34, s[6:7]
Maav 100,224,220
ds_read_b128 a[172:175], v1 offset:11264
v_cvt_pk_bf16_f32 v230, v120, v121
v_cndmask_b32 v172, v4, v34, vcc
v_cndmask_b32 v188, v8, v34, vcc
Maav 40,232,196
v_exp_f32 v126, v86
v_cmp_gt_i32 vcc, 1, v33
v_cndmask_b32 v181, v8, v34, s[6:7]
Maav 44,232,204
ds_read_b128 a[176:179], v1 offset:12288
v_cvt_pk_bf16_f32 v231, v122, v123
v_cndmask_b32 v173, v4, v34, vcc
v_cndmask_b32 v189, v8, v34, vcc
Maav 104,232,212
v_exp_f32 v127, v87
v_cmp_gt_i32 vcc, 2, v33
v_cndmask_b32 v182, v8, v34, s[6:7]
Maav 108,232,220
ds_read_b128 a[180:183], v1 offset:13312
v_cvt_pk_bf16_f32 v238, v124, v125
v_cndmask_b32 v174, v4, v34, vcc
v_cndmask_b32 v190, v8, v34, vcc
Maav 48,240,196
v_exp_f32 v120, v92
v_cmp_gt_i32 vcc, 3, v33
v_cndmask_b32 v183, v8, v34, s[6:7]
Maav 52,240,204
ds_read_b128 a[184:187], v1 offset:14336
v_cvt_pk_bf16_f32 v239, v126, v127
v_cndmask_b32 v175, v4, v34, vcc
v_cndmask_b32 v191, v8, v34, vcc
Maav 112,240,212
s_add_u32 s94, s94, 1
ds_read_b128 a[188:191], v1 offset:15360
v_add_u32 v2, s81, v0
Maav 116,240,220
s_add_u32 s92, s92, 0x4000
v_exp_f32 v121, v93
Maav 56,248,196
s_and_b32 s92, s92, 0xc000
v_exp_f32 v122, v94
Maav 60,248,204
s_add_u32 s80, s92, 0x4000
v_exp_f32 v123, v95
Maav 120,248,212
s_and_b32 s80, s80, 0xc000
s_waitcnt lgkmcnt(0)
v_cvt_pk_bf16_f32 v254, v120, v121
v_cvt_pk_bf16_f32 v255, v122, v123
Maav 124,248,220
s_add_u32 s93, s93, 1
s_add_u32 s68, s68, s63
s_add_u32 s71, s71, s64
s_bitcmp1_b64 s[34:35], s93
s_cbranch_scc1 .Lhd1
.Lhd1_r:
Cvav 32,128,128,148
s_waitcnt vmcnt(20)
s_barrier
Mvav 32,132,136
s_mov_b32 s85, 0x80000000
v_add_u32 v1, s80, v0
v_readlane_b32 s5, v12, s94
Mvav 32,136,144
v_readlane_b32 s82, v14, s94
v_mov_b32 v56, 0xff800000
v_mov_b32 v57, 0xff800000
Mvav 32,140,152
s_mov_b32 s99, 0x80000000
v_mov_b32 v58, 0xff800000
v_mov_b32 v59, 0xff800000
Cvvv 112,96,228,116
v_mov_b32 v214, 0
v_mov_b32 v215, 0
Mvvv 112,100,236
Mvvv 112,104,244
s_or_b32 m0, s70, s92
Mvvv 112,108,252
buffer_load_dwordx4 v0, s[48:51], s5 offen sc1 lds
Cvav 36,128,160,8
buffer_load_dwordx4 v0, s[48:51], s5 offen offset:1024 sc1 lds
Mvav 36,132,168
buffer_load_dwordx4 v0, s[48:51], s5 offen offset:2048 sc1 lds
Mvav 36,136,176
v_exp_f32 v120, v32
Mvav 36,140,184
Cvav 40,144,128,156
v_exp_f32 v121, v33
Mvav 40,148,136
v_add_f32 v26, v112, v113
v_add_f32 v27, v114, v115
Mvav 40,152,144
v_exp_f32 v122, v34
Mvav 40,156,152
v_add_f32 v26, v26, v27
v_cmp_nge_f32 vcc, 0x53800000, v26
Cvav 44,144,160,164
v_exp_f32 v123, v35
Mvav 44,148,168
buffer_load_dwordx4 v0, s[48:51], s5 offen offset:3072 sc1 lds
Mvav 44,152,176
s_xor_b32 s58, s92, 0x18000
s_or_b32 m0, s70, s58
v_exp_f32 v124, v36
Mvav 44,156,184
buffer_load_dwordx4 v3, s[12:15], s82 offen sc1 lds
Cvav 48,160,128,172
s_or_b32 s81, s92, 0x10000
v_exp_f32 v125, v37
Mvav 48,164,136
v_cvt_pk_bf16_f32 v196, v120, v121
v_cvt_pk_bf16_f32 v197, v122, v123
Mvav 48,168,144
s_add_u32 s72, s82, s24
v_exp_f32 v126, v38
Mvav 48,172,152
buffer_load_dwordx4 v3, s[12:15], s72 offen offset:1024 sc1 lds
Cvav 52,160,160,180
s_add_u32 s72, s82, s25
v_exp_f32 v127, v39
Mvav 52,164,168
ds_read_b128 a[128:131], v1 offset:0
buffer_load_dwordx4 v3, s[12:15], s72 offen offset:2048 sc1 lds
Mvav 52,168,176
v_exp_f32 v120, v40
Mvav 52,172,184
ds_read_b128 a[132:135], v1 offset:1024
v_cvt_pk_bf16_f32 v204, v124, v125
Cvav 60,176,160,188
s_add_u32 s72, s82, s26
v_exp_f32 v121, v41
Mvav 60,180,168
ds_read_b128 a[136:139], v1 offset:2048
buffer_load_dwordx4 v3, s[12:15], s72 offen offset:3072 sc1 lds
Mvav 60,184,176
v_exp_f32 v122, v42
Mvav 60,188,184
s_cbranch_vccnz .Ly
.Ly_r:
Maav 0,196,228
v_exp_f32 v123, v43
Maav 4,196,236
ds_read_b128 a[140:143], v1 offset:3072
v_cvt_pk_bf16_f32 v205, v126, v127
Maav 64,196,244
v_exp_f32 v124, v44
Maav 68,196,252
ds_read_b128 a[144:147], v1 offset:4096
v_cvt_pk_bf16_f32 v198, v120, v121
Maav 8,204,228
v_exp_f32 v125, v45
Maav 12,204,236
s_cmp_lt_u32 s93, s73
s_cselect_b32 m0, s71, 0x30000
ds_read_b128 a[148:151], v1 offset:5120
Maav 72,204,244
v_exp_f32 v126, v46
Maav 76,204,252
ds_read_b128 a[152:155], v1 offset:6144
buffer_load_dwordx4 v25, s[36:39], s68 offen lds
Maav 16,212,228
v_exp_f32 v127, v47
Maav 20,212,236
s_add_u32 m0, m0, 1024
ds_read_b128 a[156:159], v1 offset:7168
v_cvt_pk_bf16_f32 v199, v122, v123
Maav 80,212,244
s_add_u32 s72, s68, s62
v_exp_f32 v120, v48
Maav 84,212,252
ds_read_b128 a[160:163], v1 offset:8192
buffer_load_dwordx4 v25, s[36:39], s72 offen lds
Maav 24,220,228
v_exp_f32 v121, v49
Maav 28,220,236
ds_read_b128 a[164:167], v1 offset:9216
v_cvt_pk_bf16_f32 v206, v124, v125
Maav 88,220,244
s_add_u32 s94, s94, 1
v_exp_f32 v122, v50
Maav 92,220,252
s_add_u32 s92, s92, 0x4000
ds_read_b128 a[168:171], v1 offset:10240
v_cvt_pk_bf16_f32 v207, v126, v127
Maav 32,228,228
s_and_b32 s92, s92, 0xc000
v_exp_f32 v123, v51
Maav 36,228,236
s_add_u32 s80, s92, 0x4000
ds_read_b128 a[172:175], v1 offset:11264
v_cvt_pk_bf16_f32 v212, v120, v121
Maav 96,228,244
s_and_b32 s80, s80, 0xc000
v_exp_f32 v124, v52
Maav 100,228,252
s_add_u32 s93, s93, 1
ds_read_b128 a[176:179], v1 offset:12288
v_cvt_pk_bf16_f32 v213, v122, v123
Maav 40,236,228
s_add_u32 s68, s68, s63
v_exp_f32 v125, v53
Maav 44,236,236
s_add_u32 s71, s71, s64
ds_read_b128 a[180:183], v1 offset:13312
v_cvt_pk_bf16_f32 v220, v124, v125
Maav 104,236,244
v_exp_f32 v126, v54
Maav 108,236,252
ds_read_b128 a[184:187], v1 offset:14336
v_add_u32 v2, s81, v0
Maav 48,244,228
v_exp_f32 v127, v55
Maav 52,244,236
s_waitcnt lgkmcnt(12)
ds_read_b128 a[188:191], v1 offset:15360
v_cvt_pk_bf16_f32 v221, v126, v127
Maav 112,244,244
v_exp_f32 v120, v60
Maav 116,244,252
v_exp_f32 v121, v61
Maav 56,252,228
v_exp_f32 v122, v62
Maav 60,252,236
v_exp_f32 v123, v63
Maav 120,252,244
s_waitcnt lgkmcnt(0)
v_cvt_pk_bf16_f32 v222, v120, v121
v_cvt_pk_bf16_f32 v223, v122, v123
Maav 124,252,252
s_bitcmp1_b64 s[34:35], s93
s_cbranch_scc1 .Lhy
.Lhy_r:
s_cmp_ge_u32 s93, 33
s_cbranch_scc1 .Lend
.Laa:
Cvav 64,128,128,4
s_waitcnt vmcnt(20)
s_barrier
Mvav 64,132,136
ds_read_b128 a[192:195], v2 offset:0
v_add_u32 v1, s80, v0
Mvav 64,136,144
ds_read_b128 a[196:199], v2 offset:1024
v_readlane_b32 s5, v12, s94
Mvav 64,140,152
ds_read_b128 a[200:203], v2 offset:2048
v_readlane_b32 s82, v14, s94
Cvvv 116,96,196,112
ds_read_b128 a[204:207], v2 offset:3072
Mvvv 116,100,204
ds_read_b128 a[208:211], v2 offset:4096
Mvvv 116,96,212
ds_read_b128 a[212:215], v2 offset:5120
Mvvv 116,100,220
ds_read_b128 a[216:219], v2 offset:6144
Cvav 68,128,160,8
s_or_b32 m0, s70, s92
v_exp_f32 v120, v64
Mvav 68,132,168
ds_read_b128 a[220:223], v2 offset:7168
buffer_load_dwordx4 v0, s[48:51], s5 offen sc1 lds
Mvav 68,136,176
v_exp_f32 v121, v65
Mvav 68,140,184
ds_read_b128 a[224:227], v2 offset:8192
v_cvt_pk_bf16_f32 v228, v120, v121
Cvav 72,144,128,4
v_exp_f32 v122, v66
Mvav 72,148,136
ds_read_b128 a[228:231], v2 offset:9216
v_add_f32 v26, v116, v117
Mvav 72,152,144
v_exp_f32 v123, v67
Mvav 72,156,152
ds_read_b128 a[232:235], v2 offset:10240
v_cmp_nge_f32 vcc, 0x53800000, v26
Cvav 76,144,160,8
v_exp_f32 v124, v68
Mvav 76,148,168
ds_read_b128 a[236:239], v2 offset:11264
buffer_load_dwordx4 v0, s[48:51], s5 offen offset:1024 sc1 lds
Mvav 76,152,176
v_exp_f32 v125, v69
Mvav 76,156,184
ds_read_b128 a[240:243], v2 offset:12288
v_cvt_pk_bf16_f32 v229, v122, v123
Cvav 80,160,128,4
s_or_b32 s81, s92, 0x10000
v_exp_f32 v126, v70
Mvav 80,164,136
ds_read_b128 a[244:247], v2 offset:13312
buffer_load_dwordx4 v0, s[48:51], s5 offen offset:2048 sc1 lds
Mvav 80,168,144
v_exp_f32 v127, v71
Mvav 80,172,152
ds_read_b128 a[248:251], v2 offset:14336
v_cvt_pk_bf16_f32 v236, v124, v125
Cvav 84,160,160,8
s_waitcnt lgkmcnt(12)
v_exp_f32 v120, v72
Mvav 84,164,168
ds_read_b128 a[252:255], v2 offset:15360
buffer_load_dwordx4 v0, s[48:51], s5 offen offset:3072 sc1 lds
Mvav 84,168,176
v_exp_f32 v121, v73
Mvav 84,172,184
s_xor_b32 s58, s92, 0x18000
ds_read_b128 a[128:131], v1 offset:0
v_cvt_pk_bf16_f32 v237, v126, v127
Cvav 88,176,128,4
s_or_b32 m0, s70, s58
v_exp_f32 v122, v74
Mvav 88,180,136
ds_read_b128 a[132:135], v1 offset:1024
buffer_load_dwordx4 v3, s[12:15], s82 offen sc1 lds
Mvav 88,184,144
v_exp_f32 v123, v75
Mvav 88,188,152
s_waitcnt lgkmcnt(12)
ds_read_b128 a[136:139], v1 offset:2048
v_cvt_pk_bf16_f32 v230, v120, v121
)"
R"(
Cvav 92,176,160,8
s_add_u32 s72, s82, s24
v_exp_f32 v124, v76
Mvav 92,180,168
ds_read_b128 a[140:143], v1 offset:3072
buffer_load_dwordx4 v3, s[12:15], s72 offen offset:1024 sc1 lds
Mvav 92,184,176
v_exp_f32 v125, v77
Mvav 92,188,184
s_cbranch_vccnz .Laa1
.Laa1_r:
Maav 0,192,196
v_exp_f32 v126, v78
Maav 0,196,212
s_waitcnt lgkmcnt(11)
ds_read_b128 a[144:147], v1 offset:4096
v_cvt_pk_bf16_f32 v231, v122, v123
Maav 4,192,204
s_add_u32 s72, s82, s25
v_exp_f32 v127, v79
Maav 4,196,220
ds_read_b128 a[148:151], v1 offset:5120
buffer_load_dwordx4 v3, s[12:15], s72 offen offset:2048 sc1 lds
Maav 8,200,196
v_exp_f32 v120, v80
Maav 8,204,212
ds_read_b128 a[152:155], v1 offset:6144
v_cvt_pk_bf16_f32 v238, v124, v125
Maav 12,200,204
s_add_u32 s72, s82, s26
v_exp_f32 v121, v81
Maav 12,204,220
ds_read_b128 a[156:159], v1 offset:7168
buffer_load_dwordx4 v3, s[12:15], s72 offen offset:3072 sc1 lds
Maav 16,208,196
s_waitcnt lgkmcnt(12)
v_exp_f32 v122, v82
Maav 16,212,212
s_cmp_lt_u32 s93, s73
s_cselect_b32 m0, s71, 0x30000
ds_read_b128 a[160:163], v1 offset:8192
v_add_u32 v2, s81, v0
Maav 20,208,204
v_exp_f32 v123, v83
Maav 20,212,220
ds_read_b128 a[164:167], v1 offset:9216
buffer_load_dwordx4 v25, s[36:39], s68 offen lds
Maav 24,216,196
s_waitcnt lgkmcnt(8)
v_exp_f32 v124, v84
Maav 24,220,212
s_add_u32 m0, m0, 1024
ds_read_b128 a[168:171], v1 offset:10240
v_cvt_pk_bf16_f32 v239, v126, v127
Maav 28,216,204
s_add_u32 s72, s68, s62
v_exp_f32 v125, v85
Maav 28,220,220
ds_read_b128 a[172:175], v1 offset:11264
buffer_load_dwordx4 v25, s[36:39], s72 offen lds
Maav 32,224,196
s_add_u32 s94, s94, 1
v_exp_f32 v126, v86
Maav 32,228,212
s_add_u32 s92, s92, 0x4000
ds_read_b128 a[176:179], v1 offset:12288
v_cvt_pk_bf16_f32 v244, v120, v121
Maav 36,224,204
s_and_b32 s92, s92, 0xc000
v_exp_f32 v127, v87
Maav 36,228,220
s_add_u32 s80, s92, 0x4000
ds_read_b128 a[180:183], v1 offset:13312
v_cvt_pk_bf16_f32 v245, v122, v123
Maav 40,232,196
s_and_b32 s80, s80, 0xc000
v_exp_f32 v120, v88
Maav 40,236,212
s_add_u32 s93, s93, 1
ds_read_b128 a[184:187], v1 offset:14336
v_cvt_pk_bf16_f32 v252, v124, v125
Maav 44,232,204
s_add_u32 s68, s68, s63
ds_read_b128 a[188:191], v1 offset:15360
v_cvt_pk_bf16_f32 v253, v126, v127
Maav 44,236,220
s_add_u32 s71, s71, s64
v_exp_f32 v121, v89
Maav 48,240,196
v_exp_f32 v122, v90
Maav 48,244,212
v_exp_f32 v123, v91
Maav 52,240,204
v_exp_f32 v124, v92
Maav 52,244,220
v_exp_f32 v125, v93
Maav 56,248,196
v_exp_f32 v126, v94
Maav 56,252,212
v_exp_f32 v127, v95
Maav 60,248,204
s_waitcnt lgkmcnt(0)
v_cvt_pk_bf16_f32 v246, v120, v121
v_cvt_pk_bf16_f32 v247, v122, v123
Maav 60,252,220
v_cvt_pk_bf16_f32 v254, v124, v125
v_cvt_pk_bf16_f32 v255, v126, v127
s_bitcmp1_b64 s[34:35], s93
s_cbranch_scc1 .Lhaa1
.Lhaa1_r:
Cvav 32,128,128,4
s_waitcnt vmcnt(20)
s_barrier
Mvav 32,132,136
ds_read_b128 a[192:195], v2 offset:0
v_add_u32 v1, s80, v0
Mvav 32,136,144
ds_read_b128 a[196:199], v2 offset:1024
v_readlane_b32 s5, v12, s94
Mvav 32,140,152
ds_read_b128 a[200:203], v2 offset:2048
v_readlane_b32 s82, v14, s94
Cvvv 112,96,228,116
ds_read_b128 a[204:207], v2 offset:3072
Mvvv 112,100,236
ds_read_b128 a[208:211], v2 offset:4096
Mvvv 112,96,244
ds_read_b128 a[212:215], v2 offset:5120
Mvvv 112,100,252
ds_read_b128 a[216:219], v2 offset:6144
Cvav 36,128,160,8
s_or_b32 m0, s70, s92
v_exp_f32 v120, v32
Mvav 36,132,168
ds_read_b128 a[220:223], v2 offset:7168
buffer_load_dwordx4 v0, s[48:51], s5 offen sc1 lds
Mvav 36,136,176
v_exp_f32 v121, v33
Mvav 36,140,184
ds_read_b128 a[224:227], v2 offset:8192
v_cvt_pk_bf16_f32 v196, v120, v121
Cvav 40,144,128,4
v_exp_f32 v122, v34
Mvav 40,148,136
ds_read_b128 a[228:231], v2 offset:9216
v_add_f32 v26, v112, v113
Mvav 40,152,144
v_exp_f32 v123, v35
Mvav 40,156,152
ds_read_b128 a[232:235], v2 offset:10240
v_cmp_nge_f32 vcc, 0x53800000, v26
Cvav 44,144,160,8
v_exp_f32 v124, v36
Mvav 44,148,168
ds_read_b128 a[236:239], v2 offset:11264
buffer_load_dwordx4 v0, s[48:51], s5 offen offset:1024 sc1 lds
Mvav 44,152,176
v_exp_f32 v125, v37
Mvav 44,156,184
ds_read_b128 a[240:243], v2 offset:12288
v_cvt_pk_bf16_f32 v197, v122, v123
Cvav 48,160,128,4
s_or_b32 s81, s92, 0x10000
v_exp_f32 v126, v38
Mvav 48,164,136
ds_read_b128 a[244:247], v2 offset:13312
buffer_load_dwordx4 v0, s[48:51], s5 offen offset:2048 sc1 lds
Mvav 48,168,144
v_exp_f32 v127, v39
Mvav 48,172,152
ds_read_b128 a[248:251], v2 offset:14336
v_cvt_pk_bf16_f32 v204, v124, v125
Cvav 52,160,160,8
s_waitcnt lgkmcnt(12)
v_exp_f32 v120, v40
Mvav 52,164,168
ds_read_b128 a[252:255], v2 offset:15360
buffer_load_dwordx4 v0, s[48:51], s5 offen offset:3072 sc1 lds
Mvav 52,168,176
v_exp_f32 v121, v41
Mvav 52,172,184
s_xor_b32 s58, s92, 0x18000
ds_read_b128 a[128:131], v1 offset:0
v_cvt_pk_bf16_f32 v205, v126, v127
Cvav 56,176,128,4
s_or_b32 m0, s70, s58
v_exp_f32 v122, v42
Mvav 56,180,136
ds_read_b128 a[132:135], v1 offset:1024
buffer_load_dwordx4 v3, s[12:15], s82 offen sc1 lds
Mvav 56,184,144
v_exp_f32 v123, v43
Mvav 56,188,152
s_waitcnt lgkmcnt(12)
ds_read_b128 a[136:139], v1 offset:2048
v_cvt_pk_bf16_f32 v198, v120, v121
Cvav 60,176,160,8
s_add_u32 s72, s82, s24
v_exp_f32 v124, v44
Mvav 60,180,168
ds_read_b128 a[140:143], v1 offset:3072
buffer_load_dwordx4 v3, s[12:15], s72 offen offset:1024 sc1 lds
Mvav 60,184,176
v_exp_f32 v125, v45
Mvav 60,188,184
s_cbranch_vccnz .Laa0
.Laa0_r:
Maav 0,192,228
s_waitcnt lgkmcnt(11)
v_exp_f32 v126, v46
Maav 0,196,244
ds_read_b128 a[144:147], v1 offset:4096
buffer_store_dwordx2 v[132:133], v13, s[48:51], s85 offen sc1
v_cvt_pk_bf16_f32 v199, v122, v123
Maav 4,192,236
s_add_u32 s72, s82, s25
v_exp_f32 v127, v47
Maav 4,196,252
ds_read_b128 a[148:151], v1 offset:5120
buffer_load_dwordx4 v3, s[12:15], s72 offen offset:2048 sc1 lds
Maav 8,200,228
s_mov_b32 s85, s99
v_exp_f32 v120, v48
Maav 8,204,244
s_mov_b32 s99, 0x80000000
ds_read_b128 a[152:155], v1 offset:6144
v_cvt_pk_bf16_f32 v206, v124, v125
Maav 12,200,236
s_add_u32 s72, s82, s26
v_exp_f32 v121, v49
Maav 12,204,252
ds_read_b128 a[156:159], v1 offset:7168
buffer_load_dwordx4 v3, s[12:15], s72 offen offset:3072 sc1 lds
Maav 16,208,228
s_waitcnt lgkmcnt(12)
v_exp_f32 v122, v50
Maav 16,212,244
s_cmp_lt_u32 s93, s73
s_cselect_b32 m0, s71, 0x30000
ds_read_b128 a[160:163], v1 offset:8192
v_add_u32 v2, s81, v0
Maav 20,208,236
v_exp_f32 v123, v51
Maav 20,212,252
ds_read_b128 a[164:167], v1 offset:9216
buffer_load_dwordx4 v25, s[36:39], s68 offen lds
Maav 24,216,228
s_waitcnt lgkmcnt(8)
v_exp_f32 v124, v52
Maav 24,220,244
s_add_u32 m0, m0, 1024
ds_read_b128 a[168:171], v1 offset:10240
v_cvt_pk_bf16_f32 v207, v126, v127
Maav 28,216,236
s_add_u32 s72, s68, s62
v_exp_f32 v125, v53
Maav 28,220,252
ds_read_b128 a[172:175], v1 offset:11264
buffer_load_dwordx4 v25, s[36:39], s72 offen lds
Maav 32,224,228
s_add_u32 s94, s94, 1
v_exp_f32 v126, v54
Maav 32,228,244
s_add_u32 s92, s92, 0x4000
ds_read_b128 a[176:179], v1 offset:12288
v_cvt_pk_bf16_f32 v212, v120, v121
Maav 36,224,236
s_and_b32 s92, s92, 0xc000
v_exp_f32 v127, v55
Maav 36,228,252
s_add_u32 s80, s92, 0x4000
ds_read_b128 a[180:183], v1 offset:13312
v_cvt_pk_bf16_f32 v213, v122, v123
Maav 40,232,228
s_and_b32 s80, s80, 0xc000
v_exp_f32 v120, v56
Maav 40,236,244
s_add_u32 s93, s93, 1
ds_read_b128 a[184:187], v1 offset:14336
v_cvt_pk_bf16_f32 v220, v124, v125
Maav 44,232,236
s_add_u32 s68, s68, s63
ds_read_b128 a[188:191], v1 offset:15360
v_cvt_pk_bf16_f32 v221, v126, v127
Maav 44,236,252
s_add_u32 s71, s71, s64
v_exp_f32 v121, v57
Maav 48,240,228
v_exp_f32 v122, v58
Maav 48,244,244
v_exp_f32 v123, v59
Maav 52,240,236
v_exp_f32 v124, v60
Maav 52,244,252
v_exp_f32 v125, v61
Maav 56,248,228
v_exp_f32 v126, v62
Maav 56,252,244
v_exp_f32 v127, v63
Maav 60,248,236
s_waitcnt lgkmcnt(0)
v_cvt_pk_bf16_f32 v214, v120, v121
v_cvt_pk_bf16_f32 v215, v122, v123
Maav 60,252,252
v_cvt_pk_bf16_f32 v222, v124, v125
v_cvt_pk_bf16_f32 v223, v126, v127
s_bitcmp1_b64 s[34:35], s93
s_cbranch_scc1 .Lhaa0
.Lhaa0_r:
s_cmp_lt_u32 s93, 33
s_cbranch_scc1 .Laa
.Lend:
v_lshrrev_b32 v26, 9, v0
v_bfe_u32 v27, v0, 4, 5
v_lshlrev_b32 v27, 4, v27
v_lshl_add_u32 v26, v26, 11, v27
s_lshr_b32 s58, s70, 3
s_add_u32 s58, s58, 0x20000
v_add_u32 v3, s58, v26
ds_read_b64_tr_b16 a[64:65], v3 offset:0
ds_read_b64_tr_b16 a[66:67], v3 offset:4096
ds_read_b64_tr_b16 a[72:73], v3 offset:8192
ds_read_b64_tr_b16 a[74:75], v3 offset:12288
ds_read_b64_tr_b16 a[80:81], v3 offset:16384
ds_read_b64_tr_b16 a[82:83], v3 offset:20480
ds_read_b64_tr_b16 a[88:89], v3 offset:24576
ds_read_b64_tr_b16 a[90:91], v3 offset:28672
Cvvv 116,96,196,112
s_waitcnt lgkmcnt(0)
s_waitcnt vmcnt(20)
s_barrier
Mvvv 116,100,204
s_lshl_b32 s24, s91, 14
ds_read_b64_tr_b16 a[68:69], v3 offset:8
ds_read_b64_tr_b16 a[70:71], v3 offset:4104
v_bfe_u32 v5, v0, 4, 4
v_lshrrev_b32 v6, 8, v0
v_lshlrev_b32 v7, 4, v6
Mvvv 116,96,212
s_lshl_b32 s59, s57, 7
ds_read_b64_tr_b16 a[76:77], v3 offset:8200
ds_read_b64_tr_b16 a[78:79], v3 offset:12296
v_lshl_add_u32 v4, v5, 11, v7
v_mul_u32_u24 v5, 2176, v5
v_add_u32 v5, v5, v7
Mvvv 116,100,220
s_add_u32 s24, s24, s59
ds_read_b64_tr_b16 a[84:85], v3 offset:16392
ds_read_b64_tr_b16 a[86:87], v3 offset:20488
v_bfe_u32 v9, v0, 5, 3
v_lshlrev_b32 v6, 5, v6
v_lshl_add_u32 v6, v9, 7, v6
Zvaa 64,64,128
s_add_u32 s24, s24, 1048576
s_lshl3_add_u32 s63, s79, s86
ds_read_b64_tr_b16 a[92:93], v3 offset:24584
ds_read_b64_tr_b16 a[94:95], v3 offset:28680
v_and_b32 v9, 16, v0
v_add_u32 v6, v6, v9
Mvaa 64,72,132
s_mul_i32 s25, s57, 73728
s_lshl2_add_u32 s64, s61, s57
s_lshl_b32 s62, s64, 6
s_lshl_b32 s68, s63, 15
ds_read_b128 a[192:195], v2 offset:0
ds_read_b128 a[196:199], v2 offset:1024
Mvaa 64,80,136
s_add_u32 s66, s66, s25
s_add_u32 s68, s68, s62
s_mul_i32 s71, s63, 34816
s_add_u32 s71, s71, s62
ds_read_b128 a[200:203], v2 offset:2048
ds_read_b128 a[204:207], v2 offset:3072
Mvaa 64,88,140
s_waitcnt lgkmcnt(10)
s_mul_i32 s58, s76, 1179648
ds_read_b128 a[208:211], v2 offset:4096
v_subrev_u32 v1, 0x10000, v3
v_lshrrev_b32 v11, 8, v0
v_lshlrev_b32 v11, 12, v11
v_and_b32 v10, 16, v0
Zvaa 68,68,128
s_waitcnt lgkmcnt(9)
s_add_u32 s12, s66, s58
ds_read_b128 a[212:215], v2 offset:5120
buffer_load_dwordx4 v[16:19], v0, s[48:51], s83 offen sc1
v_add_u32 v12, 0x10000, v0
v_lshl_add_u32 v11, v10, 5, v11
v_bfe_u32 v10, v0, 5, 3
v_lshl_add_u32 v11, v10, 4, v11
Mvaa 68,76,132
s_waitcnt lgkmcnt(8)
s_mul_i32 s58, s77, 1179648
ds_read_b128 a[216:219], v2 offset:6144
v_add_f32 v26, v116, v117
Mvaa 68,84,136
s_waitcnt lgkmcnt(7)
s_add_u32 s13, s66, s58
ds_read_b128 a[220:223], v2 offset:7168
v_cmp_nge_f32 vcc, 0x53800000, v26
Mvaa 68,92,140
s_mul_i32 s58, s78, 1179648
ds_read_b128 a[224:227], v2 offset:8192
v_add_u32 v14, 0x20000, v0
Zvaa 72,64,144
s_add_u32 s14, s66, s58
s_mul_i32 s58, s79, 1179648
ds_read_b128 a[228:231], v2 offset:9216
Mvaa 72,72,148
s_waitcnt lgkmcnt(7)
s_add_u32 s15, s66, s58
s_lshl3_add_u32 s58, s86, s61
ds_read_b128 a[232:235], v2 offset:10240
Mvaa 72,80,152
s_lshl_b32 s58, s58, 19
s_lshr_b32 s59, s70, 1
ds_read_b128 a[236:239], v2 offset:11264
Mvaa 72,88,156
s_add_u32 s58, s58, s59
s_add_u32 s58, s58, 75497472
ds_read_b128 a[240:243], v2 offset:12288
Zvaa 76,68,144
s_lshl_b32 s59, s79, 13
s_add_u32 s26, s58, s59
ds_read_b128 a[244:247], v2 offset:13312
Mvaa 76,76,148
s_lshl_b32 s59, s79, 17
s_add_u32 s27, s58, s59
ds_read_b128 a[248:251], v2 offset:14336
Mvaa 76,84,152
s_and_b32 s59, s61, 1
s_lshl_b32 s32, s59, 12
ds_read_b128 a[252:255], v2 offset:15360
Mvaa 76,92,156
s_cbranch_vccnz .Ldr
.Ldr_r:
s_lshr_b32 s59, s70, 2
Maav 0,192,196
s_sub_u32 s32, s32, s59
s_lshl3_add_u32 s58, s86, s61
s_lshl2_add_u32 s58, s58, s57
Maav 0,196,212
s_lshl_b32 s58, s58, 7
s_add_u32 s33, s58, 49152
s_lshl3_add_u32 s34, s79, s33
v_rcp_f32 v32, v116
Maav 4,192,204
s_and_b32 s20, s74, 0xff
s_bfe_u32 s21, s74, 0x80008
s_bfe_u32 s22, s74, 0x80010
v_rcp_f32 v33, v117
Maav 4,196,220
s_lshr_b32 s23, s74, 24
s_xor_b32 s35, s98, 0x30f0
s_add_u32 s84, s97, 4096
Maav 8,200,196
s_waitcnt vmcnt(1) lgkmcnt(0)
Maav 8,204,212
s_barrier
Maav 12,200,204
s_add_u32 m0, s70, 131072
s_add_u32 s72, s67, 524288
)"
R"(
buffer_load_dwordx4 v25, s[28:31], s72 offen lds
Maav 12,204,220
s_add_u32 m0, s70, 131968
s_add_u32 s72, s67, 524288
buffer_load_dwordx4 v25, s[28:31], s72 offen offset:128 lds
Maav 16,208,196
s_add_u32 m0, s70, 133120
s_add_u32 s72, s67, 540672
buffer_load_dwordx4 v25, s[28:31], s72 offen lds
Maav 16,212,212
s_add_u32 m0, s70, 134016
s_add_u32 s72, s67, 540672
buffer_load_dwordx4 v25, s[28:31], s72 offen offset:128 lds
Maav 20,208,204
s_add_u32 m0, s70, 147456
s_add_u32 s72, s67, 655360
buffer_load_dwordx4 v25, s[28:31], s72 offen lds
Maav 20,212,220
s_add_u32 m0, s70, 148352
s_add_u32 s72, s67, 655360
buffer_load_dwordx4 v25, s[28:31], s72 offen offset:128 lds
Maav 24,216,196
s_add_u32 m0, s70, 149504
s_add_u32 s72, s67, 671744
buffer_load_dwordx4 v25, s[28:31], s72 offen lds
v_accvgpr_read_b32 v34, a0
v_accvgpr_read_b32 v35, a1
v_mul_f32 v34, v34, v32
v_mul_f32 v35, v35, v32
Maav 24,220,212
s_add_u32 m0, s70, 150400
s_add_u32 s72, s67, 671744
buffer_load_dwordx4 v25, s[28:31], s72 offen offset:128 lds
v_cvt_pk_bf16_f32 v148, v34, v35
v_accvgpr_read_b32 v34, a2
v_accvgpr_read_b32 v35, a3
v_mul_f32 v34, v34, v32
Maav 28,216,204
s_add_u32 m0, s70, 98304
s_add_u32 s72, s67, 786432
buffer_load_dwordx4 v25, s[28:31], s72 offen lds
v_mul_f32 v35, v35, v32
v_cvt_pk_bf16_f32 v149, v34, v35
v_accvgpr_read_b32 v34, a8
v_accvgpr_read_b32 v35, a9
Maav 28,220,220
s_add_u32 m0, s70, 99200
s_add_u32 s72, s67, 786432
buffer_load_dwordx4 v25, s[28:31], s72 offen offset:128 lds
v_mul_f32 v34, v34, v32
v_mul_f32 v35, v35, v32
v_cvt_pk_bf16_f32 v150, v34, v35
v_accvgpr_read_b32 v34, a10
Maav 32,224,196
s_add_u32 m0, s70, 100352
s_add_u32 s72, s67, 802816
buffer_load_dwordx4 v25, s[28:31], s72 offen lds
v_accvgpr_read_b32 v35, a11
v_mul_f32 v34, v34, v32
v_mul_f32 v35, v35, v32
v_cvt_pk_bf16_f32 v151, v34, v35
Maav 32,228,212
s_add_u32 m0, s70, 101248
s_add_u32 s72, s67, 802816
buffer_load_dwordx4 v25, s[28:31], s72 offen offset:128 lds
v_accvgpr_read_b32 v34, a4
v_accvgpr_read_b32 v35, a5
v_mul_f32 v34, v34, v33
v_mul_f32 v35, v35, v33
Maav 36,224,204
s_add_u32 m0, s70, 114688
s_add_u32 s72, s67, 917504
buffer_load_dwordx4 v25, s[28:31], s72 offen lds
v_cvt_pk_bf16_f32 v156, v34, v35
v_accvgpr_read_b32 v34, a6
v_accvgpr_read_b32 v35, a7
v_mul_f32 v34, v34, v33
Maav 36,228,220
s_add_u32 m0, s70, 115584
s_add_u32 s72, s67, 917504
buffer_load_dwordx4 v25, s[28:31], s72 offen offset:128 lds
v_mul_f32 v35, v35, v33
v_cvt_pk_bf16_f32 v157, v34, v35
v_accvgpr_read_b32 v34, a12
v_accvgpr_read_b32 v35, a13
Maav 40,232,196
s_add_u32 m0, s70, 116736
s_add_u32 s72, s67, 933888
buffer_load_dwordx4 v25, s[28:31], s72 offen lds
v_mul_f32 v34, v34, v33
v_mul_f32 v35, v35, v33
v_cvt_pk_bf16_f32 v158, v34, v35
v_accvgpr_read_b32 v34, a14
Maav 40,236,212
s_add_u32 m0, s70, 117632
s_add_u32 s72, s67, 933888
buffer_load_dwordx4 v25, s[28:31], s72 offen offset:128 lds
v_accvgpr_read_b32 v35, a15
v_mul_f32 v34, v34, v33
v_mul_f32 v35, v35, v33
v_cvt_pk_bf16_f32 v159, v34, v35
Maav 44,232,204
s_add_u32 m0, s70, 0
s_add_u32 s72, s87, 147456
buffer_load_dwordx4 v0, s[48:51], s72 offen sc1 lds
v_accvgpr_read_b32 v34, a16
v_accvgpr_read_b32 v35, a17
v_mul_f32 v34, v34, v32
v_mul_f32 v35, v35, v32
Maav 44,236,220
buffer_load_dwordx4 v0, s[48:51], s72 offen offset:1024 sc1 lds
v_cvt_pk_bf16_f32 v164, v34, v35
v_accvgpr_read_b32 v34, a18
v_accvgpr_read_b32 v35, a19
v_mul_f32 v34, v34, v32
Maav 48,240,196
buffer_load_dwordx4 v0, s[48:51], s72 offen offset:2048 sc1 lds
v_mul_f32 v35, v35, v32
v_cvt_pk_bf16_f32 v165, v34, v35
v_accvgpr_read_b32 v34, a24
v_accvgpr_read_b32 v35, a25
Maav 48,244,212
buffer_load_dwordx4 v0, s[48:51], s72 offen offset:3072 sc1 lds
v_mul_f32 v34, v34, v32
v_mul_f32 v35, v35, v32
v_cvt_pk_bf16_f32 v166, v34, v35
v_accvgpr_read_b32 v34, a26
Maav 52,240,204
ds_read_b128 v[128:131], v0 offset:16384
buffer_store_dwordx4 v[148:151], v0, s[48:51], s97 offen sc1
v_accvgpr_read_b32 v35, a27
v_mul_f32 v34, v34, v32
v_mul_f32 v35, v35, v32
Maav 52,244,220
ds_read_b128 v[136:139], v0 offset:17408
v_cvt_pk_bf16_f32 v167, v34, v35
v_accvgpr_read_b32 v34, a20
v_accvgpr_read_b32 v35, a21
v_mul_f32 v34, v34, v33
v_mul_f32 v35, v35, v33
Maav 56,248,196
ds_read_b128 v[144:147], v0 offset:18432
buffer_store_dwordx4 v[156:159], v0, s[48:51], s84 offen sc1
v_cvt_pk_bf16_f32 v172, v34, v35
v_accvgpr_read_b32 v34, a22
v_accvgpr_read_b32 v35, a23
Maav 56,252,212
ds_read_b128 v[152:155], v0 offset:19456
v_mul_f32 v34, v34, v33
v_mul_f32 v35, v35, v33
v_cvt_pk_bf16_f32 v173, v34, v35
v_accvgpr_read_b32 v34, a28
v_accvgpr_read_b32 v35, a29
Maav 60,248,204
ds_read_b64_tr_b16 a[96:97], v1 offset:0
buffer_store_dwordx4 v[164:167], v0, s[48:51], s97 offen offset:1024 sc1
v_mul_f32 v34, v34, v33
v_mul_f32 v35, v35, v33
v_cvt_pk_bf16_f32 v174, v34, v35
Maav 60,252,220
ds_read_b64_tr_b16 a[98:99], v1 offset:4096
v_accvgpr_read_b32 v34, a30
v_accvgpr_read_b32 v35, a31
v_mul_f32 v34, v34, v33
v_mul_f32 v35, v35, v33
v_cvt_pk_bf16_f32 v175, v34, v35
Zvaa 80,64,160
ds_read_b64_tr_b16 a[104:105], v1 offset:8192
buffer_store_dwordx4 v[172:175], v0, s[48:51], s84 offen offset:1024 sc1
v_accvgpr_read_b32 v34, a32
v_accvgpr_read_b32 v35, a33
v_mul_f32 v34, v34, v32
Mvaa 80,72,164
ds_read_b64_tr_b16 a[106:107], v1 offset:12288
v_mul_f32 v35, v35, v32
v_cvt_pk_bf16_f32 v228, v34, v35
v_accvgpr_read_b32 v34, a34
v_accvgpr_read_b32 v35, a35
v_mul_f32 v34, v34, v32
Mvaa 80,80,168
ds_read_b64_tr_b16 a[112:113], v1 offset:16384
v_mul_f32 v35, v35, v32
v_cvt_pk_bf16_f32 v229, v34, v35
v_accvgpr_read_b32 v34, a40
v_accvgpr_read_b32 v35, a41
v_mul_f32 v34, v34, v32
Mvaa 80,88,172
s_waitcnt lgkmcnt(6)
ds_read_b64_tr_b16 a[114:115], v1 offset:20480
v_mul_f32 v35, v35, v32
v_cvt_pk_bf16_f32 v230, v34, v35
v_accvgpr_read_b32 v34, a42
v_accvgpr_read_b32 v35, a43
v_mul_f32 v34, v34, v32
Zvaa 84,68,160
ds_read_b64_tr_b16 a[120:121], v1 offset:24576
v_mul_f32 v35, v35, v32
v_cvt_pk_bf16_f32 v231, v34, v35
v_accvgpr_read_b32 v34, a36
v_accvgpr_read_b32 v35, a37
v_mul_f32 v34, v34, v33
Mvaa 84,76,164
ds_read_b64_tr_b16 a[122:123], v1 offset:28672
buffer_store_dwordx4 v[228:231], v0, s[48:51], s97 offen offset:2048 sc1
v_mul_f32 v35, v35, v33
v_cvt_pk_bf16_f32 v236, v34, v35
v_accvgpr_read_b32 v34, a38
Mvaa 84,84,168
s_waitcnt lgkmcnt(6)
ds_read_b64_tr_b16 a[100:101], v1 offset:8
v_accvgpr_read_b32 v35, a39
v_mul_f32 v34, v34, v33
v_mul_f32 v35, v35, v33
v_cvt_pk_bf16_f32 v237, v34, v35
v_accvgpr_read_b32 v34, a44
Mvaa 84,92,172
ds_read_b64_tr_b16 a[102:103], v1 offset:4104
v_accvgpr_read_b32 v35, a45
v_mul_f32 v34, v34, v33
v_mul_f32 v35, v35, v33
v_cvt_pk_bf16_f32 v238, v34, v35
v_accvgpr_read_b32 v34, a46
Zvaa 88,64,176
ds_read_b64_tr_b16 a[108:109], v1 offset:8200
v_accvgpr_read_b32 v35, a47
v_mul_f32 v34, v34, v33
v_mul_f32 v35, v35, v33
v_cvt_pk_bf16_f32 v239, v34, v35
v_accvgpr_read_b32 v34, a48
Mvaa 88,72,180
s_waitcnt lgkmcnt(6)
ds_read_b64_tr_b16 a[110:111], v1 offset:12296
buffer_store_dwordx4 v[236:239], v0, s[48:51], s84 offen offset:2048 sc1
v_accvgpr_read_b32 v35, a49
v_mul_f32 v34, v34, v32
v_mul_f32 v35, v35, v32
Mvaa 88,80,184
s_waitcnt lgkmcnt(6)
ds_read_b64_tr_b16 a[116:117], v1 offset:16392
v_cvt_pk_bf16_f32 v244, v34, v35
v_accvgpr_read_b32 v34, a50
v_accvgpr_read_b32 v35, a51
v_mul_f32 v34, v34, v32
v_mul_f32 v35, v35, v32
Mvaa 88,88,188
ds_read_b64_tr_b16 a[118:119], v1 offset:20488
v_cvt_pk_bf16_f32 v245, v34, v35
v_accvgpr_read_b32 v34, a56
v_accvgpr_read_b32 v35, a57
v_mul_f32 v34, v34, v32
v_mul_f32 v35, v35, v32
Zvaa 92,68,176
s_waitcnt lgkmcnt(6)
ds_read_b64_tr_b16 a[124:125], v1 offset:24584
v_cvt_pk_bf16_f32 v246, v34, v35
v_accvgpr_read_b32 v34, a58
v_accvgpr_read_b32 v35, a59
v_mul_f32 v34, v34, v32
v_mul_f32 v35, v35, v32
Mvaa 92,76,180
ds_read_b64_tr_b16 a[126:127], v1 offset:28680
v_cvt_pk_bf16_f32 v247, v34, v35
v_accvgpr_read_b32 v34, a52
v_accvgpr_read_b32 v35, a53
v_mul_f32 v34, v34, v33
v_mul_f32 v35, v35, v33
Mvaa 92,84,184
ds_read_b128 v[160:163], v0 offset:20480
buffer_store_dwordx4 v[244:247], v0, s[48:51], s97 offen offset:3072 sc1
v_cvt_pk_bf16_f32 v252, v34, v35
v_accvgpr_read_b32 v34, a54
v_accvgpr_read_b32 v35, a55
Mvaa 92,92,188
ds_read_b128 v[168:171], v0 offset:21504
v_mul_f32 v34, v34, v33
v_mul_f32 v35, v35, v33
v_cvt_pk_bf16_f32 v253, v34, v35
v_accvgpr_read_b32 v34, a60
v_accvgpr_read_b32 v35, a61
Mvav 64,96,128
ds_read_b128 v[176:179], v0 offset:22528
v_mul_f32 v34, v34, v33
v_mul_f32 v35, v35, v33
v_cvt_pk_bf16_f32 v254, v34, v35
v_accvgpr_read_b32 v34, a62
v_accvgpr_read_b32 v35, a63
Mvav 64,104,136
ds_read_b128 v[184:187], v0 offset:23552
v_mul_f32 v34, v34, v33
v_mul_f32 v35, v35, v33
v_cvt_pk_bf16_f32 v255, v34, v35
Mvav 64,112,144
ds_read_b128 v[192:195], v0 offset:24576
buffer_store_dwordx4 v[252:255], v0, s[48:51], s84 offen offset:3072 sc1
Mvav 64,120,152
s_waitcnt lgkmcnt(11)
ds_read_b128 v[200:203], v0 offset:25600
Mvav 68,100,128
s_waitcnt lgkmcnt(10)
ds_read_b128 v[208:211], v0 offset:26624
Mvav 68,108,136
s_waitcnt lgkmcnt(9)
ds_read_b128 v[216:219], v0 offset:27648
Mvav 68,116,144
s_waitcnt lgkmcnt(8)
ds_read_b128 v[224:227], v0 offset:28672
Mvav 68,124,152
s_waitcnt lgkmcnt(8)
ds_read_b128 v[232:235], v0 offset:29696
Mvav 72,96,160
s_waitcnt lgkmcnt(8)
ds_read_b128 v[240:243], v0 offset:30720
ds_read_b128 v[248:251], v0 offset:31744
Mvav 72,104,168
s_waitcnt vmcnt(20) lgkmcnt(7)
s_barrier
Mvav 72,112,176
s_waitcnt lgkmcnt(6)
s_add_u32 m0, s70, 65536
s_add_u32 s72, s67, 1048576
ds_read_b128 v[128:131], v0 offset:32768
Mvav 72,120,184
s_waitcnt lgkmcnt(6)
ds_read_b128 v[136:139], v0 offset:33792
buffer_load_dwordx4 v25, s[28:31], s72 offen lds
Mvav 76,100,160
s_waitcnt lgkmcnt(6)
s_add_u32 m0, s70, 66432
s_add_u32 s72, s67, 1048576
ds_read_b128 v[144:147], v0 offset:34816
Mvav 76,108,168
ds_read_b128 v[152:155], v0 offset:35840
buffer_load_dwordx4 v25, s[28:31], s72 offen offset:128 lds
Mvav 76,116,176
s_add_u32 m0, s70, 67584
s_add_u32 s72, s67, 1064960
ds_read_b64_tr_b16 a[128:129], v3 offset:0
Mvav 76,124,184
s_waitcnt lgkmcnt(6)
ds_read_b64_tr_b16 a[130:131], v3 offset:4096
buffer_load_dwordx4 v25, s[28:31], s72 offen lds
Mvav 80,96,192
s_add_u32 m0, s70, 68480
s_add_u32 s72, s67, 1064960
ds_read_b64_tr_b16 a[136:137], v3 offset:8192
Mvav 80,104,200
ds_read_b64_tr_b16 a[138:139], v3 offset:12288
buffer_load_dwordx4 v25, s[28:31], s72 offen offset:128 lds
Mvav 80,112,208
s_waitcnt lgkmcnt(6)
s_add_u32 m0, s70, 81920
s_add_u32 s72, s67, 1179648
ds_read_b64_tr_b16 a[144:145], v3 offset:16384
Mvav 80,120,216
ds_read_b64_tr_b16 a[146:147], v3 offset:20480
buffer_load_dwordx4 v25, s[28:31], s72 offen lds
Mvav 84,100,192
s_add_u32 m0, s70, 82816
s_add_u32 s72, s67, 1179648
ds_read_b64_tr_b16 a[152:153], v3 offset:24576
Mvav 84,108,200
ds_read_b64_tr_b16 a[154:155], v3 offset:28672
buffer_load_dwordx4 v25, s[28:31], s72 offen offset:128 lds
Mvav 84,116,208
s_add_u32 m0, s70, 83968
s_add_u32 s72, s67, 1196032
ds_read_b64_tr_b16 a[132:133], v3 offset:8
Mvav 84,124,216
ds_read_b64_tr_b16 a[134:135], v3 offset:4104
buffer_load_dwordx4 v25, s[28:31], s72 offen lds
Mvav 88,96,224
s_add_u32 m0, s70, 84864
s_add_u32 s72, s67, 1196032
ds_read_b64_tr_b16 a[140:141], v3 offset:8200
Mvav 88,104,232
ds_read_b64_tr_b16 a[142:143], v3 offset:12296
buffer_load_dwordx4 v25, s[28:31], s72 offen offset:128 lds
Mvav 88,112,240
ds_read_b64_tr_b16 a[148:149], v3 offset:16392
Mvav 88,120,248
s_waitcnt lgkmcnt(12)
ds_read_b64_tr_b16 a[150:151], v3 offset:20488
v_xor_b32 v16, s74, v16
Mvav 92,100,224
ds_read_b64_tr_b16 a[156:157], v3 offset:24584
v_bitop3_b32 v16, v17, s75, v16 bitop3:0xbe
Mvav 92,108,232
ds_read_b64_tr_b16 a[158:159], v3 offset:28680
v_bitop3_b32 v16, v18, s74, v16 bitop3:0xbe
Mvav 92,116,240
s_waitcnt lgkmcnt(12)
ds_read_b128 v[160:163], v0 offset:36864
v_bitop3_b32 v16, v19, s75, v16 bitop3:0xbe
Mvav 92,124,248
ds_read_b128 v[168:171], v0 offset:37888
v_cmp_ne_u32 vcc, 0, v16
Mvav 64,128,128
s_and_b32 vcc_lo, vcc_lo, 0x700070
ds_read_b128 v[176:179], v0 offset:38912
Mvav 64,136,136
s_waitcnt lgkmcnt(7)
)"
R"(
s_and_b32 vcc_hi, vcc_hi, 0x700070
ds_read_b128 v[184:187], v0 offset:39936
Mvav 64,144,144
s_waitcnt lgkmcnt(6)
s_cbranch_vccnz .Lcf2
.Lcf2_r:
ds_read_b128 v[192:195], v0 offset:40960
Mvav 64,152,152
s_mul_i32 s58, s61, 36864
s_lshr_b32 s59, s70, 2
ds_read_b128 v[200:203], v0 offset:41984
Mvav 68,132,128
s_add_u32 s58, s58, s59
s_sub_u32 s58, s58, s25
ds_read_b128 v[208:211], v0 offset:43008
Mvav 68,140,136
s_add_u32 s58, s58, 4718592
s_add_u32 s59, s58, s13
ds_read_b128 v[216:219], v0 offset:44032
Mvav 68,148,144
s_waitcnt lgkmcnt(7)
s_add_u32 s58, s58, s12
ds_read_b128 v[224:227], v0 offset:45056
Mvav 68,156,152
ds_read_b128 v[232:235], v0 offset:46080
buffer_load_dwordx4 v[28:31], v0, s[48:51], s58 offen sc1
Mvav 72,128,160
s_waitcnt lgkmcnt(8)
ds_read_b128 v[240:243], v0 offset:47104
ds_read_b128 v[248:251], v0 offset:48128
Mvav 72,136,168
s_waitcnt vmcnt(21) lgkmcnt(7)
s_barrier
Mvav 72,144,176
s_waitcnt lgkmcnt(6)
s_add_u32 m0, s70, 131072
s_add_u32 s72, s67, 1310720
ds_read_b128 v[128:131], v0 offset:49152
buffer_load_dwordx4 v[28:31], v0, s[48:51], s59 offen sc1
Mvav 72,152,184
s_waitcnt lgkmcnt(6)
ds_read_b128 v[136:139], v0 offset:50176
buffer_load_dwordx4 v25, s[28:31], s72 offen lds
Mvav 76,132,160
s_waitcnt lgkmcnt(6)
s_add_u32 m0, s70, 131968
s_add_u32 s72, s67, 1310720
ds_read_b128 v[144:147], v0 offset:51200
Mvav 76,140,168
ds_read_b128 v[152:155], v0 offset:52224
buffer_load_dwordx4 v25, s[28:31], s72 offen offset:128 lds
Mvav 76,148,176
s_add_u32 m0, s70, 133120
s_add_u32 s72, s67, 1327104
ds_read_b64_tr_b16 a[160:161], v1 offset:32768
Mvav 76,156,184
s_waitcnt lgkmcnt(6)
ds_read_b64_tr_b16 a[162:163], v1 offset:36864
buffer_load_dwordx4 v25, s[28:31], s72 offen lds
Mvav 80,128,192
s_add_u32 m0, s70, 134016
s_add_u32 s72, s67, 1327104
ds_read_b64_tr_b16 a[168:169], v1 offset:40960
Mvav 80,136,200
ds_read_b64_tr_b16 a[170:171], v1 offset:45056
buffer_load_dwordx4 v25, s[28:31], s72 offen offset:128 lds
Mvav 80,144,208
s_waitcnt lgkmcnt(6)
s_add_u32 m0, s70, 147456
s_add_u32 s72, s67, 1441792
ds_read_b64_tr_b16 a[176:177], v1 offset:49152
Mvav 80,152,216
ds_read_b64_tr_b16 a[178:179], v1 offset:53248
buffer_load_dwordx4 v25, s[28:31], s72 offen lds
Mvav 84,132,192
s_add_u32 m0, s70, 148352
s_add_u32 s72, s67, 1441792
ds_read_b64_tr_b16 a[184:185], v1 offset:57344
Mvav 84,140,200
s_waitcnt lgkmcnt(6)
ds_read_b64_tr_b16 a[186:187], v1 offset:61440
buffer_load_dwordx4 v25, s[28:31], s72 offen offset:128 lds
Mvav 84,148,208
s_add_u32 m0, s70, 149504
s_add_u32 s72, s67, 1458176
ds_read_b64_tr_b16 a[164:165], v1 offset:32776
Mvav 84,156,216
ds_read_b64_tr_b16 a[166:167], v1 offset:36872
buffer_load_dwordx4 v25, s[28:31], s72 offen lds
Mvav 88,128,224
s_waitcnt lgkmcnt(6)
s_add_u32 m0, s70, 150400
s_add_u32 s72, s67, 1458176
ds_read_b64_tr_b16 a[172:173], v1 offset:40968
Mvav 88,136,232
ds_read_b64_tr_b16 a[174:175], v1 offset:45064
buffer_load_dwordx4 v25, s[28:31], s72 offen offset:128 lds
Mvav 88,144,240
s_add_u32 m0, s70, 16384
s_add_u32 s72, s87, 184320
ds_read_b64_tr_b16 a[180:181], v1 offset:49160
Mvav 88,152,248
s_waitcnt lgkmcnt(6)
ds_read_b64_tr_b16 a[182:183], v1 offset:53256
buffer_load_dwordx4 v0, s[48:51], s72 offen sc1 lds
Mvav 92,132,224
s_waitcnt lgkmcnt(6)
s_lshr_b32 s58, s60, 3
ds_read_b64_tr_b16 a[188:189], v1 offset:57352
v_mov_b32 v20, s74
Mvav 92,140,232
ds_read_b64_tr_b16 a[190:191], v1 offset:61448
buffer_load_dwordx4 v0, s[48:51], s72 offen offset:1024 sc1 lds
Mvav 92,148,240
s_waitcnt lgkmcnt(6)
s_lshl2_add_u32 s58, s58, s57
ds_read_b128 v[160:163], v0 offset:53248
v_mov_b32 v21, s75
Mvav 92,156,248
ds_read_b128 v[168:171], v0 offset:54272
buffer_load_dwordx4 v0, s[48:51], s72 offen offset:2048 sc1 lds
Mvav 64,160,128
s_waitcnt lgkmcnt(6)
s_lshl_b32 s59, s58, 7
ds_read_b128 v[176:179], v0 offset:55296
v_mov_b32 v22, 0x80000000
Mvav 64,168,136
ds_read_b128 v[184:187], v0 offset:56320
buffer_load_dwordx4 v0, s[48:51], s72 offen offset:3072 sc1 lds
Mvav 64,176,144
s_waitcnt lgkmcnt(6)
s_lshl_b32 s36, s91, 17
ds_read_b128 v[192:195], v0 offset:57344
v_writelane_b32 v22, s98, 0
Mvav 64,184,152
s_add_u32 s36, s36, s59
ds_read_b128 v[200:203], v0 offset:58368
v_writelane_b32 v22, s35, 1
Mvav 68,164,128
s_waitcnt lgkmcnt(6)
s_mul_i32 s37, s58, 14336
s_lshl_b32 s59, s91, 7
ds_read_b128 v[208:211], v0 offset:59392
Mvav 68,172,136
s_waitcnt lgkmcnt(6)
s_add_u32 s37, s37, s59
s_add_u32 s37, s37, 2097152
ds_read_b128 v[216:219], v0 offset:60416
Mvav 68,180,144
s_cmp_eq_u32 s61, 0
s_cselect_b32 s36, 0x80000000, s36
ds_read_b128 v[224:227], v0 offset:61440
Mvav 68,188,152
s_cmp_eq_u32 s61, 0
s_cselect_b32 s37, 0x80000000, s37
ds_read_b128 v[232:235], v0 offset:62464
Mvav 72,160,160
s_waitcnt lgkmcnt(8)
ds_read_b128 v[240:243], v0 offset:63488
ds_read_b128 v[248:251], v0 offset:64512
Mvav 72,168,168
s_waitcnt vmcnt(14) lgkmcnt(7)
s_barrier
Mvav 72,176,176
s_waitcnt lgkmcnt(6)
s_add_u32 m0, s70, 98304
s_add_u32 s72, s67, 1572864
ds_read_b128 v[128:131], v0 offset:0
buffer_store_dwordx2 v[20:21], v22, s[48:51], 0 offen sc1
Mvav 72,184,184
s_waitcnt lgkmcnt(6)
ds_read_b128 v[136:139], v0 offset:1024
buffer_load_dwordx4 v25, s[28:31], s72 offen lds
Mvav 76,164,160
s_waitcnt lgkmcnt(6)
s_add_u32 m0, s70, 99200
s_add_u32 s72, s67, 1572864
ds_read_b128 v[144:147], v0 offset:2048
Mvav 76,172,168
ds_read_b128 v[152:155], v0 offset:3072
buffer_load_dwordx4 v25, s[28:31], s72 offen offset:128 lds
Mvav 76,180,176
s_add_u32 m0, s70, 100352
s_add_u32 s72, s67, 1589248
ds_read_b64_tr_b16 a[192:193], v1 offset:0
Mvav 76,188,184
s_waitcnt lgkmcnt(6)
ds_read_b64_tr_b16 a[194:195], v1 offset:4096
buffer_load_dwordx4 v25, s[28:31], s72 offen lds
Mvav 80,160,192
s_add_u32 m0, s70, 101248
s_add_u32 s72, s67, 1589248
ds_read_b64_tr_b16 a[200:201], v1 offset:8192
Mvav 80,168,200
ds_read_b64_tr_b16 a[202:203], v1 offset:12288
buffer_load_dwordx4 v25, s[28:31], s72 offen offset:128 lds
Mvav 80,176,208
s_waitcnt lgkmcnt(6)
s_add_u32 m0, s70, 114688
s_add_u32 s72, s67, 1703936
ds_read_b64_tr_b16 a[208:209], v1 offset:16384
Mvav 80,184,216
ds_read_b64_tr_b16 a[210:211], v1 offset:20480
buffer_load_dwordx4 v25, s[28:31], s72 offen lds
Mvav 84,164,192
s_add_u32 m0, s70, 115584
s_add_u32 s72, s67, 1703936
ds_read_b64_tr_b16 a[216:217], v1 offset:24576
Mvav 84,172,200
s_waitcnt lgkmcnt(6)
ds_read_b64_tr_b16 a[218:219], v1 offset:28672
buffer_load_dwordx4 v25, s[28:31], s72 offen offset:128 lds
Mvav 84,180,208
s_add_u32 m0, s70, 116736
s_add_u32 s72, s67, 1720320
ds_read_b64_tr_b16 a[196:197], v1 offset:8
Mvav 84,188,216
ds_read_b64_tr_b16 a[198:199], v1 offset:4104
buffer_load_dwordx4 v25, s[28:31], s72 offen lds
Mvav 88,160,224
s_add_u32 m0, s70, 117632
s_add_u32 s72, s67, 1720320
ds_read_b64_tr_b16 a[204:205], v1 offset:8200
Mvav 88,168,232
ds_read_b64_tr_b16 a[206:207], v1 offset:12296
buffer_load_dwordx4 v25, s[28:31], s72 offen offset:128 lds
Mvav 88,176,240
s_add_u32 m0, s70, 32768
s_add_u32 s72, s87, 221184
ds_read_b64_tr_b16 a[212:213], v1 offset:16392
Mvav 88,184,248
ds_read_b64_tr_b16 a[214:215], v1 offset:20488
buffer_load_dwordx4 v0, s[48:51], s72 offen sc1 lds
Mvav 92,164,224
s_cmp_eq_u32 s61, 0
s_cselect_b64 s[38:39], 0, -1
ds_read_b64_tr_b16 a[220:221], v1 offset:24584
Mvav 92,172,232
ds_read_b64_tr_b16 a[222:223], v1 offset:28680
buffer_load_dwordx4 v0, s[48:51], s72 offen offset:1024 sc1 lds
Mvav 92,180,240
s_waitcnt lgkmcnt(12)
ds_read_b128 v[160:163], v0 offset:4096
Mvav 92,188,248
ds_read_b128 v[168:171], v0 offset:5120
buffer_load_dwordx4 v0, s[48:51], s72 offen offset:2048 sc1 lds
Mvav 64,192,128
s_waitcnt lgkmcnt(11)
ds_read_b128 v[176:179], v0 offset:6144
Mvav 64,200,136
ds_read_b128 v[184:187], v0 offset:7168
buffer_load_dwordx4 v0, s[48:51], s72 offen offset:3072 sc1 lds
Mvav 64,208,144
s_waitcnt lgkmcnt(12)
ds_read_b128 v[192:195], v0 offset:8192
Mvav 64,216,152
s_waitcnt lgkmcnt(11)
ds_read_b128 v[200:203], v0 offset:9216
Mvav 68,196,128
s_waitcnt lgkmcnt(10)
ds_read_b128 v[208:211], v0 offset:10240
Mvav 68,204,136
s_waitcnt lgkmcnt(9)
ds_read_b128 v[216:219], v0 offset:11264
Mvav 68,212,144
s_waitcnt lgkmcnt(8)
ds_read_b128 v[224:227], v0 offset:12288
Mvav 68,220,152
s_waitcnt lgkmcnt(8)
ds_read_b128 v[232:235], v0 offset:13312
Mvav 72,192,160
s_waitcnt lgkmcnt(8)
ds_read_b128 v[240:243], v0 offset:14336
ds_read_b128 v[248:251], v0 offset:15360
Mvav 72,200,168
s_waitcnt vmcnt(13) lgkmcnt(7)
s_barrier
Mvav 72,208,176
s_waitcnt lgkmcnt(6)
s_add_u32 m0, s70, 65536
s_add_u32 s72, s67, 1835008
ds_read_b128 v[128:131], v0 offset:16384
Mvav 72,216,184
s_waitcnt lgkmcnt(6)
ds_read_b128 v[136:139], v0 offset:17408
buffer_load_dwordx4 v25, s[28:31], s72 offen lds
Mvav 76,196,160
s_waitcnt lgkmcnt(6)
s_add_u32 m0, s70, 66432
s_add_u32 s72, s67, 1835008
ds_read_b128 v[144:147], v0 offset:18432
Mvav 76,204,168
ds_read_b128 v[152:155], v0 offset:19456
buffer_load_dwordx4 v25, s[28:31], s72 offen offset:128 lds
Mvav 76,212,176
s_add_u32 m0, s70, 67584
s_add_u32 s72, s67, 1851392
ds_read_b64_tr_b16 a[224:225], v3 offset:0
Mvav 76,220,184
s_waitcnt lgkmcnt(6)
ds_read_b64_tr_b16 a[226:227], v3 offset:4096
buffer_load_dwordx4 v25, s[28:31], s72 offen lds
Mvav 80,192,192
s_add_u32 m0, s70, 68480
s_add_u32 s72, s67, 1851392
ds_read_b64_tr_b16 a[232:233], v3 offset:8192
Mvav 80,200,200
ds_read_b64_tr_b16 a[234:235], v3 offset:12288
buffer_load_dwordx4 v25, s[28:31], s72 offen offset:128 lds
Mvav 80,208,208
s_waitcnt lgkmcnt(6)
s_add_u32 m0, s70, 81920
s_add_u32 s72, s67, 1966080
ds_read_b64_tr_b16 a[240:241], v3 offset:16384
Mvav 80,216,216
ds_read_b64_tr_b16 a[242:243], v3 offset:20480
buffer_load_dwordx4 v25, s[28:31], s72 offen lds
Mvav 84,196,192
s_add_u32 m0, s70, 82816
s_add_u32 s72, s67, 1966080
ds_read_b64_tr_b16 a[248:249], v3 offset:24576
Mvav 84,204,200
s_waitcnt lgkmcnt(6)
ds_read_b64_tr_b16 a[250:251], v3 offset:28672
buffer_load_dwordx4 v25, s[28:31], s72 offen offset:128 lds
Mvav 84,212,208
s_add_u32 m0, s70, 83968
s_add_u32 s72, s67, 1982464
ds_read_b64_tr_b16 a[228:229], v3 offset:8
Mvav 84,220,216
ds_read_b64_tr_b16 a[230:231], v3 offset:4104
buffer_load_dwordx4 v25, s[28:31], s72 offen lds
Mvav 88,192,224
s_add_u32 m0, s70, 84864
s_add_u32 s72, s67, 1982464
ds_read_b64_tr_b16 a[236:237], v3 offset:8200
Mvav 88,200,232
ds_read_b64_tr_b16 a[238:239], v3 offset:12296
buffer_load_dwordx4 v25, s[28:31], s72 offen offset:128 lds
Mvav 88,208,240
s_add_u32 m0, s70, 49152
s_add_u32 s72, s87, 258048
ds_read_b64_tr_b16 a[244:245], v3 offset:16392
Mvav 88,216,248
ds_read_b64_tr_b16 a[246:247], v3 offset:20488
buffer_load_dwordx4 v0, s[48:51], s72 offen sc1 lds
Mvav 92,196,224
ds_read_b64_tr_b16 a[252:253], v3 offset:24584
Mvav 92,204,232
ds_read_b64_tr_b16 a[254:255], v3 offset:28680
buffer_load_dwordx4 v0, s[48:51], s72 offen offset:1024 sc1 lds
Mvav 92,212,240
s_waitcnt lgkmcnt(12)
ds_read_b128 v[160:163], v0 offset:20480
Mvav 92,220,248
ds_read_b128 v[168:171], v0 offset:21504
buffer_load_dwordx4 v0, s[48:51], s72 offen offset:2048 sc1 lds
Mvav 64,224,128
s_waitcnt lgkmcnt(11)
ds_read_b128 v[176:179], v0 offset:22528
Mvav 64,232,136
ds_read_b128 v[184:187], v0 offset:23552
buffer_load_dwordx4 v0, s[48:51], s72 offen offset:3072 sc1 lds
Mvav 64,240,144
s_waitcnt lgkmcnt(12)
ds_read_b128 v[192:195], v0 offset:24576
Mvav 64,248,152
s_waitcnt lgkmcnt(11)
ds_read_b128 v[200:203], v0 offset:25600
Mvav 68,228,128
s_waitcnt lgkmcnt(10)
ds_read_b128 v[208:211], v0 offset:26624
Mvav 68,236,136
s_waitcnt lgkmcnt(9)
ds_read_b128 v[216:219], v0 offset:27648
Mvav 68,244,144
s_waitcnt lgkmcnt(8)
ds_read_b128 v[224:227], v0 offset:28672
Mvav 68,252,152
s_waitcnt lgkmcnt(8)
ds_read_b128 v[232:235], v0 offset:29696
Mvav 72,224,160
s_waitcnt lgkmcnt(8)
ds_read_b128 v[240:243], v0 offset:30720
ds_read_b128 v[248:251], v0 offset:31744
Mvav 72,232,168
s_waitcnt vmcnt(12) lgkmcnt(8)
s_barrier
Mvav 72,240,176
s_add_u32 m0, s69, 131072
s_add_u32 s72, s12, 4718592
ds_read_b128 v[128:131], v0 offset:32768
Mvav 72,248,184
ds_read_b128 v[136:139], v0 offset:33792
buffer_load_dwordx4 v0, s[48:51], s72 offen sc1 lds
)"
R"(
Mvav 76,228,160
ds_read_b128 v[144:147], v0 offset:34816
Mvav 76,236,168
ds_read_b128 v[152:155], v0 offset:35840
buffer_load_dwordx4 v0, s[48:51], s72 offen offset:1024 sc1 lds
Mvav 76,244,176
s_waitcnt lgkmcnt(11)
ds_read_b64_tr_b16 a[0:1], v1 offset:32768
Mvav 76,252,184
ds_read_b64_tr_b16 a[2:3], v1 offset:36864
buffer_load_dwordx4 v0, s[48:51], s72 offen offset:2048 sc1 lds
Mvav 80,224,192
s_waitcnt lgkmcnt(10)
ds_read_b64_tr_b16 a[8:9], v1 offset:40960
Mvav 80,232,200
ds_read_b64_tr_b16 a[10:11], v1 offset:45056
buffer_load_dwordx4 v0, s[48:51], s72 offen offset:3072 sc1 lds
Mvav 80,240,208
s_add_u32 m0, s69, 135168
s_add_u32 s72, s12, 4755456
ds_read_b64_tr_b16 a[16:17], v1 offset:49152
Mvav 80,248,216
ds_read_b64_tr_b16 a[18:19], v1 offset:53248
buffer_load_dwordx4 v0, s[48:51], s72 offen sc1 lds
Mvav 84,228,192
s_waitcnt lgkmcnt(11)
ds_read_b64_tr_b16 a[24:25], v1 offset:57344
Mvav 84,236,200
ds_read_b64_tr_b16 a[26:27], v1 offset:61440
buffer_load_dwordx4 v0, s[48:51], s72 offen offset:1024 sc1 lds
Mvav 84,244,208
ds_read_b64_tr_b16 a[4:5], v1 offset:32776
Mvav 84,252,216
ds_read_b64_tr_b16 a[6:7], v1 offset:36872
buffer_load_dwordx4 v0, s[48:51], s72 offen offset:2048 sc1 lds
Mvav 88,224,224
s_waitcnt lgkmcnt(12)
ds_read_b64_tr_b16 a[12:13], v1 offset:40968
Mvav 88,232,232
ds_read_b64_tr_b16 a[14:15], v1 offset:45064
buffer_load_dwordx4 v0, s[48:51], s72 offen offset:3072 sc1 lds
Mvav 88,240,240
ds_read_b64_tr_b16 a[20:21], v1 offset:49160
Mvav 88,248,248
s_waitcnt lgkmcnt(12)
ds_read_b64_tr_b16 a[22:23], v1 offset:53256
Mvav 92,228,224
ds_read_b64_tr_b16 a[28:29], v1 offset:57352
Mvav 92,236,232
ds_read_b64_tr_b16 a[30:31], v1 offset:61448
Mvav 92,244,240
s_waitcnt lgkmcnt(12)
ds_read_b128 v[160:163], v0 offset:36864
Mvav 92,252,248
ds_read_b128 v[168:171], v0 offset:37888
Mvav 64,0,128
ds_read_b128 v[176:179], v0 offset:38912
Mvav 64,8,136
s_waitcnt lgkmcnt(12)
ds_read_b128 v[184:187], v0 offset:39936
Mvav 64,16,144
s_waitcnt lgkmcnt(12)
ds_read_b128 v[192:195], v0 offset:40960
Mvav 64,24,152
s_waitcnt lgkmcnt(11)
ds_read_b128 v[200:203], v0 offset:41984
Mvav 68,4,128
s_waitcnt lgkmcnt(10)
ds_read_b128 v[208:211], v0 offset:43008
Mvav 68,12,136
s_waitcnt lgkmcnt(9)
ds_read_b128 v[216:219], v0 offset:44032
Mvav 68,20,144
s_waitcnt lgkmcnt(8)
ds_read_b128 v[224:227], v0 offset:45056
Mvav 68,28,152
s_waitcnt lgkmcnt(8)
ds_read_b128 v[232:235], v0 offset:46080
Mvav 72,0,160
s_waitcnt lgkmcnt(8)
ds_read_b128 v[240:243], v0 offset:47104
ds_read_b128 v[248:251], v0 offset:48128
Mvav 72,8,168
s_waitcnt vmcnt(8) lgkmcnt(8)
s_barrier
Mvav 72,16,176
s_add_u32 m0, s69, 98304
s_add_u32 s72, s13, 4718592
ds_read_b128 v[128:131], v0 offset:49152
Mvav 72,24,184
ds_read_b128 v[136:139], v0 offset:50176
buffer_load_dwordx4 v0, s[48:51], s72 offen sc1 lds
Mvav 76,4,160
ds_read_b128 v[144:147], v0 offset:51200
Mvav 76,12,168
ds_read_b128 v[152:155], v0 offset:52224
Mvav 76,20,176
s_waitcnt lgkmcnt(11)
ds_read_b64_tr_b16 a[32:33], v1 offset:0
Mvav 76,28,184
ds_read_b64_tr_b16 a[34:35], v1 offset:4096
buffer_load_dwordx4 v0, s[48:51], s72 offen offset:1024 sc1 lds
Mvav 80,0,192
s_waitcnt lgkmcnt(12)
ds_read_b64_tr_b16 a[40:41], v1 offset:8192
Mvav 80,8,200
s_waitcnt lgkmcnt(12)
ds_read_b64_tr_b16 a[42:43], v1 offset:12288
Mvav 80,16,208
s_waitcnt lgkmcnt(12)
ds_read_b64_tr_b16 a[48:49], v1 offset:16384
Mvav 80,24,216
ds_read_b64_tr_b16 a[50:51], v1 offset:20480
buffer_load_dwordx4 v0, s[48:51], s72 offen offset:2048 sc1 lds
Mvav 84,4,192
ds_read_b64_tr_b16 a[56:57], v1 offset:24576
Mvav 84,12,200
s_waitcnt lgkmcnt(12)
ds_read_b64_tr_b16 a[58:59], v1 offset:28672
Mvav 84,20,208
ds_read_b64_tr_b16 a[36:37], v1 offset:8
Mvav 84,28,216
ds_read_b64_tr_b16 a[38:39], v1 offset:4104
buffer_load_dwordx4 v0, s[48:51], s72 offen offset:3072 sc1 lds
Mvav 88,0,224
s_waitcnt lgkmcnt(12)
ds_read_b64_tr_b16 a[44:45], v1 offset:8200
Mvav 88,8,232
s_waitcnt lgkmcnt(10)
ds_read_b64_tr_b16 a[46:47], v1 offset:12296
Mvav 88,16,240
s_add_u32 m0, s69, 102400
s_add_u32 s72, s13, 4755456
ds_read_b64_tr_b16 a[52:53], v1 offset:16392
Mvav 88,24,248
ds_read_b64_tr_b16 a[54:55], v1 offset:20488
buffer_load_dwordx4 v0, s[48:51], s72 offen sc1 lds
Mvav 92,4,224
ds_read_b64_tr_b16 a[60:61], v1 offset:24584
Mvav 92,12,232
ds_read_b64_tr_b16 a[62:63], v1 offset:28680
Mvav 92,20,240
s_waitcnt lgkmcnt(12)
ds_read_b128 v[160:163], v0 offset:53248
Mvav 92,28,248
ds_read_b128 v[168:171], v0 offset:54272
buffer_load_dwordx4 v0, s[48:51], s72 offen offset:1024 sc1 lds
Mvav 64,32,128
s_add_u32 s88, s69, 0
ds_read_b128 v[176:179], v0 offset:55296
v_bfe_u32 v10, v0, 6, 2
Mvav 64,40,136
s_waitcnt lgkmcnt(12)
ds_read_b128 v[184:187], v0 offset:56320
v_lshlrev_b32 v10, 6, v10
Mvav 64,48,144
s_waitcnt lgkmcnt(10)
ds_read_b128 v[192:195], v0 offset:57344
v_bfe_u32 v58, v0, 4, 2
Mvav 64,56,152
ds_read_b128 v[200:203], v0 offset:58368
buffer_load_dwordx4 v0, s[48:51], s72 offen offset:2048 sc1 lds
Mvav 68,36,128
s_waitcnt lgkmcnt(10)
ds_read_b128 v[208:211], v0 offset:59392
v_lshl_add_u32 v10, v58, 3, v10
Mvav 68,44,136
s_waitcnt lgkmcnt(9)
ds_read_b128 v[216:219], v0 offset:60416
v_lshrrev_b32 v58, 8, v0
Mvav 68,52,144
s_waitcnt lgkmcnt(7)
ds_read_b128 v[224:227], v0 offset:61440
v_lshl_add_u32 v10, v58, 10, v10
Mvav 68,60,152
ds_read_b128 v[232:235], v0 offset:62464
buffer_load_dwordx4 v0, s[48:51], s72 offen offset:3072 sc1 lds
Mvav 72,32,160
s_waitcnt lgkmcnt(8)
ds_read_b128 v[240:243], v0 offset:63488
ds_read_b128 v[248:251], v0 offset:64512
Mvav 72,40,168
s_waitcnt vmcnt(8) lgkmcnt(8)
s_barrier
Mvav 72,48,176
s_add_u32 m0, s69, 0
s_add_u32 s72, s36, 0
v_add_u32 v10, s88, v10
Mvav 72,56,184
buffer_load_dwordx4 v25, s[44:47], s72 offen lds
Mvav 76,36,160
Mvav 76,44,168
Mvav 76,52,176
Mvav 76,60,184
s_waitcnt lgkmcnt(6)
s_add_u32 m0, s69, 1024
s_add_u32 s72, s36, 16384
Mvav 80,32,192
ds_read_b128 v[128:131], v14 offset:0
buffer_load_dwordx4 v25, s[44:47], s72 offen lds
Mvav 80,40,200
s_waitcnt lgkmcnt(6)
ds_read_b128 v[136:139], v14 offset:1024
Mvav 80,48,208
s_waitcnt lgkmcnt(6)
buffer_load_dwordx4 v[16:19], v0, s[48:51], s83 offen sc1
Mvav 80,56,216
ds_read_b128 v[144:147], v14 offset:2048
Mvav 84,36,192
s_add_u32 m0, s69, 2048
s_add_u32 s72, s36, 32768
buffer_load_dwordx4 v[40:43], v11, s[48:51], s24 offen sc1
Mvav 84,44,200
ds_read_b128 v[152:155], v14 offset:3072
buffer_load_dwordx4 v25, s[44:47], s72 offen lds
Mvav 84,52,208
s_waitcnt lgkmcnt(6)
buffer_load_dwordx4 v[44:47], v11, s[48:51], s24 offen offset:1024 sc1
Mvav 84,60,216
ds_read_b128 v[160:163], v14 offset:4096
buffer_load_dwordx4 v[48:51], v11, s[48:51], s24 offen offset:2048 sc1
Mvav 88,32,224
ds_read_b128 v[168:171], v14 offset:5120
buffer_load_dwordx4 v[52:55], v11, s[48:51], s24 offen offset:3072 sc1
Mvav 88,40,232
s_waitcnt lgkmcnt(6)
s_add_u32 m0, s69, 3072
s_add_u32 s72, s36, 49152
Mvav 88,48,240
ds_read_b128 v[176:179], v14 offset:6144
buffer_load_dwordx4 v25, s[44:47], s72 offen lds
Mvav 88,56,248
Mvav 92,36,224
ds_read_b128 v[184:187], v14 offset:7168
Mvav 92,44,232
s_waitcnt lgkmcnt(7)
Mvav 92,52,240
s_add_u32 m0, s69, 4096
s_add_u32 s72, s36, 65536
ds_read_b128 v[192:195], v14 offset:8192
Mvav 92,60,248
ds_read_b128 v[200:203], v14 offset:9216
buffer_load_dwordx4 v25, s[44:47], s72 offen lds
Zvav 96,64,128
s_waitcnt lgkmcnt(6)
Mvav 96,72,136
s_lshl_b32 s58, s76, 17
ds_read_b128 v[208:211], v14 offset:10240
v_mov_b32 v2, 0xffffff00
Mvav 96,80,144
s_add_u32 s58, s58, s26
v_cvt_pk_bf16_f32 v64, v64, v65
v_cvt_pk_bf16_f32 v65, v66, v67
Mvav 96,88,152
s_add_u32 m0, s69, 5120
s_add_u32 s72, s36, 81920
ds_read_b128 v[216:219], v14 offset:11264
Zvav 100,68,128
s_add_u32 s58, s58, 0
buffer_load_dwordx4 v25, s[44:47], s72 offen lds
Mvav 100,76,136
s_add_u32 s58, s58, s32
ds_read_b128 v[224:227], v14 offset:12288
v_cvt_pk_bf16_f32 v66, v68, v69
Mvav 100,84,144
ds_read_b128 v[232:235], v14 offset:13312
v_cvt_pk_bf16_f32 v67, v70, v71
Mvav 100,92,152
s_waitcnt lgkmcnt(8)
buffer_store_dwordx4 v[64:67], v0, s[48:51], s58 offen sc1
Mvav 96,96,160
s_add_u32 m0, s69, 6144
s_add_u32 s72, s36, 98304
s_add_u32 s62, s71, 4194304
ds_read_b128 v[240:243], v14 offset:14336
buffer_load_dwordx4 v[132:135], v5, s[48:51], s62 offen sc1
Mvav 96,104,168
s_waitcnt lgkmcnt(7)
buffer_load_dwordx4 v25, s[44:47], s72 offen lds
Mvav 96,112,176
s_add_u32 s62, s71, 5308416
ds_read_b128 v[248:251], v14 offset:15360
buffer_load_dwordx4 v[140:143], v5, s[48:51], s62 offen sc1
Mvav 96,120,184
s_add_u32 s62, s71, 6422528
buffer_load_dwordx4 v[180:183], v5, s[48:51], s62 offen sc1
Mvav 100,100,160
s_add_u32 s62, s71, 7536640
ds_read_b128 v[128:131], v14 offset:16384
buffer_load_dwordx4 v[188:191], v5, s[48:51], s62 offen sc1
Mvav 100,108,168
s_add_u32 m0, s69, 7168
s_add_u32 s72, s36, 114688
ds_read_b128 v[136:139], v14 offset:17408
Mvav 100,116,176
buffer_load_dwordx4 v25, s[44:47], s72 offen lds
Mvav 100,124,184
s_waitcnt lgkmcnt(7)
ds_read_b128 v[144:147], v14 offset:18432
Mvav 96,128,192
s_lshl_b32 s58, s77, 17
v_cvt_pk_bf16_f32 v72, v72, v73
v_cvt_pk_bf16_f32 v73, v74, v75
Mvav 96,136,200
s_add_u32 s58, s58, s26
ds_read_b128 v[152:155], v14 offset:19456
v_cvt_pk_bf16_f32 v74, v76, v77
Mvav 96,144,208
s_waitcnt lgkmcnt(8)
s_add_u32 s58, s58, 0
v_cvt_pk_bf16_f32 v75, v78, v79
Mvav 96,152,216
s_add_u32 s58, s58, s32
ds_read_b128 v[160:163], v14 offset:20480
Mvav 100,132,192
ds_read_b128 v[168:171], v14 offset:21504
Mvav 100,140,200
buffer_store_dwordx4 v[72:75], v0, s[48:51], s58 offen sc1
Mvav 100,148,208
s_waitcnt vmcnt(16)
ds_read_b128 v[176:179], v14 offset:22528
v_xor_b32 v16, s74, v16
Mvav 100,156,216
s_waitcnt lgkmcnt(10)
v_bitop3_b32 v16, v17, s75, v16 bitop3:0xbe
Mvav 96,160,224
s_waitcnt lgkmcnt(9)
ds_read_b128 v[184:187], v14 offset:23552
v_bitop3_b32 v16, v18, s74, v16 bitop3:0xbe
Mvav 96,168,232
s_waitcnt lgkmcnt(9)
v_bitop3_b32 v16, v19, s75, v16 bitop3:0xbe
Mvav 96,176,240
s_waitcnt lgkmcnt(8)
ds_read_b128 v[192:195], v14 offset:24576
v_cmp_ne_u32 vcc, 0, v16
Mvav 96,184,248
s_cbranch_vccnz .Lcf3
.Lcf3_r:
ds_read_b128 v[200:203], v14 offset:25600
Mvav 100,164,224
Mvav 100,172,232
ds_read_b128 v[208:211], v14 offset:26624
Mvav 100,180,240
s_add_u32 m0, s69, 65536
Mvav 100,188,248
s_waitcnt lgkmcnt(7)
s_add_u32 s72, s14, 4718592
ds_read_b128 v[216:219], v14 offset:27648
Mvav 96,192,128
s_lshl_b32 s58, s78, 17
buffer_load_dwordx4 v0, s[48:51], s72 offen sc1 lds
Mvav 96,200,136
s_add_u32 s58, s58, s26
ds_read_b128 v[224:227], v14 offset:28672
v_cvt_pk_bf16_f32 v80, v80, v81
Mvav 96,208,144
s_add_u32 s58, s58, 0
ds_read_b128 v[232:235], v14 offset:29696
v_cvt_pk_bf16_f32 v81, v82, v83
Mvav 96,216,152
s_add_u32 s58, s58, s32
buffer_load_dwordx4 v0, s[48:51], s72 offen offset:1024 sc1 lds
Mvav 100,196,128
ds_read_b128 v[240:243], v14 offset:30720
v_cvt_pk_bf16_f32 v82, v84, v85
Mvav 100,204,136
s_waitcnt vmcnt(17)
v_cvt_pk_bf16_f32 v83, v86, v87
v_xor_b32 v40, s20, v40
Mvav 100,212,144
ds_read_b128 v[248:251], v14 offset:31744
v_xor_b32 v41, s21, v41
Mvav 100,220,152
s_waitcnt lgkmcnt(11)
buffer_load_dwordx4 v0, s[48:51], s72 offen offset:2048 sc1 lds
Mvav 96,224,160
s_waitcnt lgkmcnt(10)
buffer_store_dwordx4 v[80:83], v0, s[48:51], s58 offen sc1
Mvav 96,232,168
s_waitcnt lgkmcnt(9)
v_xor_b32 v42, s22, v42
v_xor_b32 v43, s23, v43
Mvav 96,240,176
s_waitcnt lgkmcnt(8)
buffer_load_dwordx4 v0, s[48:51], s72 offen offset:3072 sc1 lds
Mvav 96,248,184
s_waitcnt vmcnt(18)
v_xor_b32 v44, s20, v44
v_xor_b32 v45, s21, v45
Mvav 100,228,160
v_xor_b32 v46, s22, v46
v_xor_b32 v47, s23, v47
Mvav 100,236,168
s_waitcnt vmcnt(17)
v_xor_b32 v48, s20, v48
v_xor_b32 v49, s21, v49
Mvav 100,244,176
s_waitcnt lgkmcnt(0)
v_xor_b32 v50, s22, v50
v_xor_b32 v51, s23, v51
Mvav 100,252,184
s_barrier
Mvav 96,0,192
s_waitcnt vmcnt(16)
s_add_u32 m0, s69, 69632
ds_read_b128 v[128:131], v12 offset:32768
v_xor_b32 v52, s20, v52
Mvav 96,8,200
s_add_u32 s72, s14, 4755456
ds_read_b128 v[136:139], v12 offset:33792
v_xor_b32 v53, s21, v53
Mvav 96,16,208
buffer_load_dwordx4 v0, s[48:51], s72 offen sc1 lds
Mvav 96,24,216
ds_read_b128 v[144:147], v12 offset:34816
)"
R"(
v_xor_b32 v54, s22, v54
Mvav 100,4,192
v_xor_b32 v55, s23, v55
v_or3_b32 v56, v40, v41, v42
Mvav 100,12,200
ds_read_b128 v[152:155], v12 offset:35840
v_or3_b32 v56, v56, v43, v44
Mvav 100,20,208
buffer_load_dwordx4 v0, s[48:51], s72 offen offset:1024 sc1 lds
Mvav 100,28,216
ds_read_b128 v[160:163], v12 offset:36864
v_or3_b32 v56, v56, v45, v46
Mvav 96,32,224
ds_read_b128 v[168:171], v12 offset:37888
v_or3_b32 v56, v56, v47, v48
Mvav 96,40,232
buffer_load_dwordx4 v0, s[48:51], s72 offen offset:2048 sc1 lds
Mvav 96,48,240
ds_read_b128 v[176:179], v12 offset:38912
v_or3_b32 v56, v56, v49, v50
Mvav 96,56,248
v_or3_b32 v56, v56, v51, v52
v_or3_b32 v56, v56, v53, v54
Mvav 100,36,224
ds_read_b128 v[184:187], v12 offset:39936
v_or_b32 v56, v56, v55
Mvav 100,44,232
buffer_load_dwordx4 v0, s[48:51], s72 offen offset:3072 sc1 lds
Mvav 100,52,240
ds_read_b128 v[192:195], v12 offset:40960
v_and_b32 v56, 0xff, v56
Mvav 100,60,248
s_waitcnt lgkmcnt(7)
ds_read_b128 v[200:203], v12 offset:41984
v_cmp_ne_u32 vcc, 0, v56
Zvav 104,64,128
s_add_u32 m0, s69, 32768
v_mov_b32 v62, s74
v_mov_b32 v63, s75
Mvav 104,72,136
s_waitcnt vmcnt(15) lgkmcnt(6)
s_add_u32 s72, s12, 9437184
ds_read_b128 v[208:211], v12 offset:43008
v_lshlrev_b32 v64, 16, v132
Mvav 104,80,144
s_and_b64 vcc, vcc, s[38:39]
buffer_load_dwordx4 v0, s[48:51], s72 offen sc1 lds
Mvav 104,88,152
ds_read_b128 v[216:219], v12 offset:44032
v_mul_f32 v24, 0xbfb8aa3b, v64
Zvav 108,68,128
s_cbranch_vccnz .Lgu
.Lgu_r:
s_lshl_b32 s59, s57, 5
Mvav 108,76,136
ds_read_b128 v[224:227], v12 offset:45056
v_add_f32 v40, v40, v44
Mvav 108,84,144
ds_read_b128 v[232:235], v12 offset:46080
v_add_f32 v41, v41, v45
Mvav 108,92,152
s_waitcnt lgkmcnt(6)
buffer_load_dwordx4 v0, s[48:51], s72 offen offset:1024 sc1 lds
Mvav 104,96,160
s_lshl_b32 s58, s76, 17
ds_read_b128 v[240:243], v12 offset:47104
v_cvt_pk_bf16_f32 v96, v96, v97
Mvav 104,104,168
s_add_u32 s58, s58, s26
v_exp_f32 v24, v24
Mvav 104,112,176
s_add_u32 s58, s58, 32768
ds_read_b128 v[248:251], v12 offset:48128
v_cvt_pk_bf16_f32 v97, v98, v99
Mvav 104,120,184
s_add_u32 s58, s58, s32
buffer_load_dwordx4 v0, s[48:51], s72 offen offset:2048 sc1 lds
Mvav 108,100,160
ds_read_b128 v[128:131], v12 offset:49152
v_cvt_pk_bf16_f32 v98, v100, v101
Mvav 108,108,168
ds_read_b128 v[136:139], v12 offset:50176
v_cvt_pk_bf16_f32 v99, v102, v103
Mvav 108,116,176
buffer_load_dwordx4 v0, s[48:51], s72 offen offset:3072 sc1 lds
Mvav 108,124,184
s_waitcnt lgkmcnt(9)
ds_read_b128 v[144:147], v12 offset:51200
v_add_f32 v42, v42, v46
Mvav 104,128,192
s_waitcnt lgkmcnt(9)
buffer_store_dwordx4 v[96:99], v0, s[48:51], s58 offen sc1
Mvav 104,136,200
s_waitcnt lgkmcnt(8)
ds_read_b128 v[152:155], v12 offset:52224
v_add_f32 v43, v43, v47
Mvav 104,144,208
s_waitcnt lgkmcnt(8)
v_add_f32 v48, v48, v52
v_add_f32 v49, v49, v53
Mvav 104,152,216
s_add_u32 m0, s69, 36864
ds_read_b128 v[160:163], v12 offset:53248
v_add_f32 v50, v50, v54
Mvav 108,132,192
s_add_u32 s72, s12, 9474048
ds_read_b128 v[168:171], v12 offset:54272
v_add_f32 v51, v51, v55
Mvav 108,140,200
buffer_load_dwordx4 v0, s[48:51], s72 offen sc1 lds
Mvav 108,148,208
ds_read_b128 v[176:179], v12 offset:55296
v_add_f32 v40, v40, v48
Mvav 108,156,216
s_waitcnt lgkmcnt(10)
v_add_f32 v41, v41, v49
v_add_f32 v42, v42, v50
Mvav 104,160,224
s_waitcnt lgkmcnt(9)
ds_read_b128 v[184:187], v12 offset:56320
v_add_f32 v43, v43, v51
Mvav 104,168,232
s_waitcnt lgkmcnt(9)
buffer_load_dwordx4 v0, s[48:51], s72 offen offset:1024 sc1 lds
Mvav 104,176,240
s_waitcnt vmcnt(17) lgkmcnt(8)
ds_read_b128 v[192:195], v12 offset:57344
v_permlane16_swap_b32 v40, v41
Mvav 104,184,248
ds_read_b128 v[200:203], v12 offset:58368
v_permlane16_swap_b32 v42, v43
Mvav 108,164,224
buffer_load_dwordx4 v0, s[48:51], s72 offen offset:2048 sc1 lds
Mvav 108,172,232
ds_read_b128 v[208:211], v12 offset:59392
v_add_f32 v40, v40, v41
Mvav 108,180,240
ds_read_b64_tr_b16 v[148:149], v10 offset:0
v_add_f32 v42, v42, v43
Mvav 108,188,248
s_waitcnt lgkmcnt(11)
ds_read_b128 v[216:219], v12 offset:60416
v_bfe_u32 v56, v0, 5, 3
Mvav 104,192,128
s_waitcnt lgkmcnt(11)
buffer_load_dwordx4 v0, s[48:51], s72 offen offset:3072 sc1 lds
Mvav 104,200,136
s_waitcnt lgkmcnt(10)
ds_read_b128 v[224:227], v12 offset:61440
v_permlane32_swap_b32 v40, v42
Mvav 104,208,144
s_waitcnt lgkmcnt(10)
ds_read_b128 v[232:235], v12 offset:62464
v_add_f32 v40, v40, v42
Mvav 104,216,152
s_add_u32 m0, s69, 131072
ds_read_b64_tr_b16 v[150:151], v10 offset:256
v_cvt_pk_bf16_f32 v40, 0, v40
Mvav 108,196,128
s_add_u32 s72, s13, 9437184
ds_read_b128 v[240:243], v12 offset:63488
v_lshrrev_b32 v57, 9, v0
Mvav 108,204,136
buffer_load_dwordx4 v0, s[48:51], s72 offen sc1 lds
Mvav 108,212,144
ds_read_b128 v[248:251], v12 offset:64512
v_mov_b32_dpp v41, v40 quad_perm:[1,1,3,3] row_mask:0xf bank_mask:0xf bound_ctrl:1
Mvav 108,220,152
s_waitcnt lgkmcnt(13)
ds_read_b64_tr_b16 v[156:157], v10 offset:4096
v_mov_b32_dpp v40, v40 quad_perm:[0,0,2,2] row_mask:0xf bank_mask:0xf bound_ctrl:1
Mvav 104,224,160
s_waitcnt lgkmcnt(13)
buffer_load_dwordx4 v0, s[48:51], s72 offen offset:1024 sc1 lds
Mvav 104,232,168
s_waitcnt lgkmcnt(12)
ds_read_b64_tr_b16 v[158:159], v10 offset:4352
v_mul_f32 v42, 0xbfb8aa3b, v40
Mvav 104,240,176
s_waitcnt lgkmcnt(12)
v_exp_f32 v42, v42
Mvav 104,248,184
buffer_load_dwordx4 v0, s[48:51], s72 offen offset:2048 sc1 lds
Mvav 108,228,160
ds_read_b64_tr_b16 v[164:165], v10 offset:32
v_add_f32 v42, 1.0, v42
Mvav 108,236,168
v_rcp_f32 v42, v42
Mvav 108,244,176
s_waitcnt vmcnt(12) lgkmcnt(0)
buffer_load_dwordx4 v0, s[48:51], s72 offen offset:3072 sc1 lds
Mvav 108,252,184
s_barrier
Mvav 104,0,192
ds_read_b128 v[128:131], v12 offset:0
v_mul_f32 v40, v40, v42
Mvav 104,8,200
ds_read_b128 v[136:139], v12 offset:1024
v_cvt_pk_bf16_f32 v40, 0, v40
Mvav 104,16,208
s_add_u32 m0, s69, 135168
buffer_store_dwordx2 v[62:63], v13, s[48:51], s34 offen sc1
Mvav 104,24,216
s_add_u32 s72, s13, 9474048
ds_read_b128 v[144:147], v12 offset:2048
v_mul_f32 v40, v40, v41
Mvav 108,4,192
buffer_load_dwordx4 v0, s[48:51], s72 offen sc1 lds
Mvav 108,12,200
ds_read_b128 v[152:155], v12 offset:3072
v_cvt_pk_bf16_f32 v40, v40, v40
Mvav 108,20,208
ds_read_b64_tr_b16 v[166:167], v10 offset:288
v_lshl_add_u32 v56, v56, 1, v57
Mvav 108,28,216
ds_read_b128 v[160:163], v12 offset:4096
v_bfe_u32 v57, v0, 8, 1
Mvav 104,32,224
ds_read_b128 v[168:171], v12 offset:5120
v_lshl_add_u32 v56, v57, 6, v56
Mvav 104,40,232
buffer_load_dwordx4 v0, s[48:51], s72 offen offset:1024 sc1 lds
Mvav 104,48,240
ds_read_b128 v[176:179], v12 offset:6144
v_lshl_add_u32 v56, v56, 1, s59
Mvav 104,56,248
ds_read_b64_tr_b16 v[172:173], v10 offset:4128
v_lshrrev_b32 v57, 8, v0
Mvav 108,36,224
ds_read_b128 v[184:187], v12 offset:7168
v_lshlrev_b32 v57, 4, v57
Mvav 108,44,232
buffer_load_dwordx4 v0, s[48:51], s72 offen offset:2048 sc1 lds
Mvav 108,52,240
ds_read_b128 v[192:195], v12 offset:8192
v_add_f32 v24, 1.0, v24
Mvav 108,60,248
s_waitcnt lgkmcnt(10)
ds_read_b128 v[200:203], v12 offset:9216
v_and_b32 v65, 0xffff0000, v132
Zvav 96,64,128
s_waitcnt lgkmcnt(10)
buffer_load_dwordx4 v0, s[48:51], s72 offen offset:3072 sc1 lds
Mvav 96,72,136
s_waitcnt lgkmcnt(9)
ds_read_b128 v[208:211], v12 offset:10240
v_mul_f32 v25, 0xbfb8aa3b, v65
Mvav 96,80,144
s_waitcnt lgkmcnt(9)
ds_read_b64_tr_b16 v[174:175], v10 offset:4384
v_lshlrev_b32 v66, 16, v133
Mvav 96,88,152
ds_read_b128 v[216:219], v12 offset:11264
v_mul_f32 v26, 0xbfb8aa3b, v66
Zvav 100,68,128
ds_read_b64_tr_b16 v[228:229], v10 offset:512
v_and_b32 v67, 0xffff0000, v133
Mvav 100,76,136
s_add_u32 m0, s69, 98304
ds_read_b128 v[224:227], v12 offset:12288
v_mul_f32 v27, 0xbfb8aa3b, v67
Mvav 100,84,144
s_add_u32 s72, s14, 9437184
ds_read_b128 v[232:235], v12 offset:13312
v_lshlrev_b32 v68, 16, v134
Mvav 100,92,152
s_waitcnt lgkmcnt(8)
buffer_load_dwordx4 v0, s[48:51], s72 offen sc1 lds
Mvav 96,96,160
s_lshl_b32 s58, s77, 17
ds_read_b128 v[240:243], v12 offset:14336
v_cvt_pk_bf16_f32 v104, v104, v105
Mvav 96,104,168
s_add_u32 s58, s58, s26
ds_read_b64_tr_b16 v[230:231], v10 offset:768
v_cvt_pk_bf16_f32 v105, v106, v107
Mvav 96,112,176
s_add_u32 s58, s58, 32768
ds_read_b128 v[248:251], v12 offset:15360
v_cvt_pk_bf16_f32 v106, v108, v109
Mvav 96,120,184
s_add_u32 s58, s58, s32
buffer_load_dwordx4 v0, s[48:51], s72 offen offset:1024 sc1 lds
Mvav 100,100,160
ds_read_b128 v[128:131], v12 offset:16384
v_cvt_pk_bf16_f32 v107, v110, v111
Mvav 100,108,168
ds_read_b128 v[136:139], v12 offset:17408
v_mul_f32 v28, 0xbfb8aa3b, v68
Mvav 100,116,176
buffer_load_dwordx4 v0, s[48:51], s72 offen offset:2048 sc1 lds
Mvav 100,124,184
s_waitcnt lgkmcnt(12)
ds_read_b128 v[144:147], v12 offset:18432
v_and_b32 v69, 0xffff0000, v134
Mvav 96,128,192
s_waitcnt lgkmcnt(12)
buffer_store_dwordx4 v[104:107], v0, s[48:51], s58 offen sc1
Mvav 96,136,200
s_waitcnt lgkmcnt(11)
ds_read_b128 v[152:155], v12 offset:19456
v_mul_f32 v29, 0xbfb8aa3b, v69
Mvav 96,144,208
s_waitcnt lgkmcnt(10)
buffer_load_dwordx4 v0, s[48:51], s72 offen offset:3072 sc1 lds
Mvav 96,152,216
ds_read_b128 v[160:163], v12 offset:20480
v_lshlrev_b32 v70, 16, v135
Mvav 100,132,192
ds_read_b128 v[168:171], v12 offset:21504
v_mul_f32 v30, 0xbfb8aa3b, v70
Mvav 100,140,200
s_add_u32 m0, s69, 102400
ds_read_b64_tr_b16 v[236:237], v10 offset:4608
v_and_b32 v71, 0xffff0000, v135
Mvav 100,148,208
s_add_u32 s72, s14, 9474048
ds_read_b128 v[176:179], v12 offset:22528
v_mul_f32 v31, 0xbfb8aa3b, v71
Mvav 100,156,216
s_waitcnt lgkmcnt(12)
buffer_load_dwordx4 v0, s[48:51], s72 offen sc1 lds
Mvav 96,160,224
s_waitcnt lgkmcnt(11)
ds_read_b128 v[184:187], v12 offset:23552
v_lshlrev_b32 v72, 16, v140
Mvav 96,168,232
s_waitcnt lgkmcnt(11)
ds_read_b64_tr_b16 v[238:239], v10 offset:4864
v_and_b32 v73, 0xffff0000, v140
Mvav 96,176,240
s_waitcnt lgkmcnt(10)
ds_read_b128 v[192:195], v12 offset:24576
v_lshlrev_b32 v74, 16, v141
Mvav 96,184,248
ds_read_b128 v[200:203], v12 offset:25600
v_and_b32 v75, 0xffff0000, v141
Mvav 100,164,224
buffer_load_dwordx4 v0, s[48:51], s72 offen offset:1024 sc1 lds
Mvav 100,172,232
ds_read_b128 v[208:211], v12 offset:26624
v_lshlrev_b32 v76, 16, v142
Mvav 100,180,240
ds_read_b64_tr_b16 v[244:245], v10 offset:544
v_and_b32 v77, 0xffff0000, v142
Mvav 100,188,248
s_waitcnt lgkmcnt(13)
ds_read_b128 v[216:219], v12 offset:27648
v_lshlrev_b32 v78, 16, v143
Mvav 96,192,128
s_waitcnt lgkmcnt(13)
buffer_load_dwordx4 v0, s[48:51], s72 offen offset:2048 sc1 lds
Mvav 96,200,136
s_waitcnt lgkmcnt(12)
ds_read_b128 v[224:227], v12 offset:28672
v_and_b32 v79, 0xffff0000, v143
Mvav 96,208,144
s_waitcnt lgkmcnt(12)
ds_read_b128 v[232:235], v12 offset:29696
v_lshlrev_b32 v80, 16, v180
Mvav 96,216,152
buffer_load_dwordx4 v0, s[48:51], s72 offen offset:3072 sc1 lds
Mvav 100,196,128
ds_read_b128 v[240:243], v12 offset:30720
v_and_b32 v81, 0xffff0000, v180
Mvav 100,204,136
ds_read_b64_tr_b16 v[246:247], v10 offset:800
v_lshlrev_b32 v82, 16, v181
Mvav 100,212,144
s_waitcnt lgkmcnt(12)
ds_read_b128 v[248:251], v12 offset:31744
v_and_b32 v83, 0xffff0000, v181
Mvav 100,220,152
ds_read_b64_tr_b16 v[252:253], v10 offset:4640
v_lshlrev_b32 v84, 16, v182
Mvav 96,224,160
ds_read_b64_tr_b16 v[254:255], v10 offset:4896
v_and_b32 v85, 0xffff0000, v182
Mvav 96,232,168
s_waitcnt lgkmcnt(14)
v_rcp_f32 v24, v24
Mvav 96,240,176
s_waitcnt lgkmcnt(13)
v_exp_f32 v25, v25
Mvav 96,248,184
v_exp_f32 v26, v26
Mvav 100,228,160
v_exp_f32 v27, v27
Mvav 100,236,168
v_exp_f32 v28, v28
Mvav 100,244,176
s_waitcnt vmcnt(18) lgkmcnt(0)
v_exp_f32 v29, v29
Mvav 100,252,184
s_barrier
Mvav 96,0,192
s_add_u32 m0, s69, 65536
ds_read_b128 v[128:131], v0 offset:32768
v_mul_f32 v64, v64, v24
Mvav 96,8,200
s_add_u32 s72, s12, 14155776
ds_read_b128 v[136:139], v0 offset:33792
v_add_f32 v25, 1.0, v25
Mvav 96,16,208
buffer_load_dwordx4 v0, s[48:51], s72 offen sc1 lds
Mvav 96,24,216
ds_read_b128 v[144:147], v0 offset:34816
v_add_f32 v26, 1.0, v26
Mvav 100,4,192
ds_write_b16 v56, v40
Mvav 100,12,200
ds_read_b128 v[152:155], v0 offset:35840
v_and_b32 v56, 16, v0
Mvav 100,20,208
v_rcp_f32 v25, v25
Mvav 100,28,216
ds_read_b128 v[160:163], v0 offset:36864
)"
R"(
v_lshl_add_u32 v56, v56, 3, v57
Mvav 96,32,224
ds_read_b128 v[168:171], v0 offset:37888
v_mul_f32 v65, v65, v25
Mvav 96,40,232
buffer_load_dwordx4 v0, s[48:51], s72 offen offset:1024 sc1 lds
Mvav 96,48,240
ds_read_b128 v[176:179], v0 offset:38912
v_add_f32 v27, 1.0, v27
Mvav 96,56,248
v_rcp_f32 v26, v26
Mvav 100,36,224
ds_read_b128 v[184:187], v0 offset:39936
v_mul_f32 v66, v66, v26
Mvav 100,44,232
v_rcp_f32 v27, v27
Mvav 100,52,240
ds_read_b128 v[192:195], v0 offset:40960
v_mul_f32 v67, v67, v27
Mvav 100,60,248
s_waitcnt lgkmcnt(9)
ds_read_b128 v[200:203], v0 offset:41984
v_add_f32 v28, 1.0, v28
Zvav 104,64,128
s_waitcnt lgkmcnt(9)
buffer_load_dwordx4 v0, s[48:51], s72 offen offset:2048 sc1 lds
Mvav 104,72,136
s_waitcnt lgkmcnt(8)
ds_read_b128 v[208:211], v0 offset:43008
v_bfe_u32 v8, v0, 4, 4
Mvav 104,80,144
s_waitcnt lgkmcnt(7)
v_rcp_f32 v28, v28
Mvav 104,88,152
ds_read_b128 v[216:219], v0 offset:44032
v_lshlrev_b32 v8, 3, v8
Zvav 108,68,128
buffer_load_dwordx2 v[60:61], v8, s[48:51], s33 offen sc1
Mvav 108,76,136
ds_read_b128 v[224:227], v0 offset:45056
v_mul_f32 v68, v68, v28
Mvav 108,84,144
ds_read_b128 v[232:235], v0 offset:46080
v_add_f32 v29, 1.0, v29
Mvav 108,92,152
s_waitcnt lgkmcnt(6)
buffer_load_dwordx4 v0, s[48:51], s72 offen offset:3072 sc1 lds
Mvav 104,96,160
s_lshl_b32 s58, s78, 17
ds_read_b128 v[240:243], v0 offset:47104
v_cvt_pk_bf16_f32 v96, v96, v97
Mvav 104,104,168
s_add_u32 s58, s58, s26
v_rcp_f32 v29, v29
Mvav 104,112,176
s_add_u32 s58, s58, 32768
ds_read_b128 v[248:251], v0 offset:48128
v_cvt_pk_bf16_f32 v97, v98, v99
Mvav 104,120,184
s_add_u32 s58, s58, s32
v_exp_f32 v30, v30
Mvav 108,100,160
ds_read_b128 v[128:131], v0 offset:49152
v_cvt_pk_bf16_f32 v98, v100, v101
Mvav 108,108,168
s_waitcnt lgkmcnt(8)
ds_read_b128 v[136:139], v0 offset:50176
v_cvt_pk_bf16_f32 v99, v102, v103
Mvav 108,116,176
s_add_u32 m0, s69, 69632
buffer_store_dwordx4 v[96:99], v0, s[48:51], s58 offen sc1
Mvav 108,124,184
s_add_u32 s72, s12, 14192640
ds_read_b128 v[144:147], v0 offset:51200
v_mul_f32 v69, v69, v29
Mvav 104,128,192
s_waitcnt lgkmcnt(9)
buffer_load_dwordx4 v0, s[48:51], s72 offen sc1 lds
Mvav 104,136,200
s_waitcnt lgkmcnt(8)
ds_read_b128 v[152:155], v0 offset:52224
v_add_f32 v30, 1.0, v30
Mvav 104,144,208
s_waitcnt lgkmcnt(8)
v_rcp_f32 v30, v30
Mvav 104,152,216
ds_read_b128 v[160:163], v0 offset:53248
v_mul_f32 v70, v70, v30
Mvav 108,132,192
ds_read_b128 v[168:171], v0 offset:54272
v_mul_f32 v24, 0xbfb8aa3b, v72
Mvav 108,140,200
v_exp_f32 v31, v31
Mvav 108,148,208
ds_read_b128 v[176:179], v0 offset:55296
v_add_f32 v31, 1.0, v31
Mvav 108,156,216
s_waitcnt lgkmcnt(10)
buffer_load_dwordx4 v0, s[48:51], s72 offen offset:1024 sc1 lds
Mvav 104,160,224
s_waitcnt lgkmcnt(9)
ds_read_b128 v[184:187], v0 offset:56320
v_mul_f32 v25, 0xbfb8aa3b, v73
Mvav 104,168,232
s_waitcnt lgkmcnt(9)
v_rcp_f32 v31, v31
Mvav 104,176,240
s_waitcnt lgkmcnt(8)
ds_read_b128 v[192:195], v0 offset:57344
v_mul_f32 v71, v71, v31
Mvav 104,184,248
ds_read_b128 v[200:203], v0 offset:58368
v_mul_f32 v26, 0xbfb8aa3b, v74
Mvav 108,164,224
v_exp_f32 v24, v24
Mvav 108,172,232
ds_read_b128 v[208:211], v0 offset:59392
v_add_f32 v24, 1.0, v24
Mvav 108,180,240
buffer_load_dwordx4 v0, s[48:51], s72 offen offset:2048 sc1 lds
Mvav 108,188,248
s_waitcnt lgkmcnt(10)
ds_read_b128 v[216:219], v0 offset:60416
v_mul_f32 v27, 0xbfb8aa3b, v75
Mvav 104,192,128
s_waitcnt lgkmcnt(10)
v_rcp_f32 v24, v24
Mvav 104,200,136
s_waitcnt lgkmcnt(9)
ds_read_b128 v[224:227], v0 offset:61440
v_mul_f32 v72, v72, v24
Mvav 104,208,144
s_waitcnt lgkmcnt(9)
ds_read_b128 v[232:235], v0 offset:62464
v_mul_f32 v28, 0xbfb8aa3b, v76
Mvav 104,216,152
v_exp_f32 v25, v25
Mvav 108,196,128
ds_read_b128 v[240:243], v0 offset:63488
v_add_f32 v25, 1.0, v25
Mvav 108,204,136
buffer_load_dwordx4 v0, s[48:51], s72 offen offset:3072 sc1 lds
Mvav 108,212,144
ds_read_b128 v[248:251], v0 offset:64512
v_mul_f32 v29, 0xbfb8aa3b, v77
Mvav 108,220,152
s_waitcnt lgkmcnt(11)
v_rcp_f32 v25, v25
Mvav 104,224,160
s_waitcnt lgkmcnt(10)
v_exp_f32 v26, v26
Mvav 104,232,168
s_waitcnt lgkmcnt(9)
v_exp_f32 v27, v27
Mvav 104,240,176
s_waitcnt lgkmcnt(8)
v_exp_f32 v28, v28
Mvav 104,248,184
v_exp_f32 v29, v29
Mvav 108,228,160
v_mul_f32 v73, v73, v25
v_add_f32 v26, 1.0, v26
Mvav 108,236,168
v_rcp_f32 v26, v26
Mvav 108,244,176
s_waitcnt vmcnt(19) lgkmcnt(0)
v_mul_f32 v74, v74, v26
v_add_f32 v27, 1.0, v27
Mvav 108,252,184
s_barrier
Mvav 104,0,192
s_add_u32 m0, s69, 32768
ds_read_b128 v[128:131], v14 offset:0
v_lshrrev_b32 v57, 9, v0
Mvav 104,8,200
s_add_u32 s72, s13, 14155776
ds_read_b128 v[136:139], v14 offset:1024
v_mul_u32_u24 v57, 7040, v57
Mvav 104,16,208
s_mov_b32 vcc_lo, 0xffff0000
buffer_load_dwordx4 v0, s[48:51], s72 offen sc1 lds
Mvav 104,24,216
s_mov_b32 vcc_hi, 0xffff0000
ds_read_b128 v[144:147], v14 offset:2048
v_lshrrev_b32 v58, 2, v0
Mvav 108,4,192
ds_read_b128 v[32:35], v56 offset:0
v_add_u32 v57, v57, v58
Mvav 108,12,200
ds_read_b128 v[152:155], v14 offset:3072
v_add_f32 v28, 1.0, v28
Mvav 108,20,208
ds_read_b128 v[36:39], v56 offset:64
v_bfe_u32 v56, v0, 4, 2
Mvav 108,28,216
ds_read_b128 v[160:163], v14 offset:4096
v_lshlrev_b32 v56, 3, v56
Mvav 104,32,224
ds_read_b128 v[168:171], v14 offset:5120
v_lshrrev_b32 v56, v56, s74
Mvav 104,40,232
buffer_load_dwordx4 v0, s[48:51], s72 offen offset:1024 sc1 lds
Mvav 104,48,240
ds_read_b128 v[176:179], v14 offset:6144
v_and_b32 v56, 0xff, v56
Mvav 104,56,248
v_rcp_f32 v27, v27
Mvav 108,36,224
s_waitcnt lgkmcnt(5)
ds_read_b128 v[184:187], v14 offset:7168
v_mul_f32 v75, v75, v27
Zvvv 40,32,148
Mvav 108,44,232
v_rcp_f32 v28, v28
Zvvv 44,32,164
Mvav 108,52,240
s_waitcnt lgkmcnt(4)
ds_read_b128 v[192:195], v14 offset:8192
v_mul_f32 v76, v76, v28
Mvvv 40,36,156
Mvav 108,60,248
ds_read_b128 v[200:203], v14 offset:9216
v_add_f32 v29, 1.0, v29
Mvvv 44,36,172
Zvav 96,64,128
buffer_load_dwordx4 v0, s[48:51], s72 offen offset:2048 sc1 lds
Zvvv 48,32,228
Mvav 96,72,136
ds_read_b128 v[208:211], v14 offset:10240
v_mul_f32 v30, 0xbfb8aa3b, v78
Mvvv 48,36,236
Mvav 96,80,144
v_rcp_f32 v29, v29
Zvvv 52,32,244
Mvav 96,88,152
ds_read_b128 v[216:219], v14 offset:11264
v_mul_f32 v77, v77, v29
Mvvv 52,36,252
Zvav 100,68,128
v_exp_f32 v30, v30
Mvav 100,76,136
ds_read_b128 v[224:227], v14 offset:12288
v_add_f32 v30, 1.0, v30
Mvav 100,84,144
ds_read_b128 v[232:235], v14 offset:13312
v_mul_f32 v31, 0xbfb8aa3b, v79
Mvav 100,92,152
s_waitcnt lgkmcnt(6)
buffer_load_dwordx4 v0, s[48:51], s72 offen offset:3072 sc1 lds
Mvav 96,96,160
s_lshl_b32 s58, s76, 17
ds_read_b128 v[240:243], v14 offset:14336
v_cvt_pk_bf16_f32 v104, v104, v105
Mvav 96,104,168
s_add_u32 s58, s58, s26
v_rcp_f32 v30, v30
Mvav 96,112,176
s_add_u32 s58, s58, 65536
ds_read_b128 v[248:251], v14 offset:15360
v_cvt_pk_bf16_f32 v105, v106, v107
Mvav 96,120,184
s_add_u32 s58, s58, s32
v_exp_f32 v31, v31
Mvav 100,100,160
ds_read_b128 v[128:131], v14 offset:16384
v_cvt_pk_bf16_f32 v106, v108, v109
Mvav 100,108,168
s_waitcnt lgkmcnt(8)
ds_read_b128 v[136:139], v14 offset:17408
v_cvt_pk_bf16_f32 v107, v110, v111
Mvav 100,116,176
s_add_u32 m0, s69, 36864
buffer_store_dwordx4 v[104:107], v0, s[48:51], s58 offen sc1
Mvav 100,124,184
s_add_u32 s72, s13, 14192640
ds_read_b128 v[144:147], v14 offset:18432
v_cndmask_b32 v40, v40, v44, vcc
Mvav 96,128,192
s_waitcnt lgkmcnt(9)
buffer_load_dwordx4 v0, s[48:51], s72 offen sc1 lds
Mvav 96,136,200
s_waitcnt lgkmcnt(8)
ds_read_b128 v[152:155], v14 offset:19456
v_cndmask_b32 v41, v41, v45, vcc
Mvav 96,144,208
s_waitcnt lgkmcnt(8)
v_cndmask_b32 v48, v48, v52, vcc
v_cndmask_b32 v49, v49, v53, vcc
Mvav 96,152,216
s_mov_b64 vcc, 0xffffffff
ds_read_b128 v[160:163], v14 offset:20480
v_cndmask_b32 v58, v48, v40, vcc
Mvav 100,132,192
ds_read_b128 v[168:171], v14 offset:21504
v_cndmask_b32 v59, v49, v41, vcc
Mvav 100,140,200
v_and_or_b32 v58, v58, v2, v56
v_and_or_b32 v59, v59, v2, v56
Mvav 100,148,208
s_waitcnt vmcnt(12)
ds_read_b128 v[176:179], v14 offset:22528
v_xor_b32 v60, s74, v60
Mvav 100,156,216
s_waitcnt lgkmcnt(10)
buffer_load_dwordx4 v0, s[48:51], s72 offen offset:1024 sc1 lds
Mvav 96,160,224
s_waitcnt lgkmcnt(9)
ds_read_b128 v[184:187], v14 offset:23552
v_bitop3_b32 v60, v61, s75, v60 bitop3:0xbe
Mvav 96,168,232
s_waitcnt lgkmcnt(8)
buffer_store_dword v58, v57, s[48:51], s37 offen sc1
Mvav 96,176,240
s_add_u32 s37, s37, 229376
ds_read_b128 v[192:195], v14 offset:24576
v_cmp_ne_u32 vcc, 0, v60
Mvav 96,184,248
s_and_b64 vcc, vcc, 0xf
ds_read_b128 v[200:203], v14 offset:25600
v_mul_f32 v78, v78, v30
Mvav 100,164,224
buffer_store_dword v59, v57, s[48:51], s37 offen sc1
Mvav 100,172,232
ds_read_b128 v[208:211], v14 offset:26624
v_add_f32 v31, 1.0, v31
Mvav 100,180,240
buffer_load_dwordx4 v0, s[48:51], s72 offen offset:2048 sc1 lds
Mvav 100,188,248
s_waitcnt lgkmcnt(3)
ds_read_b128 v[216:219], v14 offset:27648
v_mul_f32 v24, 0xbfb8aa3b, v80
Mvav 96,192,128
s_cbranch_vccnz .Lpf0
.Lpf0_r:
s_lshl_b32 s73, s76, 13
Mvav 96,200,136
s_add_u32 s73, s73, s27
ds_read_b128 v[224:227], v14 offset:28672
v_mul_f32 v25, 0xbfb8aa3b, v81
Mvav 96,208,144
s_add_u32 s73, s73, s32
ds_read_b128 v[232:235], v14 offset:29696
v_mul_f32 v26, 0xbfb8aa3b, v82
Mvav 96,216,152
s_add_u32 s73, s73, 0
buffer_load_dwordx4 v[32:35], v0, s[48:51], s73 offen sc1
Mvav 100,196,128
s_lshl_b32 s73, s77, 13
ds_read_b128 v[240:243], v14 offset:30720
v_mul_f32 v27, 0xbfb8aa3b, v83
Mvav 100,204,136
s_add_u32 s73, s73, s27
buffer_load_dwordx4 v0, s[48:51], s72 offen offset:3072 sc1 lds
Mvav 100,212,144
s_add_u32 s73, s73, s32
ds_read_b128 v[248:251], v14 offset:31744
v_mul_f32 v28, 0xbfb8aa3b, v84
Mvav 100,220,152
s_add_u32 s73, s73, 0
buffer_load_dwordx4 v[36:39], v0, s[48:51], s73 offen sc1
Mvav 96,224,160
s_lshl_b32 s73, s78, 13
v_rcp_f32 v31, v31
Mvav 96,232,168
s_add_u32 s73, s73, s27
v_exp_f32 v24, v24
Mvav 96,240,176
s_add_u32 s73, s73, s32
v_exp_f32 v25, v25
Mvav 96,248,184
s_add_u32 s73, s73, 0
buffer_load_dwordx4 v[40:43], v0, s[48:51], s73 offen sc1
Mvav 100,228,160
v_exp_f32 v26, v26
Mvav 100,236,168
v_exp_f32 v27, v27
Mvav 100,244,176
s_waitcnt lgkmcnt(0)
v_exp_f32 v28, v28
Mvav 100,252,184
s_barrier
Mvav 96,0,192
s_add_u32 m0, s69, 131072
ds_read_b128 v[128:131], v12 offset:32768
v_mul_f32 v79, v79, v31
Mvav 96,8,200
s_add_u32 s72, s14, 14155776
ds_read_b128 v[136:139], v12 offset:33792
v_add_f32 v24, 1.0, v24
Mvav 96,16,208
s_waitcnt vmcnt(18)
buffer_load_dwordx4 v0, s[48:51], s72 offen sc1 lds
Mvav 96,24,216
ds_read_b128 v[144:147], v12 offset:34816
v_add_f32 v25, 1.0, v25
Mvav 100,4,192
buffer_store_dwordx2 v[62:63], v13, s[48:51], s34 offen offset:32 sc1
Mvav 100,12,200
ds_read_b128 v[152:155], v12 offset:35840
v_add_f32 v26, 1.0, v26
Mvav 100,20,208
v_rcp_f32 v24, v24
Mvav 100,28,216
ds_read_b128 v[160:163], v12 offset:36864
v_mul_f32 v80, v80, v24
Mvav 96,32,224
ds_read_b128 v[168:171], v12 offset:37888
v_add_f32 v27, 1.0, v27
Mvav 96,40,232
buffer_load_dwordx4 v0, s[48:51], s72 offen offset:1024 sc1 lds
Mvav 96,48,240
ds_read_b128 v[176:179], v12 offset:38912
v_add_f32 v28, 1.0, v28
Mvav 96,56,248
v_rcp_f32 v25, v25
Mvav 100,36,224
ds_read_b128 v[184:187], v12 offset:39936
v_mul_f32 v81, v81, v25
Mvav 100,44,232
v_rcp_f32 v26, v26
Mvav 100,52,240
ds_read_b128 v[192:195], v12 offset:40960
v_mul_f32 v82, v82, v26
Mvav 100,60,248
s_waitcnt lgkmcnt(8)
ds_read_b128 v[200:203], v12 offset:41984
v_mul_f32 v29, 0xbfb8aa3b, v85
Zvav 104,64,128
s_waitcnt lgkmcnt(8)
buffer_load_dwordx4 v0, s[48:51], s72 offen offset:2048 sc1 lds
Mvav 104,72,136
s_waitcnt lgkmcnt(7)
ds_read_b128 v[208:211], v12 offset:43008
v_lshlrev_b32 v86, 16, v183
Mvav 104,80,144
s_waitcnt lgkmcnt(7)
v_rcp_f32 v27, v27
Mvav 104,88,152
ds_read_b128 v[216:219], v12 offset:44032
v_mul_f32 v83, v83, v27
Zvav 108,68,128
v_rcp_f32 v28, v28
Mvav 108,76,136
ds_read_b128 v[224:227], v12 offset:45056
v_mul_f32 v84, v84, v28
Mvav 108,84,144
ds_read_b128 v[232:235], v12 offset:46080
v_mul_f32 v30, 0xbfb8aa3b, v86
Mvav 108,92,152
s_waitcnt lgkmcnt(6)
buffer_load_dwordx4 v0, s[48:51], s72 offen offset:3072 sc1 lds
Mvav 104,96,160
s_lshl_b32 s58, s77, 17
)"
R"(
ds_read_b128 v[240:243], v12 offset:47104
v_cvt_pk_bf16_f32 v96, v96, v97
Mvav 104,104,168
s_add_u32 s58, s58, s26
v_exp_f32 v29, v29
Mvav 104,112,176
s_add_u32 s58, s58, 65536
ds_read_b128 v[248:251], v12 offset:48128
v_cvt_pk_bf16_f32 v97, v98, v99
Mvav 104,120,184
s_add_u32 s58, s58, s32
v_exp_f32 v30, v30
Mvav 108,100,160
ds_read_b128 v[128:131], v12 offset:49152
v_cvt_pk_bf16_f32 v98, v100, v101
Mvav 108,108,168
s_waitcnt lgkmcnt(8)
ds_read_b128 v[136:139], v12 offset:50176
v_cvt_pk_bf16_f32 v99, v102, v103
Mvav 108,116,176
s_add_u32 m0, s69, 135168
buffer_store_dwordx4 v[96:99], v0, s[48:51], s58 offen sc1
Mvav 108,124,184
s_add_u32 s72, s14, 14192640
ds_read_b128 v[144:147], v12 offset:51200
v_add_f32 v29, 1.0, v29
Mvav 104,128,192
s_waitcnt lgkmcnt(9)
buffer_load_dwordx4 v0, s[48:51], s72 offen sc1 lds
Mvav 104,136,200
s_waitcnt lgkmcnt(8)
ds_read_b128 v[152:155], v12 offset:52224
v_add_f32 v30, 1.0, v30
Mvav 104,144,208
s_waitcnt lgkmcnt(8)
v_rcp_f32 v29, v29
Mvav 104,152,216
ds_read_b128 v[160:163], v12 offset:53248
v_mul_f32 v85, v85, v29
Mvav 108,132,192
ds_read_b128 v[168:171], v12 offset:54272
v_and_b32 v87, 0xffff0000, v183
Mvav 108,140,200
v_rcp_f32 v30, v30
Mvav 108,148,208
ds_read_b128 v[176:179], v12 offset:55296
v_mul_f32 v86, v86, v30
Mvav 108,156,216
s_waitcnt lgkmcnt(10)
buffer_load_dwordx4 v0, s[48:51], s72 offen offset:1024 sc1 lds
Mvav 104,160,224
s_waitcnt lgkmcnt(9)
ds_read_b128 v[184:187], v12 offset:56320
v_mul_f32 v31, 0xbfb8aa3b, v87
Mvav 104,168,232
s_waitcnt lgkmcnt(9)
v_exp_f32 v31, v31
Mvav 104,176,240
s_waitcnt lgkmcnt(8)
ds_read_b128 v[192:195], v12 offset:57344
v_add_f32 v31, 1.0, v31
Mvav 104,184,248
ds_read_b128 v[200:203], v12 offset:58368
v_lshlrev_b32 v16, 16, v188
Mvav 108,164,224
v_rcp_f32 v31, v31
Mvav 108,172,232
ds_read_b128 v[208:211], v12 offset:59392
v_mul_f32 v87, v87, v31
Mvav 108,180,240
buffer_load_dwordx4 v0, s[48:51], s72 offen offset:2048 sc1 lds
Mvav 108,188,248
s_waitcnt lgkmcnt(10)
ds_read_b128 v[216:219], v12 offset:60416
v_mul_f32 v24, 0xbfb8aa3b, v16
Mvav 104,192,128
s_waitcnt lgkmcnt(10)
v_exp_f32 v24, v24
Mvav 104,200,136
s_waitcnt lgkmcnt(9)
ds_read_b128 v[224:227], v12 offset:61440
v_add_f32 v24, 1.0, v24
Mvav 104,208,144
s_waitcnt lgkmcnt(9)
ds_read_b128 v[232:235], v12 offset:62464
v_and_b32 v17, 0xffff0000, v188
Mvav 104,216,152
v_rcp_f32 v24, v24
Mvav 108,196,128
ds_read_b128 v[240:243], v12 offset:63488
v_mul_f32 v16, v16, v24
Mvav 108,204,136
buffer_load_dwordx4 v0, s[48:51], s72 offen offset:3072 sc1 lds
Mvav 108,212,144
ds_read_b128 v[248:251], v12 offset:64512
v_mul_f32 v25, 0xbfb8aa3b, v17
Mvav 108,220,152
s_waitcnt lgkmcnt(11)
v_exp_f32 v25, v25
Mvav 104,224,160
s_waitcnt lgkmcnt(10)
buffer_load_dwordx2 v[60:61], v8, s[48:51], s33 offen sc1
Mvav 104,232,168
s_waitcnt vmcnt(14) lgkmcnt(9)
v_lshlrev_b32 v148, 16, v32
v_and_b32 v149, 0xffff0000, v32
Mvav 104,240,176
s_waitcnt lgkmcnt(8)
v_lshlrev_b32 v150, 16, v33
v_and_b32 v151, 0xffff0000, v33
Mvav 104,248,184
v_lshlrev_b32 v156, 16, v34
v_and_b32 v157, 0xffff0000, v34
Mvav 108,228,160
v_lshlrev_b32 v158, 16, v35
v_and_b32 v159, 0xffff0000, v35
Mvav 108,236,168
s_waitcnt vmcnt(12)
v_lshlrev_b32 v164, 16, v36
v_add_f32 v148, v148, v164
Mvav 108,244,176
s_waitcnt lgkmcnt(0)
v_and_b32 v165, 0xffff0000, v36
v_add_f32 v149, v149, v165
Mvav 108,252,184
s_barrier
Mvav 104,0,192
s_add_u32 m0, s69, 98304
ds_read_b128 v[128:131], v12 offset:0
v_lshlrev_b32 v166, 16, v37
Mvav 104,8,200
s_waitcnt vmcnt(11)
s_add_u32 s72, s15, 4718592
ds_read_b128 v[136:139], v12 offset:1024
v_lshlrev_b32 v164, 16, v40
Mvav 104,16,208
buffer_load_dwordx4 v0, s[48:51], s72 offen sc1 lds
Mvav 104,24,216
ds_read_b128 v[144:147], v12 offset:2048
v_add_f32 v148, v148, v164
Mvav 108,4,192
v_and_b32 v165, 0xffff0000, v40
v_add_f32 v149, v149, v165
Mvav 108,12,200
ds_read_b128 v[152:155], v12 offset:3072
v_add_f32 v150, v150, v166
Mvav 108,20,208
v_lshlrev_b32 v166, 16, v41
v_add_f32 v150, v150, v166
Mvav 108,28,216
ds_read_b128 v[160:163], v12 offset:4096
v_and_b32 v167, 0xffff0000, v37
Mvav 104,32,224
ds_read_b128 v[168:171], v12 offset:5120
v_add_f32 v151, v151, v167
Mvav 104,40,232
buffer_load_dwordx4 v0, s[48:51], s72 offen offset:1024 sc1 lds
Mvav 104,48,240
ds_read_b128 v[176:179], v12 offset:6144
v_and_b32 v167, 0xffff0000, v41
Mvav 104,56,248
v_add_f32 v151, v151, v167
v_lshlrev_b32 v164, 16, v38
Mvav 108,36,224
ds_read_b128 v[184:187], v12 offset:7168
v_add_f32 v156, v156, v164
Mvav 108,44,232
v_lshlrev_b32 v164, 16, v42
v_add_f32 v156, v156, v164
Mvav 108,52,240
ds_read_b128 v[192:195], v12 offset:8192
v_and_b32 v165, 0xffff0000, v38
Mvav 108,60,248
s_waitcnt lgkmcnt(7)
ds_read_b128 v[200:203], v12 offset:9216
v_add_f32 v157, v157, v165
Zvav 96,64,128
s_add_u32 s62, s68, 0
buffer_load_dwordx4 v0, s[48:51], s72 offen offset:2048 sc1 lds
Mvav 96,72,136
s_waitcnt lgkmcnt(7)
ds_read_b128 v[208:211], v12 offset:10240
v_and_b32 v165, 0xffff0000, v42
Mvav 96,80,144
s_waitcnt lgkmcnt(7)
buffer_load_dwordx4 v[196:199], v4, s[8:11], s62 offen
Mvav 96,88,152
s_add_u32 s62, s68, 1048576
ds_read_b128 v[216:219], v12 offset:11264
v_add_f32 v157, v157, v165
Zvav 100,68,128
buffer_load_dwordx4 v[204:207], v4, s[8:11], s62 offen
Mvav 100,76,136
s_add_u32 s62, s68, 2097152
ds_read_b128 v[224:227], v12 offset:12288
v_lshlrev_b32 v166, 16, v39
Mvav 100,84,144
ds_read_b128 v[232:235], v12 offset:13312
v_add_f32 v158, v158, v166
Mvav 100,92,152
s_waitcnt lgkmcnt(6)
buffer_load_dwordx4 v0, s[48:51], s72 offen offset:3072 sc1 lds
Mvav 96,96,160
s_lshl_b32 s58, s78, 17
ds_read_b128 v[240:243], v12 offset:14336
v_cvt_pk_bf16_f32 v104, v104, v105
Mvav 96,104,168
s_add_u32 s58, s58, s26
buffer_load_dwordx4 v[212:215], v4, s[8:11], s62 offen
Mvav 96,112,176
s_add_u32 s58, s58, 65536
ds_read_b128 v[248:251], v12 offset:15360
v_cvt_pk_bf16_f32 v105, v106, v107
Mvav 96,120,184
s_add_u32 s58, s58, s32
v_cvt_pk_bf16_f32 v106, v108, v109
v_cvt_pk_bf16_f32 v107, v110, v111
Mvav 100,100,160
s_add_u32 s62, s68, 3145728
ds_read_b128 v[128:131], v12 offset:16384
v_lshlrev_b32 v166, 16, v43
Mvav 100,108,168
s_waitcnt lgkmcnt(8)
ds_read_b128 v[136:139], v12 offset:17408
v_add_f32 v158, v158, v166
Mvav 100,116,176
s_add_u32 m0, s69, 102400
buffer_store_dwordx4 v[104:107], v0, s[48:51], s58 offen sc1
Mvav 100,124,184
s_add_u32 s72, s15, 4755456
ds_read_b128 v[144:147], v12 offset:18432
v_and_b32 v167, 0xffff0000, v39
Mvav 96,128,192
s_waitcnt lgkmcnt(9)
buffer_load_dwordx4 v0, s[48:51], s72 offen sc1 lds
Mvav 96,136,200
s_waitcnt lgkmcnt(8)
ds_read_b128 v[152:155], v12 offset:19456
v_add_f32 v159, v159, v167
Mvav 96,144,208
s_waitcnt lgkmcnt(8)
buffer_load_dwordx4 v[220:223], v4, s[8:11], s62 offen
Mvav 96,152,216
ds_read_b128 v[160:163], v12 offset:20480
v_and_b32 v167, 0xffff0000, v43
Mvav 100,132,192
ds_read_b128 v[168:171], v12 offset:21504
v_add_f32 v159, v159, v167
Mvav 100,140,200
v_add_f32 v88, v88, v148
v_add_f32 v89, v89, v149
Mvav 100,148,208
ds_read_b128 v[176:179], v12 offset:22528
v_add_f32 v90, v90, v150
Mvav 100,156,216
s_waitcnt lgkmcnt(10)
buffer_load_dwordx4 v0, s[48:51], s72 offen offset:1024 sc1 lds
Mvav 96,160,224
s_waitcnt lgkmcnt(9)
ds_read_b128 v[184:187], v12 offset:23552
v_add_f32 v91, v91, v151
Mvav 96,168,232
s_waitcnt lgkmcnt(9)
v_add_f32 v92, v92, v156
v_add_f32 v93, v93, v157
Mvav 96,176,240
s_waitcnt lgkmcnt(8)
ds_read_b128 v[192:195], v12 offset:24576
v_add_f32 v94, v94, v158
Mvav 96,184,248
ds_read_b128 v[200:203], v12 offset:25600
v_add_f32 v95, v95, v159
Mvav 100,164,224
v_add_f32 v25, 1.0, v25
v_lshlrev_b32 v18, 16, v189
Mvav 100,172,232
ds_read_b128 v[208:211], v12 offset:26624
v_mul_f32 v26, 0xbfb8aa3b, v18
Mvav 100,180,240
buffer_load_dwordx4 v0, s[48:51], s72 offen offset:2048 sc1 lds
Mvav 100,188,248
s_waitcnt lgkmcnt(10)
ds_read_b128 v[216:219], v12 offset:27648
v_and_b32 v19, 0xffff0000, v189
Mvav 96,192,128
s_waitcnt lgkmcnt(10)
v_rcp_f32 v25, v25
Mvav 96,200,136
s_waitcnt lgkmcnt(9)
ds_read_b128 v[224:227], v12 offset:28672
v_mul_f32 v17, v17, v25
Mvav 96,208,144
s_waitcnt lgkmcnt(9)
ds_read_b128 v[232:235], v12 offset:29696
v_mul_f32 v27, 0xbfb8aa3b, v19
Mvav 96,216,152
v_exp_f32 v26, v26
Mvav 100,196,128
ds_read_b128 v[240:243], v12 offset:30720
v_add_f32 v26, 1.0, v26
Mvav 100,204,136
buffer_load_dwordx4 v0, s[48:51], s72 offen offset:3072 sc1 lds
Mvav 100,212,144
ds_read_b128 v[248:251], v12 offset:31744
v_lshlrev_b32 v20, 16, v190
Mvav 100,220,152
s_waitcnt lgkmcnt(11)
v_rcp_f32 v26, v26
Mvav 96,224,160
s_waitcnt lgkmcnt(10)
v_exp_f32 v27, v27
Mvav 96,232,168
s_waitcnt lgkmcnt(9)
v_mul_f32 v18, v18, v26
v_add_f32 v27, 1.0, v27
Mvav 96,240,176
s_waitcnt lgkmcnt(8)
v_rcp_f32 v27, v27
Mvav 96,248,184
v_mul_f32 v19, v19, v27
v_mul_f32 v28, 0xbfb8aa3b, v20
Mvav 100,228,160
v_exp_f32 v28, v28
Mvav 100,236,168
v_add_f32 v28, 1.0, v28
v_and_b32 v21, 0xffff0000, v190
Mvav 100,244,176
s_waitcnt lgkmcnt(0)
v_rcp_f32 v28, v28
Mvav 100,252,184
s_barrier
Mvav 96,0,192
s_waitcnt vmcnt(13)
s_add_u32 m0, s69, 65536
ds_read_b128 v[128:131], v0 offset:32768
v_xor_b32 v60, s74, v60
Mvav 96,8,200
s_add_u32 s72, s15, 9437184
ds_read_b128 v[136:139], v0 offset:33792
v_bitop3_b32 v60, v61, s75, v60 bitop3:0xbe
Mvav 96,16,208
buffer_load_dwordx4 v0, s[48:51], s72 offen sc1 lds
Mvav 96,24,216
ds_read_b128 v[144:147], v0 offset:34816
v_cmp_ne_u32 vcc, 0, v60
Mvav 100,4,192
s_and_b64 vcc, vcc, 0xf0
s_cbranch_vccnz .Lpf1
.Lpf1_r:
Mvav 100,12,200
s_lshl_b32 s73, s76, 13
ds_read_b128 v[152:155], v0 offset:35840
v_mul_f32 v20, v20, v28
Mvav 100,20,208
s_add_u32 s73, s73, s27
v_mul_f32 v29, 0xbfb8aa3b, v21
v_lshlrev_b32 v22, 16, v191
Mvav 100,28,216
s_add_u32 s73, s73, s32
ds_read_b128 v[160:163], v0 offset:36864
v_mul_f32 v30, 0xbfb8aa3b, v22
Mvav 96,32,224
s_add_u32 s73, s73, 32768
ds_read_b128 v[168:171], v0 offset:37888
v_and_b32 v23, 0xffff0000, v191
Mvav 96,40,232
buffer_load_dwordx4 v0, s[48:51], s72 offen offset:1024 sc1 lds
Mvav 96,48,240
ds_read_b128 v[176:179], v0 offset:38912
v_mul_f32 v31, 0xbfb8aa3b, v23
Mvav 96,56,248
buffer_load_dwordx4 v[44:47], v0, s[48:51], s73 offen sc1
Mvav 100,36,224
s_lshl_b32 s73, s77, 13
ds_read_b128 v[184:187], v0 offset:39936
Mvav 100,44,232
s_add_u32 s73, s73, s27
v_exp_f32 v29, v29
Mvav 100,52,240
s_add_u32 s73, s73, s32
ds_read_b128 v[192:195], v0 offset:40960
v_add_f32 v29, 1.0, v29
Mvav 100,60,248
s_waitcnt lgkmcnt(8)
s_add_u32 s73, s73, 32768
ds_read_b128 v[200:203], v0 offset:41984
Zvav 104,64,128
s_waitcnt lgkmcnt(8)
buffer_load_dwordx4 v0, s[48:51], s72 offen offset:2048 sc1 lds
Mvav 104,72,136
s_waitcnt lgkmcnt(7)
ds_read_b128 v[208:211], v0 offset:43008
Mvav 104,80,144
s_waitcnt lgkmcnt(7)
buffer_load_dwordx4 v[48:51], v0, s[48:51], s73 offen sc1
Mvav 104,88,152
s_lshl_b32 s73, s78, 13
ds_read_b128 v[216:219], v0 offset:44032
Zvav 108,68,128
s_add_u32 s73, s73, s27
v_rcp_f32 v29, v29
Mvav 108,76,136
s_add_u32 s73, s73, s32
ds_read_b128 v[224:227], v0 offset:45056
v_mul_f32 v21, v21, v29
Mvav 108,84,144
s_add_u32 s73, s73, 32768
ds_read_b128 v[232:235], v0 offset:46080
Mvav 108,92,152
s_waitcnt lgkmcnt(6)
buffer_load_dwordx4 v0, s[48:51], s72 offen offset:3072 sc1 lds
Mvav 104,96,160
s_lshl_b32 s58, s76, 17
ds_read_b128 v[240:243], v0 offset:47104
v_and_or_b32 v96, v96, v2, s20
Mvav 104,104,168
s_add_u32 s58, s58, s26
buffer_load_dwordx4 v[52:55], v0, s[48:51], s73 offen sc1
Mvav 104,112,176
s_add_u32 s58, s58, 98304
ds_read_b128 v[248:251], v0 offset:48128
v_and_or_b32 v97, v97, v2, s21
Mvav 104,120,184
v_exp_f32 v30, v30
Mvav 108,100,160
ds_read_b128 v[128:131], v0 offset:49152
v_and_or_b32 v98, v98, v2, s22
Mvav 108,108,168
s_waitcnt lgkmcnt(8)
ds_read_b128 v[136:139], v0 offset:50176
v_and_or_b32 v99, v99, v2, s23
Mvav 108,116,176
s_add_u32 m0, s69, 69632
buffer_store_dwordx4 v[96:99], v0, s[48:51], s58 offen sc1
Mvav 108,124,184
s_add_u32 s72, s15, 9474048
ds_read_b128 v[144:147], v0 offset:51200
v_and_or_b32 v100, v100, v2, s20
Mvav 104,128,192
s_waitcnt lgkmcnt(9)
buffer_load_dwordx4 v0, s[48:51], s72 offen sc1 lds
Mvav 104,136,200
s_waitcnt lgkmcnt(8)
)"
R"(
ds_read_b128 v[152:155], v0 offset:52224
v_and_or_b32 v101, v101, v2, s21
Mvav 104,144,208
s_waitcnt lgkmcnt(8)
v_exp_f32 v31, v31
Mvav 104,152,216
ds_read_b128 v[160:163], v0 offset:53248
v_and_or_b32 v102, v102, v2, s22
Mvav 108,132,192
ds_read_b128 v[168:171], v0 offset:54272
v_and_or_b32 v103, v103, v2, s23
Mvav 108,140,200
buffer_store_dwordx4 v[100:103], v0, s[48:51], s58 offen offset:1024 sc1
Mvav 108,148,208
ds_read_b128 v[176:179], v0 offset:55296
v_add_f32 v30, 1.0, v30
Mvav 108,156,216
s_waitcnt lgkmcnt(10)
buffer_load_dwordx4 v0, s[48:51], s72 offen offset:1024 sc1 lds
Mvav 104,160,224
s_waitcnt lgkmcnt(9)
ds_read_b128 v[184:187], v0 offset:56320
v_add_f32 v31, 1.0, v31
Mvav 104,168,232
s_waitcnt lgkmcnt(9)
v_rcp_f32 v30, v30
Mvav 104,176,240
s_waitcnt lgkmcnt(8)
ds_read_b128 v[192:195], v0 offset:57344
v_mul_f32 v22, v22, v30
Mvav 104,184,248
ds_read_b128 v[200:203], v0 offset:58368
Mvav 108,164,224
v_rcp_f32 v31, v31
Mvav 108,172,232
ds_read_b128 v[208:211], v0 offset:59392
v_mul_f32 v23, v23, v31
Mvav 108,180,240
buffer_load_dwordx4 v0, s[48:51], s72 offen offset:2048 sc1 lds
Mvav 108,188,248
s_waitcnt lgkmcnt(10)
ds_read_b128 v[216:219], v0 offset:60416
Mvav 104,192,128
s_waitcnt lgkmcnt(10)
Mvav 104,200,136
s_waitcnt lgkmcnt(9)
ds_read_b128 v[224:227], v0 offset:61440
Mvav 104,208,144
s_waitcnt lgkmcnt(9)
ds_read_b128 v[232:235], v0 offset:62464
Mvav 104,216,152
Mvav 108,196,128
ds_read_b128 v[240:243], v0 offset:63488
Mvav 108,204,136
buffer_load_dwordx4 v0, s[48:51], s72 offen offset:3072 sc1 lds
Mvav 108,212,144
ds_read_b128 v[248:251], v0 offset:64512
Mvav 108,220,152
s_waitcnt lgkmcnt(11)
Mvav 104,224,160
s_waitcnt lgkmcnt(10)
Mvav 104,232,168
s_waitcnt lgkmcnt(9)
Mvav 104,240,176
s_waitcnt lgkmcnt(8)
Mvav 104,248,184
Mvav 108,228,160
Mvav 108,236,168
Mvav 108,244,176
s_waitcnt lgkmcnt(0)
Mvav 108,252,184
s_barrier
Mvav 104,0,192
s_add_u32 m0, s69, 32768
ds_read_b128 v[128:131], v14 offset:0
Mvav 104,8,200
s_add_u32 s72, s15, 14155776
ds_read_b128 v[136:139], v14 offset:1024
Mvav 104,16,208
s_waitcnt vmcnt(18)
buffer_load_dwordx4 v0, s[48:51], s72 offen sc1 lds
Mvav 104,24,216
ds_read_b128 v[144:147], v14 offset:2048
Mvav 108,4,192
buffer_store_dwordx2 v[62:63], v13, s[48:51], s34 offen offset:64 sc1
Mvav 108,12,200
ds_read_b128 v[152:155], v14 offset:3072
Mvav 108,20,208
Mvav 108,28,216
ds_read_b128 v[160:163], v14 offset:4096
Mvav 104,32,224
ds_read_b128 v[168:171], v14 offset:5120
Mvav 104,40,232
buffer_load_dwordx4 v0, s[48:51], s72 offen offset:1024 sc1 lds
Mvav 104,48,240
ds_read_b128 v[176:179], v14 offset:6144
Mvav 104,56,248
Mvav 108,36,224
ds_read_b128 v[184:187], v14 offset:7168
Mvav 108,44,232
Mvav 108,52,240
ds_read_b128 v[192:195], v14 offset:8192
Mvav 108,60,248
s_waitcnt lgkmcnt(8)
ds_read_b128 v[200:203], v14 offset:9216
Zvav 96,64,128
s_waitcnt lgkmcnt(8)
buffer_load_dwordx4 v0, s[48:51], s72 offen offset:2048 sc1 lds
Mvav 96,72,136
s_waitcnt vmcnt(14) lgkmcnt(7)
ds_read_b128 v[208:211], v14 offset:10240
v_lshlrev_b32 v112, 16, v44
Mvav 96,80,144
s_waitcnt lgkmcnt(7)
v_and_b32 v113, 0xffff0000, v44
v_lshlrev_b32 v114, 16, v45
Mvav 96,88,152
ds_read_b128 v[216:219], v14 offset:11264
v_and_b32 v115, 0xffff0000, v45
Zvav 100,68,128
v_lshlrev_b32 v116, 16, v46
v_and_b32 v117, 0xffff0000, v46
Mvav 100,76,136
ds_read_b128 v[224:227], v14 offset:12288
v_lshlrev_b32 v118, 16, v47
Mvav 100,84,144
ds_read_b128 v[232:235], v14 offset:13312
v_and_b32 v119, 0xffff0000, v47
Mvav 100,92,152
s_waitcnt lgkmcnt(6)
buffer_load_dwordx4 v0, s[48:51], s72 offen offset:3072 sc1 lds
Mvav 96,96,160
s_lshl_b32 s58, s77, 17
ds_read_b128 v[240:243], v14 offset:14336
v_and_or_b32 v104, v104, v2, s20
Mvav 96,104,168
s_add_u32 s58, s58, s26
v_and_or_b32 v105, v105, v2, s21
v_and_or_b32 v106, v106, v2, s22
Mvav 96,112,176
s_add_u32 s58, s58, 98304
ds_read_b128 v[248:251], v14 offset:15360
v_and_or_b32 v107, v107, v2, s23
Mvav 96,120,184
buffer_store_dwordx4 v[104:107], v0, s[48:51], s58 offen sc1
Mvav 100,100,160
ds_read_b128 v[128:131], v14 offset:16384
v_and_or_b32 v108, v108, v2, s20
Mvav 100,108,168
ds_read_b128 v[136:139], v14 offset:17408
v_and_or_b32 v109, v109, v2, s21
Mvav 100,116,176
s_add_u32 m0, s69, 36864
v_and_or_b32 v110, v110, v2, s22
v_and_or_b32 v111, v111, v2, s23
Mvav 100,124,184
s_waitcnt vmcnt(14) lgkmcnt(9)
s_add_u32 s72, s15, 14192640
ds_read_b128 v[144:147], v14 offset:18432
v_lshlrev_b32 v164, 16, v48
Mvav 96,128,192
s_waitcnt lgkmcnt(9)
buffer_load_dwordx4 v0, s[48:51], s72 offen sc1 lds
Mvav 96,136,200
s_waitcnt lgkmcnt(8)
ds_read_b128 v[152:155], v14 offset:19456
v_add_f32 v112, v112, v164
Mvav 96,144,208
s_waitcnt lgkmcnt(8)
buffer_store_dwordx4 v[108:111], v0, s[48:51], s58 offen offset:1024 sc1
Mvav 96,152,216
s_waitcnt vmcnt(14)
ds_read_b128 v[160:163], v14 offset:20480
v_lshlrev_b32 v164, 16, v52
Mvav 100,132,192
ds_read_b128 v[168:171], v14 offset:21504
v_add_f32 v112, v112, v164
Mvav 100,140,200
v_and_b32 v165, 0xffff0000, v48
v_add_f32 v113, v113, v165
Mvav 100,148,208
ds_read_b128 v[176:179], v14 offset:22528
v_and_b32 v165, 0xffff0000, v52
Mvav 100,156,216
s_waitcnt lgkmcnt(10)
buffer_load_dwordx4 v0, s[48:51], s72 offen offset:1024 sc1 lds
Mvav 96,160,224
s_waitcnt lgkmcnt(9)
ds_read_b128 v[184:187], v14 offset:23552
v_add_f32 v113, v113, v165
Mvav 96,168,232
s_waitcnt lgkmcnt(9)
v_lshlrev_b32 v166, 16, v49
v_add_f32 v114, v114, v166
Mvav 96,176,240
s_waitcnt lgkmcnt(8)
ds_read_b128 v[192:195], v14 offset:24576
v_lshlrev_b32 v166, 16, v53
Mvav 96,184,248
ds_read_b128 v[200:203], v14 offset:25600
v_add_f32 v114, v114, v166
Mvav 100,164,224
v_and_b32 v167, 0xffff0000, v49
v_add_f32 v115, v115, v167
Mvav 100,172,232
ds_read_b128 v[208:211], v14 offset:26624
v_and_b32 v167, 0xffff0000, v53
Mvav 100,180,240
buffer_load_dwordx4 v0, s[48:51], s72 offen offset:2048 sc1 lds
Mvav 100,188,248
s_waitcnt lgkmcnt(10)
ds_read_b128 v[216:219], v14 offset:27648
v_add_f32 v115, v115, v167
Mvav 96,192,128
s_waitcnt lgkmcnt(10)
v_lshlrev_b32 v164, 16, v50
v_add_f32 v116, v116, v164
Mvav 96,200,136
s_waitcnt lgkmcnt(9)
ds_read_b128 v[224:227], v14 offset:28672
v_lshlrev_b32 v164, 16, v54
Mvav 96,208,144
s_waitcnt lgkmcnt(9)
ds_read_b128 v[232:235], v14 offset:29696
v_add_f32 v116, v116, v164
Mvav 96,216,152
v_and_b32 v165, 0xffff0000, v50
v_add_f32 v117, v117, v165
Mvav 100,196,128
ds_read_b128 v[240:243], v14 offset:30720
v_and_b32 v165, 0xffff0000, v54
Mvav 100,204,136
buffer_load_dwordx4 v0, s[48:51], s72 offen offset:3072 sc1 lds
Mvav 100,212,144
ds_read_b128 v[248:251], v14 offset:31744
v_add_f32 v117, v117, v165
Mvav 100,220,152
s_waitcnt lgkmcnt(9)
v_lshlrev_b32 v166, 16, v51
v_add_f32 v118, v118, v166
Mvav 96,224,160
s_not_b32 s59, s89
buffer_load_dwordx2 v[30:31], v8, s[48:51], s33 offen sc1
Mvav 96,232,168
s_bfe_u32 s59, s59, 0x50004
v_lshlrev_b32 v166, 16, v55
v_add_f32 v118, v118, v166
Mvav 96,240,176
s_waitcnt lgkmcnt(8)
v_and_b32 v167, 0xffff0000, v51
v_add_f32 v119, v119, v167
Mvav 96,248,184
v_and_b32 v167, 0xffff0000, v55
v_add_f32 v119, v119, v167
Mvav 100,228,160
s_cmp_eq_u32 s59, s63
s_cbranch_scc1 .Lrt0
.Lrt0_r:
Mvav 100,236,168
s_not_b32 s59, s90
v_lshlrev_b32 v140, 16, v196
v_and_b32 v141, 0xffff0000, v196
Mvav 100,244,176
s_waitcnt lgkmcnt(0)
v_lshlrev_b32 v142, 16, v197
v_and_b32 v143, 0xffff0000, v197
Mvav 100,252,184
s_barrier
s_bfe_u32 s59, s59, 0x50004
Mvav 96,0,192
ds_read_b128 v[128:131], v12 offset:32768
v_lshlrev_b32 v180, 16, v198
Mvav 96,8,200
ds_read_b128 v[136:139], v12 offset:33792
v_and_b32 v181, 0xffff0000, v198
Mvav 96,16,208
s_cmp_eq_u32 s59, s63
s_cbranch_scc1 .Lrt1
.Lrt1_r:
Mvav 96,24,216
s_add_u32 s59, s68, 0
ds_read_b128 v[144:147], v12 offset:34816
v_lshlrev_b32 v182, 16, v199
Mvav 100,4,192
v_and_b32 v183, 0xffff0000, v199
v_fmac_f32 v140, v88, v64
Mvav 100,12,200
ds_read_b128 v[152:155], v12 offset:35840
v_fmac_f32 v141, v89, v65
Mvav 100,20,208
v_fmac_f32 v142, v90, v66
v_fmac_f32 v143, v91, v67
Mvav 100,28,216
ds_read_b128 v[160:163], v12 offset:36864
v_fmac_f32 v180, v92, v68
Mvav 96,32,224
ds_read_b128 v[168:171], v12 offset:37888
v_fmac_f32 v181, v93, v69
Mvav 96,40,232
v_fmac_f32 v182, v94, v70
v_fmac_f32 v183, v95, v71
Mvav 96,48,240
ds_read_b128 v[176:179], v12 offset:38912
v_cvt_pk_bf16_f32 v188, v140, v141
Mvav 96,56,248
v_cvt_pk_bf16_f32 v189, v142, v143
v_cvt_pk_bf16_f32 v190, v180, v181
Mvav 100,36,224
ds_read_b128 v[184:187], v12 offset:39936
v_cvt_pk_bf16_f32 v191, v182, v183
Mvav 100,44,232
buffer_store_dwordx4 v[188:191], v4, s[52:55], s59 offen
Mvav 100,52,240
s_waitcnt lgkmcnt(7)
ds_read_b128 v[192:195], v12 offset:40960
v_lshlrev_b32 v56, 16, v204
Mvav 100,60,248
s_add_u32 s59, s68, 1048576
ds_read_b128 v[200:203], v12 offset:41984
v_and_b32 v57, 0xffff0000, v204
Mvav 112,64,128
s_waitcnt lgkmcnt(8)
v_lshlrev_b32 v58, 16, v205
v_and_b32 v59, 0xffff0000, v205
Mvav 112,72,136
s_waitcnt lgkmcnt(7)
ds_read_b128 v[208:211], v12 offset:43008
v_lshlrev_b32 v24, 16, v206
Mvav 112,80,144
s_waitcnt lgkmcnt(7)
v_and_b32 v25, 0xffff0000, v206
v_lshlrev_b32 v26, 16, v207
Mvav 112,88,152
ds_read_b128 v[216:219], v12 offset:44032
v_and_b32 v27, 0xffff0000, v207
Mvav 116,68,128
v_lshlrev_b32 v140, 16, v212
v_and_b32 v141, 0xffff0000, v212
Mvav 116,76,136
ds_read_b128 v[224:227], v12 offset:45056
v_lshlrev_b32 v142, 16, v213
Mvav 116,84,144
ds_read_b128 v[232:235], v12 offset:46080
v_and_b32 v143, 0xffff0000, v213
Mvav 116,92,152
s_waitcnt lgkmcnt(6)
v_lshlrev_b32 v180, 16, v214
v_and_b32 v181, 0xffff0000, v214
Mvav 112,96,160
s_lshl_b32 s58, s78, 17
ds_read_b128 v[240:243], v12 offset:47104
v_and_or_b32 v96, v96, v2, s20
Mvav 112,104,168
s_add_u32 s58, s58, s26
v_and_or_b32 v97, v97, v2, s21
v_and_or_b32 v98, v98, v2, s22
Mvav 112,112,176
s_add_u32 s58, s58, 98304
ds_read_b128 v[248:251], v12 offset:48128
v_and_or_b32 v99, v99, v2, s23
Mvav 112,120,184
buffer_store_dwordx4 v[96:99], v0, s[48:51], s58 offen sc1
Mvav 116,100,160
ds_read_b128 v[128:131], v12 offset:49152
v_and_or_b32 v100, v100, v2, s20
Mvav 116,108,168
ds_read_b128 v[136:139], v12 offset:50176
v_and_or_b32 v101, v101, v2, s21
Mvav 116,116,176
v_and_or_b32 v102, v102, v2, s22
v_and_or_b32 v103, v103, v2, s23
Mvav 116,124,184
s_waitcnt lgkmcnt(9)
ds_read_b128 v[144:147], v12 offset:51200
v_lshlrev_b32 v182, 16, v215
Mvav 112,128,192
s_waitcnt lgkmcnt(9)
buffer_store_dwordx4 v[100:103], v0, s[48:51], s58 offen offset:1024 sc1
Mvav 112,136,200
s_waitcnt lgkmcnt(8)
ds_read_b128 v[152:155], v12 offset:52224
v_and_b32 v183, 0xffff0000, v215
Mvav 112,144,208
s_waitcnt lgkmcnt(8)
Mvav 112,152,216
ds_read_b128 v[160:163], v12 offset:53248
Mvav 116,132,192
ds_read_b128 v[168:171], v12 offset:54272
Mvav 116,140,200
Mvav 116,148,208
ds_read_b128 v[176:179], v12 offset:55296
Mvav 116,156,216
s_waitcnt lgkmcnt(10)
Mvav 112,160,224
s_waitcnt lgkmcnt(9)
ds_read_b128 v[184:187], v12 offset:56320
Mvav 112,168,232
s_waitcnt lgkmcnt(9)
Mvav 112,176,240
s_waitcnt lgkmcnt(8)
ds_read_b128 v[192:195], v12 offset:57344
Mvav 112,184,248
ds_read_b128 v[200:203], v12 offset:58368
Mvav 116,164,224
Mvav 116,172,232
ds_read_b128 v[208:211], v12 offset:59392
Mvav 116,180,240
Mvav 116,188,248
s_waitcnt lgkmcnt(10)
ds_read_b128 v[216:219], v12 offset:60416
Mvav 112,192,128
s_waitcnt lgkmcnt(10)
Mvav 112,200,136
s_waitcnt lgkmcnt(9)
ds_read_b128 v[224:227], v12 offset:61440
Mvav 112,208,144
s_waitcnt lgkmcnt(9)
ds_read_b128 v[232:235], v12 offset:62464
Mvav 112,216,152
Mvav 116,196,128
ds_read_b128 v[240:243], v12 offset:63488
Mvav 116,204,136
Mvav 116,212,144
ds_read_b128 v[248:251], v12 offset:64512
Mvav 116,220,152
s_waitcnt lgkmcnt(11)
Mvav 112,224,160
s_waitcnt lgkmcnt(10)
Mvav 112,232,168
s_waitcnt lgkmcnt(9)
Mvav 112,240,176
s_waitcnt lgkmcnt(8)
Mvav 112,248,184
Mvav 116,228,160
Mvav 116,236,168
Mvav 116,244,176
s_waitcnt vmcnt(3) lgkmcnt(0)
v_xor_b32 v30, s74, v30
v_bitop3_b32 v30, v31, s75, v30 bitop3:0xbe
Mvav 116,252,184
s_barrier
Mvav 112,0,192
ds_read_b128 v[128:131], v12 offset:0
v_cmp_ne_u32 vcc, 0, v30
Mvav 112,8,200
s_and_b64 vcc, vcc, 0xf00
ds_read_b128 v[136:139], v12 offset:1024
)"
R"(
Mvav 112,16,208
s_cbranch_vccnz .Lpf2
.Lpf2_r:
s_lshl_b32 s73, s76, 13
Mvav 112,24,216
s_add_u32 s73, s73, s27
ds_read_b128 v[144:147], v12 offset:2048
Mvav 116,4,192
s_add_u32 s73, s73, s32
Mvav 116,12,200
s_add_u32 s73, s73, 65536
ds_read_b128 v[152:155], v12 offset:3072
Mvav 116,20,208
buffer_load_dwordx4 v[32:35], v0, s[48:51], s73 offen sc1
Mvav 116,28,216
s_lshl_b32 s73, s77, 13
ds_read_b128 v[160:163], v12 offset:4096
Mvav 112,32,224
s_add_u32 s73, s73, s27
ds_read_b128 v[168:171], v12 offset:5120
Mvav 112,40,232
s_add_u32 s73, s73, s32
Mvav 112,48,240
s_add_u32 s73, s73, 65536
ds_read_b128 v[176:179], v12 offset:6144
Mvav 112,56,248
buffer_load_dwordx4 v[36:39], v0, s[48:51], s73 offen sc1
Mvav 116,36,224
s_lshl_b32 s73, s78, 13
ds_read_b128 v[184:187], v12 offset:7168
Mvav 116,44,232
s_add_u32 s73, s73, s27
Mvav 116,52,240
s_add_u32 s73, s73, s32
ds_read_b128 v[192:195], v12 offset:8192
Mvav 116,60,248
s_waitcnt lgkmcnt(7)
s_add_u32 s73, s73, 65536
ds_read_b128 v[200:203], v12 offset:9216
Zvav 120,64,128
s_lshl_b32 s92, s76, 13
buffer_load_dwordx4 v[40:43], v0, s[48:51], s73 offen sc1
Mvav 120,72,136
s_waitcnt lgkmcnt(7)
s_add_u32 s92, s92, s27
ds_read_b128 v[208:211], v12 offset:10240
Mvav 120,80,144
s_waitcnt lgkmcnt(7)
s_add_u32 s92, s92, 0x18000
Mvav 120,88,152
s_lshl_b32 s93, s77, 13
ds_read_b128 v[216:219], v12 offset:11264
Zvav 124,68,128
s_add_u32 s93, s93, s27
Mvav 124,76,136
s_add_u32 s93, s93, 0x18000
ds_read_b128 v[224:227], v12 offset:12288
Mvav 124,84,144
s_lshl_b32 s94, s78, 13
ds_read_b128 v[232:235], v12 offset:13312
Mvav 124,92,152
s_waitcnt lgkmcnt(8)
s_add_u32 s94, s94, s27
Mvav 120,96,160
s_add_u32 s94, s94, 0x18000
ds_read_b128 v[240:243], v12 offset:14336
v_fmac_f32 v56, v112, v72
Mvav 120,104,168
s_waitcnt lgkmcnt(8)
v_fmac_f32 v57, v113, v73
v_fmac_f32 v58, v114, v74
Mvav 120,112,176
s_waitcnt lgkmcnt(7)
ds_read_b128 v[248:251], v12 offset:15360
v_fmac_f32 v59, v115, v75
Mvav 120,120,184
v_fmac_f32 v24, v116, v76
v_fmac_f32 v25, v117, v77
Mvav 124,100,160
ds_read_b128 v[128:131], v12 offset:16384
v_fmac_f32 v26, v118, v78
Mvav 124,108,168
ds_read_b128 v[136:139], v12 offset:17408
v_fmac_f32 v27, v119, v79
Mvav 124,116,176
v_cvt_pk_bf16_f32 v132, v56, v57
v_cvt_pk_bf16_f32 v133, v58, v59
Mvav 124,124,184
s_waitcnt lgkmcnt(9)
ds_read_b128 v[144:147], v12 offset:18432
v_cvt_pk_bf16_f32 v134, v24, v25
Mvav 120,128,192
s_waitcnt lgkmcnt(9)
v_cvt_pk_bf16_f32 v135, v26, v27
v_lshlrev_b32 v56, 16, v220
Mvav 120,136,200
s_waitcnt lgkmcnt(8)
ds_read_b128 v[152:155], v12 offset:19456
v_and_b32 v57, 0xffff0000, v220
Mvav 120,144,208
s_waitcnt lgkmcnt(8)
buffer_store_dwordx4 v[132:135], v4, s[52:55], s59 offen
Mvav 120,152,216
ds_read_b128 v[160:163], v12 offset:20480
v_lshlrev_b32 v58, 16, v221
Mvav 124,132,192
s_add_u32 s59, s68, 2097152
ds_read_b128 v[168:171], v12 offset:21504
v_and_b32 v59, 0xffff0000, v221
Mvav 124,140,200
v_lshlrev_b32 v24, 16, v222
v_and_b32 v25, 0xffff0000, v222
Mvav 124,148,208
ds_read_b128 v[176:179], v12 offset:22528
v_lshlrev_b32 v26, 16, v223
Mvav 124,156,216
s_waitcnt lgkmcnt(10)
v_and_b32 v27, 0xffff0000, v223
Mvav 120,160,224
s_waitcnt lgkmcnt(9)
ds_read_b128 v[184:187], v12 offset:23552
Mvav 120,168,232
s_waitcnt lgkmcnt(9)
Mvav 120,176,240
s_waitcnt lgkmcnt(8)
ds_read_b128 v[192:195], v12 offset:24576
Mvav 120,184,248
ds_read_b128 v[200:203], v12 offset:25600
Mvav 124,164,224
Mvav 124,172,232
ds_read_b128 v[208:211], v12 offset:26624
Mvav 124,180,240
Mvav 124,188,248
s_waitcnt lgkmcnt(10)
ds_read_b128 v[216:219], v12 offset:27648
Mvav 120,192,128
s_waitcnt lgkmcnt(10)
Mvav 120,200,136
s_waitcnt lgkmcnt(9)
ds_read_b128 v[224:227], v12 offset:28672
Mvav 120,208,144
s_waitcnt lgkmcnt(9)
ds_read_b128 v[232:235], v12 offset:29696
Mvav 120,216,152
Mvav 124,196,128
ds_read_b128 v[240:243], v12 offset:30720
Mvav 124,204,136
Mvav 124,212,144
ds_read_b128 v[248:251], v12 offset:31744
Mvav 124,220,152
s_waitcnt lgkmcnt(11)
Mvav 120,224,160
s_waitcnt lgkmcnt(10)
Mvav 120,232,168
s_waitcnt lgkmcnt(9)
Mvav 120,240,176
s_waitcnt lgkmcnt(8)
Mvav 120,248,184
Mvav 124,228,160
Mvav 124,236,168
Mvav 124,244,176
s_waitcnt lgkmcnt(0)
Mvav 124,252,184
s_barrier
Mvav 120,0,192
ds_read_b128 v[128:131], v0 offset:32768
Mvav 120,8,200
ds_read_b128 v[136:139], v0 offset:33792
Mvav 120,16,208
Mvav 120,24,216
ds_read_b128 v[144:147], v0 offset:34816
Mvav 124,4,192
Mvav 124,12,200
ds_read_b128 v[152:155], v0 offset:35840
Mvav 124,20,208
Mvav 124,28,216
ds_read_b128 v[160:163], v0 offset:36864
Mvav 120,32,224
ds_read_b128 v[168:171], v0 offset:37888
Mvav 120,40,232
Mvav 120,48,240
ds_read_b128 v[176:179], v0 offset:38912
Mvav 120,56,248
Mvav 124,36,224
s_waitcnt vmcnt(3)
ds_read_b128 v[184:187], v0 offset:39936
v_lshlrev_b32 v44, 16, v32
Mvav 124,44,232
v_and_b32 v45, 0xffff0000, v32
v_lshlrev_b32 v46, 16, v33
Mvav 124,52,240
ds_read_b128 v[192:195], v0 offset:40960
v_and_b32 v47, 0xffff0000, v33
Mvav 124,60,248
s_waitcnt lgkmcnt(8)
ds_read_b128 v[200:203], v0 offset:41984
v_lshlrev_b32 v48, 16, v34
Zvav 96,64,128
s_waitcnt lgkmcnt(8)
v_and_b32 v49, 0xffff0000, v34
v_lshlrev_b32 v50, 16, v35
Mvav 96,72,136
s_waitcnt vmcnt(2) lgkmcnt(7)
ds_read_b128 v[208:211], v0 offset:43008
v_lshlrev_b32 v52, 16, v36
Mvav 96,80,144
s_waitcnt lgkmcnt(7)
v_add_f32 v44, v44, v52
v_and_b32 v53, 0xffff0000, v36
Mvav 96,88,152
ds_read_b128 v[216:219], v0 offset:44032
v_add_f32 v45, v45, v53
Zvav 100,68,128
buffer_load_dwordx4 v[148:151], v0, s[48:51], s92 offen sc1
Mvav 100,76,136
ds_read_b128 v[224:227], v0 offset:45056
v_lshlrev_b32 v54, 16, v37
Mvav 100,84,144
s_waitcnt vmcnt(2)
ds_read_b128 v[232:235], v0 offset:46080
v_lshlrev_b32 v52, 16, v40
Mvav 100,92,152
s_waitcnt lgkmcnt(9)
buffer_load_dwordx4 v[156:159], v0, s[48:51], s92 offen offset:1024 sc1
Mvav 96,96,160
s_waitcnt lgkmcnt(8)
ds_read_b128 v[240:243], v0 offset:47104
v_add_f32 v44, v44, v52
Mvav 96,104,168
s_waitcnt lgkmcnt(8)
buffer_load_dwordx4 v[164:167], v0, s[48:51], s93 offen sc1
Mvav 96,112,176
s_waitcnt lgkmcnt(7)
ds_read_b128 v[248:251], v0 offset:48128
v_and_b32 v53, 0xffff0000, v40
Mvav 96,120,184
buffer_load_dwordx4 v[172:175], v0, s[48:51], s93 offen offset:1024 sc1
Mvav 100,100,160
ds_read_b128 v[128:131], v0 offset:49152
v_add_f32 v45, v45, v53
Mvav 100,108,168
ds_read_b128 v[136:139], v0 offset:50176
v_add_f32 v46, v46, v54
Mvav 100,116,176
buffer_load_dwordx4 v[228:231], v0, s[48:51], s94 offen sc1
Mvav 100,124,184
s_waitcnt lgkmcnt(9)
ds_read_b128 v[144:147], v0 offset:51200
v_lshlrev_b32 v54, 16, v41
Mvav 96,128,192
s_waitcnt lgkmcnt(9)
buffer_load_dwordx4 v[236:239], v0, s[48:51], s94 offen offset:1024 sc1
Mvav 96,136,200
s_waitcnt lgkmcnt(8)
ds_read_b128 v[152:155], v0 offset:52224
v_add_f32 v46, v46, v54
Mvav 96,144,208
s_waitcnt lgkmcnt(8)
v_and_b32 v55, 0xffff0000, v37
v_add_f32 v47, v47, v55
Mvav 96,152,216
ds_read_b128 v[160:163], v0 offset:53248
v_and_b32 v55, 0xffff0000, v41
Mvav 100,132,192
ds_read_b128 v[168:171], v0 offset:54272
v_add_f32 v47, v47, v55
Mvav 100,140,200
v_lshlrev_b32 v52, 16, v38
v_add_f32 v48, v48, v52
Mvav 100,148,208
ds_read_b128 v[176:179], v0 offset:55296
v_lshlrev_b32 v52, 16, v42
Mvav 100,156,216
s_waitcnt lgkmcnt(10)
v_add_f32 v48, v48, v52
v_and_b32 v53, 0xffff0000, v38
Mvav 96,160,224
s_waitcnt lgkmcnt(9)
ds_read_b128 v[184:187], v0 offset:56320
v_add_f32 v49, v49, v53
Mvav 96,168,232
s_waitcnt lgkmcnt(9)
v_and_b32 v53, 0xffff0000, v42
v_add_f32 v49, v49, v53
Mvav 96,176,240
s_waitcnt lgkmcnt(8)
ds_read_b128 v[192:195], v0 offset:57344
v_lshlrev_b32 v54, 16, v39
Mvav 96,184,248
ds_read_b128 v[200:203], v0 offset:58368
v_add_f32 v50, v50, v54
Mvav 100,164,224
v_lshlrev_b32 v54, 16, v43
v_add_f32 v50, v50, v54
Mvav 100,172,232
ds_read_b128 v[208:211], v0 offset:59392
v_and_b32 v51, 0xffff0000, v35
Mvav 100,180,240
v_and_b32 v55, 0xffff0000, v39
v_add_f32 v51, v51, v55
Mvav 100,188,248
s_waitcnt lgkmcnt(10)
ds_read_b128 v[216:219], v0 offset:60416
v_and_b32 v55, 0xffff0000, v43
Mvav 96,192,128
s_waitcnt lgkmcnt(10)
v_add_f32 v51, v51, v55
v_add_f32 v120, v120, v44
Mvav 96,200,136
s_waitcnt lgkmcnt(9)
ds_read_b128 v[224:227], v0 offset:61440
v_add_f32 v121, v121, v45
Mvav 96,208,144
s_waitcnt lgkmcnt(9)
ds_read_b128 v[232:235], v0 offset:62464
v_add_f32 v122, v122, v46
Mvav 96,216,152
v_add_f32 v123, v123, v47
v_add_f32 v124, v124, v48
Mvav 100,196,128
ds_read_b128 v[240:243], v0 offset:63488
v_add_f32 v125, v125, v49
Mvav 100,204,136
v_add_f32 v126, v126, v50
v_add_f32 v127, v127, v51
Mvav 100,212,144
ds_read_b128 v[248:251], v0 offset:64512
v_fmac_f32 v140, v120, v80
Mvav 100,220,152
s_waitcnt lgkmcnt(11)
v_fmac_f32 v141, v121, v81
v_fmac_f32 v142, v122, v82
Mvav 96,224,160
s_waitcnt lgkmcnt(10)
v_fmac_f32 v143, v123, v83
v_fmac_f32 v180, v124, v84
Mvav 96,232,168
s_waitcnt lgkmcnt(9)
v_fmac_f32 v181, v125, v85
v_fmac_f32 v182, v126, v86
Mvav 96,240,176
s_waitcnt lgkmcnt(8)
v_fmac_f32 v183, v127, v87
v_cvt_pk_bf16_f32 v188, v140, v141
Mvav 96,248,184
v_cvt_pk_bf16_f32 v189, v142, v143
v_cvt_pk_bf16_f32 v190, v180, v181
Mvav 100,228,160
v_cvt_pk_bf16_f32 v191, v182, v183
Mvav 100,236,168
buffer_store_dwordx4 v[188:191], v4, s[52:55], s59 offen
Mvav 100,244,176
Mvav 100,252,184
s_waitcnt lgkmcnt(7)
s_add_u32 s59, s68, 3145728
Mvav 96,0,192
s_waitcnt lgkmcnt(6)
Mvav 96,8,200
s_waitcnt lgkmcnt(5)
Mvav 96,16,208
s_waitcnt lgkmcnt(4)
Mvav 96,24,216
Mvav 100,4,192
Mvav 100,12,200
Mvav 100,20,208
Mvav 100,28,216
s_waitcnt lgkmcnt(3)
Mvav 96,32,224
s_waitcnt lgkmcnt(2)
Mvav 96,40,232
s_waitcnt lgkmcnt(1)
Mvav 96,48,240
s_waitcnt lgkmcnt(0)
Mvav 96,56,248
Mvav 100,36,224
Mvav 100,44,232
Mvav 100,52,240
Mvav 100,60,248
s_waitcnt vmcnt(6)
v_xor_b32 v148, s20, v148
v_xor_b32 v149, s21, v149
v_xor_b32 v150, s22, v150
v_xor_b32 v151, s23, v151
s_waitcnt vmcnt(5)
v_xor_b32 v156, s20, v156
v_xor_b32 v157, s21, v157
v_xor_b32 v158, s22, v158
v_xor_b32 v159, s23, v159
s_waitcnt vmcnt(4)
v_xor_b32 v164, s20, v164
v_xor_b32 v165, s21, v165
v_xor_b32 v166, s22, v166
v_xor_b32 v167, s23, v167
s_waitcnt vmcnt(3)
v_xor_b32 v172, s20, v172
v_xor_b32 v173, s21, v173
v_xor_b32 v174, s22, v174
v_xor_b32 v175, s23, v175
s_waitcnt vmcnt(2)
v_xor_b32 v228, s20, v228
v_xor_b32 v229, s21, v229
v_xor_b32 v230, s22, v230
v_xor_b32 v231, s23, v231
s_waitcnt vmcnt(1)
v_xor_b32 v236, s20, v236
v_xor_b32 v237, s21, v237
v_xor_b32 v238, s22, v238
v_xor_b32 v239, s23, v239
v_or3_b32 v9, v148, v149, v150
v_or3_b32 v9, v9, v151, v156
v_or3_b32 v9, v9, v157, v158
v_or3_b32 v9, v9, v159, v164
v_or3_b32 v9, v9, v165, v166
v_or3_b32 v9, v9, v167, v172
v_or3_b32 v9, v9, v173, v174
v_or3_b32 v9, v9, v175, v228
v_or3_b32 v9, v9, v229, v230
v_or3_b32 v9, v9, v231, v236
v_or3_b32 v9, v9, v237, v238
v_or_b32 v9, v9, v239
v_and_b32 v9, 0xff, v9
v_cmp_ne_u32 vcc, 0, v9
s_nop 0
s_cbranch_vccnz .Lpc3
.Lpc3_r:
v_add_f32 v148, v148, v164
v_add_f32 v149, v149, v165
v_add_f32 v150, v150, v166
v_add_f32 v151, v151, v167
v_add_f32 v156, v156, v172
v_add_f32 v157, v157, v173
v_add_f32 v158, v158, v174
v_add_f32 v159, v159, v175
v_add_f32 v148, v148, v228
v_add_f32 v149, v149, v229
v_add_f32 v150, v150, v230
v_add_f32 v151, v151, v231
v_add_f32 v156, v156, v236
v_add_f32 v157, v157, v237
v_add_f32 v158, v158, v238
v_add_f32 v159, v159, v239
v_add_f32 v96, v96, v148
v_add_f32 v97, v97, v149
v_add_f32 v98, v98, v150
v_add_f32 v99, v99, v151
v_add_f32 v100, v100, v156
v_add_f32 v101, v101, v157
v_add_f32 v102, v102, v158
v_add_f32 v103, v103, v159
v_fmac_f32 v56, v96, v16
v_fmac_f32 v57, v97, v17
v_fmac_f32 v58, v98, v18
v_fmac_f32 v59, v99, v19
v_fmac_f32 v24, v100, v20
v_fmac_f32 v25, v101, v21
v_fmac_f32 v26, v102, v22
v_fmac_f32 v27, v103, v23
v_cvt_pk_bf16_f32 v132, v56, v57
v_cvt_pk_bf16_f32 v133, v58, v59
v_cvt_pk_bf16_f32 v134, v24, v25
v_cvt_pk_bf16_f32 v135, v26, v27
buffer_store_dwordx4 v[132:135], v4, s[52:55], s59 offen
s_endpgm
.Lbepi:
s_mov_b32 s99, s98
v_mov_b32 v132, s74
v_mov_b32 v133, s75
v_rcp_f32 v64, v114
v_rcp_f32 v65, v115
s_add_u32 s84, s96, 4096
v_accvgpr_read_b32 v66, a64
v_accvgpr_read_b32 v67, a65
)"
R"(
v_mul_f32 v66, v66, v64
v_mul_f32 v67, v67, v64
v_cvt_pk_bf16_f32 v68, v66, v67
v_accvgpr_read_b32 v66, a66
v_accvgpr_read_b32 v67, a67
v_mul_f32 v66, v66, v64
v_mul_f32 v67, v67, v64
v_cvt_pk_bf16_f32 v69, v66, v67
v_accvgpr_read_b32 v66, a72
v_accvgpr_read_b32 v67, a73
v_mul_f32 v66, v66, v64
v_mul_f32 v67, v67, v64
v_cvt_pk_bf16_f32 v70, v66, v67
v_accvgpr_read_b32 v66, a74
v_accvgpr_read_b32 v67, a75
v_mul_f32 v66, v66, v64
v_mul_f32 v67, v67, v64
v_cvt_pk_bf16_f32 v71, v66, v67
buffer_store_dwordx4 v[68:71], v0, s[48:51], s96 offen sc1
v_accvgpr_read_b32 v66, a68
v_accvgpr_read_b32 v67, a69
v_mul_f32 v66, v66, v65
v_mul_f32 v67, v67, v65
v_cvt_pk_bf16_f32 v72, v66, v67
v_accvgpr_read_b32 v66, a70
v_accvgpr_read_b32 v67, a71
v_mul_f32 v66, v66, v65
v_mul_f32 v67, v67, v65
v_cvt_pk_bf16_f32 v73, v66, v67
v_accvgpr_read_b32 v66, a76
v_accvgpr_read_b32 v67, a77
v_mul_f32 v66, v66, v65
v_mul_f32 v67, v67, v65
v_cvt_pk_bf16_f32 v74, v66, v67
v_accvgpr_read_b32 v66, a78
v_accvgpr_read_b32 v67, a79
v_mul_f32 v66, v66, v65
v_mul_f32 v67, v67, v65
v_cvt_pk_bf16_f32 v75, v66, v67
buffer_store_dwordx4 v[72:75], v0, s[48:51], s84 offen sc1
v_accvgpr_read_b32 v66, a80
v_accvgpr_read_b32 v67, a81
v_mul_f32 v66, v66, v64
v_mul_f32 v67, v67, v64
v_cvt_pk_bf16_f32 v68, v66, v67
v_accvgpr_read_b32 v66, a82
v_accvgpr_read_b32 v67, a83
v_mul_f32 v66, v66, v64
v_mul_f32 v67, v67, v64
v_cvt_pk_bf16_f32 v69, v66, v67
v_accvgpr_read_b32 v66, a88
v_accvgpr_read_b32 v67, a89
v_mul_f32 v66, v66, v64
v_mul_f32 v67, v67, v64
v_cvt_pk_bf16_f32 v70, v66, v67
v_accvgpr_read_b32 v66, a90
v_accvgpr_read_b32 v67, a91
v_mul_f32 v66, v66, v64
v_mul_f32 v67, v67, v64
v_cvt_pk_bf16_f32 v71, v66, v67
buffer_store_dwordx4 v[68:71], v0, s[48:51], s96 offen offset:1024 sc1
v_accvgpr_read_b32 v66, a84
v_accvgpr_read_b32 v67, a85
v_mul_f32 v66, v66, v65
v_mul_f32 v67, v67, v65
v_cvt_pk_bf16_f32 v72, v66, v67
v_accvgpr_read_b32 v66, a86
v_accvgpr_read_b32 v67, a87
v_mul_f32 v66, v66, v65
v_mul_f32 v67, v67, v65
v_cvt_pk_bf16_f32 v73, v66, v67
v_accvgpr_read_b32 v66, a92
v_accvgpr_read_b32 v67, a93
v_mul_f32 v66, v66, v65
v_mul_f32 v67, v67, v65
v_cvt_pk_bf16_f32 v74, v66, v67
v_accvgpr_read_b32 v66, a94
v_accvgpr_read_b32 v67, a95
v_mul_f32 v66, v66, v65
v_mul_f32 v67, v67, v65
v_cvt_pk_bf16_f32 v75, v66, v67
buffer_store_dwordx4 v[72:75], v0, s[48:51], s84 offen offset:1024 sc1
v_accvgpr_read_b32 v66, a96
v_accvgpr_read_b32 v67, a97
v_mul_f32 v66, v66, v64
v_mul_f32 v67, v67, v64
v_cvt_pk_bf16_f32 v68, v66, v67
v_accvgpr_read_b32 v66, a98
v_accvgpr_read_b32 v67, a99
v_mul_f32 v66, v66, v64
v_mul_f32 v67, v67, v64
v_cvt_pk_bf16_f32 v69, v66, v67
v_accvgpr_read_b32 v66, a104
v_accvgpr_read_b32 v67, a105
v_mul_f32 v66, v66, v64
v_mul_f32 v67, v67, v64
v_cvt_pk_bf16_f32 v70, v66, v67
v_accvgpr_read_b32 v66, a106
v_accvgpr_read_b32 v67, a107
v_mul_f32 v66, v66, v64
v_mul_f32 v67, v67, v64
v_cvt_pk_bf16_f32 v71, v66, v67
buffer_store_dwordx4 v[68:71], v0, s[48:51], s96 offen offset:2048 sc1
v_accvgpr_read_b32 v66, a100
v_accvgpr_read_b32 v67, a101
v_mul_f32 v66, v66, v65
v_mul_f32 v67, v67, v65
v_cvt_pk_bf16_f32 v72, v66, v67
v_accvgpr_read_b32 v66, a102
v_accvgpr_read_b32 v67, a103
v_mul_f32 v66, v66, v65
v_mul_f32 v67, v67, v65
v_cvt_pk_bf16_f32 v73, v66, v67
v_accvgpr_read_b32 v66, a108
v_accvgpr_read_b32 v67, a109
v_mul_f32 v66, v66, v65
v_mul_f32 v67, v67, v65
v_cvt_pk_bf16_f32 v74, v66, v67
v_accvgpr_read_b32 v66, a110
v_accvgpr_read_b32 v67, a111
v_mul_f32 v66, v66, v65
v_mul_f32 v67, v67, v65
v_cvt_pk_bf16_f32 v75, v66, v67
buffer_store_dwordx4 v[72:75], v0, s[48:51], s84 offen offset:2048 sc1
v_accvgpr_read_b32 v66, a112
v_accvgpr_read_b32 v67, a113
v_mul_f32 v66, v66, v64
v_mul_f32 v67, v67, v64
v_cvt_pk_bf16_f32 v68, v66, v67
v_accvgpr_read_b32 v66, a114
v_accvgpr_read_b32 v67, a115
v_mul_f32 v66, v66, v64
v_mul_f32 v67, v67, v64
v_cvt_pk_bf16_f32 v69, v66, v67
v_accvgpr_read_b32 v66, a120
v_accvgpr_read_b32 v67, a121
v_mul_f32 v66, v66, v64
v_mul_f32 v67, v67, v64
v_cvt_pk_bf16_f32 v70, v66, v67
v_accvgpr_read_b32 v66, a122
v_accvgpr_read_b32 v67, a123
v_mul_f32 v66, v66, v64
v_mul_f32 v67, v67, v64
v_cvt_pk_bf16_f32 v71, v66, v67
buffer_store_dwordx4 v[68:71], v0, s[48:51], s96 offen offset:3072 sc1
v_accvgpr_read_b32 v66, a116
v_accvgpr_read_b32 v67, a117
v_mul_f32 v66, v66, v65
v_mul_f32 v67, v67, v65
v_cvt_pk_bf16_f32 v72, v66, v67
v_accvgpr_read_b32 v66, a118
v_accvgpr_read_b32 v67, a119
v_mul_f32 v66, v66, v65
v_mul_f32 v67, v67, v65
v_cvt_pk_bf16_f32 v73, v66, v67
v_accvgpr_read_b32 v66, a124
v_accvgpr_read_b32 v67, a125
v_mul_f32 v66, v66, v65
v_mul_f32 v67, v67, v65
v_cvt_pk_bf16_f32 v74, v66, v67
v_accvgpr_read_b32 v66, a126
v_accvgpr_read_b32 v67, a127
v_mul_f32 v66, v66, v65
v_mul_f32 v67, v67, v65
v_cvt_pk_bf16_f32 v75, v66, v67
buffer_store_dwordx4 v[72:75], v0, s[48:51], s84 offen offset:3072 sc1
s_setpc_b64 s[16:17]
.Lsb:
v_bfe_u32 v26, v0, 6, 2
v_lshlrev_b32 v26, 6, v26
v_bfe_u32 v27, v0, 4, 2
v_lshl_add_u32 v26, v27, 3, v26
v_lshrrev_b32 v27, 8, v0
v_lshl_add_u32 v26, v27, 10, v26
s_add_u32 s88, s69, 131072
v_add_u32 v26, s88, v26
s_waitcnt vmcnt(16)
ds_read_b64_tr_b16 v[32:33], v26 offset:0
ds_read_b64_tr_b16 v[34:35], v26 offset:256
ds_read_b64_tr_b16 v[36:37], v26 offset:4096
ds_read_b64_tr_b16 v[38:39], v26 offset:4352
ds_read_b64_tr_b16 v[40:41], v26 offset:32
ds_read_b64_tr_b16 v[42:43], v26 offset:288
ds_read_b64_tr_b16 v[44:45], v26 offset:4128
ds_read_b64_tr_b16 v[46:47], v26 offset:4384
ds_read_b64_tr_b16 v[48:49], v26 offset:512
ds_read_b64_tr_b16 v[50:51], v26 offset:768
ds_read_b64_tr_b16 v[52:53], v26 offset:4608
ds_read_b64_tr_b16 v[54:55], v26 offset:4864
ds_read_b64_tr_b16 v[56:57], v26 offset:544
ds_read_b64_tr_b16 v[58:59], v26 offset:800
ds_read_b64_tr_b16 v[60:61], v26 offset:4640
s_waitcnt lgkmcnt(14)
ds_read_b64_tr_b16 v[62:63], v26 offset:4896
v_and_b32 v27, 16, v0
v_lshrrev_b32 v27, v27, s74
v_lshrrev_b32 v120, 8, v27
v_lshrrev_b32 v121, 1, v0
v_mov_b32 v124, 0xff
s_lshr_b32 s59, s60, 3
s_lshl2_add_u32 s59, s91, s59
s_lshl2_add_u32 s59, s59, s57
s_lshl_b32 s59, s59, 10
s_add_u32 s59, s59, 1048576
s_cmp_eq_u32 s61, 0
s_cselect_b32 s58, 0x80000000, s59
s_waitcnt lgkmcnt(14)
Zvvv 196,16,32
s_waitcnt lgkmcnt(12)
Mvvv 196,20,36
s_waitcnt lgkmcnt(10)
Zvvv 204,16,40
s_waitcnt lgkmcnt(8)
Mvvv 204,20,44
s_waitcnt lgkmcnt(6)
Zvvv 212,16,48
s_waitcnt lgkmcnt(4)
Mvvv 212,20,52
s_waitcnt lgkmcnt(2)
Zvvv 220,16,56
s_waitcnt lgkmcnt(0)
Mvvv 220,20,60
s_mov_b64 s[36:37], s[40:41]
s_lshr_b32 s59, s60, 3
s_lshl2_add_u32 s59, s59, s57
s_mul_i32 s59, s59, 458752
s_lshl_b32 s68, s91, 7
s_add_u32 s68, s68, s59
s_add_u32 s71, s69, 131072
s_add_u32 s73, s93, 4
s_mov_b32 s62, 57344
s_mov_b32 s63, 114688
s_mov_b32 s64, 2048
s_cmp_eq_u32 s61, 0
s_cselect_b32 s73, 0, s73
s_mov_b64 s[20:21], s[44:45]
s_mov_b64 s[22:23], s[46:47]
s_sub_u32 s95, s95, 0x800000
s_mov_b32 vcc_lo, 0xffff0000
s_mov_b32 vcc_hi, 0xffff0000
v_cndmask_b32 v196, v196, v204, vcc
v_cndmask_b32 v197, v197, v205, vcc
v_cndmask_b32 v212, v212, v220, vcc
v_cndmask_b32 v213, v213, v221, vcc
s_mov_b64 vcc, 0xffffffff
v_cndmask_b32 v122, v212, v196, vcc
v_cndmask_b32 v123, v213, v197, vcc
v_bfi_b32 v122, v124, v27, v122
v_bfi_b32 v123, v124, v120, v123
buffer_store_dwordx2 v[122:123], v121, s[48:51], s58 offen sc1
s_nop 1
s_swappc_b64 s[32:33], s[16:17]
v_bfe_u32 v26, v0, 6, 2
v_lshlrev_b32 v26, 6, v26
v_bfe_u32 v27, v0, 4, 2
v_lshl_add_u32 v26, v27, 3, v26
v_lshrrev_b32 v27, 8, v0
v_lshl_add_u32 v26, v27, 10, v26
s_add_u32 s88, s69, 131072
v_add_u32 v26, s88, v26
s_waitcnt vmcnt(16)
ds_read_b64_tr_b16 v[32:33], v26 offset:0
ds_read_b64_tr_b16 v[34:35], v26 offset:256
ds_read_b64_tr_b16 v[36:37], v26 offset:4096
ds_read_b64_tr_b16 v[38:39], v26 offset:4352
ds_read_b64_tr_b16 v[40:41], v26 offset:32
ds_read_b64_tr_b16 v[42:43], v26 offset:288
ds_read_b64_tr_b16 v[44:45], v26 offset:4128
ds_read_b64_tr_b16 v[46:47], v26 offset:4384
ds_read_b64_tr_b16 v[48:49], v26 offset:512
ds_read_b64_tr_b16 v[50:51], v26 offset:768
ds_read_b64_tr_b16 v[52:53], v26 offset:4608
ds_read_b64_tr_b16 v[54:55], v26 offset:4864
ds_read_b64_tr_b16 v[56:57], v26 offset:544
ds_read_b64_tr_b16 v[58:59], v26 offset:800
ds_read_b64_tr_b16 v[60:61], v26 offset:4640
s_waitcnt lgkmcnt(14)
ds_read_b64_tr_b16 v[62:63], v26 offset:4896
v_and_b32 v27, 16, v0
v_lshrrev_b32 v27, v27, s74
v_lshrrev_b32 v120, 8, v27
v_lshrrev_b32 v121, 1, v0
v_mov_b32 v124, 0xff
s_lshr_b32 s59, s60, 3
s_lshl2_add_u32 s59, s91, s59
s_lshl2_add_u32 s59, s59, s57
s_lshl_b32 s59, s59, 10
s_add_u32 s59, s59, 1049088
s_cmp_eq_u32 s61, 0
s_cselect_b32 s58, 0x80000000, s59
s_waitcnt lgkmcnt(14)
Zvvv 196,16,32
s_waitcnt lgkmcnt(12)
Mvvv 196,20,36
s_waitcnt lgkmcnt(10)
Zvvv 204,16,40
s_waitcnt lgkmcnt(8)
Mvvv 204,20,44
s_waitcnt lgkmcnt(6)
Zvvv 212,16,48
s_waitcnt lgkmcnt(4)
Mvvv 212,20,52
s_waitcnt lgkmcnt(2)
Zvvv 220,16,56
s_waitcnt lgkmcnt(0)
Mvvv 220,20,60
v_bfe_u32 v25, v0, 6, 3
v_lshrrev_b32 v126, 9, v0
v_lshlrev_b32 v126, 6, v126
v_lshl_add_u32 v25, v25, 11, v126
v_and_b32 v126, 48, v0
v_add_u32 v25, v25, v126
s_mov_b64 s[36:37], s[28:29]
s_mov_b32 s38, s30
s_lshl3_add_u32 s68, s70, s67
s_add_u32 s71, s69, 131072
s_add_u32 s73, s93, 4
s_mov_b32 s62, 128
s_mov_b32 s63, 16384
s_mov_b32 s64, 2048
s_lshr_b32 s83, s86, 1
s_lshl2_add_u32 s83, s83, s79
s_lshl_b32 s83, s83, 10
s_add_u32 s83, s83, 32768
buffer_load_dwordx4 v[16:19], v0, s[48:51], s83 offen sc1
s_mov_b32 vcc_lo, 0xffff0000
s_mov_b32 vcc_hi, 0xffff0000
v_cndmask_b32 v196, v196, v204, vcc
v_cndmask_b32 v197, v197, v205, vcc
v_cndmask_b32 v212, v212, v220, vcc
v_cndmask_b32 v213, v213, v221, vcc
s_mov_b64 vcc, 0xffffffff
v_cndmask_b32 v122, v212, v196, vcc
v_cndmask_b32 v123, v213, v197, vcc
v_bfi_b32 v122, v124, v27, v122
v_bfi_b32 v123, v124, v120, v123
buffer_store_dwordx2 v[122:123], v121, s[48:51], s58 offen sc1
s_nop 1
s_swappc_b64 s[32:33], s[16:17]
s_waitcnt vmcnt(16)
v_xor_b32 v16, s74, v16
v_bitop3_b32 v16, v17, s75, v16 bitop3:0xbe
v_bitop3_b32 v16, v18, s74, v16 bitop3:0xbe
v_bitop3_b32 v16, v19, s75, v16 bitop3:0xbe
v_cmp_ne_u32 vcc, 0, v16
s_nop 0
s_and_b32 vcc_lo, vcc_lo, 0xf000f
s_and_b32 vcc_hi, vcc_hi, 0xf000f
s_cbranch_vccnz .Lcf1
.Lcf1_r:
s_lshl2_add_u32 s58, s60, s57
s_lshl_b32 s58, s58, 10
s_mov_b32 s59, .Lhot_end-.Lstart
s_min_u32 s58, s58, s59
s_getpc_b64 s[18:19]
.Lcp2:
s_sub_u32 s18, s18, .Lcp2-.Lstart
s_subb_u32 s19, s19, 0
s_add_u32 s18, s18, s58
s_addc_u32 s19, s19, 0
s_and_b32 s18, s18, -16
global_load_dwordx4 v[28:31], v0, s[18:19]
s_swappc_b64 s[32:33], s[16:17]
s_waitcnt vmcnt(21)
s_cmp_eq_u32 s61, 6
s_cselect_b32 s58, s98, 0x80000000
buffer_store_dwordx2 v[132:133], v13, s[48:51], s58 offen sc1
v_mov_b32 v3, v25
s_mov_b64 s[12:13], s[28:29]
s_mov_b64 s[14:15], s[30:31]
s_mov_b32 s24, -896
s_mov_b32 s25, 14336
s_mov_b32 s26, 13440
s_swappc_b64 s[32:33], s[16:17]
.Lhab1:
s_getpc_b64 s[16:17]
.Lhab1_g:
s_sub_u32 s16, s16, .Lhab1_g-.Lhab1_r
s_subb_u32 s17, s17, 0
s_and_b32 s58, s93, 3
s_cmp_eq_u32 s58, 1
s_cbranch_scc1 .Lbepi
s_setpc_b64 s[32:33]
.Lhd0:
s_getpc_b64 s[16:17]
.Lhd0_g:
s_sub_u32 s16, s16, .Lhd0_g-.Lhd0_r
s_subb_u32 s17, s17, 0
s_and_b32 s58, s93, 3
s_cmp_eq_u32 s58, 1
s_cbranch_scc1 .Lbepi
s_setpc_b64 s[32:33]
.Lhd1:
s_getpc_b64 s[16:17]
.Lhd1_g:
s_sub_u32 s16, s16, .Lhd1_g-.Lhd1_r
s_subb_u32 s17, s17, 0
s_and_b32 s58, s93, 3
s_cmp_eq_u32 s58, 1
s_cbranch_scc1 .Lbepi
s_setpc_b64 s[32:33]
.Lhy:
s_getpc_b64 s[16:17]
.Lhy_g:
s_sub_u32 s16, s16, .Lhy_g-.Lhy_r
s_subb_u32 s17, s17, 0
s_and_b32 s58, s93, 3
s_cmp_eq_u32 s58, 1
s_cbranch_scc1 .Lbepi
s_setpc_b64 s[32:33]
.Lhaa1:
s_getpc_b64 s[16:17]
.Lhaa1_g:
s_sub_u32 s16, s16, .Lhaa1_g-.Lhaa1_r
s_subb_u32 s17, s17, 0
s_and_b32 s58, s93, 3
s_cmp_eq_u32 s58, 1
s_cbranch_scc1 .Lbepi
s_setpc_b64 s[32:33]
.Lhaa0:
s_getpc_b64 s[16:17]
.Lhaa0_g:
s_sub_u32 s16, s16, .Lhaa0_g-.Lhaa0_r
s_subb_u32 s17, s17, 0
s_and_b32 s58, s93, 3
s_cmp_eq_u32 s58, 1
s_cbranch_scc1 .Lbepi
s_setpc_b64 s[32:33]
.Lrt0:
s_mul_i32 s37, s64, 7168
s_add_u32 s37, s37, 2097152
s_add_u32 s36, s37, 4096
)"
R"(
s_not_b32 s24, s89
s_and_b32 s24, s24, 0x7ff
s_mul_i32 s59, s24, 2176
s_lshl_b32 s88, s64, 6
s_add_u32 s59, s59, s88
s_add_u32 s59, s59, 4194304
buffer_load_dwordx4 v[24:27], v7, s[48:51], s59 offen sc1
buffer_load_dwordx4 v[148:151], v6, s[48:51], s37 offen sc1
buffer_load_dwordx4 v[156:159], v6, s[48:51], s37 offen offset:1024 sc1
buffer_load_dwordx4 v[164:167], v6, s[48:51], s37 offen offset:2048 sc1
buffer_load_dwordx4 v[172:175], v6, s[48:51], s37 offen offset:3072 sc1
buffer_load_dwordx4 v[228:231], v6, s[48:51], s36 offen sc1
buffer_load_dwordx4 v[236:239], v6, s[48:51], s36 offen offset:1024 sc1
buffer_load_dwordx4 v[244:247], v6, s[48:51], s36 offen offset:2048 sc1
s_and_b32 s59, s89, 0xffff0000
s_bitcmp1_b32 s89, 31
s_cselect_b32 s88, 0, 0x7fff0000
s_xor_b32 s59, s59, s88
v_mov_b32 v253, s59
v_mul_f32 v253, 0x3fb8aa3b, v253
v_exp_f32 v253, v253
s_nop 0
v_add_f32 v253, 1.0, v253
v_rcp_f32 v253, v253
s_nop 0
v_cvt_pk_bf16_f32 v253, 0, v253
s_waitcnt vmcnt(0)
v_xor_b32 v148, s20, v148
v_xor_b32 v149, s21, v149
v_xor_b32 v150, s22, v150
v_xor_b32 v151, s23, v151
v_xor_b32 v156, s20, v156
v_xor_b32 v157, s21, v157
v_xor_b32 v158, s22, v158
v_xor_b32 v159, s23, v159
v_xor_b32 v164, s20, v164
v_xor_b32 v165, s21, v165
v_xor_b32 v166, s22, v166
v_xor_b32 v167, s23, v167
v_xor_b32 v172, s20, v172
v_xor_b32 v173, s21, v173
v_xor_b32 v174, s22, v174
v_xor_b32 v175, s23, v175
v_xor_b32 v228, s20, v228
v_xor_b32 v229, s21, v229
v_xor_b32 v230, s22, v230
v_xor_b32 v231, s23, v231
v_xor_b32 v236, s20, v236
v_xor_b32 v237, s21, v237
v_xor_b32 v238, s22, v238
v_xor_b32 v239, s23, v239
v_xor_b32 v244, s20, v244
v_xor_b32 v245, s21, v245
v_xor_b32 v246, s22, v246
v_xor_b32 v247, s23, v247
v_or3_b32 v252, v148, v149, v150
v_or3_b32 v252, v252, v151, v156
v_or3_b32 v252, v252, v157, v158
v_or3_b32 v252, v252, v159, v164
v_or3_b32 v252, v252, v165, v166
v_or3_b32 v252, v252, v167, v172
v_or3_b32 v252, v252, v173, v174
v_or3_b32 v252, v252, v175, v228
v_or3_b32 v252, v252, v229, v230
v_or3_b32 v252, v252, v231, v236
v_or3_b32 v252, v252, v237, v238
v_or3_b32 v252, v252, v239, v244
v_or3_b32 v252, v252, v245, v246
v_or_b32 v252, v252, v247
v_and_b32 v252, 0xff, v252
v_cmp_ne_u32 vcc, 0, v252
s_nop 0
s_cbranch_vccnz .Ly0
.Ly0_r:
v_add_f32 v148, v148, v156
v_add_f32 v149, v149, v157
v_add_f32 v150, v150, v158
v_add_f32 v151, v151, v159
v_add_f32 v164, v164, v172
v_add_f32 v165, v165, v173
v_add_f32 v166, v166, v174
v_add_f32 v167, v167, v175
v_add_f32 v228, v228, v236
v_add_f32 v229, v229, v237
v_add_f32 v230, v230, v238
v_add_f32 v231, v231, v239
v_add_f32 v148, v148, v164
v_add_f32 v149, v149, v165
v_add_f32 v150, v150, v166
v_add_f32 v151, v151, v167
v_add_f32 v228, v228, v244
v_add_f32 v229, v229, v245
v_add_f32 v230, v230, v246
v_add_f32 v231, v231, v247
v_add_f32 v148, v148, v228
v_add_f32 v149, v149, v229
v_add_f32 v150, v150, v230
v_add_f32 v151, v151, v231
v_add_f32_dpp v148, v148, v148 row_ror:2 row_mask:0xf bank_mask:0xf bound_ctrl:1
v_add_f32_dpp v149, v149, v149 row_ror:2 row_mask:0xf bank_mask:0xf bound_ctrl:1
v_add_f32_dpp v150, v150, v150 row_ror:2 row_mask:0xf bank_mask:0xf bound_ctrl:1
v_add_f32_dpp v151, v151, v151 row_ror:2 row_mask:0xf bank_mask:0xf bound_ctrl:1
v_add_f32_dpp v148, v148, v148 row_ror:4 row_mask:0xf bank_mask:0xf bound_ctrl:1
v_add_f32_dpp v149, v149, v149 row_ror:4 row_mask:0xf bank_mask:0xf bound_ctrl:1
v_add_f32_dpp v150, v150, v150 row_ror:4 row_mask:0xf bank_mask:0xf bound_ctrl:1
v_add_f32_dpp v151, v151, v151 row_ror:4 row_mask:0xf bank_mask:0xf bound_ctrl:1
v_add_f32_dpp v148, v148, v148 row_ror:8 row_mask:0xf bank_mask:0xf bound_ctrl:1
v_add_f32_dpp v149, v149, v149 row_ror:8 row_mask:0xf bank_mask:0xf bound_ctrl:1
v_add_f32_dpp v150, v150, v150 row_ror:8 row_mask:0xf bank_mask:0xf bound_ctrl:1
v_add_f32_dpp v151, v151, v151 row_ror:8 row_mask:0xf bank_mask:0xf bound_ctrl:1
v_cvt_pk_bf16_f32 v148, 0, v148
v_cvt_pk_bf16_f32 v149, 0, v149
v_cvt_pk_bf16_f32 v150, 0, v150
v_cvt_pk_bf16_f32 v151, 0, v151
v_mul_f32 v148, v148, v253
v_mul_f32 v149, v149, v253
v_mul_f32 v150, v150, v253
v_mul_f32 v151, v151, v253
v_cvt_pk_bf16_f32 v148, 0, v148
v_cvt_pk_bf16_f32 v149, 0, v149
v_cvt_pk_bf16_f32 v150, 0, v150
v_cvt_pk_bf16_f32 v151, 0, v151
v_lshlrev_b32 v156, 16, v24
v_lshlrev_b32 v158, 16, v25
v_lshlrev_b32 v164, 16, v26
v_lshlrev_b32 v166, 16, v27
v_and_b32 v157, 0xffff0000, v24
v_and_b32 v159, 0xffff0000, v25
v_and_b32 v165, 0xffff0000, v26
v_and_b32 v167, 0xffff0000, v27
v_add_f32_dpp v156, v148, v156 quad_perm:[0,0,2,2] row_mask:0xf bank_mask:0xf bound_ctrl:1
v_add_f32_dpp v157, v149, v157 quad_perm:[0,0,2,2] row_mask:0xf bank_mask:0xf bound_ctrl:1
v_add_f32_dpp v158, v150, v158 quad_perm:[0,0,2,2] row_mask:0xf bank_mask:0xf bound_ctrl:1
v_add_f32_dpp v159, v151, v159 quad_perm:[0,0,2,2] row_mask:0xf bank_mask:0xf bound_ctrl:1
v_add_f32_dpp v164, v148, v164 quad_perm:[1,1,3,3] row_mask:0xf bank_mask:0xf bound_ctrl:1
v_add_f32_dpp v165, v149, v165 quad_perm:[1,1,3,3] row_mask:0xf bank_mask:0xf bound_ctrl:1
v_add_f32_dpp v166, v150, v166 quad_perm:[1,1,3,3] row_mask:0xf bank_mask:0xf bound_ctrl:1
v_add_f32_dpp v167, v151, v167 quad_perm:[1,1,3,3] row_mask:0xf bank_mask:0xf bound_ctrl:1
v_cvt_pk_bf16_f32 v24, v156, v157
v_cvt_pk_bf16_f32 v25, v158, v159
v_cvt_pk_bf16_f32 v26, v164, v165
v_cvt_pk_bf16_f32 v27, v166, v167
v_lshlrev_b32 v148, 16, v24
v_and_b32 v149, 0xffff0000, v24
v_lshlrev_b32 v150, 16, v25
v_and_b32 v151, 0xffff0000, v25
v_lshlrev_b32 v156, 16, v26
v_and_b32 v157, 0xffff0000, v26
v_lshlrev_b32 v158, 16, v27
v_and_b32 v159, 0xffff0000, v27
v_mul_f32 v164, 0xbfb8aa3b, v148
v_mul_f32 v165, 0xbfb8aa3b, v149
v_mul_f32 v166, 0xbfb8aa3b, v150
v_mul_f32 v167, 0xbfb8aa3b, v151
v_mul_f32 v172, 0xbfb8aa3b, v156
v_mul_f32 v173, 0xbfb8aa3b, v157
v_mul_f32 v174, 0xbfb8aa3b, v158
v_mul_f32 v175, 0xbfb8aa3b, v159
v_exp_f32 v164, v164
v_exp_f32 v165, v165
v_exp_f32 v166, v166
v_exp_f32 v167, v167
v_exp_f32 v172, v172
v_exp_f32 v173, v173
v_exp_f32 v174, v174
v_exp_f32 v175, v175
v_add_f32 v164, 1.0, v164
v_add_f32 v165, 1.0, v165
v_add_f32 v166, 1.0, v166
v_add_f32 v167, 1.0, v167
v_add_f32 v172, 1.0, v172
v_add_f32 v173, 1.0, v173
v_add_f32 v174, 1.0, v174
v_add_f32 v175, 1.0, v175
v_rcp_f32 v164, v164
v_rcp_f32 v165, v165
v_rcp_f32 v166, v166
v_rcp_f32 v167, v167
v_rcp_f32 v172, v172
v_rcp_f32 v173, v173
v_rcp_f32 v174, v174
v_rcp_f32 v175, v175
v_mul_f32 v148, v148, v164
v_mul_f32 v149, v149, v165
v_mul_f32 v150, v150, v166
v_mul_f32 v151, v151, v167
v_mul_f32 v156, v156, v172
v_mul_f32 v157, v157, v173
v_mul_f32 v158, v158, v174
v_mul_f32 v159, v159, v175
s_and_b32 s59, s24, 15
s_lshl_b32 s59, 0x10001, s59
s_lshr_b32 s24, s24, 9
s_cmp_eq_u32 s24, 0
s_cselect_b32 vcc_lo, s59, 0
s_cselect_b32 vcc_hi, s59, 0
v_cndmask_b32 v64, v64, v148, vcc
v_cndmask_b32 v65, v65, v149, vcc
v_cndmask_b32 v66, v66, v150, vcc
v_cndmask_b32 v67, v67, v151, vcc
v_cndmask_b32 v68, v68, v156, vcc
v_cndmask_b32 v69, v69, v157, vcc
v_cndmask_b32 v70, v70, v158, vcc
v_cndmask_b32 v71, v71, v159, vcc
s_cmp_eq_u32 s24, 1
s_cselect_b32 vcc_lo, s59, 0
s_cselect_b32 vcc_hi, s59, 0
v_cndmask_b32 v72, v72, v148, vcc
v_cndmask_b32 v73, v73, v149, vcc
v_cndmask_b32 v74, v74, v150, vcc
v_cndmask_b32 v75, v75, v151, vcc
v_cndmask_b32 v76, v76, v156, vcc
v_cndmask_b32 v77, v77, v157, vcc
v_cndmask_b32 v78, v78, v158, vcc
v_cndmask_b32 v79, v79, v159, vcc
s_cmp_eq_u32 s24, 2
s_cselect_b32 vcc_lo, s59, 0
s_cselect_b32 vcc_hi, s59, 0
v_cndmask_b32 v80, v80, v148, vcc
v_cndmask_b32 v81, v81, v149, vcc
v_cndmask_b32 v82, v82, v150, vcc
v_cndmask_b32 v83, v83, v151, vcc
v_cndmask_b32 v84, v84, v156, vcc
v_cndmask_b32 v85, v85, v157, vcc
v_cndmask_b32 v86, v86, v158, vcc
v_cndmask_b32 v87, v87, v159, vcc
s_cmp_eq_u32 s24, 3
s_cselect_b32 vcc_lo, s59, 0
s_cselect_b32 vcc_hi, s59, 0
v_cndmask_b32 v16, v16, v148, vcc
v_cndmask_b32 v17, v17, v149, vcc
v_cndmask_b32 v18, v18, v150, vcc
v_cndmask_b32 v19, v19, v151, vcc
v_cndmask_b32 v20, v20, v156, vcc
v_cndmask_b32 v21, v21, v157, vcc
v_cndmask_b32 v22, v22, v158, vcc
v_cndmask_b32 v23, v23, v159, vcc
s_branch .Lrt0_r
.Lrt1:
s_mul_i32 s37, s64, 7168
s_add_u32 s37, s37, 2326528
s_add_u32 s36, s37, 4096
s_not_b32 s24, s90
s_and_b32 s24, s24, 0x7ff
s_mul_i32 s59, s24, 2176
s_lshl_b32 s88, s64, 6
s_add_u32 s59, s59, s88
s_add_u32 s59, s59, 4194304
buffer_load_dwordx4 v[24:27], v7, s[48:51], s59 offen sc1
buffer_load_dwordx4 v[148:151], v6, s[48:51], s37 offen sc1
buffer_load_dwordx4 v[156:159], v6, s[48:51], s37 offen offset:1024 sc1
buffer_load_dwordx4 v[164:167], v6, s[48:51], s37 offen offset:2048 sc1
buffer_load_dwordx4 v[172:175], v6, s[48:51], s37 offen offset:3072 sc1
buffer_load_dwordx4 v[228:231], v6, s[48:51], s36 offen sc1
buffer_load_dwordx4 v[236:239], v6, s[48:51], s36 offen offset:1024 sc1
buffer_load_dwordx4 v[244:247], v6, s[48:51], s36 offen offset:2048 sc1
s_and_b32 s59, s90, 0xffff0000
s_bitcmp1_b32 s90, 31
s_cselect_b32 s88, 0, 0x7fff0000
s_xor_b32 s59, s59, s88
v_mov_b32 v253, s59
v_mul_f32 v253, 0x3fb8aa3b, v253
v_exp_f32 v253, v253
s_nop 0
v_add_f32 v253, 1.0, v253
v_rcp_f32 v253, v253
s_nop 0
v_cvt_pk_bf16_f32 v253, 0, v253
s_waitcnt vmcnt(0)
v_xor_b32 v148, s20, v148
v_xor_b32 v149, s21, v149
v_xor_b32 v150, s22, v150
v_xor_b32 v151, s23, v151
v_xor_b32 v156, s20, v156
v_xor_b32 v157, s21, v157
v_xor_b32 v158, s22, v158
v_xor_b32 v159, s23, v159
v_xor_b32 v164, s20, v164
v_xor_b32 v165, s21, v165
v_xor_b32 v166, s22, v166
v_xor_b32 v167, s23, v167
v_xor_b32 v172, s20, v172
v_xor_b32 v173, s21, v173
v_xor_b32 v174, s22, v174
v_xor_b32 v175, s23, v175
v_xor_b32 v228, s20, v228
v_xor_b32 v229, s21, v229
v_xor_b32 v230, s22, v230
v_xor_b32 v231, s23, v231
v_xor_b32 v236, s20, v236
v_xor_b32 v237, s21, v237
v_xor_b32 v238, s22, v238
v_xor_b32 v239, s23, v239
v_xor_b32 v244, s20, v244
v_xor_b32 v245, s21, v245
v_xor_b32 v246, s22, v246
v_xor_b32 v247, s23, v247
v_or3_b32 v252, v148, v149, v150
v_or3_b32 v252, v252, v151, v156
v_or3_b32 v252, v252, v157, v158
v_or3_b32 v252, v252, v159, v164
v_or3_b32 v252, v252, v165, v166
v_or3_b32 v252, v252, v167, v172
v_or3_b32 v252, v252, v173, v174
v_or3_b32 v252, v252, v175, v228
v_or3_b32 v252, v252, v229, v230
v_or3_b32 v252, v252, v231, v236
v_or3_b32 v252, v252, v237, v238
v_or3_b32 v252, v252, v239, v244
v_or3_b32 v252, v252, v245, v246
v_or_b32 v252, v252, v247
v_and_b32 v252, 0xff, v252
v_cmp_ne_u32 vcc, 0, v252
s_nop 0
s_cbranch_vccnz .Ly1
.Ly1_r:
v_add_f32 v148, v148, v156
v_add_f32 v149, v149, v157
v_add_f32 v150, v150, v158
v_add_f32 v151, v151, v159
v_add_f32 v164, v164, v172
v_add_f32 v165, v165, v173
v_add_f32 v166, v166, v174
v_add_f32 v167, v167, v175
v_add_f32 v228, v228, v236
v_add_f32 v229, v229, v237
v_add_f32 v230, v230, v238
v_add_f32 v231, v231, v239
v_add_f32 v148, v148, v164
v_add_f32 v149, v149, v165
v_add_f32 v150, v150, v166
v_add_f32 v151, v151, v167
v_add_f32 v228, v228, v244
v_add_f32 v229, v229, v245
v_add_f32 v230, v230, v246
v_add_f32 v231, v231, v247
v_add_f32 v148, v148, v228
v_add_f32 v149, v149, v229
v_add_f32 v150, v150, v230
v_add_f32 v151, v151, v231
v_add_f32_dpp v148, v148, v148 row_ror:2 row_mask:0xf bank_mask:0xf bound_ctrl:1
v_add_f32_dpp v149, v149, v149 row_ror:2 row_mask:0xf bank_mask:0xf bound_ctrl:1
v_add_f32_dpp v150, v150, v150 row_ror:2 row_mask:0xf bank_mask:0xf bound_ctrl:1
v_add_f32_dpp v151, v151, v151 row_ror:2 row_mask:0xf bank_mask:0xf bound_ctrl:1
v_add_f32_dpp v148, v148, v148 row_ror:4 row_mask:0xf bank_mask:0xf bound_ctrl:1
v_add_f32_dpp v149, v149, v149 row_ror:4 row_mask:0xf bank_mask:0xf bound_ctrl:1
v_add_f32_dpp v150, v150, v150 row_ror:4 row_mask:0xf bank_mask:0xf bound_ctrl:1
v_add_f32_dpp v151, v151, v151 row_ror:4 row_mask:0xf bank_mask:0xf bound_ctrl:1
v_add_f32_dpp v148, v148, v148 row_ror:8 row_mask:0xf bank_mask:0xf bound_ctrl:1
v_add_f32_dpp v149, v149, v149 row_ror:8 row_mask:0xf bank_mask:0xf bound_ctrl:1
v_add_f32_dpp v150, v150, v150 row_ror:8 row_mask:0xf bank_mask:0xf bound_ctrl:1
v_add_f32_dpp v151, v151, v151 row_ror:8 row_mask:0xf bank_mask:0xf bound_ctrl:1
)"
R"(
v_cvt_pk_bf16_f32 v148, 0, v148
v_cvt_pk_bf16_f32 v149, 0, v149
v_cvt_pk_bf16_f32 v150, 0, v150
v_cvt_pk_bf16_f32 v151, 0, v151
v_mul_f32 v148, v148, v253
v_mul_f32 v149, v149, v253
v_mul_f32 v150, v150, v253
v_mul_f32 v151, v151, v253
v_cvt_pk_bf16_f32 v148, 0, v148
v_cvt_pk_bf16_f32 v149, 0, v149
v_cvt_pk_bf16_f32 v150, 0, v150
v_cvt_pk_bf16_f32 v151, 0, v151
v_lshlrev_b32 v156, 16, v24
v_lshlrev_b32 v158, 16, v25
v_lshlrev_b32 v164, 16, v26
v_lshlrev_b32 v166, 16, v27
v_and_b32 v157, 0xffff0000, v24
v_and_b32 v159, 0xffff0000, v25
v_and_b32 v165, 0xffff0000, v26
v_and_b32 v167, 0xffff0000, v27
v_add_f32_dpp v156, v148, v156 quad_perm:[0,0,2,2] row_mask:0xf bank_mask:0xf bound_ctrl:1
v_add_f32_dpp v157, v149, v157 quad_perm:[0,0,2,2] row_mask:0xf bank_mask:0xf bound_ctrl:1
v_add_f32_dpp v158, v150, v158 quad_perm:[0,0,2,2] row_mask:0xf bank_mask:0xf bound_ctrl:1
v_add_f32_dpp v159, v151, v159 quad_perm:[0,0,2,2] row_mask:0xf bank_mask:0xf bound_ctrl:1
v_add_f32_dpp v164, v148, v164 quad_perm:[1,1,3,3] row_mask:0xf bank_mask:0xf bound_ctrl:1
v_add_f32_dpp v165, v149, v165 quad_perm:[1,1,3,3] row_mask:0xf bank_mask:0xf bound_ctrl:1
v_add_f32_dpp v166, v150, v166 quad_perm:[1,1,3,3] row_mask:0xf bank_mask:0xf bound_ctrl:1
v_add_f32_dpp v167, v151, v167 quad_perm:[1,1,3,3] row_mask:0xf bank_mask:0xf bound_ctrl:1
v_cvt_pk_bf16_f32 v24, v156, v157
v_cvt_pk_bf16_f32 v25, v158, v159
v_cvt_pk_bf16_f32 v26, v164, v165
v_cvt_pk_bf16_f32 v27, v166, v167
v_lshlrev_b32 v148, 16, v24
v_and_b32 v149, 0xffff0000, v24
v_lshlrev_b32 v150, 16, v25
v_and_b32 v151, 0xffff0000, v25
v_lshlrev_b32 v156, 16, v26
v_and_b32 v157, 0xffff0000, v26
v_lshlrev_b32 v158, 16, v27
v_and_b32 v159, 0xffff0000, v27
v_mul_f32 v164, 0xbfb8aa3b, v148
v_mul_f32 v165, 0xbfb8aa3b, v149
v_mul_f32 v166, 0xbfb8aa3b, v150
v_mul_f32 v167, 0xbfb8aa3b, v151
v_mul_f32 v172, 0xbfb8aa3b, v156
v_mul_f32 v173, 0xbfb8aa3b, v157
v_mul_f32 v174, 0xbfb8aa3b, v158
v_mul_f32 v175, 0xbfb8aa3b, v159
v_exp_f32 v164, v164
v_exp_f32 v165, v165
v_exp_f32 v166, v166
v_exp_f32 v167, v167
v_exp_f32 v172, v172
v_exp_f32 v173, v173
v_exp_f32 v174, v174
v_exp_f32 v175, v175
v_add_f32 v164, 1.0, v164
v_add_f32 v165, 1.0, v165
v_add_f32 v166, 1.0, v166
v_add_f32 v167, 1.0, v167
v_add_f32 v172, 1.0, v172
v_add_f32 v173, 1.0, v173
v_add_f32 v174, 1.0, v174
v_add_f32 v175, 1.0, v175
v_rcp_f32 v164, v164
v_rcp_f32 v165, v165
v_rcp_f32 v166, v166
v_rcp_f32 v167, v167
v_rcp_f32 v172, v172
v_rcp_f32 v173, v173
v_rcp_f32 v174, v174
v_rcp_f32 v175, v175
v_mul_f32 v148, v148, v164
v_mul_f32 v149, v149, v165
v_mul_f32 v150, v150, v166
v_mul_f32 v151, v151, v167
v_mul_f32 v156, v156, v172
v_mul_f32 v157, v157, v173
v_mul_f32 v158, v158, v174
v_mul_f32 v159, v159, v175
s_and_b32 s59, s24, 15
s_lshl_b32 s59, 0x10001, s59
s_lshr_b32 s24, s24, 9
s_cmp_eq_u32 s24, 0
s_cselect_b32 vcc_lo, s59, 0
s_cselect_b32 vcc_hi, s59, 0
v_cndmask_b32 v64, v64, v148, vcc
v_cndmask_b32 v65, v65, v149, vcc
v_cndmask_b32 v66, v66, v150, vcc
v_cndmask_b32 v67, v67, v151, vcc
v_cndmask_b32 v68, v68, v156, vcc
v_cndmask_b32 v69, v69, v157, vcc
v_cndmask_b32 v70, v70, v158, vcc
v_cndmask_b32 v71, v71, v159, vcc
s_cmp_eq_u32 s24, 1
s_cselect_b32 vcc_lo, s59, 0
s_cselect_b32 vcc_hi, s59, 0
v_cndmask_b32 v72, v72, v148, vcc
v_cndmask_b32 v73, v73, v149, vcc
v_cndmask_b32 v74, v74, v150, vcc
v_cndmask_b32 v75, v75, v151, vcc
v_cndmask_b32 v76, v76, v156, vcc
v_cndmask_b32 v77, v77, v157, vcc
v_cndmask_b32 v78, v78, v158, vcc
v_cndmask_b32 v79, v79, v159, vcc
s_cmp_eq_u32 s24, 2
s_cselect_b32 vcc_lo, s59, 0
s_cselect_b32 vcc_hi, s59, 0
v_cndmask_b32 v80, v80, v148, vcc
v_cndmask_b32 v81, v81, v149, vcc
v_cndmask_b32 v82, v82, v150, vcc
v_cndmask_b32 v83, v83, v151, vcc
v_cndmask_b32 v84, v84, v156, vcc
v_cndmask_b32 v85, v85, v157, vcc
v_cndmask_b32 v86, v86, v158, vcc
v_cndmask_b32 v87, v87, v159, vcc
s_cmp_eq_u32 s24, 3
s_cselect_b32 vcc_lo, s59, 0
s_cselect_b32 vcc_hi, s59, 0
v_cndmask_b32 v16, v16, v148, vcc
v_cndmask_b32 v17, v17, v149, vcc
v_cndmask_b32 v18, v18, v150, vcc
v_cndmask_b32 v19, v19, v151, vcc
v_cndmask_b32 v20, v20, v156, vcc
v_cndmask_b32 v21, v21, v157, vcc
v_cndmask_b32 v22, v22, v158, vcc
v_cndmask_b32 v23, v23, v159, vcc
s_branch .Lrt1_r
.Lhot_end:
.Lscan:
s_mov_b32 s88, 1048576
.Lscan_l:
s_sleep 8
buffer_load_dwordx4 v[32:35], v0, s[48:51], s79 offen sc1
buffer_load_dwordx4 v[36:39], v0, s[48:51], s79 offen offset:1024 sc1
buffer_load_dwordx4 v[40:43], v0, s[48:51], s79 offen offset:2048 sc1
buffer_load_dwordx4 v[44:47], v0, s[48:51], s79 offen offset:3072 sc1
s_waitcnt vmcnt(0)
v_xor_b32 v50, s74, v33
v_bitop3_b32 v50, v35, s75, v50 bitop3:0xbe
v_bitop3_b32 v50, v37, s74, v50 bitop3:0xbe
v_bitop3_b32 v50, v39, s75, v50 bitop3:0xbe
v_bitop3_b32 v50, v41, s74, v50 bitop3:0xbe
v_bitop3_b32 v50, v43, s75, v50 bitop3:0xbe
v_bitop3_b32 v50, v45, s74, v50 bitop3:0xbe
v_bitop3_b32 v50, v47, s75, v50 bitop3:0xbe
v_cmp_ne_u32 vcc, 0, v50
s_nop 0
s_cbranch_vccz .Lscan_r
s_sub_u32 s88, s88, 1
s_cmp_lg_u32 s88, 0
s_cbranch_scc1 .Lscan_l
s_branch .Lscan_r
.Lcf1:
s_mov_b32 s88, 1048576
.Lcf1_l:
s_sleep 4
buffer_load_dwordx4 v[16:19], v0, s[48:51], s83 offen sc1
s_waitcnt vmcnt(0)
v_xor_b32 v16, s74, v16
v_bitop3_b32 v16, v17, s75, v16 bitop3:0xbe
v_bitop3_b32 v16, v18, s74, v16 bitop3:0xbe
v_bitop3_b32 v16, v19, s75, v16 bitop3:0xbe
v_cmp_ne_u32 vcc, 0, v16
s_nop 0
s_and_b32 vcc_lo, vcc_lo, 0xf000f
s_and_b32 vcc_hi, vcc_hi, 0xf000f
s_cbranch_vccz .Lcf1_r
s_sub_u32 s88, s88, 1
s_cmp_lg_u32 s88, 0
s_cbranch_scc1 .Lcf1_l
s_branch .Lcf1_r
.Lkvf:
s_mov_b32 s88, 1048576
.Lkvf_l:
s_sleep 4
buffer_load_dwordx2 v[20:21], v1, s[48:51], s84 offen sc1
s_waitcnt vmcnt(0)
v_xor_b32 v20, s74, v20
v_bitop3_b32 v20, v21, s75, v20 bitop3:0xbe
v_cmp_ne_u32 vcc, 0, v20
s_nop 0
s_cbranch_vccz .Lkvf_r
s_sub_u32 s88, s88, 1
s_cmp_lg_u32 s88, 0
s_cbranch_scc1 .Lkvf_l
s_branch .Lkvf_r
.Lscale4:
v_accvgpr_read_b32 v28, a64
v_accvgpr_read_b32 v29, a65
v_accvgpr_read_b32 v30, a66
v_accvgpr_read_b32 v31, a67
v_mul_f32 v28, v28, v126
v_mul_f32 v29, v29, v126
v_mul_f32 v30, v30, v126
v_mul_f32 v31, v31, v126
v_accvgpr_write_b32 a64, v28
v_accvgpr_write_b32 a65, v29
v_accvgpr_write_b32 a66, v30
v_accvgpr_write_b32 a67, v31
v_accvgpr_read_b32 v28, a68
v_accvgpr_read_b32 v29, a69
v_accvgpr_read_b32 v30, a70
v_accvgpr_read_b32 v31, a71
v_mul_f32 v28, v28, v127
v_mul_f32 v29, v29, v127
v_mul_f32 v30, v30, v127
v_mul_f32 v31, v31, v127
v_accvgpr_write_b32 a68, v28
v_accvgpr_write_b32 a69, v29
v_accvgpr_write_b32 a70, v30
v_accvgpr_write_b32 a71, v31
v_accvgpr_read_b32 v28, a72
v_accvgpr_read_b32 v29, a73
v_accvgpr_read_b32 v30, a74
v_accvgpr_read_b32 v31, a75
v_mul_f32 v28, v28, v126
v_mul_f32 v29, v29, v126
v_mul_f32 v30, v30, v126
v_mul_f32 v31, v31, v126
v_accvgpr_write_b32 a72, v28
v_accvgpr_write_b32 a73, v29
v_accvgpr_write_b32 a74, v30
v_accvgpr_write_b32 a75, v31
v_accvgpr_read_b32 v28, a76
v_accvgpr_read_b32 v29, a77
v_accvgpr_read_b32 v30, a78
v_accvgpr_read_b32 v31, a79
v_mul_f32 v28, v28, v127
v_mul_f32 v29, v29, v127
v_mul_f32 v30, v30, v127
v_mul_f32 v31, v31, v127
v_accvgpr_write_b32 a76, v28
v_accvgpr_write_b32 a77, v29
v_accvgpr_write_b32 a78, v30
v_accvgpr_write_b32 a79, v31
v_accvgpr_read_b32 v28, a80
v_accvgpr_read_b32 v29, a81
v_accvgpr_read_b32 v30, a82
v_accvgpr_read_b32 v31, a83
v_mul_f32 v28, v28, v126
v_mul_f32 v29, v29, v126
v_mul_f32 v30, v30, v126
v_mul_f32 v31, v31, v126
v_accvgpr_write_b32 a80, v28
v_accvgpr_write_b32 a81, v29
v_accvgpr_write_b32 a82, v30
v_accvgpr_write_b32 a83, v31
v_accvgpr_read_b32 v28, a84
v_accvgpr_read_b32 v29, a85
v_accvgpr_read_b32 v30, a86
v_accvgpr_read_b32 v31, a87
v_mul_f32 v28, v28, v127
v_mul_f32 v29, v29, v127
v_mul_f32 v30, v30, v127
v_mul_f32 v31, v31, v127
v_accvgpr_write_b32 a84, v28
v_accvgpr_write_b32 a85, v29
v_accvgpr_write_b32 a86, v30
v_accvgpr_write_b32 a87, v31
v_accvgpr_read_b32 v28, a88
v_accvgpr_read_b32 v29, a89
v_accvgpr_read_b32 v30, a90
v_accvgpr_read_b32 v31, a91
v_mul_f32 v28, v28, v126
v_mul_f32 v29, v29, v126
v_mul_f32 v30, v30, v126
v_mul_f32 v31, v31, v126
v_accvgpr_write_b32 a88, v28
v_accvgpr_write_b32 a89, v29
v_accvgpr_write_b32 a90, v30
v_accvgpr_write_b32 a91, v31
v_accvgpr_read_b32 v28, a92
v_accvgpr_read_b32 v29, a93
v_accvgpr_read_b32 v30, a94
v_accvgpr_read_b32 v31, a95
v_mul_f32 v28, v28, v127
v_mul_f32 v29, v29, v127
v_mul_f32 v30, v30, v127
v_mul_f32 v31, v31, v127
v_accvgpr_write_b32 a92, v28
v_accvgpr_write_b32 a93, v29
v_accvgpr_write_b32 a94, v30
v_accvgpr_write_b32 a95, v31
v_accvgpr_read_b32 v28, a96
v_accvgpr_read_b32 v29, a97
v_accvgpr_read_b32 v30, a98
v_accvgpr_read_b32 v31, a99
v_mul_f32 v28, v28, v126
v_mul_f32 v29, v29, v126
v_mul_f32 v30, v30, v126
v_mul_f32 v31, v31, v126
v_accvgpr_write_b32 a96, v28
v_accvgpr_write_b32 a97, v29
v_accvgpr_write_b32 a98, v30
v_accvgpr_write_b32 a99, v31
v_accvgpr_read_b32 v28, a100
v_accvgpr_read_b32 v29, a101
v_accvgpr_read_b32 v30, a102
v_accvgpr_read_b32 v31, a103
v_mul_f32 v28, v28, v127
v_mul_f32 v29, v29, v127
v_mul_f32 v30, v30, v127
v_mul_f32 v31, v31, v127
v_accvgpr_write_b32 a100, v28
v_accvgpr_write_b32 a101, v29
v_accvgpr_write_b32 a102, v30
v_accvgpr_write_b32 a103, v31
v_accvgpr_read_b32 v28, a104
v_accvgpr_read_b32 v29, a105
v_accvgpr_read_b32 v30, a106
v_accvgpr_read_b32 v31, a107
v_mul_f32 v28, v28, v126
v_mul_f32 v29, v29, v126
v_mul_f32 v30, v30, v126
v_mul_f32 v31, v31, v126
v_accvgpr_write_b32 a104, v28
v_accvgpr_write_b32 a105, v29
v_accvgpr_write_b32 a106, v30
v_accvgpr_write_b32 a107, v31
v_accvgpr_read_b32 v28, a108
v_accvgpr_read_b32 v29, a109
v_accvgpr_read_b32 v30, a110
v_accvgpr_read_b32 v31, a111
v_mul_f32 v28, v28, v127
v_mul_f32 v29, v29, v127
v_mul_f32 v30, v30, v127
v_mul_f32 v31, v31, v127
v_accvgpr_write_b32 a108, v28
v_accvgpr_write_b32 a109, v29
v_accvgpr_write_b32 a110, v30
v_accvgpr_write_b32 a111, v31
v_accvgpr_read_b32 v28, a112
v_accvgpr_read_b32 v29, a113
v_accvgpr_read_b32 v30, a114
v_accvgpr_read_b32 v31, a115
v_mul_f32 v28, v28, v126
v_mul_f32 v29, v29, v126
v_mul_f32 v30, v30, v126
v_mul_f32 v31, v31, v126
v_accvgpr_write_b32 a112, v28
v_accvgpr_write_b32 a113, v29
v_accvgpr_write_b32 a114, v30
v_accvgpr_write_b32 a115, v31
v_accvgpr_read_b32 v28, a116
v_accvgpr_read_b32 v29, a117
v_accvgpr_read_b32 v30, a118
v_accvgpr_read_b32 v31, a119
v_mul_f32 v28, v28, v127
v_mul_f32 v29, v29, v127
v_mul_f32 v30, v30, v127
v_mul_f32 v31, v31, v127
v_accvgpr_write_b32 a116, v28
v_accvgpr_write_b32 a117, v29
v_accvgpr_write_b32 a118, v30
v_accvgpr_write_b32 a119, v31
v_accvgpr_read_b32 v28, a120
v_accvgpr_read_b32 v29, a121
v_accvgpr_read_b32 v30, a122
v_accvgpr_read_b32 v31, a123
v_mul_f32 v28, v28, v126
v_mul_f32 v29, v29, v126
v_mul_f32 v30, v30, v126
v_mul_f32 v31, v31, v126
v_accvgpr_write_b32 a120, v28
v_accvgpr_write_b32 a121, v29
v_accvgpr_write_b32 a122, v30
v_accvgpr_write_b32 a123, v31
v_accvgpr_read_b32 v28, a124
v_accvgpr_read_b32 v29, a125
v_accvgpr_read_b32 v30, a126
v_accvgpr_read_b32 v31, a127
v_mul_f32 v28, v28, v127
v_mul_f32 v29, v29, v127
v_mul_f32 v30, v30, v127
v_mul_f32 v31, v31, v127
v_accvgpr_write_b32 a124, v28
v_accvgpr_write_b32 a125, v29
v_accvgpr_write_b32 a126, v30
v_accvgpr_write_b32 a127, v31
.Lscale2:
v_accvgpr_read_b32 v28, a0
v_accvgpr_read_b32 v29, a1
v_accvgpr_read_b32 v30, a2
v_accvgpr_read_b32 v31, a3
v_mul_f32 v28, v28, v124
v_mul_f32 v29, v29, v124
v_mul_f32 v30, v30, v124
v_mul_f32 v31, v31, v124
v_accvgpr_write_b32 a0, v28
v_accvgpr_write_b32 a1, v29
v_accvgpr_write_b32 a2, v30
v_accvgpr_write_b32 a3, v31
v_accvgpr_read_b32 v28, a4
v_accvgpr_read_b32 v29, a5
v_accvgpr_read_b32 v30, a6
v_accvgpr_read_b32 v31, a7
v_mul_f32 v28, v28, v125
v_mul_f32 v29, v29, v125
v_mul_f32 v30, v30, v125
v_mul_f32 v31, v31, v125
v_accvgpr_write_b32 a4, v28
v_accvgpr_write_b32 a5, v29
v_accvgpr_write_b32 a6, v30
v_accvgpr_write_b32 a7, v31
v_accvgpr_read_b32 v28, a8
v_accvgpr_read_b32 v29, a9
v_accvgpr_read_b32 v30, a10
v_accvgpr_read_b32 v31, a11
v_mul_f32 v28, v28, v124
v_mul_f32 v29, v29, v124
v_mul_f32 v30, v30, v124
)"
R"(
v_mul_f32 v31, v31, v124
v_accvgpr_write_b32 a8, v28
v_accvgpr_write_b32 a9, v29
v_accvgpr_write_b32 a10, v30
v_accvgpr_write_b32 a11, v31
v_accvgpr_read_b32 v28, a12
v_accvgpr_read_b32 v29, a13
v_accvgpr_read_b32 v30, a14
v_accvgpr_read_b32 v31, a15
v_mul_f32 v28, v28, v125
v_mul_f32 v29, v29, v125
v_mul_f32 v30, v30, v125
v_mul_f32 v31, v31, v125
v_accvgpr_write_b32 a12, v28
v_accvgpr_write_b32 a13, v29
v_accvgpr_write_b32 a14, v30
v_accvgpr_write_b32 a15, v31
v_accvgpr_read_b32 v28, a16
v_accvgpr_read_b32 v29, a17
v_accvgpr_read_b32 v30, a18
v_accvgpr_read_b32 v31, a19
v_mul_f32 v28, v28, v124
v_mul_f32 v29, v29, v124
v_mul_f32 v30, v30, v124
v_mul_f32 v31, v31, v124
v_accvgpr_write_b32 a16, v28
v_accvgpr_write_b32 a17, v29
v_accvgpr_write_b32 a18, v30
v_accvgpr_write_b32 a19, v31
v_accvgpr_read_b32 v28, a20
v_accvgpr_read_b32 v29, a21
v_accvgpr_read_b32 v30, a22
v_accvgpr_read_b32 v31, a23
v_mul_f32 v28, v28, v125
v_mul_f32 v29, v29, v125
v_mul_f32 v30, v30, v125
v_mul_f32 v31, v31, v125
v_accvgpr_write_b32 a20, v28
v_accvgpr_write_b32 a21, v29
v_accvgpr_write_b32 a22, v30
v_accvgpr_write_b32 a23, v31
v_accvgpr_read_b32 v28, a24
v_accvgpr_read_b32 v29, a25
v_accvgpr_read_b32 v30, a26
v_accvgpr_read_b32 v31, a27
v_mul_f32 v28, v28, v124
v_mul_f32 v29, v29, v124
v_mul_f32 v30, v30, v124
v_mul_f32 v31, v31, v124
v_accvgpr_write_b32 a24, v28
v_accvgpr_write_b32 a25, v29
v_accvgpr_write_b32 a26, v30
v_accvgpr_write_b32 a27, v31
v_accvgpr_read_b32 v28, a28
v_accvgpr_read_b32 v29, a29
v_accvgpr_read_b32 v30, a30
v_accvgpr_read_b32 v31, a31
v_mul_f32 v28, v28, v125
v_mul_f32 v29, v29, v125
v_mul_f32 v30, v30, v125
v_mul_f32 v31, v31, v125
v_accvgpr_write_b32 a28, v28
v_accvgpr_write_b32 a29, v29
v_accvgpr_write_b32 a30, v30
v_accvgpr_write_b32 a31, v31
v_accvgpr_read_b32 v28, a32
v_accvgpr_read_b32 v29, a33
v_accvgpr_read_b32 v30, a34
v_accvgpr_read_b32 v31, a35
v_mul_f32 v28, v28, v124
v_mul_f32 v29, v29, v124
v_mul_f32 v30, v30, v124
v_mul_f32 v31, v31, v124
v_accvgpr_write_b32 a32, v28
v_accvgpr_write_b32 a33, v29
v_accvgpr_write_b32 a34, v30
v_accvgpr_write_b32 a35, v31
v_accvgpr_read_b32 v28, a36
v_accvgpr_read_b32 v29, a37
v_accvgpr_read_b32 v30, a38
v_accvgpr_read_b32 v31, a39
v_mul_f32 v28, v28, v125
v_mul_f32 v29, v29, v125
v_mul_f32 v30, v30, v125
v_mul_f32 v31, v31, v125
v_accvgpr_write_b32 a36, v28
v_accvgpr_write_b32 a37, v29
v_accvgpr_write_b32 a38, v30
v_accvgpr_write_b32 a39, v31
v_accvgpr_read_b32 v28, a40
v_accvgpr_read_b32 v29, a41
v_accvgpr_read_b32 v30, a42
v_accvgpr_read_b32 v31, a43
v_mul_f32 v28, v28, v124
v_mul_f32 v29, v29, v124
v_mul_f32 v30, v30, v124
v_mul_f32 v31, v31, v124
v_accvgpr_write_b32 a40, v28
v_accvgpr_write_b32 a41, v29
v_accvgpr_write_b32 a42, v30
v_accvgpr_write_b32 a43, v31
v_accvgpr_read_b32 v28, a44
v_accvgpr_read_b32 v29, a45
v_accvgpr_read_b32 v30, a46
v_accvgpr_read_b32 v31, a47
v_mul_f32 v28, v28, v125
v_mul_f32 v29, v29, v125
v_mul_f32 v30, v30, v125
v_mul_f32 v31, v31, v125
v_accvgpr_write_b32 a44, v28
v_accvgpr_write_b32 a45, v29
v_accvgpr_write_b32 a46, v30
v_accvgpr_write_b32 a47, v31
v_accvgpr_read_b32 v28, a48
v_accvgpr_read_b32 v29, a49
v_accvgpr_read_b32 v30, a50
v_accvgpr_read_b32 v31, a51
v_mul_f32 v28, v28, v124
v_mul_f32 v29, v29, v124
v_mul_f32 v30, v30, v124
v_mul_f32 v31, v31, v124
v_accvgpr_write_b32 a48, v28
v_accvgpr_write_b32 a49, v29
v_accvgpr_write_b32 a50, v30
v_accvgpr_write_b32 a51, v31
v_accvgpr_read_b32 v28, a52
v_accvgpr_read_b32 v29, a53
v_accvgpr_read_b32 v30, a54
v_accvgpr_read_b32 v31, a55
v_mul_f32 v28, v28, v125
v_mul_f32 v29, v29, v125
v_mul_f32 v30, v30, v125
v_mul_f32 v31, v31, v125
v_accvgpr_write_b32 a52, v28
v_accvgpr_write_b32 a53, v29
v_accvgpr_write_b32 a54, v30
v_accvgpr_write_b32 a55, v31
v_accvgpr_read_b32 v28, a56
v_accvgpr_read_b32 v29, a57
v_accvgpr_read_b32 v30, a58
v_accvgpr_read_b32 v31, a59
v_mul_f32 v28, v28, v124
v_mul_f32 v29, v29, v124
v_mul_f32 v30, v30, v124
v_mul_f32 v31, v31, v124
v_accvgpr_write_b32 a56, v28
v_accvgpr_write_b32 a57, v29
v_accvgpr_write_b32 a58, v30
v_accvgpr_write_b32 a59, v31
v_accvgpr_read_b32 v28, a60
v_accvgpr_read_b32 v29, a61
v_accvgpr_read_b32 v30, a62
v_accvgpr_read_b32 v31, a63
v_mul_f32 v28, v28, v125
v_mul_f32 v29, v29, v125
v_mul_f32 v30, v30, v125
v_mul_f32 v31, v31, v125
v_accvgpr_write_b32 a60, v28
v_accvgpr_write_b32 a61, v29
v_accvgpr_write_b32 a62, v30
v_accvgpr_write_b32 a63, v31
s_setpc_b64 s[16:17]
.Lab0:
s_waitcnt vmcnt(0)
s_nop 7
v_max3_f32 v120, v64, v65, v66
v_max3_f32 v120, v120, v67, v80
v_max3_f32 v120, v120, v81, v82
v_max_f32 v120, v120, v83
v_mov_b32 v26, v120
s_nop 1
v_permlane16_swap_b32 v120, v26
v_max_f32 v120, v120, v26
v_mov_b32 v26, v120
s_nop 1
v_permlane32_swap_b32 v120, v26
v_max_f32 v120, v120, v26
v_max_f32 v120, 0, v120
v_sub_f32 v26, 0, v120
v_exp_f32 v124, v26
s_nop 0
v_mul_f32 v116, v116, v124
v_max3_f32 v121, v68, v69, v70
v_max3_f32 v121, v121, v71, v84
v_max3_f32 v121, v121, v85, v86
v_max_f32 v121, v121, v87
v_mov_b32 v26, v121
s_nop 1
v_permlane16_swap_b32 v121, v26
v_max_f32 v121, v121, v26
v_mov_b32 v26, v121
s_nop 1
v_permlane32_swap_b32 v121, v26
v_max_f32 v121, v121, v26
v_max_f32 v121, 0, v121
v_sub_f32 v26, 0, v121
v_exp_f32 v125, v26
s_nop 0
v_mul_f32 v117, v117, v125
v_max3_f32 v122, v72, v73, v74
v_max3_f32 v122, v122, v75, v88
v_max3_f32 v122, v122, v89, v90
v_max_f32 v122, v122, v91
v_mov_b32 v26, v122
s_nop 1
v_permlane16_swap_b32 v122, v26
v_max_f32 v122, v122, v26
v_mov_b32 v26, v122
s_nop 1
v_permlane32_swap_b32 v122, v26
v_max_f32 v122, v122, v26
v_max_f32 v122, 0, v122
v_sub_f32 v26, 0, v122
v_exp_f32 v126, v26
s_nop 0
v_mul_f32 v118, v118, v126
v_max3_f32 v123, v76, v77, v78
v_max3_f32 v123, v123, v79, v92
v_max3_f32 v123, v123, v93, v94
v_max_f32 v123, v123, v95
v_mov_b32 v26, v123
s_nop 1
v_permlane16_swap_b32 v123, v26
v_max_f32 v123, v123, v26
v_mov_b32 v26, v123
s_nop 1
v_permlane32_swap_b32 v123, v26
v_max_f32 v123, v123, v26
v_max_f32 v123, 0, v123
v_sub_f32 v26, 0, v123
v_exp_f32 v127, v26
s_nop 0
v_mul_f32 v119, v119, v127
v_sub_f32 v28, v64, v120
v_sub_f32 v29, v65, v120
v_exp_f32 v28, v28
v_exp_f32 v29, v29
s_nop 0
v_cvt_pk_bf16_f32 v228, v28, v29
v_sub_f32 v28, v66, v120
v_sub_f32 v29, v67, v120
v_exp_f32 v28, v28
v_exp_f32 v29, v29
s_nop 0
v_cvt_pk_bf16_f32 v229, v28, v29
v_sub_f32 v32, v32, v120
v_sub_f32 v33, v33, v120
v_sub_f32 v34, v34, v120
v_sub_f32 v35, v35, v120
v_sub_f32 v28, v68, v121
v_sub_f32 v29, v69, v121
v_exp_f32 v28, v28
v_exp_f32 v29, v29
s_nop 0
v_cvt_pk_bf16_f32 v236, v28, v29
v_sub_f32 v28, v70, v121
v_sub_f32 v29, v71, v121
v_exp_f32 v28, v28
v_exp_f32 v29, v29
s_nop 0
v_cvt_pk_bf16_f32 v237, v28, v29
v_sub_f32 v36, v36, v121
v_sub_f32 v37, v37, v121
v_sub_f32 v38, v38, v121
v_sub_f32 v39, v39, v121
v_sub_f32 v28, v72, v122
v_sub_f32 v29, v73, v122
v_exp_f32 v28, v28
v_exp_f32 v29, v29
s_nop 0
v_cvt_pk_bf16_f32 v244, v28, v29
v_sub_f32 v28, v74, v122
v_sub_f32 v29, v75, v122
v_exp_f32 v28, v28
v_exp_f32 v29, v29
s_nop 0
v_cvt_pk_bf16_f32 v245, v28, v29
v_sub_f32 v40, v40, v122
v_sub_f32 v41, v41, v122
v_sub_f32 v42, v42, v122
v_sub_f32 v43, v43, v122
v_sub_f32 v28, v76, v123
v_sub_f32 v29, v77, v123
v_exp_f32 v28, v28
v_exp_f32 v29, v29
s_nop 0
v_cvt_pk_bf16_f32 v252, v28, v29
v_sub_f32 v28, v78, v123
v_sub_f32 v29, v79, v123
v_exp_f32 v28, v28
v_exp_f32 v29, v29
s_nop 0
v_cvt_pk_bf16_f32 v253, v28, v29
v_sub_f32 v44, v44, v123
v_sub_f32 v45, v45, v123
v_sub_f32 v46, v46, v123
v_sub_f32 v47, v47, v123
v_sub_f32 v28, v80, v120
v_sub_f32 v29, v81, v120
v_exp_f32 v28, v28
v_exp_f32 v29, v29
s_nop 0
v_cvt_pk_bf16_f32 v230, v28, v29
v_sub_f32 v28, v82, v120
v_sub_f32 v29, v83, v120
v_exp_f32 v28, v28
v_exp_f32 v29, v29
s_nop 0
v_cvt_pk_bf16_f32 v231, v28, v29
v_sub_f32 v48, v48, v120
v_sub_f32 v49, v49, v120
v_sub_f32 v50, v50, v120
v_sub_f32 v51, v51, v120
v_sub_f32 v28, v84, v121
v_sub_f32 v29, v85, v121
v_exp_f32 v28, v28
v_exp_f32 v29, v29
s_nop 0
v_cvt_pk_bf16_f32 v238, v28, v29
v_sub_f32 v28, v86, v121
v_sub_f32 v29, v87, v121
v_exp_f32 v28, v28
v_exp_f32 v29, v29
s_nop 0
v_cvt_pk_bf16_f32 v239, v28, v29
v_sub_f32 v52, v52, v121
v_sub_f32 v53, v53, v121
v_sub_f32 v54, v54, v121
v_sub_f32 v55, v55, v121
v_sub_f32 v28, v88, v122
v_sub_f32 v29, v89, v122
v_exp_f32 v28, v28
v_exp_f32 v29, v29
s_nop 0
v_cvt_pk_bf16_f32 v246, v28, v29
v_sub_f32 v28, v90, v122
v_sub_f32 v29, v91, v122
v_exp_f32 v28, v28
v_exp_f32 v29, v29
s_nop 0
v_cvt_pk_bf16_f32 v247, v28, v29
v_sub_f32 v56, v56, v122
v_sub_f32 v57, v57, v122
v_sub_f32 v58, v58, v122
v_sub_f32 v59, v59, v122
v_sub_f32 v28, v92, v123
v_sub_f32 v29, v93, v123
v_exp_f32 v28, v28
v_exp_f32 v29, v29
s_nop 0
v_cvt_pk_bf16_f32 v254, v28, v29
v_sub_f32 v28, v94, v123
v_sub_f32 v29, v95, v123
v_exp_f32 v28, v28
v_exp_f32 v29, v29
s_nop 0
v_cvt_pk_bf16_f32 v255, v28, v29
v_sub_f32 v60, v60, v123
v_sub_f32 v61, v61, v123
v_sub_f32 v62, v62, v123
v_sub_f32 v63, v63, v123
v_sub_f32 v4, v4, v120
v_sub_f32 v5, v5, v120
v_sub_f32 v6, v6, v120
v_sub_f32 v7, v7, v120
v_sub_f32 v8, v8, v121
v_sub_f32 v9, v9, v121
v_sub_f32 v10, v10, v121
v_sub_f32 v11, v11, v121
v_sub_f32 v132, v132, v122
v_sub_f32 v133, v133, v122
v_sub_f32 v134, v134, v122
v_sub_f32 v135, v135, v122
v_sub_f32 v140, v140, v123
v_sub_f32 v141, v141, v123
v_sub_f32 v142, v142, v123
v_sub_f32 v143, v143, v123
v_sub_f32 v148, v148, v122
v_sub_f32 v149, v149, v122
v_sub_f32 v150, v150, v122
v_sub_f32 v151, v151, v122
v_sub_f32 v156, v156, v122
v_sub_f32 v157, v157, v122
v_sub_f32 v158, v158, v122
v_sub_f32 v159, v159, v122
v_sub_f32 v164, v164, v123
v_sub_f32 v165, v165, v123
v_sub_f32 v166, v166, v123
v_sub_f32 v167, v167, v123
v_sub_f32 v172, v172, v122
v_sub_f32 v173, v173, v122
v_sub_f32 v174, v174, v122
v_sub_f32 v175, v175, v122
v_sub_f32 v180, v180, v123
v_sub_f32 v181, v181, v123
v_sub_f32 v182, v182, v123
v_sub_f32 v183, v183, v123
v_sub_f32 v188, v188, v123
v_sub_f32 v189, v189, v123
v_sub_f32 v190, v190, v123
v_sub_f32 v191, v191, v123
s_getpc_b64 s[16:17]
.Lab0_g:
s_add_u32 s16, s16, .Lab0_b-.Lab0_g
s_addc_u32 s17, s17, 0
s_branch .Lscale4
.Lab0_b:
Cvvv 112,96,228,116
Mvvv 112,100,236
Mvvv 112,104,244
Mvvv 112,108,252
v_exp_f32 v120, v32
v_exp_f32 v121, v33
v_exp_f32 v122, v34
v_exp_f32 v123, v35
v_exp_f32 v124, v36
v_cvt_pk_bf16_f32 v196, v120, v121
v_cvt_pk_bf16_f32 v197, v122, v123
v_exp_f32 v125, v37
v_exp_f32 v126, v38
v_exp_f32 v127, v39
v_cvt_pk_bf16_f32 v204, v124, v125
v_cvt_pk_bf16_f32 v205, v126, v127
v_exp_f32 v120, v40
v_exp_f32 v121, v41
s_nop 0
v_cvt_pk_bf16_f32 v212, v120, v121
v_exp_f32 v122, v42
v_exp_f32 v123, v43
s_nop 0
v_cvt_pk_bf16_f32 v213, v122, v123
v_exp_f32 v124, v44
s_nop 7
s_branch .Lab0_r
.Lab1:
s_waitcnt vmcnt(0)
s_nop 7
v_max3_f32 v120, v32, v33, v34
v_max3_f32 v120, v120, v35, v48
v_max3_f32 v120, v120, v49, v50
v_max_f32 v120, v120, v51
v_mov_b32 v26, v120
s_nop 1
v_permlane16_swap_b32 v120, v26
v_max_f32 v120, v120, v26
v_mov_b32 v26, v120
s_nop 1
v_permlane32_swap_b32 v120, v26
v_max_f32 v120, v120, v26
v_max_f32 v120, 0, v120
v_sub_f32 v26, 0, v120
v_exp_f32 v124, v26
s_nop 0
v_mul_f32 v112, v112, v124
v_max3_f32 v121, v36, v37, v38
v_max3_f32 v121, v121, v39, v52
v_max3_f32 v121, v121, v53, v54
v_max_f32 v121, v121, v55
v_mov_b32 v26, v121
s_nop 1
v_permlane16_swap_b32 v121, v26
v_max_f32 v121, v121, v26
v_mov_b32 v26, v121
s_nop 1
v_permlane32_swap_b32 v121, v26
v_max_f32 v121, v121, v26
v_max_f32 v121, 0, v121
v_sub_f32 v26, 0, v121
v_exp_f32 v125, v26
s_nop 0
v_mul_f32 v113, v113, v125
v_max3_f32 v122, v40, v41, v42
v_max3_f32 v122, v122, v43, v56
v_max3_f32 v122, v122, v57, v58
v_max_f32 v122, v122, v59
v_mov_b32 v26, v122
s_nop 1
v_permlane16_swap_b32 v122, v26
v_max_f32 v122, v122, v26
v_mov_b32 v26, v122
s_nop 1
v_permlane32_swap_b32 v122, v26
v_max_f32 v122, v122, v26
v_max_f32 v122, 0, v122
v_sub_f32 v26, 0, v122
v_exp_f32 v126, v26
s_nop 0
v_mul_f32 v114, v114, v126
v_max3_f32 v123, v44, v45, v46
v_max3_f32 v123, v123, v47, v60
v_max3_f32 v123, v123, v61, v62
v_max_f32 v123, v123, v63
v_mov_b32 v26, v123
)"
R"(
s_nop 1
v_permlane16_swap_b32 v123, v26
v_max_f32 v123, v123, v26
v_mov_b32 v26, v123
s_nop 1
v_permlane32_swap_b32 v123, v26
v_max_f32 v123, v123, v26
v_max_f32 v123, 0, v123
v_sub_f32 v26, 0, v123
v_exp_f32 v127, v26
s_nop 0
v_mul_f32 v115, v115, v127
v_sub_f32 v28, v32, v120
v_sub_f32 v29, v33, v120
v_exp_f32 v28, v28
v_exp_f32 v29, v29
s_nop 0
v_cvt_pk_bf16_f32 v196, v28, v29
v_sub_f32 v28, v34, v120
v_sub_f32 v29, v35, v120
v_exp_f32 v28, v28
v_exp_f32 v29, v29
s_nop 0
v_cvt_pk_bf16_f32 v197, v28, v29
v_sub_f32 v64, v64, v120
v_sub_f32 v65, v65, v120
v_sub_f32 v66, v66, v120
v_sub_f32 v67, v67, v120
v_sub_f32 v28, v36, v121
v_sub_f32 v29, v37, v121
v_exp_f32 v28, v28
v_exp_f32 v29, v29
s_nop 0
v_cvt_pk_bf16_f32 v204, v28, v29
v_sub_f32 v28, v38, v121
v_sub_f32 v29, v39, v121
v_exp_f32 v28, v28
v_exp_f32 v29, v29
s_nop 0
v_cvt_pk_bf16_f32 v205, v28, v29
v_sub_f32 v68, v68, v121
v_sub_f32 v69, v69, v121
v_sub_f32 v70, v70, v121
v_sub_f32 v71, v71, v121
v_sub_f32 v28, v40, v122
v_sub_f32 v29, v41, v122
v_exp_f32 v28, v28
v_exp_f32 v29, v29
s_nop 0
v_cvt_pk_bf16_f32 v212, v28, v29
v_sub_f32 v28, v42, v122
v_sub_f32 v29, v43, v122
v_exp_f32 v28, v28
v_exp_f32 v29, v29
s_nop 0
v_cvt_pk_bf16_f32 v213, v28, v29
v_sub_f32 v72, v72, v122
v_sub_f32 v73, v73, v122
v_sub_f32 v74, v74, v122
v_sub_f32 v75, v75, v122
v_sub_f32 v28, v44, v123
v_sub_f32 v29, v45, v123
v_exp_f32 v28, v28
v_exp_f32 v29, v29
s_nop 0
v_cvt_pk_bf16_f32 v220, v28, v29
v_sub_f32 v28, v46, v123
v_sub_f32 v29, v47, v123
v_exp_f32 v28, v28
v_exp_f32 v29, v29
s_nop 0
v_cvt_pk_bf16_f32 v221, v28, v29
v_sub_f32 v76, v76, v123
v_sub_f32 v77, v77, v123
v_sub_f32 v78, v78, v123
v_sub_f32 v79, v79, v123
v_sub_f32 v28, v48, v120
v_sub_f32 v29, v49, v120
v_exp_f32 v28, v28
v_exp_f32 v29, v29
s_nop 0
v_cvt_pk_bf16_f32 v198, v28, v29
v_sub_f32 v28, v50, v120
v_sub_f32 v29, v51, v120
v_exp_f32 v28, v28
v_exp_f32 v29, v29
s_nop 0
v_cvt_pk_bf16_f32 v199, v28, v29
v_sub_f32 v80, v80, v120
v_sub_f32 v81, v81, v120
v_sub_f32 v82, v82, v120
v_sub_f32 v83, v83, v120
v_sub_f32 v28, v52, v121
v_sub_f32 v29, v53, v121
v_exp_f32 v28, v28
v_exp_f32 v29, v29
s_nop 0
v_cvt_pk_bf16_f32 v206, v28, v29
v_sub_f32 v28, v54, v121
v_sub_f32 v29, v55, v121
v_exp_f32 v28, v28
v_exp_f32 v29, v29
s_nop 0
v_cvt_pk_bf16_f32 v207, v28, v29
v_sub_f32 v84, v84, v121
v_sub_f32 v85, v85, v121
v_sub_f32 v86, v86, v121
v_sub_f32 v87, v87, v121
v_sub_f32 v28, v56, v122
v_sub_f32 v29, v57, v122
v_exp_f32 v28, v28
v_exp_f32 v29, v29
s_nop 0
v_cvt_pk_bf16_f32 v214, v28, v29
v_sub_f32 v28, v58, v122
v_sub_f32 v29, v59, v122
v_exp_f32 v28, v28
v_exp_f32 v29, v29
s_nop 0
v_cvt_pk_bf16_f32 v215, v28, v29
v_sub_f32 v88, v88, v122
v_sub_f32 v89, v89, v122
v_sub_f32 v90, v90, v122
v_sub_f32 v91, v91, v122
v_sub_f32 v28, v60, v123
v_sub_f32 v29, v61, v123
v_exp_f32 v28, v28
v_exp_f32 v29, v29
s_nop 0
v_cvt_pk_bf16_f32 v222, v28, v29
v_sub_f32 v28, v62, v123
v_sub_f32 v29, v63, v123
v_exp_f32 v28, v28
v_exp_f32 v29, v29
s_nop 0
v_cvt_pk_bf16_f32 v223, v28, v29
v_sub_f32 v92, v92, v123
v_sub_f32 v93, v93, v123
v_sub_f32 v94, v94, v123
v_sub_f32 v95, v95, v123
v_sub_f32 v4, v4, v120
v_sub_f32 v5, v5, v120
v_sub_f32 v6, v6, v120
v_sub_f32 v7, v7, v120
v_sub_f32 v8, v8, v121
v_sub_f32 v9, v9, v121
v_sub_f32 v10, v10, v121
v_sub_f32 v11, v11, v121
v_sub_f32 v132, v132, v122
v_sub_f32 v133, v133, v122
v_sub_f32 v134, v134, v122
v_sub_f32 v135, v135, v122
v_sub_f32 v140, v140, v123
v_sub_f32 v141, v141, v123
v_sub_f32 v142, v142, v123
v_sub_f32 v143, v143, v123
v_sub_f32 v148, v148, v122
v_sub_f32 v149, v149, v122
v_sub_f32 v150, v150, v122
v_sub_f32 v151, v151, v122
v_sub_f32 v156, v156, v122
v_sub_f32 v157, v157, v122
v_sub_f32 v158, v158, v122
v_sub_f32 v159, v159, v122
v_sub_f32 v164, v164, v123
v_sub_f32 v165, v165, v123
v_sub_f32 v166, v166, v123
v_sub_f32 v167, v167, v123
v_sub_f32 v172, v172, v122
v_sub_f32 v173, v173, v122
v_sub_f32 v174, v174, v122
v_sub_f32 v175, v175, v122
v_sub_f32 v180, v180, v123
v_sub_f32 v181, v181, v123
v_sub_f32 v182, v182, v123
v_sub_f32 v183, v183, v123
v_sub_f32 v188, v188, v123
v_sub_f32 v189, v189, v123
v_sub_f32 v190, v190, v123
v_sub_f32 v191, v191, v123
s_getpc_b64 s[16:17]
.Lab1_g:
s_add_u32 s16, s16, .Lab1_b-.Lab1_g
s_addc_u32 s17, s17, 0
s_branch .Lscale4
.Lab1_b:
Cvvv 116,96,196,112
Mvvv 116,100,204
Mvvv 116,104,212
Mvvv 116,108,220
v_exp_f32 v120, v64
v_exp_f32 v121, v65
s_nop 0
v_cvt_pk_bf16_f32 v228, v120, v121
v_exp_f32 v122, v66
v_exp_f32 v123, v67
v_exp_f32 v124, v68
v_exp_f32 v125, v69
v_exp_f32 v126, v70
v_cvt_pk_bf16_f32 v229, v122, v123
v_exp_f32 v127, v71
v_exp_f32 v120, v72
v_exp_f32 v121, v73
v_cvt_pk_bf16_f32 v236, v124, v125
v_exp_f32 v122, v74
v_cvt_pk_bf16_f32 v237, v126, v127
v_exp_f32 v123, v75
v_exp_f32 v124, v76
v_cvt_pk_bf16_f32 v244, v120, v121
v_exp_f32 v125, v77
s_nop 7
s_branch .Lab1_r
.Ld0:
s_waitcnt vmcnt(0)
s_nop 7
v_max3_f32 v120, v64, v65, v66
v_max3_f32 v120, v120, v67, v80
v_max3_f32 v120, v120, v81, v82
v_max_f32 v120, v120, v83
v_mov_b32 v26, v120
s_nop 1
v_permlane16_swap_b32 v120, v26
v_max_f32 v120, v120, v26
v_mov_b32 v26, v120
s_nop 1
v_permlane32_swap_b32 v120, v26
v_max_f32 v120, v120, v26
v_max_f32 v120, 0, v120
v_sub_f32 v26, 0, v120
v_exp_f32 v124, v26
s_nop 0
v_mul_f32 v116, v116, v124
v_max3_f32 v121, v68, v69, v70
v_max3_f32 v121, v121, v71, v84
v_max3_f32 v121, v121, v85, v86
v_max_f32 v121, v121, v87
v_mov_b32 v26, v121
s_nop 1
v_permlane16_swap_b32 v121, v26
v_max_f32 v121, v121, v26
v_mov_b32 v26, v121
s_nop 1
v_permlane32_swap_b32 v121, v26
v_max_f32 v121, v121, v26
v_max_f32 v121, 0, v121
v_sub_f32 v26, 0, v121
v_exp_f32 v125, v26
s_nop 0
v_mul_f32 v117, v117, v125
v_max3_f32 v122, v72, v73, v74
v_max3_f32 v122, v122, v75, v88
v_max3_f32 v122, v122, v89, v90
v_max_f32 v122, v122, v91
v_mov_b32 v26, v122
s_nop 1
v_permlane16_swap_b32 v122, v26
v_max_f32 v122, v122, v26
v_mov_b32 v26, v122
s_nop 1
v_permlane32_swap_b32 v122, v26
v_max_f32 v122, v122, v26
v_max_f32 v122, 0, v122
v_sub_f32 v26, 0, v122
v_exp_f32 v126, v26
s_nop 0
v_mul_f32 v118, v118, v126
v_max3_f32 v123, v76, v77, v78
v_max3_f32 v123, v123, v79, v92
v_max3_f32 v123, v123, v93, v94
v_max_f32 v123, v123, v95
v_mov_b32 v26, v123
s_nop 1
v_permlane16_swap_b32 v123, v26
v_max_f32 v123, v123, v26
v_mov_b32 v26, v123
s_nop 1
v_permlane32_swap_b32 v123, v26
v_max_f32 v123, v123, v26
v_max_f32 v123, 0, v123
v_sub_f32 v26, 0, v123
v_exp_f32 v127, v26
s_nop 0
v_mul_f32 v119, v119, v127
v_sub_f32 v28, v64, v120
v_sub_f32 v29, v65, v120
v_exp_f32 v28, v28
v_exp_f32 v29, v29
s_nop 0
v_cvt_pk_bf16_f32 v228, v28, v29
v_sub_f32 v28, v66, v120
v_sub_f32 v29, v67, v120
v_exp_f32 v28, v28
v_exp_f32 v29, v29
s_nop 0
v_cvt_pk_bf16_f32 v229, v28, v29
v_sub_f32 v32, v32, v120
v_sub_f32 v33, v33, v120
v_sub_f32 v34, v34, v120
v_sub_f32 v35, v35, v120
v_sub_f32 v28, v68, v121
v_sub_f32 v29, v69, v121
v_exp_f32 v28, v28
v_exp_f32 v29, v29
s_nop 0
v_cvt_pk_bf16_f32 v236, v28, v29
v_sub_f32 v28, v70, v121
v_sub_f32 v29, v71, v121
v_exp_f32 v28, v28
v_exp_f32 v29, v29
s_nop 0
v_cvt_pk_bf16_f32 v237, v28, v29
v_sub_f32 v36, v36, v121
v_sub_f32 v37, v37, v121
v_sub_f32 v38, v38, v121
v_sub_f32 v39, v39, v121
v_sub_f32 v28, v72, v122
v_sub_f32 v29, v73, v122
v_exp_f32 v28, v28
v_exp_f32 v29, v29
s_nop 0
v_cvt_pk_bf16_f32 v244, v28, v29
v_sub_f32 v28, v74, v122
v_sub_f32 v29, v75, v122
v_exp_f32 v28, v28
v_exp_f32 v29, v29
s_nop 0
v_cvt_pk_bf16_f32 v245, v28, v29
v_sub_f32 v40, v40, v122
v_sub_f32 v41, v41, v122
v_sub_f32 v42, v42, v122
v_sub_f32 v43, v43, v122
v_sub_f32 v28, v76, v123
v_sub_f32 v29, v77, v123
v_exp_f32 v28, v28
v_exp_f32 v29, v29
s_nop 0
v_cvt_pk_bf16_f32 v252, v28, v29
v_sub_f32 v28, v78, v123
v_sub_f32 v29, v79, v123
v_exp_f32 v28, v28
v_exp_f32 v29, v29
s_nop 0
v_cvt_pk_bf16_f32 v253, v28, v29
v_sub_f32 v44, v44, v123
v_sub_f32 v45, v45, v123
v_sub_f32 v46, v46, v123
v_sub_f32 v47, v47, v123
v_sub_f32 v28, v80, v120
v_sub_f32 v29, v81, v120
v_exp_f32 v28, v28
v_exp_f32 v29, v29
s_nop 0
v_cvt_pk_bf16_f32 v230, v28, v29
v_sub_f32 v28, v82, v120
v_sub_f32 v29, v83, v120
v_exp_f32 v28, v28
v_exp_f32 v29, v29
s_nop 0
v_cvt_pk_bf16_f32 v231, v28, v29
v_sub_f32 v48, v48, v120
v_sub_f32 v49, v49, v120
v_sub_f32 v50, v50, v120
v_sub_f32 v51, v51, v120
v_sub_f32 v28, v84, v121
v_sub_f32 v29, v85, v121
v_exp_f32 v28, v28
v_exp_f32 v29, v29
s_nop 0
v_cvt_pk_bf16_f32 v238, v28, v29
v_sub_f32 v28, v86, v121
v_sub_f32 v29, v87, v121
v_exp_f32 v28, v28
v_exp_f32 v29, v29
s_nop 0
v_cvt_pk_bf16_f32 v239, v28, v29
v_sub_f32 v52, v52, v121
v_sub_f32 v53, v53, v121
v_sub_f32 v54, v54, v121
v_sub_f32 v55, v55, v121
v_sub_f32 v28, v88, v122
v_sub_f32 v29, v89, v122
v_exp_f32 v28, v28
v_exp_f32 v29, v29
s_nop 0
v_cvt_pk_bf16_f32 v246, v28, v29
v_sub_f32 v28, v90, v122
v_sub_f32 v29, v91, v122
v_exp_f32 v28, v28
v_exp_f32 v29, v29
s_nop 0
v_cvt_pk_bf16_f32 v247, v28, v29
v_sub_f32 v56, v56, v122
v_sub_f32 v57, v57, v122
v_sub_f32 v58, v58, v122
v_sub_f32 v59, v59, v122
v_sub_f32 v28, v92, v123
v_sub_f32 v29, v93, v123
v_exp_f32 v28, v28
v_exp_f32 v29, v29
s_nop 0
v_cvt_pk_bf16_f32 v254, v28, v29
v_sub_f32 v28, v94, v123
v_sub_f32 v29, v95, v123
v_exp_f32 v28, v28
v_exp_f32 v29, v29
s_nop 0
v_cvt_pk_bf16_f32 v255, v28, v29
v_sub_f32 v60, v60, v123
v_sub_f32 v61, v61, v123
v_sub_f32 v62, v62, v123
v_sub_f32 v63, v63, v123
v_sub_f32 v4, v4, v120
v_sub_f32 v5, v5, v120
v_sub_f32 v6, v6, v120
v_sub_f32 v7, v7, v120
v_sub_f32 v8, v8, v121
v_sub_f32 v9, v9, v121
v_sub_f32 v10, v10, v121
v_sub_f32 v11, v11, v121
v_sub_f32 v132, v132, v122
v_sub_f32 v133, v133, v122
v_sub_f32 v134, v134, v122
v_sub_f32 v135, v135, v122
v_sub_f32 v140, v140, v123
v_sub_f32 v141, v141, v123
v_sub_f32 v142, v142, v123
v_sub_f32 v143, v143, v123
v_sub_f32 v148, v148, v122
v_sub_f32 v149, v149, v122
v_sub_f32 v150, v150, v122
v_sub_f32 v151, v151, v122
v_sub_f32 v156, v156, v122
v_sub_f32 v157, v157, v122
v_sub_f32 v158, v158, v122
v_sub_f32 v159, v159, v122
v_sub_f32 v164, v164, v123
v_sub_f32 v165, v165, v123
v_sub_f32 v166, v166, v123
v_sub_f32 v167, v167, v123
v_sub_f32 v172, v172, v122
v_sub_f32 v173, v173, v122
v_sub_f32 v174, v174, v122
v_sub_f32 v175, v175, v122
v_sub_f32 v180, v180, v123
v_sub_f32 v181, v181, v123
v_sub_f32 v182, v182, v123
v_sub_f32 v183, v183, v123
v_sub_f32 v188, v188, v123
v_sub_f32 v189, v189, v123
v_sub_f32 v190, v190, v123
v_sub_f32 v191, v191, v123
s_getpc_b64 s[16:17]
.Ld0_g:
s_add_u32 s16, s16, .Ld0_b-.Ld0_g
s_addc_u32 s17, s17, 0
s_branch .Lscale4
.Ld0_b:
Cvvv 112,96,228,116
Mvvv 112,100,236
Mvvv 112,104,244
Mvvv 112,108,252
v_exp_f32 v120, v32
v_exp_f32 v121, v33
v_exp_f32 v122, v34
v_exp_f32 v123, v35
v_cvt_pk_bf16_f32 v196, v120, v121
v_cvt_pk_bf16_f32 v197, v122, v123
v_exp_f32 v124, v36
v_exp_f32 v125, v37
s_nop 0
v_cvt_pk_bf16_f32 v204, v124, v125
v_exp_f32 v126, v38
v_exp_f32 v127, v39
v_exp_f32 v120, v40
v_cvt_pk_bf16_f32 v205, v126, v127
v_exp_f32 v121, v41
v_exp_f32 v122, v42
v_cvt_pk_bf16_f32 v212, v120, v121
v_exp_f32 v123, v43
v_exp_f32 v124, v44
s_nop 7
s_branch .Ld0_r
.Ld1:
s_waitcnt vmcnt(0)
s_nop 7
v_max3_f32 v120, v32, v33, v34
v_max3_f32 v120, v120, v35, v48
v_max3_f32 v120, v120, v49, v50
v_max_f32 v120, v120, v51
v_mov_b32 v26, v120
s_nop 1
v_permlane16_swap_b32 v120, v26
v_max_f32 v120, v120, v26
v_mov_b32 v26, v120
s_nop 1
v_permlane32_swap_b32 v120, v26
v_max_f32 v120, v120, v26
v_max_f32 v120, 0, v120
v_sub_f32 v26, 0, v120
v_exp_f32 v124, v26
s_nop 0
v_mul_f32 v112, v112, v124
v_max3_f32 v121, v36, v37, v38
v_max3_f32 v121, v121, v39, v52
v_max3_f32 v121, v121, v53, v54
v_max_f32 v121, v121, v55
v_mov_b32 v26, v121
s_nop 1
v_permlane16_swap_b32 v121, v26
v_max_f32 v121, v121, v26
v_mov_b32 v26, v121
s_nop 1
v_permlane32_swap_b32 v121, v26
v_max_f32 v121, v121, v26
v_max_f32 v121, 0, v121
v_sub_f32 v26, 0, v121
v_exp_f32 v125, v26
s_nop 0
v_mul_f32 v113, v113, v125
v_max3_f32 v122, v40, v41, v42
v_max3_f32 v122, v122, v43, v56
v_max3_f32 v122, v122, v57, v58
v_max_f32 v122, v122, v59
v_mov_b32 v26, v122
s_nop 1
v_permlane16_swap_b32 v122, v26
)"
R"(
v_max_f32 v122, v122, v26
v_mov_b32 v26, v122
s_nop 1
v_permlane32_swap_b32 v122, v26
v_max_f32 v122, v122, v26
v_max_f32 v122, 0, v122
v_sub_f32 v26, 0, v122
v_exp_f32 v126, v26
s_nop 0
v_mul_f32 v114, v114, v126
v_max3_f32 v123, v44, v45, v46
v_max3_f32 v123, v123, v47, v60
v_max3_f32 v123, v123, v61, v62
v_max_f32 v123, v123, v63
v_mov_b32 v26, v123
s_nop 1
v_permlane16_swap_b32 v123, v26
v_max_f32 v123, v123, v26
v_mov_b32 v26, v123
s_nop 1
v_permlane32_swap_b32 v123, v26
v_max_f32 v123, v123, v26
v_max_f32 v123, 0, v123
v_sub_f32 v26, 0, v123
v_exp_f32 v127, v26
s_nop 0
v_mul_f32 v115, v115, v127
v_sub_f32 v28, v32, v120
v_sub_f32 v29, v33, v120
v_exp_f32 v28, v28
v_exp_f32 v29, v29
s_nop 0
v_cvt_pk_bf16_f32 v196, v28, v29
v_sub_f32 v28, v34, v120
v_sub_f32 v29, v35, v120
v_exp_f32 v28, v28
v_exp_f32 v29, v29
s_nop 0
v_cvt_pk_bf16_f32 v197, v28, v29
v_sub_f32 v64, v64, v120
v_sub_f32 v65, v65, v120
v_sub_f32 v66, v66, v120
v_sub_f32 v67, v67, v120
v_sub_f32 v28, v36, v121
v_sub_f32 v29, v37, v121
v_exp_f32 v28, v28
v_exp_f32 v29, v29
s_nop 0
v_cvt_pk_bf16_f32 v204, v28, v29
v_sub_f32 v28, v38, v121
v_sub_f32 v29, v39, v121
v_exp_f32 v28, v28
v_exp_f32 v29, v29
s_nop 0
v_cvt_pk_bf16_f32 v205, v28, v29
v_sub_f32 v68, v68, v121
v_sub_f32 v69, v69, v121
v_sub_f32 v70, v70, v121
v_sub_f32 v71, v71, v121
v_sub_f32 v28, v40, v122
v_sub_f32 v29, v41, v122
v_exp_f32 v28, v28
v_exp_f32 v29, v29
s_nop 0
v_cvt_pk_bf16_f32 v212, v28, v29
v_sub_f32 v28, v42, v122
v_sub_f32 v29, v43, v122
v_exp_f32 v28, v28
v_exp_f32 v29, v29
s_nop 0
v_cvt_pk_bf16_f32 v213, v28, v29
v_sub_f32 v72, v72, v122
v_sub_f32 v73, v73, v122
v_sub_f32 v74, v74, v122
v_sub_f32 v75, v75, v122
v_sub_f32 v28, v44, v123
v_sub_f32 v29, v45, v123
v_exp_f32 v28, v28
v_exp_f32 v29, v29
s_nop 0
v_cvt_pk_bf16_f32 v220, v28, v29
v_sub_f32 v28, v46, v123
v_sub_f32 v29, v47, v123
v_exp_f32 v28, v28
v_exp_f32 v29, v29
s_nop 0
v_cvt_pk_bf16_f32 v221, v28, v29
v_sub_f32 v76, v76, v123
v_sub_f32 v77, v77, v123
v_sub_f32 v78, v78, v123
v_sub_f32 v79, v79, v123
v_sub_f32 v28, v48, v120
v_sub_f32 v29, v49, v120
v_exp_f32 v28, v28
v_exp_f32 v29, v29
s_nop 0
v_cvt_pk_bf16_f32 v198, v28, v29
v_sub_f32 v28, v50, v120
v_sub_f32 v29, v51, v120
v_exp_f32 v28, v28
v_exp_f32 v29, v29
s_nop 0
v_cvt_pk_bf16_f32 v199, v28, v29
v_sub_f32 v80, v80, v120
v_sub_f32 v81, v81, v120
v_sub_f32 v82, v82, v120
v_sub_f32 v83, v83, v120
v_sub_f32 v28, v52, v121
v_sub_f32 v29, v53, v121
v_exp_f32 v28, v28
v_exp_f32 v29, v29
s_nop 0
v_cvt_pk_bf16_f32 v206, v28, v29
v_sub_f32 v28, v54, v121
v_sub_f32 v29, v55, v121
v_exp_f32 v28, v28
v_exp_f32 v29, v29
s_nop 0
v_cvt_pk_bf16_f32 v207, v28, v29
v_sub_f32 v84, v84, v121
v_sub_f32 v85, v85, v121
v_sub_f32 v86, v86, v121
v_sub_f32 v87, v87, v121
v_sub_f32 v28, v56, v122
v_sub_f32 v29, v57, v122
v_exp_f32 v28, v28
v_exp_f32 v29, v29
s_nop 0
v_cvt_pk_bf16_f32 v214, v28, v29
v_sub_f32 v28, v58, v122
v_sub_f32 v29, v59, v122
v_exp_f32 v28, v28
v_exp_f32 v29, v29
s_nop 0
v_cvt_pk_bf16_f32 v215, v28, v29
v_sub_f32 v88, v88, v122
v_sub_f32 v89, v89, v122
v_sub_f32 v90, v90, v122
v_sub_f32 v91, v91, v122
v_sub_f32 v28, v60, v123
v_sub_f32 v29, v61, v123
v_exp_f32 v28, v28
v_exp_f32 v29, v29
s_nop 0
v_cvt_pk_bf16_f32 v222, v28, v29
v_sub_f32 v28, v62, v123
v_sub_f32 v29, v63, v123
v_exp_f32 v28, v28
v_exp_f32 v29, v29
s_nop 0
v_cvt_pk_bf16_f32 v223, v28, v29
v_sub_f32 v92, v92, v123
v_sub_f32 v93, v93, v123
v_sub_f32 v94, v94, v123
v_sub_f32 v95, v95, v123
v_sub_f32 v4, v4, v120
v_sub_f32 v5, v5, v120
v_sub_f32 v6, v6, v120
v_sub_f32 v7, v7, v120
v_sub_f32 v8, v8, v121
v_sub_f32 v9, v9, v121
v_sub_f32 v10, v10, v121
v_sub_f32 v11, v11, v121
v_sub_f32 v132, v132, v122
v_sub_f32 v133, v133, v122
v_sub_f32 v134, v134, v122
v_sub_f32 v135, v135, v122
v_sub_f32 v140, v140, v123
v_sub_f32 v141, v141, v123
v_sub_f32 v142, v142, v123
v_sub_f32 v143, v143, v123
v_sub_f32 v148, v148, v122
v_sub_f32 v149, v149, v122
v_sub_f32 v150, v150, v122
v_sub_f32 v151, v151, v122
v_sub_f32 v156, v156, v122
v_sub_f32 v157, v157, v122
v_sub_f32 v158, v158, v122
v_sub_f32 v159, v159, v122
v_sub_f32 v164, v164, v123
v_sub_f32 v165, v165, v123
v_sub_f32 v166, v166, v123
v_sub_f32 v167, v167, v123
v_sub_f32 v172, v172, v122
v_sub_f32 v173, v173, v122
v_sub_f32 v174, v174, v122
v_sub_f32 v175, v175, v122
v_sub_f32 v180, v180, v123
v_sub_f32 v181, v181, v123
v_sub_f32 v182, v182, v123
v_sub_f32 v183, v183, v123
v_sub_f32 v188, v188, v123
v_sub_f32 v189, v189, v123
v_sub_f32 v190, v190, v123
v_sub_f32 v191, v191, v123
s_getpc_b64 s[16:17]
.Ld1_g:
s_add_u32 s16, s16, .Ld1_b-.Ld1_g
s_addc_u32 s17, s17, 0
s_branch .Lscale4
.Ld1_b:
Cvvv 116,96,196,112
Mvvv 116,100,204
Mvvv 116,104,212
Mvvv 116,108,220
v_exp_f32 v120, v64
v_exp_f32 v121, v65
v_exp_f32 v122, v66
v_exp_f32 v123, v67
s_nop 0
v_cvt_pk_bf16_f32 v229, v122, v123
v_exp_f32 v124, v68
v_exp_f32 v125, v69
v_exp_f32 v126, v70
v_exp_f32 v127, v71
v_cvt_pk_bf16_f32 v228, v120, v121
v_cvt_pk_bf16_f32 v237, v126, v127
v_exp_f32 v120, v72
v_exp_f32 v121, v73
v_exp_f32 v122, v74
v_cvt_pk_bf16_f32 v236, v124, v125
v_exp_f32 v123, v75
s_nop 7
s_branch .Ld1_r
.Ly:
s_waitcnt vmcnt(0)
s_nop 7
v_max3_f32 v120, v64, v65, v66
v_max3_f32 v120, v120, v67, v80
v_max3_f32 v120, v120, v81, v82
v_max_f32 v120, v120, v83
v_mov_b32 v26, v120
s_nop 1
v_permlane16_swap_b32 v120, v26
v_max_f32 v120, v120, v26
v_mov_b32 v26, v120
s_nop 1
v_permlane32_swap_b32 v120, v26
v_max_f32 v120, v120, v26
v_max_f32 v120, 0, v120
v_sub_f32 v26, 0, v120
v_exp_f32 v124, v26
s_nop 0
v_mul_f32 v116, v116, v124
v_max3_f32 v121, v68, v69, v70
v_max3_f32 v121, v121, v71, v84
v_max3_f32 v121, v121, v85, v86
v_max_f32 v121, v121, v87
v_mov_b32 v26, v121
s_nop 1
v_permlane16_swap_b32 v121, v26
v_max_f32 v121, v121, v26
v_mov_b32 v26, v121
s_nop 1
v_permlane32_swap_b32 v121, v26
v_max_f32 v121, v121, v26
v_max_f32 v121, 0, v121
v_sub_f32 v26, 0, v121
v_exp_f32 v125, v26
s_nop 0
v_mul_f32 v117, v117, v125
v_max3_f32 v122, v72, v73, v74
v_max3_f32 v122, v122, v75, v88
v_max3_f32 v122, v122, v89, v90
v_max_f32 v122, v122, v91
v_mov_b32 v26, v122
s_nop 1
v_permlane16_swap_b32 v122, v26
v_max_f32 v122, v122, v26
v_mov_b32 v26, v122
s_nop 1
v_permlane32_swap_b32 v122, v26
v_max_f32 v122, v122, v26
v_max_f32 v122, 0, v122
v_sub_f32 v26, 0, v122
v_exp_f32 v126, v26
s_nop 0
v_mul_f32 v118, v118, v126
v_max3_f32 v123, v76, v77, v78
v_max3_f32 v123, v123, v79, v92
v_max3_f32 v123, v123, v93, v94
v_max_f32 v123, v123, v95
v_mov_b32 v26, v123
s_nop 1
v_permlane16_swap_b32 v123, v26
v_max_f32 v123, v123, v26
v_mov_b32 v26, v123
s_nop 1
v_permlane32_swap_b32 v123, v26
v_max_f32 v123, v123, v26
v_max_f32 v123, 0, v123
v_sub_f32 v26, 0, v123
v_exp_f32 v127, v26
s_nop 0
v_mul_f32 v119, v119, v127
v_sub_f32 v28, v64, v120
v_sub_f32 v29, v65, v120
v_exp_f32 v28, v28
v_exp_f32 v29, v29
s_nop 0
v_cvt_pk_bf16_f32 v228, v28, v29
v_sub_f32 v28, v66, v120
v_sub_f32 v29, v67, v120
v_exp_f32 v28, v28
v_exp_f32 v29, v29
s_nop 0
v_cvt_pk_bf16_f32 v229, v28, v29
v_sub_f32 v32, v32, v120
v_sub_f32 v33, v33, v120
v_sub_f32 v34, v34, v120
v_sub_f32 v35, v35, v120
v_sub_f32 v28, v68, v121
v_sub_f32 v29, v69, v121
v_exp_f32 v28, v28
v_exp_f32 v29, v29
s_nop 0
v_cvt_pk_bf16_f32 v236, v28, v29
v_sub_f32 v28, v70, v121
v_sub_f32 v29, v71, v121
v_exp_f32 v28, v28
v_exp_f32 v29, v29
s_nop 0
v_cvt_pk_bf16_f32 v237, v28, v29
v_sub_f32 v36, v36, v121
v_sub_f32 v37, v37, v121
v_sub_f32 v38, v38, v121
v_sub_f32 v39, v39, v121
v_sub_f32 v28, v72, v122
v_sub_f32 v29, v73, v122
v_exp_f32 v28, v28
v_exp_f32 v29, v29
s_nop 0
v_cvt_pk_bf16_f32 v244, v28, v29
v_sub_f32 v28, v74, v122
v_sub_f32 v29, v75, v122
v_exp_f32 v28, v28
v_exp_f32 v29, v29
s_nop 0
v_cvt_pk_bf16_f32 v245, v28, v29
v_sub_f32 v40, v40, v120
v_sub_f32 v41, v41, v120
v_sub_f32 v42, v42, v120
v_sub_f32 v43, v43, v120
v_sub_f32 v28, v76, v123
v_sub_f32 v29, v77, v123
v_exp_f32 v28, v28
v_exp_f32 v29, v29
s_nop 0
v_cvt_pk_bf16_f32 v252, v28, v29
v_sub_f32 v28, v78, v123
v_sub_f32 v29, v79, v123
v_exp_f32 v28, v28
v_exp_f32 v29, v29
s_nop 0
v_cvt_pk_bf16_f32 v253, v28, v29
v_sub_f32 v44, v44, v121
v_sub_f32 v45, v45, v121
v_sub_f32 v46, v46, v121
v_sub_f32 v47, v47, v121
v_sub_f32 v28, v80, v120
v_sub_f32 v29, v81, v120
v_exp_f32 v28, v28
v_exp_f32 v29, v29
s_nop 0
v_cvt_pk_bf16_f32 v230, v28, v29
v_sub_f32 v28, v82, v120
v_sub_f32 v29, v83, v120
v_exp_f32 v28, v28
v_exp_f32 v29, v29
s_nop 0
v_cvt_pk_bf16_f32 v231, v28, v29
v_sub_f32 v48, v48, v120
v_sub_f32 v49, v49, v120
v_sub_f32 v50, v50, v120
v_sub_f32 v51, v51, v120
v_sub_f32 v28, v84, v121
v_sub_f32 v29, v85, v121
v_exp_f32 v28, v28
v_exp_f32 v29, v29
s_nop 0
v_cvt_pk_bf16_f32 v238, v28, v29
v_sub_f32 v28, v86, v121
v_sub_f32 v29, v87, v121
v_exp_f32 v28, v28
v_exp_f32 v29, v29
s_nop 0
v_cvt_pk_bf16_f32 v239, v28, v29
v_sub_f32 v52, v52, v121
v_sub_f32 v53, v53, v121
v_sub_f32 v54, v54, v121
v_sub_f32 v55, v55, v121
v_sub_f32 v28, v88, v122
v_sub_f32 v29, v89, v122
v_exp_f32 v28, v28
v_exp_f32 v29, v29
s_nop 0
v_cvt_pk_bf16_f32 v246, v28, v29
v_sub_f32 v28, v90, v122
v_sub_f32 v29, v91, v122
v_exp_f32 v28, v28
v_exp_f32 v29, v29
s_nop 0
v_cvt_pk_bf16_f32 v247, v28, v29
v_sub_f32 v56, v56, v120
v_sub_f32 v57, v57, v120
v_sub_f32 v58, v58, v120
v_sub_f32 v59, v59, v120
v_sub_f32 v28, v92, v123
v_sub_f32 v29, v93, v123
v_exp_f32 v28, v28
v_exp_f32 v29, v29
s_nop 0
v_cvt_pk_bf16_f32 v254, v28, v29
v_sub_f32 v28, v94, v123
v_sub_f32 v29, v95, v123
v_exp_f32 v28, v28
v_exp_f32 v29, v29
s_nop 0
v_cvt_pk_bf16_f32 v255, v28, v29
v_sub_f32 v60, v60, v121
v_sub_f32 v61, v61, v121
v_sub_f32 v62, v62, v121
v_sub_f32 v63, v63, v121
v_sub_f32 v4, v4, v120
v_sub_f32 v5, v5, v120
v_sub_f32 v6, v6, v120
v_sub_f32 v7, v7, v120
v_sub_f32 v8, v8, v121
v_sub_f32 v9, v9, v121
v_sub_f32 v10, v10, v121
v_sub_f32 v11, v11, v121
v_sub_f32 v132, v132, v122
v_sub_f32 v133, v133, v122
v_sub_f32 v134, v134, v122
v_sub_f32 v135, v135, v122
v_sub_f32 v140, v140, v123
v_sub_f32 v141, v141, v123
v_sub_f32 v142, v142, v123
v_sub_f32 v143, v143, v123
s_getpc_b64 s[16:17]
.Ly_g:
s_add_u32 s16, s16, .Ly_b-.Ly_g
s_addc_u32 s17, s17, 0
s_branch .Lscale4
.Ly_b:
Cvvv 112,96,228,116
Mvvv 112,100,236
Mvvv 112,104,244
Mvvv 112,108,252
v_exp_f32 v120, v32
v_exp_f32 v121, v33
v_exp_f32 v122, v34
v_exp_f32 v123, v35
v_exp_f32 v124, v36
v_exp_f32 v125, v37
v_cvt_pk_bf16_f32 v196, v120, v121
v_cvt_pk_bf16_f32 v197, v122, v123
v_exp_f32 v126, v38
v_exp_f32 v127, v39
v_exp_f32 v120, v40
v_cvt_pk_bf16_f32 v204, v124, v125
v_exp_f32 v121, v41
v_exp_f32 v122, v42
s_nop 7
s_branch .Ly_r
.Laa1:
s_waitcnt vmcnt(0)
s_nop 7
v_max3_f32 v120, v32, v33, v34
v_max3_f32 v120, v120, v35, v40
v_max3_f32 v120, v120, v41, v42
v_max3_f32 v120, v120, v43, v48
v_max3_f32 v120, v120, v49, v50
v_max3_f32 v120, v120, v51, v56
v_max3_f32 v120, v120, v57, v58
v_max_f32 v120, v120, v59
v_mov_b32 v26, v120
s_nop 1
v_permlane16_swap_b32 v120, v26
v_max_f32 v120, v120, v26
v_mov_b32 v26, v120
s_nop 1
v_permlane32_swap_b32 v120, v26
v_max_f32 v120, v120, v26
v_max_f32 v120, 0, v120
v_sub_f32 v26, 0, v120
v_exp_f32 v124, v26
s_nop 0
v_mul_f32 v112, v112, v124
v_max3_f32 v121, v36, v37, v38
v_max3_f32 v121, v121, v39, v44
v_max3_f32 v121, v121, v45, v46
v_max3_f32 v121, v121, v47, v52
v_max3_f32 v121, v121, v53, v54
v_max3_f32 v121, v121, v55, v60
v_max3_f32 v121, v121, v61, v62
v_max_f32 v121, v121, v63
v_mov_b32 v26, v121
s_nop 1
v_permlane16_swap_b32 v121, v26
v_max_f32 v121, v121, v26
v_mov_b32 v26, v121
s_nop 1
v_permlane32_swap_b32 v121, v26
v_max_f32 v121, v121, v26
v_max_f32 v121, 0, v121
v_sub_f32 v26, 0, v121
v_exp_f32 v125, v26
s_nop 0
v_mul_f32 v113, v113, v125
v_sub_f32 v28, v32, v120
v_sub_f32 v29, v33, v120
v_exp_f32 v28, v28
v_exp_f32 v29, v29
s_nop 0
v_cvt_pk_bf16_f32 v196, v28, v29
v_sub_f32 v28, v34, v120
v_sub_f32 v29, v35, v120
v_exp_f32 v28, v28
v_exp_f32 v29, v29
s_nop 0
v_cvt_pk_bf16_f32 v197, v28, v29
v_sub_f32 v64, v64, v120
v_sub_f32 v65, v65, v120
v_sub_f32 v66, v66, v120
v_sub_f32 v67, v67, v120
v_sub_f32 v28, v36, v121
v_sub_f32 v29, v37, v121
)"
R"(
v_exp_f32 v28, v28
v_exp_f32 v29, v29
s_nop 0
v_cvt_pk_bf16_f32 v204, v28, v29
v_sub_f32 v28, v38, v121
v_sub_f32 v29, v39, v121
v_exp_f32 v28, v28
v_exp_f32 v29, v29
s_nop 0
v_cvt_pk_bf16_f32 v205, v28, v29
v_sub_f32 v68, v68, v121
v_sub_f32 v69, v69, v121
v_sub_f32 v70, v70, v121
v_sub_f32 v71, v71, v121
v_sub_f32 v28, v40, v120
v_sub_f32 v29, v41, v120
v_exp_f32 v28, v28
v_exp_f32 v29, v29
s_nop 0
v_cvt_pk_bf16_f32 v198, v28, v29
v_sub_f32 v28, v42, v120
v_sub_f32 v29, v43, v120
v_exp_f32 v28, v28
v_exp_f32 v29, v29
s_nop 0
v_cvt_pk_bf16_f32 v199, v28, v29
v_sub_f32 v72, v72, v120
v_sub_f32 v73, v73, v120
v_sub_f32 v74, v74, v120
v_sub_f32 v75, v75, v120
v_sub_f32 v28, v44, v121
v_sub_f32 v29, v45, v121
v_exp_f32 v28, v28
v_exp_f32 v29, v29
s_nop 0
v_cvt_pk_bf16_f32 v206, v28, v29
v_sub_f32 v28, v46, v121
v_sub_f32 v29, v47, v121
v_exp_f32 v28, v28
v_exp_f32 v29, v29
s_nop 0
v_cvt_pk_bf16_f32 v207, v28, v29
v_sub_f32 v76, v76, v121
v_sub_f32 v77, v77, v121
v_sub_f32 v78, v78, v121
v_sub_f32 v79, v79, v121
v_sub_f32 v28, v48, v120
v_sub_f32 v29, v49, v120
v_exp_f32 v28, v28
v_exp_f32 v29, v29
s_nop 0
v_cvt_pk_bf16_f32 v212, v28, v29
v_sub_f32 v28, v50, v120
v_sub_f32 v29, v51, v120
v_exp_f32 v28, v28
v_exp_f32 v29, v29
s_nop 0
v_cvt_pk_bf16_f32 v213, v28, v29
v_sub_f32 v80, v80, v120
v_sub_f32 v81, v81, v120
v_sub_f32 v82, v82, v120
v_sub_f32 v83, v83, v120
v_sub_f32 v28, v52, v121
v_sub_f32 v29, v53, v121
v_exp_f32 v28, v28
v_exp_f32 v29, v29
s_nop 0
v_cvt_pk_bf16_f32 v220, v28, v29
v_sub_f32 v28, v54, v121
v_sub_f32 v29, v55, v121
v_exp_f32 v28, v28
v_exp_f32 v29, v29
s_nop 0
v_cvt_pk_bf16_f32 v221, v28, v29
v_sub_f32 v84, v84, v121
v_sub_f32 v85, v85, v121
v_sub_f32 v86, v86, v121
v_sub_f32 v87, v87, v121
v_sub_f32 v28, v56, v120
v_sub_f32 v29, v57, v120
v_exp_f32 v28, v28
v_exp_f32 v29, v29
s_nop 0
v_cvt_pk_bf16_f32 v214, v28, v29
v_sub_f32 v28, v58, v120
v_sub_f32 v29, v59, v120
v_exp_f32 v28, v28
v_exp_f32 v29, v29
s_nop 0
v_cvt_pk_bf16_f32 v215, v28, v29
v_sub_f32 v88, v88, v120
v_sub_f32 v89, v89, v120
v_sub_f32 v90, v90, v120
v_sub_f32 v91, v91, v120
v_sub_f32 v28, v60, v121
v_sub_f32 v29, v61, v121
v_exp_f32 v28, v28
v_exp_f32 v29, v29
s_nop 0
v_cvt_pk_bf16_f32 v222, v28, v29
v_sub_f32 v28, v62, v121
v_sub_f32 v29, v63, v121
v_exp_f32 v28, v28
v_exp_f32 v29, v29
s_nop 0
v_cvt_pk_bf16_f32 v223, v28, v29
v_sub_f32 v92, v92, v121
v_sub_f32 v93, v93, v121
v_sub_f32 v94, v94, v121
v_sub_f32 v95, v95, v121
v_sub_f32 v4, v4, v120
v_sub_f32 v5, v5, v120
v_sub_f32 v6, v6, v120
v_sub_f32 v7, v7, v120
v_sub_f32 v8, v8, v121
v_sub_f32 v9, v9, v121
v_sub_f32 v10, v10, v121
v_sub_f32 v11, v11, v121
s_getpc_b64 s[16:17]
.Laa1_g:
s_add_u32 s16, s16, .Laa1_b-.Laa1_g
s_addc_u32 s17, s17, 0
s_branch .Lscale2
.Laa1_b:
Cvvv 116,96,196,112
Mvvv 116,100,204
Mvvv 116,96,212
Mvvv 116,100,220
v_exp_f32 v120, v64
v_exp_f32 v121, v65
s_nop 0
v_cvt_pk_bf16_f32 v228, v120, v121
v_exp_f32 v122, v66
v_exp_f32 v123, v67
v_exp_f32 v124, v68
v_exp_f32 v125, v69
v_cvt_pk_bf16_f32 v229, v122, v123
v_exp_f32 v126, v70
v_exp_f32 v127, v71
v_cvt_pk_bf16_f32 v236, v124, v125
v_exp_f32 v120, v72
v_exp_f32 v121, v73
v_cvt_pk_bf16_f32 v237, v126, v127
v_exp_f32 v122, v74
v_exp_f32 v123, v75
v_cvt_pk_bf16_f32 v230, v120, v121
v_exp_f32 v124, v76
v_exp_f32 v125, v77
s_nop 7
s_branch .Laa1_r
.Laa0:
s_waitcnt vmcnt(0)
s_nop 7
v_max3_f32 v120, v64, v65, v66
v_max3_f32 v120, v120, v67, v72
v_max3_f32 v120, v120, v73, v74
v_max3_f32 v120, v120, v75, v80
v_max3_f32 v120, v120, v81, v82
v_max3_f32 v120, v120, v83, v88
v_max3_f32 v120, v120, v89, v90
v_max_f32 v120, v120, v91
v_mov_b32 v26, v120
s_nop 1
v_permlane16_swap_b32 v120, v26
v_max_f32 v120, v120, v26
v_mov_b32 v26, v120
s_nop 1
v_permlane32_swap_b32 v120, v26
v_max_f32 v120, v120, v26
v_max_f32 v120, 0, v120
v_sub_f32 v26, 0, v120
v_exp_f32 v124, v26
s_nop 0
v_mul_f32 v116, v116, v124
v_max3_f32 v121, v68, v69, v70
v_max3_f32 v121, v121, v71, v76
v_max3_f32 v121, v121, v77, v78
v_max3_f32 v121, v121, v79, v84
v_max3_f32 v121, v121, v85, v86
v_max3_f32 v121, v121, v87, v92
v_max3_f32 v121, v121, v93, v94
v_max_f32 v121, v121, v95
v_mov_b32 v26, v121
s_nop 1
v_permlane16_swap_b32 v121, v26
v_max_f32 v121, v121, v26
v_mov_b32 v26, v121
s_nop 1
v_permlane32_swap_b32 v121, v26
v_max_f32 v121, v121, v26
v_max_f32 v121, 0, v121
v_sub_f32 v26, 0, v121
v_exp_f32 v125, v26
s_nop 0
v_mul_f32 v117, v117, v125
v_sub_f32 v28, v64, v120
v_sub_f32 v29, v65, v120
v_exp_f32 v28, v28
v_exp_f32 v29, v29
s_nop 0
v_cvt_pk_bf16_f32 v228, v28, v29
v_sub_f32 v28, v66, v120
v_sub_f32 v29, v67, v120
v_exp_f32 v28, v28
v_exp_f32 v29, v29
s_nop 0
v_cvt_pk_bf16_f32 v229, v28, v29
v_sub_f32 v32, v32, v120
v_sub_f32 v33, v33, v120
v_sub_f32 v34, v34, v120
v_sub_f32 v35, v35, v120
v_sub_f32 v28, v68, v121
v_sub_f32 v29, v69, v121
v_exp_f32 v28, v28
v_exp_f32 v29, v29
s_nop 0
v_cvt_pk_bf16_f32 v236, v28, v29
v_sub_f32 v28, v70, v121
v_sub_f32 v29, v71, v121
v_exp_f32 v28, v28
v_exp_f32 v29, v29
s_nop 0
v_cvt_pk_bf16_f32 v237, v28, v29
v_sub_f32 v36, v36, v121
v_sub_f32 v37, v37, v121
v_sub_f32 v38, v38, v121
v_sub_f32 v39, v39, v121
v_sub_f32 v28, v72, v120
v_sub_f32 v29, v73, v120
v_exp_f32 v28, v28
v_exp_f32 v29, v29
s_nop 0
v_cvt_pk_bf16_f32 v230, v28, v29
v_sub_f32 v28, v74, v120
v_sub_f32 v29, v75, v120
v_exp_f32 v28, v28
v_exp_f32 v29, v29
s_nop 0
v_cvt_pk_bf16_f32 v231, v28, v29
v_sub_f32 v40, v40, v120
v_sub_f32 v41, v41, v120
v_sub_f32 v42, v42, v120
v_sub_f32 v43, v43, v120
v_sub_f32 v28, v76, v121
v_sub_f32 v29, v77, v121
v_exp_f32 v28, v28
v_exp_f32 v29, v29
s_nop 0
v_cvt_pk_bf16_f32 v238, v28, v29
v_sub_f32 v28, v78, v121
v_sub_f32 v29, v79, v121
v_exp_f32 v28, v28
v_exp_f32 v29, v29
s_nop 0
v_cvt_pk_bf16_f32 v239, v28, v29
v_sub_f32 v44, v44, v121
v_sub_f32 v45, v45, v121
v_sub_f32 v46, v46, v121
v_sub_f32 v47, v47, v121
v_sub_f32 v28, v80, v120
v_sub_f32 v29, v81, v120
v_exp_f32 v28, v28
v_exp_f32 v29, v29
s_nop 0
v_cvt_pk_bf16_f32 v244, v28, v29
v_sub_f32 v28, v82, v120
v_sub_f32 v29, v83, v120
v_exp_f32 v28, v28
v_exp_f32 v29, v29
s_nop 0
v_cvt_pk_bf16_f32 v245, v28, v29
v_sub_f32 v48, v48, v120
v_sub_f32 v49, v49, v120
v_sub_f32 v50, v50, v120
v_sub_f32 v51, v51, v120
v_sub_f32 v28, v84, v121
v_sub_f32 v29, v85, v121
v_exp_f32 v28, v28
v_exp_f32 v29, v29
s_nop 0
v_cvt_pk_bf16_f32 v252, v28, v29
v_sub_f32 v28, v86, v121
v_sub_f32 v29, v87, v121
v_exp_f32 v28, v28
v_exp_f32 v29, v29
s_nop 0
v_cvt_pk_bf16_f32 v253, v28, v29
v_sub_f32 v52, v52, v121
v_sub_f32 v53, v53, v121
v_sub_f32 v54, v54, v121
v_sub_f32 v55, v55, v121
v_sub_f32 v28, v88, v120
v_sub_f32 v29, v89, v120
v_exp_f32 v28, v28
v_exp_f32 v29, v29
s_nop 0
v_cvt_pk_bf16_f32 v246, v28, v29
v_sub_f32 v28, v90, v120
v_sub_f32 v29, v91, v120
v_exp_f32 v28, v28
v_exp_f32 v29, v29
s_nop 0
v_cvt_pk_bf16_f32 v247, v28, v29
v_sub_f32 v56, v56, v120
v_sub_f32 v57, v57, v120
v_sub_f32 v58, v58, v120
v_sub_f32 v59, v59, v120
v_sub_f32 v28, v92, v121
v_sub_f32 v29, v93, v121
v_exp_f32 v28, v28
v_exp_f32 v29, v29
s_nop 0
v_cvt_pk_bf16_f32 v254, v28, v29
v_sub_f32 v28, v94, v121
v_sub_f32 v29, v95, v121
v_exp_f32 v28, v28
v_exp_f32 v29, v29
s_nop 0
v_cvt_pk_bf16_f32 v255, v28, v29
v_sub_f32 v60, v60, v121
v_sub_f32 v61, v61, v121
v_sub_f32 v62, v62, v121
v_sub_f32 v63, v63, v121
v_sub_f32 v4, v4, v120
v_sub_f32 v5, v5, v120
v_sub_f32 v6, v6, v120
v_sub_f32 v7, v7, v120
v_sub_f32 v8, v8, v121
v_sub_f32 v9, v9, v121
v_sub_f32 v10, v10, v121
v_sub_f32 v11, v11, v121
s_getpc_b64 s[16:17]
.Laa0_g:
s_add_u32 s16, s16, .Laa0_b-.Laa0_g
s_addc_u32 s17, s17, 0
s_branch .Lscale2
.Laa0_b:
Cvvv 112,96,228,116
Mvvv 112,100,236
Mvvv 112,96,244
Mvvv 112,100,252
v_exp_f32 v120, v32
v_exp_f32 v121, v33
s_nop 0
v_cvt_pk_bf16_f32 v196, v120, v121
v_exp_f32 v122, v34
v_exp_f32 v123, v35
v_exp_f32 v124, v36
v_exp_f32 v125, v37
v_cvt_pk_bf16_f32 v197, v122, v123
v_exp_f32 v126, v38
v_exp_f32 v127, v39
v_cvt_pk_bf16_f32 v204, v124, v125
v_exp_f32 v120, v40
v_exp_f32 v121, v41
v_cvt_pk_bf16_f32 v205, v126, v127
v_exp_f32 v122, v42
v_exp_f32 v123, v43
v_cvt_pk_bf16_f32 v198, v120, v121
v_exp_f32 v124, v44
v_exp_f32 v125, v45
s_nop 7
s_branch .Laa0_r
.Ldr:
s_waitcnt vmcnt(0)
s_nop 7
v_max3_f32 v120, v32, v33, v34
v_max3_f32 v120, v120, v35, v40
v_max3_f32 v120, v120, v41, v42
v_max3_f32 v120, v120, v43, v48
v_max3_f32 v120, v120, v49, v50
v_max3_f32 v120, v120, v51, v56
v_max3_f32 v120, v120, v57, v58
v_max_f32 v120, v120, v59
v_mov_b32 v26, v120
s_nop 1
v_permlane16_swap_b32 v120, v26
v_max_f32 v120, v120, v26
v_mov_b32 v26, v120
s_nop 1
v_permlane32_swap_b32 v120, v26
v_max_f32 v120, v120, v26
v_max_f32 v120, 0, v120
v_sub_f32 v26, 0, v120
v_exp_f32 v124, v26
s_nop 0
v_mul_f32 v112, v112, v124
v_max3_f32 v121, v36, v37, v38
v_max3_f32 v121, v121, v39, v44
v_max3_f32 v121, v121, v45, v46
v_max3_f32 v121, v121, v47, v52
v_max3_f32 v121, v121, v53, v54
v_max3_f32 v121, v121, v55, v60
v_max3_f32 v121, v121, v61, v62
v_max_f32 v121, v121, v63
v_mov_b32 v26, v121
s_nop 1
v_permlane16_swap_b32 v121, v26
v_max_f32 v121, v121, v26
v_mov_b32 v26, v121
s_nop 1
v_permlane32_swap_b32 v121, v26
v_max_f32 v121, v121, v26
v_max_f32 v121, 0, v121
v_sub_f32 v26, 0, v121
v_exp_f32 v125, v26
s_nop 0
v_mul_f32 v113, v113, v125
v_sub_f32 v28, v32, v120
v_sub_f32 v29, v33, v120
v_exp_f32 v28, v28
v_exp_f32 v29, v29
s_nop 0
v_cvt_pk_bf16_f32 v196, v28, v29
v_sub_f32 v28, v34, v120
v_sub_f32 v29, v35, v120
v_exp_f32 v28, v28
v_exp_f32 v29, v29
s_nop 0
v_cvt_pk_bf16_f32 v197, v28, v29
v_sub_f32 v28, v36, v121
v_sub_f32 v29, v37, v121
v_exp_f32 v28, v28
v_exp_f32 v29, v29
s_nop 0
v_cvt_pk_bf16_f32 v204, v28, v29
v_sub_f32 v28, v38, v121
v_sub_f32 v29, v39, v121
v_exp_f32 v28, v28
v_exp_f32 v29, v29
s_nop 0
v_cvt_pk_bf16_f32 v205, v28, v29
v_sub_f32 v28, v40, v120
v_sub_f32 v29, v41, v120
v_exp_f32 v28, v28
v_exp_f32 v29, v29
s_nop 0
v_cvt_pk_bf16_f32 v198, v28, v29
v_sub_f32 v28, v42, v120
v_sub_f32 v29, v43, v120
v_exp_f32 v28, v28
v_exp_f32 v29, v29
s_nop 0
v_cvt_pk_bf16_f32 v199, v28, v29
v_sub_f32 v28, v44, v121
v_sub_f32 v29, v45, v121
v_exp_f32 v28, v28
v_exp_f32 v29, v29
s_nop 0
v_cvt_pk_bf16_f32 v206, v28, v29
v_sub_f32 v28, v46, v121
v_sub_f32 v29, v47, v121
v_exp_f32 v28, v28
v_exp_f32 v29, v29
s_nop 0
v_cvt_pk_bf16_f32 v207, v28, v29
v_sub_f32 v28, v48, v120
v_sub_f32 v29, v49, v120
v_exp_f32 v28, v28
v_exp_f32 v29, v29
s_nop 0
v_cvt_pk_bf16_f32 v212, v28, v29
v_sub_f32 v28, v50, v120
v_sub_f32 v29, v51, v120
v_exp_f32 v28, v28
v_exp_f32 v29, v29
s_nop 0
v_cvt_pk_bf16_f32 v213, v28, v29
v_sub_f32 v28, v52, v121
v_sub_f32 v29, v53, v121
v_exp_f32 v28, v28
v_exp_f32 v29, v29
s_nop 0
v_cvt_pk_bf16_f32 v220, v28, v29
v_sub_f32 v28, v54, v121
v_sub_f32 v29, v55, v121
v_exp_f32 v28, v28
v_exp_f32 v29, v29
s_nop 0
v_cvt_pk_bf16_f32 v221, v28, v29
v_sub_f32 v28, v56, v120
v_sub_f32 v29, v57, v120
v_exp_f32 v28, v28
v_exp_f32 v29, v29
s_nop 0
v_cvt_pk_bf16_f32 v214, v28, v29
v_sub_f32 v28, v58, v120
v_sub_f32 v29, v59, v120
v_exp_f32 v28, v28
v_exp_f32 v29, v29
s_nop 0
v_cvt_pk_bf16_f32 v215, v28, v29
v_sub_f32 v28, v60, v121
v_sub_f32 v29, v61, v121
v_exp_f32 v28, v28
v_exp_f32 v29, v29
s_nop 0
v_cvt_pk_bf16_f32 v222, v28, v29
v_sub_f32 v28, v62, v121
v_sub_f32 v29, v63, v121
v_exp_f32 v28, v28
v_exp_f32 v29, v29
s_nop 0
v_cvt_pk_bf16_f32 v223, v28, v29
s_getpc_b64 s[16:17]
.Ldr_g:
s_add_u32 s16, s16, .Ldr_b-.Ldr_g
s_addc_u32 s17, s17, 0
s_branch .Lscale2
.Ldr_b:
Cvvv 116,96,196,112
Mvvv 116,100,204
Mvvv 116,96,212
Mvvv 116,100,220
s_nop 7
s_branch .Ldr_r
.Lcf2:
s_mov_b32 s88, 1048576
.Lcf2_l:
s_sleep 4
buffer_load_dwordx4 v[16:19], v0, s[48:51], s83 offen sc1
s_waitcnt vmcnt(0)
v_xor_b32 v16, s74, v16
v_bitop3_b32 v16, v17, s75, v16 bitop3:0xbe
v_bitop3_b32 v16, v18, s74, v16 bitop3:0xbe
v_bitop3_b32 v16, v19, s75, v16 bitop3:0xbe
v_cmp_ne_u32 vcc, 0, v16
s_nop 0
s_and_b32 vcc_lo, vcc_lo, 0x700070
s_and_b32 vcc_hi, vcc_hi, 0x700070
s_cbranch_vccz .Lcf2_r
s_sub_u32 s88, s88, 1
s_cmp_lg_u32 s88, 0
s_cbranch_scc1 .Lcf2_l
s_branch .Lcf2_r
.Lcf3:
s_mov_b32 s88, 1048576
.Lcf3_l:
)"
R"(
s_sleep 4
buffer_load_dwordx4 v[16:19], v0, s[48:51], s83 offen sc1
s_waitcnt vmcnt(0)
v_xor_b32 v16, s74, v16
v_bitop3_b32 v16, v17, s75, v16 bitop3:0xbe
v_bitop3_b32 v16, v18, s74, v16 bitop3:0xbe
v_bitop3_b32 v16, v19, s75, v16 bitop3:0xbe
v_cmp_ne_u32 vcc, 0, v16
s_nop 0
s_cbranch_vccz .Lcf3_r
s_sub_u32 s88, s88, 1
s_cmp_lg_u32 s88, 0
s_cbranch_scc1 .Lcf3_l
s_branch .Lcf3_r
.Lgu:
s_mov_b32 s88, 1048576
.Lgu_l:
s_sleep 4
buffer_load_dwordx4 v[40:43], v11, s[48:51], s24 offen sc1
buffer_load_dwordx4 v[44:47], v11, s[48:51], s24 offen offset:1024 sc1
buffer_load_dwordx4 v[48:51], v11, s[48:51], s24 offen offset:2048 sc1
buffer_load_dwordx4 v[52:55], v11, s[48:51], s24 offen offset:3072 sc1
s_waitcnt vmcnt(0)
v_xor_b32 v40, s20, v40
v_xor_b32 v41, s21, v41
v_xor_b32 v42, s22, v42
v_xor_b32 v43, s23, v43
v_xor_b32 v44, s20, v44
v_xor_b32 v45, s21, v45
v_xor_b32 v46, s22, v46
v_xor_b32 v47, s23, v47
v_xor_b32 v48, s20, v48
v_xor_b32 v49, s21, v49
v_xor_b32 v50, s22, v50
v_xor_b32 v51, s23, v51
v_xor_b32 v52, s20, v52
v_xor_b32 v53, s21, v53
v_xor_b32 v54, s22, v54
v_xor_b32 v55, s23, v55
v_or3_b32 v56, v40, v41, v42
v_or3_b32 v56, v56, v43, v44
v_or3_b32 v56, v56, v45, v46
v_or3_b32 v56, v56, v47, v48
v_or3_b32 v56, v56, v49, v50
v_or3_b32 v56, v56, v51, v52
v_or3_b32 v56, v56, v53, v54
v_or_b32 v56, v56, v55
v_and_b32 v56, 0xff, v56
v_cmp_ne_u32 vcc, 0, v56
s_nop 0
s_and_b64 vcc, vcc, s[38:39]
s_cbranch_vccz .Lgu_r
s_sub_u32 s88, s88, 1
s_cmp_lg_u32 s88, 0
s_cbranch_scc1 .Lgu_l
s_branch .Lgu_r
.Lpf0:
s_mov_b32 s88, 1048576
.Lpf0_l:
s_sleep 1
buffer_load_dwordx2 v[60:61], v8, s[48:51], s33 offen sc1
s_waitcnt vmcnt(0)
v_xor_b32 v60, s74, v60
v_bitop3_b32 v60, v61, s75, v60 bitop3:0xbe
v_cmp_ne_u32 vcc, 0, v60
s_nop 0
s_and_b64 vcc, vcc, 0xf
s_cbranch_vccz .Lpf0_r
s_sub_u32 s88, s88, 1
s_cmp_lg_u32 s88, 0
s_cbranch_scc1 .Lpf0_l
s_branch .Lpf0_r
.Lpf1:
s_mov_b32 s88, 1048576
.Lpf1_l:
s_sleep 1
buffer_load_dwordx2 v[60:61], v8, s[48:51], s33 offen sc1
s_waitcnt vmcnt(0)
v_xor_b32 v60, s74, v60
v_bitop3_b32 v60, v61, s75, v60 bitop3:0xbe
v_cmp_ne_u32 vcc, 0, v60
s_nop 0
s_and_b64 vcc, vcc, 0xf0
s_cbranch_vccz .Lpf1_r
s_sub_u32 s88, s88, 1
s_cmp_lg_u32 s88, 0
s_cbranch_scc1 .Lpf1_l
s_branch .Lpf1_r
.Ly0:
s_mov_b32 s88, 1048576
.Ly0_l:
s_sleep 4
buffer_load_dwordx4 v[148:151], v6, s[48:51], s37 offen sc1
buffer_load_dwordx4 v[156:159], v6, s[48:51], s37 offen offset:1024 sc1
buffer_load_dwordx4 v[164:167], v6, s[48:51], s37 offen offset:2048 sc1
buffer_load_dwordx4 v[172:175], v6, s[48:51], s37 offen offset:3072 sc1
buffer_load_dwordx4 v[228:231], v6, s[48:51], s36 offen sc1
buffer_load_dwordx4 v[236:239], v6, s[48:51], s36 offen offset:1024 sc1
buffer_load_dwordx4 v[244:247], v6, s[48:51], s36 offen offset:2048 sc1
s_waitcnt vmcnt(0)
v_xor_b32 v148, s20, v148
v_xor_b32 v149, s21, v149
v_xor_b32 v150, s22, v150
v_xor_b32 v151, s23, v151
v_xor_b32 v156, s20, v156
v_xor_b32 v157, s21, v157
v_xor_b32 v158, s22, v158
v_xor_b32 v159, s23, v159
v_xor_b32 v164, s20, v164
v_xor_b32 v165, s21, v165
v_xor_b32 v166, s22, v166
v_xor_b32 v167, s23, v167
v_xor_b32 v172, s20, v172
v_xor_b32 v173, s21, v173
v_xor_b32 v174, s22, v174
v_xor_b32 v175, s23, v175
v_xor_b32 v228, s20, v228
v_xor_b32 v229, s21, v229
v_xor_b32 v230, s22, v230
v_xor_b32 v231, s23, v231
v_xor_b32 v236, s20, v236
v_xor_b32 v237, s21, v237
v_xor_b32 v238, s22, v238
v_xor_b32 v239, s23, v239
v_xor_b32 v244, s20, v244
v_xor_b32 v245, s21, v245
v_xor_b32 v246, s22, v246
v_xor_b32 v247, s23, v247
v_or3_b32 v252, v148, v149, v150
v_or3_b32 v252, v252, v151, v156
v_or3_b32 v252, v252, v157, v158
v_or3_b32 v252, v252, v159, v164
v_or3_b32 v252, v252, v165, v166
v_or3_b32 v252, v252, v167, v172
v_or3_b32 v252, v252, v173, v174
v_or3_b32 v252, v252, v175, v228
v_or3_b32 v252, v252, v229, v230
v_or3_b32 v252, v252, v231, v236
v_or3_b32 v252, v252, v237, v238
v_or3_b32 v252, v252, v239, v244
v_or3_b32 v252, v252, v245, v246
v_or_b32 v252, v252, v247
v_and_b32 v252, 0xff, v252
v_cmp_ne_u32 vcc, 0, v252
s_nop 0
s_cbranch_vccz .Ly0_r
s_sub_u32 s88, s88, 1
s_cmp_lg_u32 s88, 0
s_cbranch_scc1 .Ly0_l
s_branch .Ly0_r
.Ly1:
s_mov_b32 s88, 1048576
.Ly1_l:
s_sleep 4
buffer_load_dwordx4 v[148:151], v6, s[48:51], s37 offen sc1
buffer_load_dwordx4 v[156:159], v6, s[48:51], s37 offen offset:1024 sc1
buffer_load_dwordx4 v[164:167], v6, s[48:51], s37 offen offset:2048 sc1
buffer_load_dwordx4 v[172:175], v6, s[48:51], s37 offen offset:3072 sc1
buffer_load_dwordx4 v[228:231], v6, s[48:51], s36 offen sc1
buffer_load_dwordx4 v[236:239], v6, s[48:51], s36 offen offset:1024 sc1
buffer_load_dwordx4 v[244:247], v6, s[48:51], s36 offen offset:2048 sc1
s_waitcnt vmcnt(0)
v_xor_b32 v148, s20, v148
v_xor_b32 v149, s21, v149
v_xor_b32 v150, s22, v150
v_xor_b32 v151, s23, v151
v_xor_b32 v156, s20, v156
v_xor_b32 v157, s21, v157
v_xor_b32 v158, s22, v158
v_xor_b32 v159, s23, v159
v_xor_b32 v164, s20, v164
v_xor_b32 v165, s21, v165
v_xor_b32 v166, s22, v166
v_xor_b32 v167, s23, v167
v_xor_b32 v172, s20, v172
v_xor_b32 v173, s21, v173
v_xor_b32 v174, s22, v174
v_xor_b32 v175, s23, v175
v_xor_b32 v228, s20, v228
v_xor_b32 v229, s21, v229
v_xor_b32 v230, s22, v230
v_xor_b32 v231, s23, v231
v_xor_b32 v236, s20, v236
v_xor_b32 v237, s21, v237
v_xor_b32 v238, s22, v238
v_xor_b32 v239, s23, v239
v_xor_b32 v244, s20, v244
v_xor_b32 v245, s21, v245
v_xor_b32 v246, s22, v246
v_xor_b32 v247, s23, v247
v_or3_b32 v252, v148, v149, v150
v_or3_b32 v252, v252, v151, v156
v_or3_b32 v252, v252, v157, v158
v_or3_b32 v252, v252, v159, v164
v_or3_b32 v252, v252, v165, v166
v_or3_b32 v252, v252, v167, v172
v_or3_b32 v252, v252, v173, v174
v_or3_b32 v252, v252, v175, v228
v_or3_b32 v252, v252, v229, v230
v_or3_b32 v252, v252, v231, v236
v_or3_b32 v252, v252, v237, v238
v_or3_b32 v252, v252, v239, v244
v_or3_b32 v252, v252, v245, v246
v_or_b32 v252, v252, v247
v_and_b32 v252, 0xff, v252
v_cmp_ne_u32 vcc, 0, v252
s_nop 0
s_cbranch_vccz .Ly1_r
s_sub_u32 s88, s88, 1
s_cmp_lg_u32 s88, 0
s_cbranch_scc1 .Ly1_l
s_branch .Ly1_r
.Lpf2:
s_mov_b32 s88, 1048576
.Lpf2_l:
s_sleep 1
buffer_load_dwordx2 v[30:31], v8, s[48:51], s33 offen sc1
s_waitcnt vmcnt(0)
v_xor_b32 v30, s74, v30
v_bitop3_b32 v30, v31, s75, v30 bitop3:0xbe
v_cmp_ne_u32 vcc, 0, v30
s_nop 0
s_and_b64 vcc, vcc, 0xf00
s_cbranch_vccz .Lpf2_r
s_sub_u32 s88, s88, 1
s_cmp_lg_u32 s88, 0
s_cbranch_scc1 .Lpf2_l
s_branch .Lpf2_r
.Lpc3:
s_mov_b32 s88, 1048576
.Lpc3_l:
s_sleep 1
buffer_load_dwordx4 v[148:151], v0, s[48:51], s92 offen sc1
buffer_load_dwordx4 v[156:159], v0, s[48:51], s92 offen offset:1024 sc1
buffer_load_dwordx4 v[164:167], v0, s[48:51], s93 offen sc1
buffer_load_dwordx4 v[172:175], v0, s[48:51], s93 offen offset:1024 sc1
buffer_load_dwordx4 v[228:231], v0, s[48:51], s94 offen sc1
buffer_load_dwordx4 v[236:239], v0, s[48:51], s94 offen offset:1024 sc1
s_waitcnt vmcnt(0)
v_xor_b32 v148, s20, v148
v_xor_b32 v149, s21, v149
v_xor_b32 v150, s22, v150
v_xor_b32 v151, s23, v151
v_xor_b32 v156, s20, v156
v_xor_b32 v157, s21, v157
v_xor_b32 v158, s22, v158
v_xor_b32 v159, s23, v159
v_xor_b32 v164, s20, v164
v_xor_b32 v165, s21, v165
v_xor_b32 v166, s22, v166
v_xor_b32 v167, s23, v167
v_xor_b32 v172, s20, v172
v_xor_b32 v173, s21, v173
v_xor_b32 v174, s22, v174
v_xor_b32 v175, s23, v175
v_xor_b32 v228, s20, v228
v_xor_b32 v229, s21, v229
v_xor_b32 v230, s22, v230
v_xor_b32 v231, s23, v231
v_xor_b32 v236, s20, v236
v_xor_b32 v237, s21, v237
v_xor_b32 v238, s22, v238
v_xor_b32 v239, s23, v239
v_or3_b32 v9, v148, v149, v150
v_or3_b32 v9, v9, v151, v156
v_or3_b32 v9, v9, v157, v158
v_or3_b32 v9, v9, v159, v164
v_or3_b32 v9, v9, v165, v166
v_or3_b32 v9, v9, v167, v172
v_or3_b32 v9, v9, v173, v174
v_or3_b32 v9, v9, v175, v228
v_or3_b32 v9, v9, v229, v230
v_or3_b32 v9, v9, v231, v236
v_or3_b32 v9, v9, v237, v238
v_or_b32 v9, v9, v239
v_and_b32 v9, 0xff, v9
v_cmp_ne_u32 vcc, 0, v9
s_nop 0
s_cbranch_vccz .Lpc3_r
s_sub_u32 s88, s88, 1
s_cmp_lg_u32 s88, 0
s_cbranch_scc1 .Lpc3_l
s_branch .Lpc3_r
s_endpgm
s_nop 0
s_nop 0
s_nop 0
s_nop 0
s_nop 0
s_nop 0
s_nop 0
s_nop 0
s_nop 0
s_nop 0
s_nop 0
s_nop 0
s_nop 0
s_nop 0
s_nop 0
s_nop 0
s_nop 0
s_nop 0
s_nop 0
s_nop 0
s_nop 0
s_nop 0
s_nop 0
s_nop 0
s_nop 0
s_nop 0
s_nop 0
s_nop 0
s_nop 0
s_nop 0
s_nop 0
s_nop 0
s_nop 0
s_nop 0
s_nop 0
s_nop 0
s_nop 0
s_nop 0
s_nop 0
s_nop 0
s_nop 0
s_nop 0
s_nop 0
s_nop 0
s_nop 0
s_nop 0
s_nop 0
s_nop 0
s_nop 0
s_nop 0
s_nop 0
s_nop 0
s_nop 0
s_nop 0
s_nop 0
s_nop 0
s_nop 0
s_nop 0
s_nop 0
s_nop 0
s_nop 0
s_nop 0
s_nop 0
s_nop 0
s_nop 0
s_nop 0
s_nop 0
s_nop 0
s_nop 0
s_nop 0
s_nop 0
s_nop 0
s_nop 0
s_nop 0
s_nop 0
s_nop 0
s_nop 0
s_nop 0
s_nop 0
s_nop 0
s_nop 0
s_nop 0
s_nop 0
s_nop 0
s_nop 0
s_nop 0
s_nop 0
s_nop 0
s_nop 0
s_nop 0
s_nop 0
s_nop 0
s_nop 0
s_nop 0
s_nop 0
s_nop 0
s_nop 0
s_nop 0
s_nop 0
s_nop 0
s_nop 0
s_nop 0
s_nop 0
s_nop 0
s_nop 0
s_nop 0
s_nop 0
s_nop 0
s_nop 0
s_nop 0
s_nop 0
s_nop 0
s_nop 0
s_nop 0
s_nop 0
s_nop 0
s_nop 0
s_nop 0
s_nop 0
s_nop 0
s_nop 0
s_nop 0
s_nop 0
s_nop 0
s_nop 0
s_nop 0
s_nop 0
s_nop 0
s_nop 0
s_nop 0
s_nop 0
s_nop 0
s_nop 0
s_nop 0
s_nop 0
s_nop 0
s_nop 0
s_nop 0
s_nop 0
s_nop 0
s_nop 0
s_nop 0
s_nop 0
s_nop 0
s_nop 0
s_nop 0
s_nop 0
s_nop 0
s_nop 0
s_nop 0
s_nop 0
s_nop 0
s_nop 0
s_nop 0
s_nop 0
s_nop 0
s_nop 0
s_nop 0
s_nop 0
s_nop 0
s_nop 0
s_nop 0
s_nop 0
s_nop 0
s_nop 0
s_nop 0
s_nop 0
s_nop 0
s_nop 0
s_nop 0
s_nop 0
s_nop 0
s_nop 0
s_nop 0
s_nop 0
s_nop 0
s_nop 0
s_nop 0
s_nop 0
s_nop 0
s_nop 0
s_nop 0
s_nop 0
s_nop 0
s_nop 0
s_nop 0
s_nop 0
s_nop 0
s_nop 0
s_nop 0
s_nop 0
s_nop 0
s_nop 0
s_nop 0
s_nop 0
s_nop 0
s_nop 0
s_nop 0
s_nop 0
s_nop 0
s_nop 0
s_nop 0
s_nop 0
s_nop 0
s_nop 0
s_nop 0
s_nop 0
s_nop 0
s_nop 0
s_nop 0
s_nop 0
s_nop 0
s_nop 0
s_nop 0
s_nop 0
s_nop 0
s_nop 0
s_nop 0
s_nop 0
s_nop 0
s_nop 0
s_nop 0
s_nop 0
s_nop 0
s_nop 0
s_nop 0
s_nop 0
s_nop 0
s_nop 0
s_nop 0
s_nop 0
s_nop 0
s_nop 0
s_nop 0
s_nop 0
s_nop 0
s_nop 0
s_nop 0
s_nop 0
s_nop 0
s_nop 0
s_nop 0
s_nop 0
s_nop 0
s_nop 0
s_nop 0
s_nop 0
s_nop 0
s_nop 0
s_nop 0
s_nop 0
s_nop 0
s_nop 0
s_nop 0
s_nop 0
s_nop 0
s_nop 0
s_nop 0
s_nop 0
s_nop 0
)"

        :: [ka] "s"(__builtin_amdgcn_kernarg_segment_ptr()), [b] "s"(blockIdx.x), [w] "s"(wave)
        : "memory", "scc", "vcc", "v0", "v1", "v2", "v3", "v4", "v5", "v6", "v7", "v8", "v9", "v10", "v11", "v12", "v13", "v14", "v15", "v16", "v17", "v18", "v19", "v20", "v21", "v22", "v23", "v24", "v25", "v26", "v27", "v28", "v29", "v30", "v31", "v32", "v33", "v34", "v35", "v36", "v37", "v38", "v39", "v40", "v41", "v42", "v43", "v44", "v45", "v46", "v47", "v48", "v49", "v50", "v51", "v52", "v53", "v54", "v55", "v56", "v57", "v58", "v59", "v60", "v61", "v62", "v63", "v64", "v65", "v66", "v67", "v68", "v69", "v70", "v71", "v72", "v73", "v74", "v75", "v76", "v77", "v78", "v79", "v80", "v81", "v82", "v83", "v84", "v85", "v86", "v87", "v88", "v89", "v90", "v91", "v92", "v93", "v94", "v95", "v96", "v97", "v98", "v99", "v100", "v101", "v102", "v103", "v104", "v105", "v106", "v107", "v108", "v109", "v110", "v111", "v112", "v113", "v114", "v115", "v116", "v117", "v118", "v119", "v120", "v121", "v122", "v123", "v124", "v125", "v126", "v127", "v128", "v129", "v130", "v131", "v132", "v133", "v134", "v135", "v136", "v137", "v138", "v139", "v140", "v141", "v142", "v143", "v144", "v145", "v146", "v147", "v148", "v149", "v150", "v151", "v152", "v153", "v154", "v155", "v156", "v157", "v158", "v159", "v160", "v161", "v162", "v163", "v164", "v165", "v166", "v167", "v168", "v169", "v170", "v171", "v172", "v173", "v174", "v175", "v176", "v177", "v178", "v179", "v180", "v181", "v182", "v183", "v184", "v185", "v186", "v187", "v188", "v189", "v190", "v191", "v192", "v193", "v194", "v195", "v196", "v197", "v198", "v199", "v200", "v201", "v202", "v203", "v204", "v205", "v206", "v207", "v208", "v209", "v210", "v211", "v212", "v213", "v214", "v215", "v216", "v217", "v218", "v219", "v220", "v221", "v222", "v223", "v224", "v225", "v226", "v227", "v228", "v229", "v230", "v231", "v232", "v233", "v234", "v235", "v236", "v237", "v238", "v239", "v240", "v241", "v242", "v243", "v244", "v245", "v246", "v247", "v248", "v249", "v250", "v251", "v252", "v253", "v254", "v255", "a0", "a1", "a2", "a3", "a4", "a5", "a6", "a7", "a8", "a9", "a10", "a11", "a12", "a13", "a14", "a15", "a16", "a17", "a18", "a19", "a20", "a21", "a22", "a23", "a24", "a25", "a26", "a27", "a28", "a29", "a30", "a31", "a32", "a33", "a34", "a35", "a36", "a37", "a38", "a39", "a40", "a41", "a42", "a43", "a44", "a45", "a46", "a47", "a48", "a49", "a50", "a51", "a52", "a53", "a54", "a55", "a56", "a57", "a58", "a59", "a60", "a61", "a62", "a63", "a64", "a65", "a66", "a67", "a68", "a69", "a70", "a71", "a72", "a73", "a74", "a75", "a76", "a77", "a78", "a79", "a80", "a81", "a82", "a83", "a84", "a85", "a86", "a87", "a88", "a89", "a90", "a91", "a92", "a93", "a94", "a95", "a96", "a97", "a98", "a99", "a100", "a101", "a102", "a103", "a104", "a105", "a106", "a107", "a108", "a109", "a110", "a111", "a112", "a113", "a114", "a115", "a116", "a117", "a118", "a119", "a120", "a121", "a122", "a123", "a124", "a125", "a126", "a127", "a128", "a129", "a130", "a131", "a132", "a133", "a134", "a135", "a136", "a137", "a138", "a139", "a140", "a141", "a142", "a143", "a144", "a145", "a146", "a147", "a148", "a149", "a150", "a151", "a152", "a153", "a154", "a155", "a156", "a157", "a158", "a159", "a160", "a161", "a162", "a163", "a164", "a165", "a166", "a167", "a168", "a169", "a170", "a171", "a172", "a173", "a174", "a175", "a176", "a177", "a178", "a179", "a180", "a181", "a182", "a183", "a184", "a185", "a186", "a187", "a188", "a189", "a190", "a191", "a192", "a193", "a194", "a195", "a196", "a197", "a198", "a199", "a200", "a201", "a202", "a203", "a204", "a205", "a206", "a207", "a208", "a209", "a210", "a211", "a212", "a213", "a214", "a215", "a216", "a217", "a218", "a219", "a220", "a221", "a222", "a223", "a224", "a225", "a226", "a227", "a228", "a229", "a230", "a231", "a232", "a233", "a234", "a235", "a236", "a237", "a238", "a239", "a240", "a241", "a242", "a243", "a244", "a245", "a246", "a247", "a248", "a249", "a250", "a251", "a252", "a253", "a254", "a255", "s4", "s5", "s6", "s7", "s8", "s9", "s10", "s11", "s12", "s13", "s14", "s15", "s16", "s17", "s18", "s19", "s20", "s21", "s22", "s23", "s24", "s25", "s26", "s27", "s28", "s29", "s30", "s31", "s32", "s33", "s34", "s35", "s36", "s37", "s38", "s39", "s40", "s41", "s42", "s43", "s44", "s45", "s46", "s47", "s48", "s49", "s50", "s51", "s52", "s53", "s54", "s55", "s56", "s57", "s58", "s59", "s60", "s61", "s62", "s63", "s64", "s65", "s66", "s67", "s68", "s69", "s70", "s71", "s72", "s73", "s74", "s75", "s76", "s77", "s78", "s79", "s80", "s81", "s82", "s83", "s84", "s85", "s86", "s87", "s88", "s89", "s90", "s91", "s92", "s93", "s94", "s95", "s96", "s97", "s98", "s99");
}
extern "C" void kernel_launch(void* const* d_in, const int* in_sizes, int n_in, void* d_out, int out_size, void* d_ws, size_t ws_size, hipStream_t stream) {
    (void)n_in; (void)ws_size;
    (void)hipFuncSetAttribute((const void*)k_all, hipFuncAttributeMaxDynamicSharedMemorySize, 163840);
#define IN(i) d_in[i], 2u * (unsigned)in_sizes[i], 0x20000u
    k_all<<<256, 256, 163840, stream>>>(IN(0), IN(1), IN(2), IN(3), IN(4), IN(5), IN(7), IN(8), IN(9), IN(10), d_ws, 109051904u, 0x20000u, d_out, 2u * (unsigned)out_size, 0x20000u);
}
```

```cpp
#pragma clang diagnostic ignored "-Winline-asm"
#include <hip/hip_runtime.h>
#include <stdint.h>
#define SRD(n) const void* p##n, unsigned n##_bytes, unsigned n##_fmt
__global__ __launch_bounds__(256, 1) void k_all(SRD(x), SRD(ckv), SRD(wq), SRD(wuk), SRD(wuv), SRD(wo), SRD(wr), SRD(wg), SRD(wu), SRD(wd), SRD(ws), SRD(out)) {
    const int wave = __builtin_amdgcn_readfirstlane(threadIdx.x >> 6);
    asm volatile(
R"(
.macro Mvvv d,a,b
v_mfma_f32_16x16x32_bf16 v[\d:\d+3], v[\a:\a+3], v[\b:\b+3], v[\d:\d+3]
.endm
.macro Zvvv d,a,b
v_mfma_f32_16x16x32_bf16 v[\d:\d+3], v[\a:\a+3], v[\b:\b+3], 0
.endm
.macro Cvvv d,a,b,c
v_mfma_f32_16x16x32_bf16 v[\d:\d+3], v[\a:\a+3], v[\b:\b+3], v[\c:\c+3]
.endm
.macro Mvav d,a,b
v_mfma_f32_16x16x32_bf16 v[\d:\d+3], a[\a:\a+3], v[\b:\b+3], v[\d:\d+3]
.endm
.macro Zvav d,a,b
v_mfma_f32_16x16x32_bf16 v[\d:\d+3], a[\a:\a+3], v[\b:\b+3], 0
.endm
.macro Cvav d,a,b,c
v_mfma_f32_16x16x32_bf16 v[\d:\d+3], a[\a:\a+3], v[\b:\b+3], v[\c:\c+3]
.endm
.macro Mvaa d,a,b
v_mfma_f32_16x16x32_bf16 v[\d:\d+3], a[\a:\a+3], a[\b:\b+3], v[\d:\d+3]
.endm
.macro Zvaa d,a,b
v_mfma_f32_16x16x32_bf16 v[\d:\d+3], a[\a:\a+3], a[\b:\b+3], 0
.endm
.macro Mavv d,a,b
v_mfma_f32_16x16x32_bf16 a[\d:\d+3], v[\a:\a+3], v[\b:\b+3], a[\d:\d+3]
.endm
.macro Zavv d,a,b
v_mfma_f32_16x16x32_bf16 a[\d:\d+3], v[\a:\a+3], v[\b:\b+3], 0
.endm
.macro Maav d,a,b
v_mfma_f32_16x16x32_bf16 a[\d:\d+3], a[\a:\a+3], v[\b:\b+3], a[\d:\d+3]
.endm
.Lstart:
s_mov_b32 s56, %[b]
s_mov_b32 s57, %[w]
s_load_dwordx16 s[8:23], %[ka], 0x0
s_load_dwordx16 s[24:39], %[ka], 0x40
s_load_dwordx16 s[40:55], %[ka], 0x80
v_mbcnt_lo_u32_b32 v16, -1, 0
v_mbcnt_hi_u32_b32 v16, -1, v16
v_lshlrev_b32 v0, 4, v16
s_lshr_b32 s60, s56, 3
s_lshl2_add_u32 s58, s60, s57
s_lshl_b32 s58, s58, 10
s_mov_b32 s59, .Lhot_end-.Lstart
s_min_u32 s58, s58, s59
s_getpc_b64 s[62:63]
.Lcp1:
s_sub_u32 s62, s62, .Lcp1-.Lstart
s_subb_u32 s63, s63, 0
s_add_u32 s62, s62, s58
s_addc_u32 s63, s63, 0
s_and_b32 s62, s62, -16
global_load_dwordx4 v[28:31], v0, s[62:63]
s_and_b32 s61, s60, 7
s_and_b32 s65, s56, 7
s_lshl_b32 s65, s65, 2
s_lshr_b32 s65, 0x73625140, s65
s_and_b32 s65, s65, 7
s_lshr_b32 s58, s60, 3
s_lshl2_add_u32 s64, s65, s58
v_lshrrev_b32 v17, 3, v16
v_and_b32 v18, 7, v16
v_and_b32 v19, 15, v16
v_lshrrev_b32 v20, 4, v16
s_and_b32 s59, s57, 1
s_lshl_b32 s58, s59, 6
v_xor_b32 v21, v18, v20
v_lshlrev_b32 v21, 4, v21
v_lshl_add_u32 v1, v17, 10, v21
v_xor_b32 v1, s58, v1
s_lshl_b32 s58, s59, 5
v_bfe_u32 v22, v16, 2, 3
v_lshrrev_b32 v23, 5, v16
v_lshlrev_b32 v23, 6, v23
v_lshl_add_u32 v22, v22, 13, v23
v_and_b32 v23, 3, v16
v_lshl_add_u32 v3, v23, 4, v22
v_xor_b32 v3, s58, v3
s_lshr_b32 s58, s57, 1
s_lshl_b32 s66, s58, 14
s_lshl_b32 s58, s59, 10
s_add_u32 s66, s66, s58
s_lshl_b32 s67, s61, 18
s_lshl3_add_u32 s67, s66, s67
s_lshl_b32 s68, s57, 16
s_lshl_b32 s58, s64, 8
s_add_u32 s68, s68, s58
s_lshl_b32 s69, s57, 13
s_lshl_b32 s70, s57, 12
s_lshl_b32 s71, s57, 11
s_add_u32 m0, s70, 98304
s_add_u32 s72, s68, 0
s_waitcnt lgkmcnt(0)
buffer_load_dwordx4 v3, s[20:23], s72 offen lds
s_add_u32 m0, s70, 99200
s_add_u32 s72, s68, 0
buffer_load_dwordx4 v3, s[20:23], s72 offen offset:128 lds
s_add_u32 m0, s70, 100352
s_add_u32 s72, s68, 0
buffer_load_dwordx4 v3, s[24:27], s72 offen lds
s_add_u32 m0, s70, 101248
s_add_u32 s72, s68, 0
buffer_load_dwordx4 v3, s[24:27], s72 offen offset:128 lds
s_add_u32 m0, s66, 0
s_add_u32 s72, s67, 0
buffer_load_dwordx4 v1, s[12:15], s72 offen lds
s_add_u32 m0, s66, 2048
s_add_u32 s72, s67, 16384
buffer_load_dwordx4 v1, s[12:15], s72 offen lds
s_add_u32 m0, s66, 4096
s_add_u32 s72, s67, 32768
buffer_load_dwordx4 v1, s[12:15], s72 offen lds
s_add_u32 m0, s66, 6144
s_add_u32 s72, s67, 49152
buffer_load_dwordx4 v1, s[12:15], s72 offen lds
s_add_u32 m0, s66, 8192
s_add_u32 s72, s67, 65536
buffer_load_dwordx4 v1, s[12:15], s72 offen lds
s_add_u32 m0, s66, 10240
s_add_u32 s72, s67, 81920
buffer_load_dwordx4 v1, s[12:15], s72 offen lds
s_add_u32 m0, s66, 12288
s_add_u32 s72, s67, 98304
buffer_load_dwordx4 v1, s[12:15], s72 offen lds
s_add_u32 m0, s66, 14336
s_add_u32 s72, s67, 114688
buffer_load_dwordx4 v1, s[12:15], s72 offen lds
s_add_u32 m0, s70, 114688
s_add_u32 s72, s68, 262144
buffer_load_dwordx4 v3, s[20:23], s72 offen lds
s_add_u32 m0, s70, 115584
s_add_u32 s72, s68, 262144
buffer_load_dwordx4 v3, s[20:23], s72 offen offset:128 lds
s_add_u32 m0, s70, 116736
s_add_u32 s72, s68, 262144
buffer_load_dwordx4 v3, s[24:27], s72 offen lds
s_add_u32 m0, s70, 117632
s_add_u32 s72, s68, 262144
buffer_load_dwordx4 v3, s[24:27], s72 offen offset:128 lds
s_load_dwordx2 s[74:75], s[48:49], 0x0
s_add_u32 m0, s70, 131072
s_add_u32 s72, s68, 524288
buffer_load_dwordx4 v3, s[20:23], s72 offen lds
s_add_u32 m0, s70, 131968
s_add_u32 s72, s68, 524288
buffer_load_dwordx4 v3, s[20:23], s72 offen offset:128 lds
s_add_u32 m0, s70, 133120
s_add_u32 s72, s68, 524288
buffer_load_dwordx4 v3, s[24:27], s72 offen lds
s_add_u32 m0, s70, 134016
s_add_u32 s72, s68, 524288
buffer_load_dwordx4 v3, s[24:27], s72 offen offset:128 lds
s_add_u32 m0, s70, 147456
s_add_u32 s72, s68, 786432
buffer_load_dwordx4 v3, s[20:23], s72 offen lds
s_add_u32 m0, s70, 148352
s_add_u32 s72, s68, 786432
buffer_load_dwordx4 v3, s[20:23], s72 offen offset:128 lds
s_add_u32 m0, s70, 149504
s_add_u32 s72, s68, 786432
buffer_load_dwordx4 v3, s[24:27], s72 offen lds
s_add_u32 m0, s70, 150400
s_add_u32 s72, s68, 786432
buffer_load_dwordx4 v3, s[24:27], s72 offen offset:128 lds
v_lshl_add_u32 v21, v20, 3, v18
v_lshlrev_b32 v21, 4, v21
v_and_b32 v22, 1, v17
v_lshl_add_u32 v79, v22, 11, v21
v_mov_b32 v84, v21
s_lshl2_add_u32 s58, s56, s57
s_lshl_b32 s73, s58, 12
s_lshl2_add_u32 s76, s60, s57
s_lshl3_add_u32 s76, s76, s65
s_lshl_b32 s76, s76, 10
s_add_u32 s72, s76, 1048576
buffer_load_dwordx4 v[80:83], v0, s[20:23], s72 offen
s_add_u32 s72, s76, 1048576
buffer_load_dwordx4 v[80:83], v0, s[24:27], s72 offen
v_mul_u32_u24 v77, 2176, v22
v_add_u32 v77, v77, v21
s_mul_i32 s77, s58, 4352
s_add_u32 s77, s77, 4194304
s_lshl4_add_u32 s78, s58, 4096
s_lshl_b32 s58, s58, 1
s_sub_u32 s58, 0x7ff, s58
v_sub_u32 v78, s58, v22
s_add_u32 s79, s70, 4096
v_cmp_eq_u32 vcc, 0, v16
v_mov_b32 v23, 0x80000000
s_nop 0
v_cndmask_b32 v13, v23, 0, vcc
v_lshrrev_b32 v21, 1, v19
v_xor_b32 v21, v21, v20
v_lshlrev_b32 v21, 4, v21
v_lshl_add_u32 v21, v19, 7, v21
s_lshr_b32 s58, s57, 1
s_lshl_b32 s59, s58, 14
v_add_u32 v5, s59, v21
v_xor_b32 v6, 64, v5
v_add_u32 v7, 0x10000, v5
v_add_u32 v8, 0x10000, v6
s_add_u32 s59, s69, 0x10000
v_add_u32 v24, s59, v21
v_xor_b32 v25, 64, v24
v_lshrrev_b32 v21, 2, v19
v_lshlrev_b32 v21, 6, v21
v_and_b32 v22, 1, v20
v_lshl_add_u32 v21, v22, 5, v21
v_and_b32 v22, 3, v19
v_lshl_add_u32 v21, v22, 3, v21
v_add_u32 v21, 0x18000, v21
v_lshl_add_u32 v26, v20, 11, v21
v_xor_b32 v27, 32, v26
v_lshl_add_u32 v21, v20, 12, v21
s_and_b32 s59, s57, 1
s_lshl_b32 s72, s59, 10
v_add_u32 v9, s72, v21
v_xor_b32 v10, 32, v9
s_lshl_b32 s80, s64, 4
s_lshl1_add_u32 s80, s61, s80
s_add_u32 s80, s80, s58
s_mul_i32 s80, s80, 69632
s_lshl_b32 s81, s59, 13
s_add_u32 s81, s81, s80
s_add_u32 s81, s81, 16793600
s_lshl_b32 s59, s59, 11
s_add_u32 s80, s80, s59
s_add_u32 s80, s80, 16777216
s_lshl2_add_u32 s58, s61, s57
s_mul_i32 s82, s58, 69632
s_sub_u32 s83, 8581120, s82
s_add_u32 s82, s82, 4194304
v_xor_b32 v21, v18, v20
v_lshlrev_b32 v21, 4, v21
v_mul_u32_u24 v22, 2176, v17
v_add_u32 v11, v22, v21
v_xor_b32 v21, 64, v21
v_add_u32 v22, 17408, v22
v_add_u32 v12, v22, v21
s_lshl_b32 s84, s64, 5
s_add_u32 s58, s58, s84
s_lshl3_add_u32 s85, s58, 24576
s_lshl3_add_u32 s84, s84, 24576
s_waitcnt lgkmcnt(0)
s_add_u32 s74, s74, 0x7f4a7c15
s_addc_u32 s75, s75, 0x9e3779b9
s_waitcnt vmcnt(19)
s_barrier
ds_read_b128 v[96:99], v5 offset:0
ds_read_b64_tr_b16 v[128:129], v9 offset:0
ds_read_b64_tr_b16 v[130:131], v9 offset:256
ds_read_b64_tr_b16 v[136:137], v10 offset:0
ds_read_b64_tr_b16 v[138:139], v10 offset:256
ds_read_b64_tr_b16 v[144:145], v9 offset:512
ds_read_b64_tr_b16 v[146:147], v9 offset:768
ds_read_b64_tr_b16 v[152:153], v10 offset:512
ds_read_b64_tr_b16 v[154:155], v10 offset:768
ds_read_b64_tr_b16 v[160:161], v9 offset:2048
ds_read_b64_tr_b16 v[162:163], v9 offset:2304
s_waitcnt lgkmcnt(8)
Zavv 0,128,96
s_waitcnt lgkmcnt(6)
ds_read_b64_tr_b16 v[168:169], v10 offset:2048
ds_read_b64_tr_b16 v[170:171], v10 offset:2304
Zavv 4,136,96
s_waitcnt lgkmcnt(6)
ds_read_b64_tr_b16 v[176:177], v9 offset:2560
ds_read_b64_tr_b16 v[178:179], v9 offset:2816
Zavv 8,144,96
s_waitcnt lgkmcnt(6)
ds_read_b64_tr_b16 v[184:185], v10 offset:2560
ds_read_b64_tr_b16 v[186:187], v10 offset:2816
Zavv 12,152,96
s_waitcnt lgkmcnt(6)
ds_read_b128 v[104:107], v5 offset:2048
Zavv 16,96,160
s_waitcnt lgkmcnt(5)
ds_read_b128 v[112:115], v5 offset:4096
Zavv 20,96,168
s_waitcnt lgkmcnt(4)
s_add_u32 m0, s66, 32768
s_add_u32 s72, s67, 128
buffer_load_dwordx4 v1, s[12:15], s72 offen lds
Zavv 24,96,176
s_waitcnt lgkmcnt(2)
Zavv 28,96,184
s_waitcnt lgkmcnt(1)
s_add_u32 m0, s66, 34816
s_add_u32 s72, s67, 16512
buffer_load_dwordx4 v1, s[12:15], s72 offen lds
Zavv 32,128,104
Zavv 36,136,104
s_add_u32 m0, s66, 36864
s_add_u32 s72, s67, 32896
buffer_load_dwordx4 v1, s[12:15], s72 offen lds
Zavv 40,144,104
Zavv 44,152,104
s_add_u32 m0, s66, 38912
s_add_u32 s72, s67, 49280
buffer_load_dwordx4 v1, s[12:15], s72 offen lds
Zavv 48,104,160
s_waitcnt vmcnt(18)
Zavv 52,104,168
s_barrier
Zavv 56,104,176
s_add_u32 m0, s66, 40960
s_add_u32 s72, s67, 65664
ds_read_b128 v[120:123], v5 offset:6144
buffer_load_dwordx4 v1, s[12:15], s72 offen lds
Zavv 60,104,184
s_waitcnt lgkmcnt(1)
Zavv 64,128,112
s_add_u32 m0, s66, 43008
s_add_u32 s72, s67, 82048
ds_read_b128 v[96:99], v5 offset:8192
buffer_load_dwordx4 v1, s[12:15], s72 offen lds
Zavv 68,136,112
Zavv 72,144,112
s_add_u32 m0, s66, 45056
s_add_u32 s72, s67, 98432
buffer_load_dwordx4 v1, s[12:15], s72 offen lds
Zavv 76,152,112
Zavv 80,112,160
s_add_u32 m0, s66, 47104
s_add_u32 s72, s67, 114816
buffer_load_dwordx4 v1, s[12:15], s72 offen lds
Zavv 84,112,168
Zavv 88,112,176
buffer_load_dwordx4 v[32:35], v79, s[8:11], s73 offen
Zavv 92,112,184
s_waitcnt lgkmcnt(1)
Zavv 96,128,120
ds_read_b128 v[104:107], v5 offset:10240
buffer_load_dwordx4 v[36:39], v79, s[8:11], s73 offen offset:512
Zavv 100,136,120
s_waitcnt vmcnt(20)
Zavv 104,144,120
s_barrier
Zavv 108,152,120
ds_read_b64_tr_b16 v[132:133], v9 offset:16384
buffer_load_dwordx4 v[40:43], v79, s[8:11], s73 offen offset:1024
Zavv 112,120,160
ds_read_b64_tr_b16 v[134:135], v9 offset:16640
Zavv 116,120,168
ds_read_b64_tr_b16 v[140:141], v10 offset:16384
buffer_load_dwordx4 v[44:47], v79, s[8:11], s73 offen offset:1536
Zavv 120,120,176
s_waitcnt lgkmcnt(4)
ds_read_b64_tr_b16 v[142:143], v10 offset:16640
Zavv 124,120,184
ds_read_b64_tr_b16 v[148:149], v9 offset:16896
buffer_load_dwordx4 v[48:51], v84, s[32:35], 0 offen
Zavv 128,128,96
ds_read_b128 v[112:115], v5 offset:12288
Zavv 132,136,96
ds_read_b64_tr_b16 v[150:151], v9 offset:17152
buffer_load_dwordx4 v[52:55], v84, s[32:35], 0 offen offset:512
Zavv 136,144,96
ds_read_b64_tr_b16 v[156:157], v10 offset:16896
Zavv 140,152,96
ds_read_b64_tr_b16 v[158:159], v10 offset:17152
buffer_load_dwordx4 v[56:59], v84, s[32:35], 0 offen offset:1024
Zavv 144,96,160
ds_read_b64_tr_b16 v[164:165], v9 offset:18432
Zavv 148,96,168
ds_read_b64_tr_b16 v[166:167], v9 offset:18688
buffer_load_dwordx4 v[60:63], v84, s[32:35], 0 offen offset:1536
Zavv 152,96,176
ds_read_b64_tr_b16 v[172:173], v10 offset:18432
Zavv 156,96,184
s_waitcnt lgkmcnt(12)
ds_read_b64_tr_b16 v[174:175], v10 offset:18688
Zavv 160,128,104
ds_read_b128 v[120:123], v5 offset:14336
Zavv 164,136,104
ds_read_b64_tr_b16 v[180:181], v9 offset:18944
Zavv 168,144,104
s_waitcnt lgkmcnt(12)
ds_read_b128 v[100:103], v6 offset:0
Zavv 172,152,104
ds_read_b64_tr_b16 v[182:183], v9 offset:19200
Zavv 176,104,160
ds_read_b128 v[108:111], v6 offset:2048
Zavv 180,104,168
s_waitcnt lgkmcnt(12)
ds_read_b64_tr_b16 v[188:189], v10 offset:18944
Zavv 184,104,176
ds_read_b128 v[116:119], v6 offset:4096
Zavv 188,104,184
ds_read_b64_tr_b16 v[190:191], v10 offset:19200
Zavv 192,128,112
s_waitcnt lgkmcnt(12)
ds_read_b128 v[124:127], v6 offset:6144
Zavv 196,136,112
Zavv 200,144,112
Zavv 204,152,112
s_waitcnt vmcnt(18) lgkmcnt(0)
Zavv 208,112,160
s_barrier
Zavv 212,112,168
s_add_u32 m0, s70, 98304
s_add_u32 s72, s68, 1048576
ds_read_b64_tr_b16 v[192:193], v9 offset:32768
buffer_load_dwordx4 v3, s[20:23], s72 offen lds
Zavv 216,112,176
)"
R"(
ds_read_b64_tr_b16 v[194:195], v9 offset:33024
Zavv 220,112,184
s_add_u32 m0, s70, 99200
s_add_u32 s72, s68, 1048576
ds_read_b64_tr_b16 v[196:197], v9 offset:49152
buffer_load_dwordx4 v3, s[20:23], s72 offen offset:128 lds
Zavv 224,128,120
ds_read_b64_tr_b16 v[198:199], v9 offset:49408
Zavv 228,136,120
s_add_u32 m0, s70, 100352
s_add_u32 s72, s68, 1048576
ds_read_b64_tr_b16 v[200:201], v10 offset:32768
buffer_load_dwordx4 v3, s[24:27], s72 offen lds
Zavv 232,144,120
ds_read_b64_tr_b16 v[202:203], v10 offset:33024
Zavv 236,152,120
s_add_u32 m0, s70, 101248
s_add_u32 s72, s68, 1048576
ds_read_b64_tr_b16 v[204:205], v10 offset:49152
buffer_load_dwordx4 v3, s[24:27], s72 offen offset:128 lds
Zavv 240,120,160
ds_read_b64_tr_b16 v[206:207], v10 offset:49408
Zavv 244,120,168
s_add_u32 m0, s70, 114688
s_add_u32 s72, s68, 1310720
ds_read_b64_tr_b16 v[208:209], v9 offset:33280
buffer_load_dwordx4 v3, s[20:23], s72 offen lds
Zavv 248,120,176
ds_read_b64_tr_b16 v[210:211], v9 offset:33536
Zavv 252,120,184
s_add_u32 m0, s70, 115584
s_add_u32 s72, s68, 1310720
ds_read_b64_tr_b16 v[212:213], v9 offset:49664
buffer_load_dwordx4 v3, s[20:23], s72 offen offset:128 lds
Mavv 0,132,100
ds_read_b64_tr_b16 v[214:215], v9 offset:49920
Mavv 4,140,100
s_add_u32 m0, s70, 116736
s_add_u32 s72, s68, 1310720
ds_read_b64_tr_b16 v[216:217], v10 offset:33280
buffer_load_dwordx4 v3, s[24:27], s72 offen lds
Mavv 8,148,100
ds_read_b64_tr_b16 v[218:219], v10 offset:33536
Mavv 12,156,100
s_add_u32 m0, s70, 117632
s_add_u32 s72, s68, 1310720
ds_read_b64_tr_b16 v[220:221], v10 offset:49664
buffer_load_dwordx4 v3, s[24:27], s72 offen offset:128 lds
Mavv 16,100,164
s_waitcnt lgkmcnt(12)
ds_read_b64_tr_b16 v[222:223], v10 offset:49920
Mavv 20,100,172
s_add_u32 m0, s66, 65536
s_add_u32 s72, s67, 256
ds_read_b64_tr_b16 v[224:225], v9 offset:34816
buffer_load_dwordx4 v1, s[12:15], s72 offen lds
Mavv 24,100,180
s_waitcnt lgkmcnt(11)
ds_read_b64_tr_b16 v[226:227], v9 offset:35072
Mavv 28,100,188
s_add_u32 m0, s66, 67584
s_add_u32 s72, s67, 16640
ds_read_b64_tr_b16 v[228:229], v9 offset:51200
buffer_load_dwordx4 v1, s[12:15], s72 offen lds
Mavv 32,132,108
ds_read_b128 v[100:103], v6 offset:8192
Mavv 36,140,108
s_add_u32 m0, s66, 69632
s_add_u32 s72, s67, 33024
ds_read_b64_tr_b16 v[230:231], v9 offset:51456
buffer_load_dwordx4 v1, s[12:15], s72 offen lds
Mavv 40,148,108
s_waitcnt lgkmcnt(12)
ds_read_b64_tr_b16 v[232:233], v10 offset:34816
Mavv 44,156,108
s_add_u32 m0, s66, 71680
s_add_u32 s72, s67, 49408
ds_read_b64_tr_b16 v[234:235], v10 offset:35072
buffer_load_dwordx4 v1, s[12:15], s72 offen lds
Mavv 48,108,164
s_waitcnt lgkmcnt(11)
ds_read_b64_tr_b16 v[236:237], v10 offset:51200
Mavv 52,108,172
s_add_u32 m0, s66, 73728
s_add_u32 s72, s67, 65792
ds_read_b64_tr_b16 v[238:239], v10 offset:51456
buffer_load_dwordx4 v1, s[12:15], s72 offen lds
Mavv 56,108,180
ds_read_b64_tr_b16 v[240:241], v9 offset:35328
Mavv 60,108,188
s_add_u32 m0, s66, 75776
s_add_u32 s72, s67, 82176
ds_read_b64_tr_b16 v[242:243], v9 offset:35584
buffer_load_dwordx4 v1, s[12:15], s72 offen lds
Mavv 64,132,116
s_waitcnt lgkmcnt(12)
ds_read_b128 v[108:111], v6 offset:10240
Mavv 68,140,116
s_add_u32 m0, s66, 77824
s_add_u32 s72, s67, 98560
ds_read_b64_tr_b16 v[244:245], v9 offset:51712
buffer_load_dwordx4 v1, s[12:15], s72 offen lds
Mavv 72,148,116
s_waitcnt lgkmcnt(11)
ds_read_b64_tr_b16 v[246:247], v9 offset:51968
Mavv 76,156,116
s_add_u32 m0, s66, 79872
s_add_u32 s72, s67, 114944
ds_read_b64_tr_b16 v[248:249], v10 offset:35328
buffer_load_dwordx4 v1, s[12:15], s72 offen lds
Mavv 80,116,164
ds_read_b64_tr_b16 v[250:251], v10 offset:35584
Mavv 84,116,172
ds_read_b64_tr_b16 v[252:253], v10 offset:51712
Mavv 88,116,180
s_waitcnt lgkmcnt(12)
ds_read_b64_tr_b16 v[254:255], v10 offset:51968
Mavv 92,116,188
Mavv 96,132,124
ds_read_b128 v[116:119], v6 offset:12288
Mavv 100,140,124
Mavv 104,148,124
Mavv 108,156,124
Mavv 112,124,164
Mavv 116,124,172
Mavv 120,124,180
Mavv 124,124,188
Mavv 128,132,100
ds_read_b128 v[124:127], v6 offset:14336
Mavv 132,140,100
Mavv 136,148,100
Mavv 140,156,100
Mavv 144,100,164
Mavv 148,100,172
Mavv 152,100,180
s_waitcnt vmcnt(24) lgkmcnt(0)
Mavv 156,100,188
s_barrier
Mavv 160,132,108
Mavv 164,140,108
s_add_u32 s72, s76, 2097152
ds_read_b128 v[96:99], v5 offset:32768
Mavv 168,148,108
buffer_load_dwordx4 v[80:83], v0, s[20:23], s72 offen
Mavv 172,156,108
s_add_u32 s72, s76, 2097152
ds_read_b128 v[100:103], v6 offset:32768
Mavv 176,108,164
buffer_load_dwordx4 v[80:83], v0, s[24:27], s72 offen
Mavv 180,108,172
s_add_u32 m0, s70, 131072
s_add_u32 s72, s68, 1572864
Mavv 184,108,180
buffer_load_dwordx4 v3, s[20:23], s72 offen lds
Mavv 188,108,188
Mavv 192,132,116
Mavv 196,140,116
Mavv 200,148,116
s_add_u32 m0, s70, 131968
s_add_u32 s72, s68, 1572864
Mavv 204,156,116
buffer_load_dwordx4 v3, s[20:23], s72 offen offset:128 lds
Mavv 208,116,164
Mavv 212,116,172
ds_read_b128 v[104:107], v5 offset:34816
Mavv 216,116,180
Mavv 220,116,188
s_add_u32 m0, s70, 133120
s_add_u32 s72, s68, 1572864
ds_read_b128 v[108:111], v6 offset:34816
Mavv 224,132,124
buffer_load_dwordx4 v3, s[24:27], s72 offen lds
Mavv 228,140,124
Mavv 232,148,124
Mavv 236,156,124
Mavv 240,124,164
s_add_u32 m0, s70, 134016
s_add_u32 s72, s68, 1572864
Mavv 244,124,172
buffer_load_dwordx4 v3, s[24:27], s72 offen offset:128 lds
Mavv 248,124,180
Mavv 252,124,188
s_waitcnt lgkmcnt(3)
Mavv 0,192,96
s_waitcnt lgkmcnt(2)
ds_read_b128 v[112:115], v5 offset:36864
Mavv 0,196,100
Mavv 4,200,96
s_add_u32 m0, s70, 147456
s_add_u32 s72, s68, 1835008
ds_read_b128 v[116:119], v6 offset:36864
Mavv 4,204,100
buffer_load_dwordx4 v3, s[20:23], s72 offen lds
Mavv 8,208,96
Mavv 8,212,100
Mavv 12,216,96
Mavv 12,220,100
s_add_u32 m0, s70, 148352
s_add_u32 s72, s68, 1835008
Mavv 16,96,224
buffer_load_dwordx4 v3, s[20:23], s72 offen offset:128 lds
Mavv 16,100,228
Mavv 20,96,232
Mavv 20,100,236
Mavv 24,96,240
s_add_u32 m0, s70, 149504
s_add_u32 s72, s68, 1835008
Mavv 24,100,244
buffer_load_dwordx4 v3, s[24:27], s72 offen lds
Mavv 28,96,248
Mavv 28,100,252
s_waitcnt lgkmcnt(3)
Mavv 32,192,104
s_waitcnt lgkmcnt(2)
ds_read_b128 v[120:123], v5 offset:38912
Mavv 32,196,108
Mavv 36,200,104
s_add_u32 m0, s70, 150400
s_add_u32 s72, s68, 1835008
ds_read_b128 v[124:127], v6 offset:38912
Mavv 36,204,108
buffer_load_dwordx4 v3, s[24:27], s72 offen offset:128 lds
Mavv 40,208,104
Mavv 40,212,108
Mavv 44,216,104
Mavv 44,220,108
s_add_u32 m0, s66, 0
s_add_u32 s72, s67, 384
Mavv 48,104,224
buffer_load_dwordx4 v1, s[12:15], s72 offen lds
Mavv 48,108,228
Mavv 52,104,232
Mavv 52,108,236
Mavv 56,104,240
s_add_u32 m0, s66, 2048
s_add_u32 s72, s67, 16768
Mavv 56,108,244
buffer_load_dwordx4 v1, s[12:15], s72 offen lds
Mavv 60,104,248
Mavv 60,108,252
s_waitcnt lgkmcnt(3)
Mavv 64,192,112
s_waitcnt lgkmcnt(2)
ds_read_b128 v[96:99], v5 offset:40960
Mavv 64,196,116
Mavv 68,200,112
s_add_u32 m0, s66, 4096
s_add_u32 s72, s67, 33152
ds_read_b128 v[100:103], v6 offset:40960
Mavv 68,204,116
buffer_load_dwordx4 v1, s[12:15], s72 offen lds
Mavv 72,208,112
Mavv 72,212,116
Mavv 76,216,112
Mavv 76,220,116
s_add_u32 m0, s66, 6144
s_add_u32 s72, s67, 49536
Mavv 80,112,224
buffer_load_dwordx4 v1, s[12:15], s72 offen lds
Mavv 80,116,228
Mavv 84,112,232
Mavv 84,116,236
Mavv 88,112,240
s_add_u32 m0, s66, 8192
s_add_u32 s72, s67, 65920
Mavv 88,116,244
buffer_load_dwordx4 v1, s[12:15], s72 offen lds
Mavv 92,112,248
Mavv 92,116,252
s_waitcnt lgkmcnt(3)
Mavv 96,192,120
s_waitcnt lgkmcnt(2)
ds_read_b128 v[104:107], v5 offset:43008
Mavv 96,196,124
Mavv 100,200,120
s_add_u32 m0, s66, 10240
s_add_u32 s72, s67, 82304
ds_read_b128 v[108:111], v6 offset:43008
Mavv 100,204,124
s_waitcnt vmcnt(23)
buffer_load_dwordx4 v1, s[12:15], s72 offen lds
Mavv 104,208,120
s_barrier
Mavv 104,212,124
ds_read_b64_tr_b16 v[128:129], v9 offset:0
Mavv 108,216,120
ds_read_b64_tr_b16 v[130:131], v9 offset:256
Mavv 108,220,124
ds_read_b64_tr_b16 v[132:133], v9 offset:16384
Mavv 112,120,224
ds_read_b64_tr_b16 v[134:135], v9 offset:16640
Mavv 112,124,228
ds_read_b64_tr_b16 v[136:137], v10 offset:0
Mavv 116,120,232
ds_read_b64_tr_b16 v[138:139], v10 offset:256
Mavv 116,124,236
ds_read_b64_tr_b16 v[140:141], v10 offset:16384
Mavv 120,120,240
ds_read_b64_tr_b16 v[142:143], v10 offset:16640
Mavv 120,124,244
ds_read_b64_tr_b16 v[144:145], v9 offset:512
Mavv 124,120,248
ds_read_b64_tr_b16 v[146:147], v9 offset:768
v_mov_b32 v64, 0x3f803f80
Mavv 124,124,252
s_waitcnt lgkmcnt(13)
ds_read_b64_tr_b16 v[148:149], v9 offset:16896
v_mov_b32 v65, 0x3f803f80
Mavv 128,192,96
s_waitcnt lgkmcnt(13)
ds_read_b128 v[112:115], v5 offset:45056
v_mov_b32 v66, 0x3f803f80
Mavv 128,196,100
ds_read_b64_tr_b16 v[150:151], v9 offset:17152
v_mov_b32 v67, 0x3f803f80
Mavv 132,200,96
s_waitcnt lgkmcnt(12)
ds_read_b128 v[116:119], v6 offset:45056
Mavv 132,204,100
ds_read_b64_tr_b16 v[152:153], v10 offset:512
Zvvv 68,64,32
Mavv 136,208,96
ds_read_b64_tr_b16 v[154:155], v10 offset:768
Mvvv 68,64,36
Mavv 136,212,100
s_waitcnt lgkmcnt(12)
ds_read_b64_tr_b16 v[156:157], v10 offset:16896
Mvvv 68,64,40
Mavv 140,216,96
ds_read_b64_tr_b16 v[158:159], v10 offset:17152
Mvvv 68,64,44
Mavv 140,220,100
ds_read_b64_tr_b16 v[160:161], v9 offset:2048
Mavv 144,96,224
s_waitcnt lgkmcnt(12)
ds_read_b64_tr_b16 v[162:163], v9 offset:2304
Mavv 144,100,228
ds_read_b64_tr_b16 v[164:165], v9 offset:18432
Mavv 148,96,232
ds_read_b64_tr_b16 v[166:167], v9 offset:18688
Mavv 148,100,236
s_waitcnt lgkmcnt(12)
ds_read_b64_tr_b16 v[168:169], v10 offset:2048
Mavv 152,96,240
ds_read_b64_tr_b16 v[170:171], v10 offset:2304
Mavv 152,100,244
ds_read_b64_tr_b16 v[172:173], v10 offset:18432
Mavv 156,96,248
s_waitcnt lgkmcnt(12)
ds_read_b64_tr_b16 v[174:175], v10 offset:18688
Mavv 156,100,252
ds_read_b64_tr_b16 v[176:177], v9 offset:2560
v_mov_b32 v72, v68
Mavv 160,192,104
ds_read_b128 v[120:123], v5 offset:47104
Mavv 160,196,108
s_waitcnt lgkmcnt(12)
ds_read_b64_tr_b16 v[178:179], v9 offset:2816
v_add_f32_dpp v72, v72, v72 quad_perm:[1,0,3,2] row_mask:0xf bank_mask:0xf bound_ctrl:1
Mavv 164,200,104
ds_read_b128 v[124:127], v6 offset:47104
Mavv 164,204,108
ds_read_b64_tr_b16 v[180:181], v9 offset:18944
v_add_f32_dpp v72, v72, v72 quad_perm:[2,3,0,1] row_mask:0xf bank_mask:0xf bound_ctrl:1
Mavv 168,208,104
s_waitcnt lgkmcnt(12)
ds_read_b64_tr_b16 v[182:183], v9 offset:19200
Mavv 168,212,108
ds_read_b64_tr_b16 v[184:185], v10 offset:2560
v_add_f32_dpp v72, v72, v72 row_half_mirror row_mask:0xf bank_mask:0xf bound_ctrl:1
Mavv 172,216,104
ds_read_b64_tr_b16 v[186:187], v10 offset:2816
v_mul_f32 v72, 0x3a800000, v72
Mavv 172,220,108
s_waitcnt lgkmcnt(12)
ds_read_b64_tr_b16 v[188:189], v10 offset:18944
v_cvt_pk_bf16_f32 v73, 0, v72
Mavv 176,104,224
s_add_u32 m0, s66, 12288
s_add_u32 s72, s67, 98688
ds_read_b64_tr_b16 v[190:191], v10 offset:19200
Mavv 176,108,228
buffer_load_dwordx4 v1, s[12:15], s72 offen lds
Mavv 180,104,232
s_add_u32 s72, s76, 3145728
v_sub_f32 v72, v72, v73
v_mul_f32 v72, v72, v72
Mavv 180,108,236
buffer_load_dwordx4 v[80:83], v0, s[20:23], s72 offen
Mavv 184,104,240
v_mul_f32 v72, 0xc2000000, v72
v_lshlrev_b32 v75, 16, v32
Mavv 184,108,244
s_add_u32 m0, s66, 14336
s_add_u32 s72, s67, 115072
v_and_b32 v76, 0xffff0000, v32
Mavv 188,104,248
buffer_load_dwordx4 v1, s[12:15], s72 offen lds
Mavv 188,108,252
s_add_u32 s72, s76, 3145728
v_sub_f32 v75, v75, v73
v_sub_f32 v76, v76, v73
Mavv 192,192,112
buffer_load_dwordx4 v[80:83], v0, s[24:27], s72 offen
Mavv 192,196,116
s_add_u32 s72, s76, 0
v_fmac_f32 v72, v75, v75
v_cvt_pk_bf16_f32 v32, v75, v76
Mavv 196,200,112
buffer_load_dwordx4 v[80:83], v0, s[16:19], s72 offen
Mavv 196,204,116
s_waitcnt vmcnt(21) lgkmcnt(0)
v_fmac_f32 v72, v76, v76
v_lshlrev_b32 v75, 16, v33
Mavv 200,208,112
s_barrier
Mavv 200,212,116
v_and_b32 v76, 0xffff0000, v33
v_sub_f32 v75, v75, v73
Mavv 204,216,112
ds_read_b128 v[96:99], v7 offset:0
v_sub_f32 v76, v76, v73
Mavv 204,220,116
v_fmac_f32 v72, v75, v75
v_cvt_pk_bf16_f32 v33, v75, v76
Mavv 208,112,224
ds_read_b128 v[100:103], v8 offset:0
v_fmac_f32 v72, v76, v76
Mavv 208,116,228
s_add_u32 m0, s70, 98304
)"
R"(
s_add_u32 s72, s68, 2097152
v_lshlrev_b32 v75, 16, v34
Mavv 212,112,232
buffer_load_dwordx4 v3, s[20:23], s72 offen lds
Mavv 212,116,236
v_and_b32 v76, 0xffff0000, v34
v_sub_f32 v75, v75, v73
Mavv 216,112,240
v_sub_f32 v76, v76, v73
v_fmac_f32 v72, v75, v75
Mavv 216,116,244
v_cvt_pk_bf16_f32 v34, v75, v76
v_fmac_f32 v72, v76, v76
Mavv 220,112,248
s_add_u32 m0, s70, 99200
s_add_u32 s72, s68, 2097152
v_lshlrev_b32 v75, 16, v35
Mavv 220,116,252
buffer_load_dwordx4 v3, s[20:23], s72 offen offset:128 lds
Mavv 224,192,120
v_and_b32 v76, 0xffff0000, v35
v_sub_f32 v75, v75, v73
Mavv 224,196,124
v_sub_f32 v76, v76, v73
v_fmac_f32 v72, v75, v75
Mavv 228,200,120
ds_read_b128 v[104:107], v7 offset:2048
v_cvt_pk_bf16_f32 v35, v75, v76
Mavv 228,204,124
v_fmac_f32 v72, v76, v76
v_lshlrev_b32 v75, 16, v36
Mavv 232,208,120
s_add_u32 m0, s70, 100352
s_add_u32 s72, s68, 2097152
ds_read_b128 v[108:111], v8 offset:2048
Mavv 232,212,124
buffer_load_dwordx4 v3, s[24:27], s72 offen lds
Mavv 236,216,120
v_and_b32 v76, 0xffff0000, v36
v_sub_f32 v75, v75, v73
Mavv 236,220,124
v_sub_f32 v76, v76, v73
v_fmac_f32 v72, v75, v75
Mavv 240,120,224
v_cvt_pk_bf16_f32 v36, v75, v76
v_fmac_f32 v72, v76, v76
Mavv 240,124,228
s_add_u32 m0, s70, 101248
s_add_u32 s72, s68, 2097152
v_lshlrev_b32 v75, 16, v37
Mavv 244,120,232
buffer_load_dwordx4 v3, s[24:27], s72 offen offset:128 lds
Mavv 244,124,236
v_and_b32 v76, 0xffff0000, v37
v_sub_f32 v75, v75, v73
Mavv 248,120,240
v_sub_f32 v76, v76, v73
v_fmac_f32 v72, v75, v75
Mavv 248,124,244
v_cvt_pk_bf16_f32 v37, v75, v76
v_fmac_f32 v72, v76, v76
Mavv 252,120,248
s_add_u32 m0, s70, 114688
s_add_u32 s72, s68, 2359296
v_lshlrev_b32 v75, 16, v38
Mavv 252,124,252
s_waitcnt lgkmcnt(3)
buffer_load_dwordx4 v3, s[20:23], s72 offen lds
Mavv 0,128,96
s_waitcnt lgkmcnt(2)
ds_read_b128 v[112:115], v7 offset:4096
v_and_b32 v76, 0xffff0000, v38
Mavv 0,132,100
v_sub_f32 v75, v75, v73
v_sub_f32 v76, v76, v73
Mavv 4,136,96
ds_read_b128 v[116:119], v8 offset:4096
v_fmac_f32 v72, v75, v75
Mavv 4,140,100
s_add_u32 m0, s70, 115584
s_add_u32 s72, s68, 2359296
v_cvt_pk_bf16_f32 v38, v75, v76
Mavv 8,144,96
buffer_load_dwordx4 v3, s[20:23], s72 offen offset:128 lds
Mavv 8,148,100
v_fmac_f32 v72, v76, v76
v_lshlrev_b32 v75, 16, v39
Mavv 12,152,96
v_and_b32 v76, 0xffff0000, v39
v_sub_f32 v75, v75, v73
Mavv 12,156,100
v_sub_f32 v76, v76, v73
v_fmac_f32 v72, v75, v75
Mavv 16,96,160
s_add_u32 m0, s70, 116736
s_add_u32 s72, s68, 2359296
v_cvt_pk_bf16_f32 v39, v75, v76
Mavv 16,100,164
buffer_load_dwordx4 v3, s[24:27], s72 offen lds
Mavv 20,96,168
v_fmac_f32 v72, v76, v76
v_lshlrev_b32 v75, 16, v40
Mavv 20,100,172
v_and_b32 v76, 0xffff0000, v40
v_sub_f32 v75, v75, v73
Mavv 24,96,176
v_sub_f32 v76, v76, v73
v_fmac_f32 v72, v75, v75
Mavv 24,100,180
s_add_u32 m0, s70, 117632
s_add_u32 s72, s68, 2359296
v_cvt_pk_bf16_f32 v40, v75, v76
Mavv 28,96,184
buffer_load_dwordx4 v3, s[24:27], s72 offen offset:128 lds
Mavv 28,100,188
s_waitcnt lgkmcnt(3)
v_fmac_f32 v72, v76, v76
v_lshlrev_b32 v75, 16, v41
Mavv 32,128,104
s_waitcnt lgkmcnt(2)
ds_read_b128 v[120:123], v7 offset:6144
v_and_b32 v76, 0xffff0000, v41
Mavv 32,132,108
v_sub_f32 v75, v75, v73
v_sub_f32 v76, v76, v73
Mavv 36,136,104
s_add_u32 m0, s66, 32768
s_add_u32 s72, s67, 512
ds_read_b128 v[124:127], v8 offset:6144
Mavv 36,140,108
buffer_load_dwordx4 v1, s[12:15], s72 offen lds
Mavv 40,144,104
v_fmac_f32 v72, v75, v75
v_cvt_pk_bf16_f32 v41, v75, v76
Mavv 40,148,108
v_fmac_f32 v72, v76, v76
v_lshlrev_b32 v75, 16, v42
Mavv 44,152,104
v_and_b32 v76, 0xffff0000, v42
v_sub_f32 v75, v75, v73
Mavv 44,156,108
s_add_u32 m0, s66, 34816
s_add_u32 s72, s67, 16896
v_sub_f32 v76, v76, v73
Mavv 48,104,160
buffer_load_dwordx4 v1, s[12:15], s72 offen lds
Mavv 48,108,164
v_fmac_f32 v72, v75, v75
v_cvt_pk_bf16_f32 v42, v75, v76
Mavv 52,104,168
v_fmac_f32 v72, v76, v76
v_lshlrev_b32 v75, 16, v43
Mavv 52,108,172
v_and_b32 v76, 0xffff0000, v43
v_sub_f32 v75, v75, v73
Mavv 56,104,176
s_add_u32 m0, s66, 36864
s_add_u32 s72, s67, 33280
v_sub_f32 v76, v76, v73
Mavv 56,108,180
buffer_load_dwordx4 v1, s[12:15], s72 offen lds
Mavv 60,104,184
v_fmac_f32 v72, v75, v75
v_cvt_pk_bf16_f32 v43, v75, v76
Mavv 60,108,188
s_waitcnt lgkmcnt(3)
v_fmac_f32 v72, v76, v76
v_lshlrev_b32 v75, 16, v44
Mavv 64,128,112
s_waitcnt lgkmcnt(2)
ds_read_b128 v[96:99], v7 offset:8192
v_and_b32 v76, 0xffff0000, v44
Mavv 64,132,116
v_sub_f32 v75, v75, v73
v_sub_f32 v76, v76, v73
Mavv 68,136,112
s_add_u32 m0, s66, 38912
s_add_u32 s72, s67, 49664
ds_read_b128 v[100:103], v8 offset:8192
Mavv 68,140,116
buffer_load_dwordx4 v1, s[12:15], s72 offen lds
Mavv 72,144,112
v_fmac_f32 v72, v75, v75
v_cvt_pk_bf16_f32 v44, v75, v76
Mavv 72,148,116
v_fmac_f32 v72, v76, v76
v_lshlrev_b32 v75, 16, v45
Mavv 76,152,112
v_and_b32 v76, 0xffff0000, v45
v_sub_f32 v75, v75, v73
Mavv 76,156,116
s_add_u32 m0, s66, 40960
s_add_u32 s72, s67, 66048
v_sub_f32 v76, v76, v73
Mavv 80,112,160
buffer_load_dwordx4 v1, s[12:15], s72 offen lds
Mavv 80,116,164
v_fmac_f32 v72, v75, v75
v_cvt_pk_bf16_f32 v45, v75, v76
Mavv 84,112,168
v_fmac_f32 v72, v76, v76
v_lshlrev_b32 v75, 16, v46
Mavv 84,116,172
v_and_b32 v76, 0xffff0000, v46
v_sub_f32 v75, v75, v73
Mavv 88,112,176
s_add_u32 m0, s66, 43008
s_add_u32 s72, s67, 82432
v_sub_f32 v76, v76, v73
Mavv 88,116,180
buffer_load_dwordx4 v1, s[12:15], s72 offen lds
Mavv 92,112,184
v_fmac_f32 v72, v75, v75
v_cvt_pk_bf16_f32 v46, v75, v76
Mavv 92,116,188
s_waitcnt lgkmcnt(3)
v_fmac_f32 v72, v76, v76
v_lshlrev_b32 v75, 16, v47
Mavv 96,128,120
s_waitcnt lgkmcnt(2)
ds_read_b128 v[104:107], v7 offset:10240
v_and_b32 v76, 0xffff0000, v47
Mavv 96,132,124
v_sub_f32 v75, v75, v73
v_sub_f32 v76, v76, v73
Mavv 100,136,120
s_add_u32 m0, s66, 45056
s_add_u32 s72, s67, 98816
ds_read_b128 v[108:111], v8 offset:10240
Mavv 100,140,124
s_waitcnt vmcnt(25)
buffer_load_dwordx4 v1, s[12:15], s72 offen lds
Mavv 104,144,120
s_barrier
Mavv 104,148,124
ds_read_b64_tr_b16 v[192:193], v9 offset:32768
v_fmac_f32 v72, v75, v75
Mavv 108,152,120
ds_read_b64_tr_b16 v[194:195], v9 offset:33024
v_cvt_pk_bf16_f32 v47, v75, v76
Mavv 108,156,124
ds_read_b64_tr_b16 v[196:197], v9 offset:49152
v_fmac_f32 v72, v76, v76
Mavv 112,120,160
ds_read_b64_tr_b16 v[198:199], v9 offset:49408
Mavv 112,124,164
ds_read_b64_tr_b16 v[200:201], v10 offset:32768
v_add_f32_dpp v72, v72, v72 quad_perm:[1,0,3,2] row_mask:0xf bank_mask:0xf bound_ctrl:1
Mavv 116,120,168
ds_read_b64_tr_b16 v[202:203], v10 offset:33024
Mavv 116,124,172
ds_read_b64_tr_b16 v[204:205], v10 offset:49152
v_add_f32_dpp v72, v72, v72 quad_perm:[2,3,0,1] row_mask:0xf bank_mask:0xf bound_ctrl:1
Mavv 120,120,176
ds_read_b64_tr_b16 v[206:207], v10 offset:49408
Mavv 120,124,180
ds_read_b64_tr_b16 v[208:209], v9 offset:33280
v_add_f32_dpp v72, v72, v72 row_half_mirror row_mask:0xf bank_mask:0xf bound_ctrl:1
Mavv 124,120,184
ds_read_b64_tr_b16 v[210:211], v9 offset:33536
v_mov_b32 v75, v72
Mavv 124,124,188
s_waitcnt lgkmcnt(13)
ds_read_b64_tr_b16 v[212:213], v9 offset:49664
Mavv 128,128,96
s_waitcnt lgkmcnt(13)
ds_read_b128 v[112:115], v7 offset:12288
v_permlane16_swap_b32 v72, v75
Mavv 128,132,100
ds_read_b64_tr_b16 v[214:215], v9 offset:49920
v_add_f32 v72, v72, v75
Mavv 132,136,96
s_waitcnt lgkmcnt(12)
ds_read_b128 v[116:119], v8 offset:12288
v_mov_b32 v75, v72
Mavv 132,140,100
ds_read_b64_tr_b16 v[216:217], v10 offset:33280
Mavv 136,144,96
ds_read_b64_tr_b16 v[218:219], v10 offset:33536
v_permlane32_swap_b32 v72, v75
Mavv 136,148,100
s_waitcnt lgkmcnt(12)
ds_read_b64_tr_b16 v[220:221], v10 offset:49664
v_add_f32 v72, v72, v75
Mavv 140,152,96
ds_read_b64_tr_b16 v[222:223], v10 offset:49920
v_mul_f32 v72, 0x3a800000, v72
Mavv 140,156,100
ds_read_b64_tr_b16 v[224:225], v9 offset:34816
v_cvt_pk_bf16_f32 v72, 0, v72
Mavv 144,96,160
s_waitcnt lgkmcnt(12)
ds_read_b64_tr_b16 v[226:227], v9 offset:35072
v_add_f32 v72, 0x3727c5ac, v72
Mavv 144,100,164
ds_read_b64_tr_b16 v[228:229], v9 offset:51200
v_cvt_pk_bf16_f32 v72, 0, v72
Mavv 148,96,168
ds_read_b64_tr_b16 v[230:231], v9 offset:51456
Mavv 148,100,172
s_waitcnt lgkmcnt(12)
ds_read_b64_tr_b16 v[232:233], v10 offset:34816
Mavv 152,96,176
ds_read_b64_tr_b16 v[234:235], v10 offset:35072
Mavv 152,100,180
ds_read_b64_tr_b16 v[236:237], v10 offset:51200
Mavv 156,96,184
s_waitcnt lgkmcnt(12)
ds_read_b64_tr_b16 v[238:239], v10 offset:51456
Mavv 156,100,188
ds_read_b64_tr_b16 v[240:241], v9 offset:35328
Mavv 160,128,104
ds_read_b128 v[120:123], v7 offset:14336
Mavv 160,132,108
s_waitcnt lgkmcnt(12)
ds_read_b64_tr_b16 v[242:243], v9 offset:35584
Mavv 164,136,104
ds_read_b128 v[124:127], v8 offset:14336
Mavv 164,140,108
ds_read_b64_tr_b16 v[244:245], v9 offset:51712
Mavv 168,144,104
s_waitcnt lgkmcnt(12)
ds_read_b64_tr_b16 v[246:247], v9 offset:51968
Mavv 168,148,108
ds_read_b64_tr_b16 v[248:249], v10 offset:35328
Mavv 172,152,104
ds_read_b64_tr_b16 v[250:251], v10 offset:35584
Mavv 172,156,108
s_waitcnt lgkmcnt(12)
ds_read_b64_tr_b16 v[252:253], v10 offset:51712
Mavv 176,104,160
s_add_u32 m0, s66, 47104
s_add_u32 s72, s67, 115200
ds_read_b64_tr_b16 v[254:255], v10 offset:51968
Mavv 176,108,164
buffer_load_dwordx4 v1, s[12:15], s72 offen lds
Mavv 180,104,168
s_add_u32 s72, s76, 1048576
v_sqrt_f32 v74, v72
Mavv 180,108,172
buffer_load_dwordx4 v[80:83], v0, s[16:19], s72 offen
Mavv 184,104,176
s_add_u32 s72, s76, 2097152
v_cvt_pk_bf16_f32 v74, 0, v74
Mavv 184,108,180
buffer_load_dwordx4 v[80:83], v0, s[16:19], s72 offen
Mavv 188,104,184
s_add_u32 s72, s76, 3145728
v_rcp_f32 v74, v74
Mavv 188,108,188
buffer_load_dwordx4 v[80:83], v0, s[16:19], s72 offen
Mavv 192,128,112
v_lshlrev_b32 v75, 16, v32
v_and_b32 v76, 0xffff0000, v32
Mavv 192,132,116
v_mul_f32 v75, v75, v74
v_mul_f32 v76, v76, v74
Mavv 196,136,112
v_cvt_pk_bf16_f32 v32, v75, v76
v_lshlrev_b32 v75, 16, v33
Mavv 196,140,116
s_waitcnt vmcnt(21) lgkmcnt(0)
v_and_b32 v76, 0xffff0000, v33
v_mul_f32 v75, v75, v74
Mavv 200,144,112
s_barrier
Mavv 200,148,116
v_mul_f32 v76, v76, v74
v_cvt_pk_bf16_f32 v33, v75, v76
Mavv 204,152,112
ds_read_b128 v[96:99], v5 offset:0
v_lshlrev_b32 v75, 16, v34
Mavv 204,156,116
v_and_b32 v76, 0xffff0000, v34
v_mul_f32 v75, v75, v74
Mavv 208,112,160
ds_read_b128 v[100:103], v6 offset:0
v_mul_f32 v76, v76, v74
Mavv 208,116,164
s_add_u32 m0, s70, 131072
s_add_u32 s72, s68, 2621440
v_cvt_pk_bf16_f32 v34, v75, v76
Mavv 212,112,168
buffer_load_dwordx4 v3, s[20:23], s72 offen lds
Mavv 212,116,172
v_lshlrev_b32 v75, 16, v35
v_and_b32 v76, 0xffff0000, v35
Mavv 216,112,176
v_mul_f32 v75, v75, v74
v_mul_f32 v76, v76, v74
Mavv 216,116,180
v_cvt_pk_bf16_f32 v35, v75, v76
Mavv 220,112,184
s_add_u32 m0, s70, 131968
s_add_u32 s72, s68, 2621440
Mavv 220,116,188
buffer_load_dwordx4 v3, s[20:23], s72 offen offset:128 lds
Mavv 224,128,120
buffer_store_dwordx4 v[32:35], v77, s[48:51], s77 offen sc1
Mavv 224,132,124
v_lshlrev_b32 v75, 16, v36
v_and_b32 v76, 0xffff0000, v36
Mavv 228,136,120
ds_read_b128 v[104:107], v5 offset:2048
v_mul_f32 v75, v75, v74
Mavv 228,140,124
v_mul_f32 v76, v76, v74
v_cvt_pk_bf16_f32 v36, v75, v76
Mavv 232,144,120
s_add_u32 m0, s70, 133120
s_add_u32 s72, s68, 2621440
ds_read_b128 v[108:111], v6 offset:2048
Mavv 232,148,124
buffer_load_dwordx4 v3, s[24:27], s72 offen lds
Mavv 236,152,120
v_lshlrev_b32 v75, 16, v37
v_and_b32 v76, 0xffff0000, v37
Mavv 236,156,124
v_mul_f32 v75, v75, v74
v_mul_f32 v76, v76, v74
Mavv 240,120,160
v_cvt_pk_bf16_f32 v37, v75, v76
v_lshlrev_b32 v75, 16, v38
Mavv 240,124,164
s_add_u32 m0, s70, 134016
s_add_u32 s72, s68, 2621440
v_and_b32 v76, 0xffff0000, v38
Mavv 244,120,168
buffer_load_dwordx4 v3, s[24:27], s72 offen offset:128 lds
Mavv 244,124,172
v_mul_f32 v75, v75, v74
v_mul_f32 v76, v76, v74
Mavv 248,120,176
v_cvt_pk_bf16_f32 v38, v75, v76
v_lshlrev_b32 v75, 16, v39
Mavv 248,124,180
v_and_b32 v76, 0xffff0000, v39
v_mul_f32 v75, v75, v74
Mavv 252,120,184
s_add_u32 m0, s70, 147456
s_add_u32 s72, s68, 2883584
v_mul_f32 v76, v76, v74
Mavv 252,124,188
s_waitcnt lgkmcnt(3)
buffer_load_dwordx4 v3, s[20:23], s72 offen lds
Mavv 0,192,96
s_waitcnt lgkmcnt(2)
ds_read_b128 v[112:115], v5 offset:4096
)"
R"(
v_cvt_pk_bf16_f32 v39, v75, v76
Mavv 0,196,100
buffer_store_dwordx4 v[36:39], v77, s[48:51], s77 offen offset:512 sc1
Mavv 4,200,96
ds_read_b128 v[116:119], v6 offset:4096
v_lshlrev_b32 v75, 16, v40
Mavv 4,204,100
s_add_u32 m0, s70, 148352
s_add_u32 s72, s68, 2883584
v_and_b32 v76, 0xffff0000, v40
Mavv 8,208,96
buffer_load_dwordx4 v3, s[20:23], s72 offen offset:128 lds
Mavv 8,212,100
v_mul_f32 v75, v75, v74
v_mul_f32 v76, v76, v74
Mavv 12,216,96
v_cvt_pk_bf16_f32 v40, v75, v76
v_lshlrev_b32 v75, 16, v41
Mavv 12,220,100
v_and_b32 v76, 0xffff0000, v41
v_mul_f32 v75, v75, v74
Mavv 16,96,224
s_add_u32 m0, s70, 149504
s_add_u32 s72, s68, 2883584
v_mul_f32 v76, v76, v74
Mavv 16,100,228
buffer_load_dwordx4 v3, s[24:27], s72 offen lds
Mavv 20,96,232
v_cvt_pk_bf16_f32 v41, v75, v76
v_lshlrev_b32 v75, 16, v42
Mavv 20,100,236
v_and_b32 v76, 0xffff0000, v42
v_mul_f32 v75, v75, v74
Mavv 24,96,240
v_mul_f32 v76, v76, v74
v_cvt_pk_bf16_f32 v42, v75, v76
Mavv 24,100,244
s_add_u32 m0, s70, 150400
s_add_u32 s72, s68, 2883584
v_lshlrev_b32 v75, 16, v43
Mavv 28,96,248
buffer_load_dwordx4 v3, s[24:27], s72 offen offset:128 lds
Mavv 28,100,252
s_waitcnt lgkmcnt(3)
v_and_b32 v76, 0xffff0000, v43
v_mul_f32 v75, v75, v74
Mavv 32,192,104
s_waitcnt lgkmcnt(2)
ds_read_b128 v[120:123], v5 offset:6144
v_mul_f32 v76, v76, v74
Mavv 32,196,108
v_cvt_pk_bf16_f32 v43, v75, v76
Mavv 36,200,104
s_add_u32 m0, s66, 65536
s_add_u32 s72, s67, 640
ds_read_b128 v[124:127], v6 offset:6144
Mavv 36,204,108
buffer_load_dwordx4 v1, s[12:15], s72 offen lds
Mavv 40,208,104
buffer_store_dwordx4 v[40:43], v77, s[48:51], s77 offen offset:1024 sc1
Mavv 40,212,108
v_lshlrev_b32 v75, 16, v44
v_and_b32 v76, 0xffff0000, v44
Mavv 44,216,104
v_mul_f32 v75, v75, v74
v_mul_f32 v76, v76, v74
Mavv 44,220,108
s_add_u32 m0, s66, 67584
s_add_u32 s72, s67, 17024
v_cvt_pk_bf16_f32 v44, v75, v76
Mavv 48,104,224
buffer_load_dwordx4 v1, s[12:15], s72 offen lds
Mavv 48,108,228
v_lshlrev_b32 v75, 16, v45
v_and_b32 v76, 0xffff0000, v45
Mavv 52,104,232
v_mul_f32 v75, v75, v74
v_mul_f32 v76, v76, v74
Mavv 52,108,236
v_cvt_pk_bf16_f32 v45, v75, v76
v_lshlrev_b32 v75, 16, v46
Mavv 56,104,240
s_add_u32 m0, s66, 69632
s_add_u32 s72, s67, 33408
v_and_b32 v76, 0xffff0000, v46
Mavv 56,108,244
buffer_load_dwordx4 v1, s[12:15], s72 offen lds
Mavv 60,104,248
v_mul_f32 v75, v75, v74
v_mul_f32 v76, v76, v74
Mavv 60,108,252
s_waitcnt lgkmcnt(3)
v_cvt_pk_bf16_f32 v46, v75, v76
v_lshlrev_b32 v75, 16, v47
Mavv 64,192,112
s_waitcnt lgkmcnt(2)
ds_read_b128 v[96:99], v5 offset:8192
v_and_b32 v76, 0xffff0000, v47
Mavv 64,196,116
v_mul_f32 v75, v75, v74
v_mul_f32 v76, v76, v74
Mavv 68,200,112
s_add_u32 m0, s66, 71680
s_add_u32 s72, s67, 49792
ds_read_b128 v[100:103], v6 offset:8192
Mavv 68,204,116
buffer_load_dwordx4 v1, s[12:15], s72 offen lds
Mavv 72,208,112
v_cvt_pk_bf16_f32 v47, v75, v76
Mavv 72,212,116
buffer_store_dwordx4 v[44:47], v77, s[48:51], s77 offen offset:1536 sc1
Zvvv 68,48,32
Mavv 76,216,112
Mvvv 68,52,36
Mavv 76,220,116
s_add_u32 m0, s66, 73728
s_add_u32 s72, s67, 66176
Mvvv 68,56,40
Mavv 80,112,224
buffer_load_dwordx4 v1, s[12:15], s72 offen lds
Mvvv 68,60,44
Mavv 80,116,228
s_mov_b32 s86, 0x100001
s_mov_b32 s87, 0x10000100
Mavv 84,112,232
Mavv 84,116,236
Mavv 88,112,240
s_add_u32 m0, s66, 75776
s_add_u32 s72, s67, 82560
Mavv 88,116,244
buffer_load_dwordx4 v1, s[12:15], s72 offen lds
Mavv 92,112,248
Mavv 92,116,252
s_waitcnt lgkmcnt(3)
Mavv 96,192,120
s_waitcnt lgkmcnt(2)
ds_read_b128 v[104:107], v5 offset:10240
Mavv 96,196,124
v_cndmask_b32 v72, 0, v68, s[86:87]
Mavv 100,200,120
s_add_u32 m0, s66, 77824
s_add_u32 s72, s67, 98944
ds_read_b128 v[108:111], v6 offset:10240
Mavv 100,204,124
s_waitcnt vmcnt(29)
buffer_load_dwordx4 v1, s[12:15], s72 offen lds
Mavv 104,208,120
s_barrier
s_lshl_b64 s[86:87], s[86:87], 1
Mavv 104,212,124
ds_read_b64_tr_b16 v[128:129], v9 offset:0
v_cndmask_b32 v72, v72, v69, s[86:87]
Mavv 108,216,120
s_lshl_b64 s[86:87], s[86:87], 1
ds_read_b64_tr_b16 v[130:131], v9 offset:256
v_cndmask_b32 v72, v72, v70, s[86:87]
Mavv 108,220,124
s_lshl_b64 s[86:87], s[86:87], 1
ds_read_b64_tr_b16 v[132:133], v9 offset:16384
v_cndmask_b32 v72, v72, v71, s[86:87]
Mavv 112,120,224
ds_read_b64_tr_b16 v[134:135], v9 offset:16640
Mavv 112,124,228
ds_read_b64_tr_b16 v[136:137], v10 offset:0
v_add_f32_dpp v72, v72, v72 quad_perm:[1,0,3,2] row_mask:0xf bank_mask:0xf bound_ctrl:1
Mavv 116,120,232
ds_read_b64_tr_b16 v[138:139], v10 offset:256
Mavv 116,124,236
ds_read_b64_tr_b16 v[140:141], v10 offset:16384
v_add_f32_dpp v72, v72, v72 quad_perm:[2,3,0,1] row_mask:0xf bank_mask:0xf bound_ctrl:1
Mavv 120,120,240
ds_read_b64_tr_b16 v[142:143], v10 offset:16640
Mavv 120,124,244
ds_read_b64_tr_b16 v[144:145], v9 offset:512
v_add_f32_dpp v72, v72, v72 row_half_mirror row_mask:0xf bank_mask:0xf bound_ctrl:1
Mavv 124,120,248
ds_read_b64_tr_b16 v[146:147], v9 offset:768
v_mov_b32 v75, v72
Mavv 124,124,252
s_waitcnt lgkmcnt(13)
ds_read_b64_tr_b16 v[148:149], v9 offset:16896
Mavv 128,192,96
s_waitcnt lgkmcnt(13)
ds_read_b128 v[112:115], v5 offset:12288
v_permlane16_swap_b32 v72, v75
Mavv 128,196,100
ds_read_b64_tr_b16 v[150:151], v9 offset:17152
v_add_f32 v72, v72, v75
Mavv 132,200,96
s_waitcnt lgkmcnt(12)
ds_read_b128 v[116:119], v6 offset:12288
v_mov_b32 v75, v72
Mavv 132,204,100
ds_read_b64_tr_b16 v[152:153], v10 offset:512
Mavv 136,208,96
ds_read_b64_tr_b16 v[154:155], v10 offset:768
v_permlane32_swap_b32 v72, v75
Mavv 136,212,100
s_waitcnt lgkmcnt(12)
ds_read_b64_tr_b16 v[156:157], v10 offset:16896
v_add_f32 v72, v72, v75
Mavv 140,216,96
ds_read_b64_tr_b16 v[158:159], v10 offset:17152
v_cvt_pk_bf16_f32 v72, 0, v72
Mavv 140,220,100
ds_read_b64_tr_b16 v[160:161], v9 offset:2048
v_add_f32 v72, 0, v72
Mavv 144,96,224
s_waitcnt lgkmcnt(12)
ds_read_b64_tr_b16 v[162:163], v9 offset:2304
v_ashrrev_i32 v75, 31, v72
Mavv 144,100,228
ds_read_b64_tr_b16 v[164:165], v9 offset:18432
v_or_b32 v75, 0x80000000, v75
Mavv 148,96,232
ds_read_b64_tr_b16 v[166:167], v9 offset:18688
v_xor_b32 v75, v75, v72
Mavv 148,100,236
s_waitcnt lgkmcnt(12)
ds_read_b64_tr_b16 v[168:169], v10 offset:2048
v_and_b32 v75, 0xffff0000, v75
Mavv 152,96,240
ds_read_b64_tr_b16 v[170:171], v10 offset:2304
v_cmp_o_f32 vcc, v72, v72
Mavv 152,100,244
ds_read_b64_tr_b16 v[172:173], v10 offset:18432
Mavv 156,96,248
s_waitcnt lgkmcnt(12)
ds_read_b64_tr_b16 v[174:175], v10 offset:18688
v_cndmask_b32 v75, 0, v75, vcc
Mavv 156,100,252
ds_read_b64_tr_b16 v[176:177], v9 offset:2560
v_or_b32 v68, v75, v78
Mavv 160,192,104
ds_read_b128 v[120:123], v5 offset:14336
Mavv 160,196,108
s_waitcnt lgkmcnt(12)
ds_read_b64_tr_b16 v[178:179], v9 offset:2816
v_mov_b32_dpp v70, v68 row_ror:8 row_mask:0xf bank_mask:0xf bound_ctrl:1
Mavv 164,200,104
ds_read_b128 v[124:127], v6 offset:14336
v_mov_b32 v69, s74
Mavv 164,204,108
ds_read_b64_tr_b16 v[180:181], v9 offset:18944
v_mov_b32 v71, s75
Mavv 168,208,104
s_waitcnt lgkmcnt(12)
ds_read_b64_tr_b16 v[182:183], v9 offset:19200
Mavv 168,212,108
ds_read_b64_tr_b16 v[184:185], v10 offset:2560
Mavv 172,216,104
ds_read_b64_tr_b16 v[186:187], v10 offset:2816
Mavv 172,220,108
s_waitcnt lgkmcnt(12)
ds_read_b64_tr_b16 v[188:189], v10 offset:18944
Mavv 176,104,224
s_add_u32 m0, s66, 79872
s_add_u32 s72, s67, 115328
ds_read_b64_tr_b16 v[190:191], v10 offset:19200
Mavv 176,108,228
buffer_load_dwordx4 v1, s[12:15], s72 offen lds
Mavv 180,104,232
s_add_u32 s72, s76, 4194304
Mavv 180,108,236
buffer_load_dwordx4 v[80:83], v0, s[16:19], s72 offen
Mavv 184,104,240
s_add_u32 s72, s76, 5242880
Mavv 184,108,244
buffer_load_dwordx4 v[80:83], v0, s[16:19], s72 offen
Mavv 188,104,248
Mavv 188,108,252
Mavv 192,192,112
Mavv 192,196,116
Mavv 196,200,112
Mavv 196,204,116
s_waitcnt vmcnt(25) lgkmcnt(0)
Mavv 200,208,112
s_barrier
Mavv 200,212,116
Mavv 204,216,112
ds_read_b128 v[96:99], v5 offset:32768
Mavv 204,220,116
Mavv 208,112,224
ds_read_b128 v[100:103], v6 offset:32768
Mavv 208,116,228
s_add_u32 m0, s70, 98304
s_add_u32 s72, s68, 3145728
Mavv 212,112,232
buffer_load_dwordx4 v3, s[20:23], s72 offen lds
Mavv 212,116,236
Mavv 216,112,240
Mavv 216,116,244
Mavv 220,112,248
s_add_u32 m0, s70, 99200
s_add_u32 s72, s68, 3145728
Mavv 220,116,252
buffer_load_dwordx4 v3, s[20:23], s72 offen offset:128 lds
Mavv 224,192,120
Mavv 224,196,124
Mavv 228,200,120
ds_read_b128 v[104:107], v5 offset:34816
Mavv 228,204,124
Mavv 232,208,120
s_add_u32 m0, s70, 100352
s_add_u32 s72, s68, 3145728
ds_read_b128 v[108:111], v6 offset:34816
Mavv 232,212,124
buffer_load_dwordx4 v3, s[24:27], s72 offen lds
Mavv 236,216,120
Mavv 236,220,124
Mavv 240,120,224
Mavv 240,124,228
s_add_u32 m0, s70, 101248
s_add_u32 s72, s68, 3145728
Mavv 244,120,232
buffer_load_dwordx4 v3, s[24:27], s72 offen offset:128 lds
Mavv 244,124,236
Mavv 248,120,240
Mavv 248,124,244
Mavv 252,120,248
s_add_u32 m0, s70, 114688
s_add_u32 s72, s68, 3407872
Mavv 252,124,252
s_waitcnt lgkmcnt(3)
buffer_load_dwordx4 v3, s[20:23], s72 offen lds
Mavv 0,128,96
s_waitcnt lgkmcnt(2)
ds_read_b128 v[112:115], v5 offset:36864
Mavv 0,132,100
Mavv 4,136,96
ds_read_b128 v[116:119], v6 offset:36864
Mavv 4,140,100
s_add_u32 m0, s70, 115584
s_add_u32 s72, s68, 3407872
Mavv 8,144,96
buffer_load_dwordx4 v3, s[20:23], s72 offen offset:128 lds
Mavv 8,148,100
Mavv 12,152,96
Mavv 12,156,100
Mavv 16,96,160
s_add_u32 m0, s70, 116736
s_add_u32 s72, s68, 3407872
Mavv 16,100,164
buffer_load_dwordx4 v3, s[24:27], s72 offen lds
Mavv 20,96,168
Mavv 20,100,172
Mavv 24,96,176
Mavv 24,100,180
s_add_u32 m0, s70, 117632
s_add_u32 s72, s68, 3407872
Mavv 28,96,184
buffer_load_dwordx4 v3, s[24:27], s72 offen offset:128 lds
Mavv 28,100,188
s_waitcnt lgkmcnt(3)
Mavv 32,128,104
s_waitcnt lgkmcnt(2)
ds_read_b128 v[120:123], v5 offset:38912
Mavv 32,132,108
Mavv 36,136,104
s_add_u32 m0, s66, 0
s_add_u32 s72, s67, 768
ds_read_b128 v[124:127], v6 offset:38912
Mavv 36,140,108
buffer_load_dwordx4 v1, s[12:15], s72 offen lds
Mavv 40,144,104
Mavv 40,148,108
Mavv 44,152,104
Mavv 44,156,108
s_add_u32 m0, s66, 2048
s_add_u32 s72, s67, 17152
Mavv 48,104,160
buffer_load_dwordx4 v1, s[12:15], s72 offen lds
Mavv 48,108,164
Mavv 52,104,168
Mavv 52,108,172
Mavv 56,104,176
s_add_u32 m0, s66, 4096
s_add_u32 s72, s67, 33536
Mavv 56,108,180
buffer_load_dwordx4 v1, s[12:15], s72 offen lds
Mavv 60,104,184
Mavv 60,108,188
s_waitcnt lgkmcnt(3)
Mavv 64,128,112
s_waitcnt lgkmcnt(2)
ds_read_b128 v[96:99], v5 offset:40960
Mavv 64,132,116
Mavv 68,136,112
s_add_u32 m0, s66, 6144
s_add_u32 s72, s67, 49920
ds_read_b128 v[100:103], v6 offset:40960
Mavv 68,140,116
buffer_load_dwordx4 v1, s[12:15], s72 offen lds
Mavv 72,144,112
Mavv 72,148,116
Mavv 76,152,112
Mavv 76,156,116
s_add_u32 m0, s66, 8192
s_add_u32 s72, s67, 66304
Mavv 80,112,160
buffer_load_dwordx4 v1, s[12:15], s72 offen lds
Mavv 80,116,164
Mavv 84,112,168
Mavv 84,116,172
Mavv 88,112,176
s_add_u32 m0, s66, 10240
s_add_u32 s72, s67, 82688
Mavv 88,116,180
buffer_load_dwordx4 v1, s[12:15], s72 offen lds
Mavv 92,112,184
Mavv 92,116,188
s_waitcnt lgkmcnt(3)
Mavv 96,128,120
s_waitcnt lgkmcnt(2)
ds_read_b128 v[104:107], v5 offset:43008
Mavv 96,132,124
Mavv 100,136,120
s_add_u32 m0, s66, 12288
s_add_u32 s72, s67, 99072
ds_read_b128 v[108:111], v6 offset:43008
Mavv 100,140,124
s_waitcnt vmcnt(26)
buffer_load_dwordx4 v1, s[12:15], s72 offen lds
Mavv 104,144,120
s_barrier
Mavv 104,148,124
ds_read_b64_tr_b16 v[192:193], v9 offset:32768
Mavv 108,152,120
ds_read_b64_tr_b16 v[194:195], v9 offset:33024
Mavv 108,156,124
ds_read_b64_tr_b16 v[196:197], v9 offset:49152
Mavv 112,120,160
ds_read_b64_tr_b16 v[198:199], v9 offset:49408
Mavv 112,124,164
ds_read_b64_tr_b16 v[200:201], v10 offset:32768
Mavv 116,120,168
ds_read_b64_tr_b16 v[202:203], v10 offset:33024
Mavv 116,124,172
ds_read_b64_tr_b16 v[204:205], v10 offset:49152
Mavv 120,120,176
ds_read_b64_tr_b16 v[206:207], v10 offset:49408
Mavv 120,124,180
ds_read_b64_tr_b16 v[208:209], v9 offset:33280
Mavv 124,120,184
ds_read_b64_tr_b16 v[210:211], v9 offset:33536
Mavv 124,124,188
s_waitcnt lgkmcnt(13)
ds_read_b64_tr_b16 v[212:213], v9 offset:49664
Mavv 128,128,96
)"
R"(
s_waitcnt lgkmcnt(13)
ds_read_b128 v[112:115], v5 offset:45056
Mavv 128,132,100
s_waitcnt vmcnt(21)
ds_read_b64_tr_b16 v[214:215], v9 offset:49920
Mavv 132,136,96
s_waitcnt lgkmcnt(12)
ds_read_b128 v[116:119], v6 offset:45056
Mavv 132,140,100
ds_read_b64_tr_b16 v[216:217], v10 offset:33280
Mavv 136,144,96
ds_read_b64_tr_b16 v[218:219], v10 offset:33536
Mavv 136,148,100
s_waitcnt lgkmcnt(12)
ds_read_b64_tr_b16 v[220:221], v10 offset:49664
Mavv 140,152,96
ds_read_b64_tr_b16 v[222:223], v10 offset:49920
Mavv 140,156,100
ds_read_b64_tr_b16 v[224:225], v9 offset:34816
Mavv 144,96,160
s_waitcnt lgkmcnt(12)
ds_read_b64_tr_b16 v[226:227], v9 offset:35072
Mavv 144,100,164
ds_read_b64_tr_b16 v[228:229], v9 offset:51200
Mavv 148,96,168
ds_read_b64_tr_b16 v[230:231], v9 offset:51456
Mavv 148,100,172
s_waitcnt lgkmcnt(12)
ds_read_b64_tr_b16 v[232:233], v10 offset:34816
Mavv 152,96,176
ds_read_b64_tr_b16 v[234:235], v10 offset:35072
Mavv 152,100,180
ds_read_b64_tr_b16 v[236:237], v10 offset:51200
Mavv 156,96,184
s_waitcnt lgkmcnt(12)
ds_read_b64_tr_b16 v[238:239], v10 offset:51456
Mavv 156,100,188
ds_read_b64_tr_b16 v[240:241], v9 offset:35328
Mavv 160,128,104
ds_read_b128 v[120:123], v5 offset:47104
Mavv 160,132,108
s_waitcnt lgkmcnt(12)
ds_read_b64_tr_b16 v[242:243], v9 offset:35584
Mavv 164,136,104
ds_read_b128 v[124:127], v6 offset:47104
Mavv 164,140,108
ds_read_b64_tr_b16 v[244:245], v9 offset:51712
Mavv 168,144,104
s_waitcnt lgkmcnt(12)
ds_read_b64_tr_b16 v[246:247], v9 offset:51968
Mavv 168,148,108
ds_read_b64_tr_b16 v[248:249], v10 offset:35328
Mavv 172,152,104
ds_read_b64_tr_b16 v[250:251], v10 offset:35584
Mavv 172,156,108
s_waitcnt lgkmcnt(12)
ds_read_b64_tr_b16 v[252:253], v10 offset:51712
Mavv 176,104,160
s_add_u32 m0, s66, 14336
s_add_u32 s72, s67, 115456
ds_read_b64_tr_b16 v[254:255], v10 offset:51968
Mavv 176,108,164
buffer_load_dwordx4 v1, s[12:15], s72 offen lds
Mavv 180,104,168
s_add_u32 s72, s76, 6291456
buffer_store_dwordx4 v[68:71], v13, s[48:51], s78 offen sc1
Mavv 180,108,172
buffer_load_dwordx4 v[80:83], v0, s[16:19], s72 offen
Mavv 184,104,176
s_add_u32 s72, s76, 7340032
Mavv 184,108,180
buffer_load_dwordx4 v[80:83], v0, s[16:19], s72 offen
Mavv 188,104,184
Mavv 188,108,188
Mavv 192,128,112
Mavv 192,132,116
Mavv 196,136,112
Mavv 196,140,116
s_waitcnt vmcnt(21) lgkmcnt(0)
Mavv 200,144,112
s_barrier
Mavv 200,148,116
Mavv 204,152,112
ds_read_b128 v[96:99], v7 offset:0
Mavv 204,156,116
Mavv 208,112,160
ds_read_b128 v[100:103], v8 offset:0
Mavv 208,116,164
s_add_u32 m0, s70, 131072
s_add_u32 s72, s68, 3670016
Mavv 212,112,168
buffer_load_dwordx4 v3, s[20:23], s72 offen lds
Mavv 212,116,172
Mavv 216,112,176
Mavv 216,116,180
Mavv 220,112,184
s_add_u32 m0, s70, 131968
s_add_u32 s72, s68, 3670016
Mavv 220,116,188
buffer_load_dwordx4 v3, s[20:23], s72 offen offset:128 lds
Mavv 224,128,120
Mavv 224,132,124
Mavv 228,136,120
ds_read_b128 v[104:107], v7 offset:2048
Mavv 228,140,124
Mavv 232,144,120
s_add_u32 m0, s70, 133120
s_add_u32 s72, s68, 3670016
ds_read_b128 v[108:111], v8 offset:2048
Mavv 232,148,124
buffer_load_dwordx4 v3, s[24:27], s72 offen lds
Mavv 236,152,120
Mavv 236,156,124
Mavv 240,120,160
Mavv 240,124,164
s_add_u32 m0, s70, 134016
s_add_u32 s72, s68, 3670016
Mavv 244,120,168
buffer_load_dwordx4 v3, s[24:27], s72 offen offset:128 lds
Mavv 244,124,172
Mavv 248,120,176
Mavv 248,124,180
Mavv 252,120,184
s_add_u32 m0, s70, 147456
s_add_u32 s72, s68, 3932160
Mavv 252,124,188
s_waitcnt lgkmcnt(3)
buffer_load_dwordx4 v3, s[20:23], s72 offen lds
Mavv 0,192,96
s_waitcnt lgkmcnt(2)
ds_read_b128 v[112:115], v7 offset:4096
Mavv 0,196,100
Mavv 4,200,96
ds_read_b128 v[116:119], v8 offset:4096
Mavv 4,204,100
s_add_u32 m0, s70, 148352
s_add_u32 s72, s68, 3932160
Mavv 8,208,96
buffer_load_dwordx4 v3, s[20:23], s72 offen offset:128 lds
Mavv 8,212,100
Mavv 12,216,96
Mavv 12,220,100
Mavv 16,96,224
s_add_u32 m0, s70, 149504
s_add_u32 s72, s68, 3932160
Mavv 16,100,228
buffer_load_dwordx4 v3, s[24:27], s72 offen lds
Mavv 20,96,232
Mavv 20,100,236
Mavv 24,96,240
Mavv 24,100,244
s_add_u32 m0, s70, 150400
s_add_u32 s72, s68, 3932160
Mavv 28,96,248
buffer_load_dwordx4 v3, s[24:27], s72 offen offset:128 lds
Mavv 28,100,252
s_waitcnt lgkmcnt(3)
Mavv 32,192,104
s_waitcnt lgkmcnt(2)
ds_read_b128 v[120:123], v7 offset:6144
Mavv 32,196,108
Mavv 36,200,104
s_add_u32 m0, s66, 32768
s_add_u32 s72, s67, 896
ds_read_b128 v[124:127], v8 offset:6144
Mavv 36,204,108
buffer_load_dwordx4 v1, s[12:15], s72 offen lds
Mavv 40,208,104
Mavv 40,212,108
Mavv 44,216,104
Mavv 44,220,108
s_add_u32 m0, s66, 34816
s_add_u32 s72, s67, 17280
Mavv 48,104,224
buffer_load_dwordx4 v1, s[12:15], s72 offen lds
Mavv 48,108,228
Mavv 52,104,232
Mavv 52,108,236
Mavv 56,104,240
s_add_u32 m0, s66, 36864
s_add_u32 s72, s67, 33664
Mavv 56,108,244
buffer_load_dwordx4 v1, s[12:15], s72 offen lds
Mavv 60,104,248
Mavv 60,108,252
s_waitcnt lgkmcnt(3)
Mavv 64,192,112
s_waitcnt lgkmcnt(2)
ds_read_b128 v[96:99], v7 offset:8192
Mavv 64,196,116
Mavv 68,200,112
s_add_u32 m0, s66, 38912
s_add_u32 s72, s67, 50048
ds_read_b128 v[100:103], v8 offset:8192
Mavv 68,204,116
buffer_load_dwordx4 v1, s[12:15], s72 offen lds
Mavv 72,208,112
Mavv 72,212,116
Mavv 76,216,112
Mavv 76,220,116
s_add_u32 m0, s66, 40960
s_add_u32 s72, s67, 66432
Mavv 80,112,224
buffer_load_dwordx4 v1, s[12:15], s72 offen lds
Mavv 80,116,228
Mavv 84,112,232
Mavv 84,116,236
Mavv 88,112,240
s_add_u32 m0, s66, 43008
s_add_u32 s72, s67, 82816
Mavv 88,116,244
buffer_load_dwordx4 v1, s[12:15], s72 offen lds
Mavv 92,112,248
Mavv 92,116,252
s_waitcnt lgkmcnt(3)
Mavv 96,192,120
s_waitcnt lgkmcnt(2)
ds_read_b128 v[104:107], v7 offset:10240
Mavv 96,196,124
Mavv 100,200,120
s_add_u32 m0, s66, 45056
s_add_u32 s72, s67, 99200
ds_read_b128 v[108:111], v8 offset:10240
Mavv 100,204,124
s_waitcnt vmcnt(25)
buffer_load_dwordx4 v1, s[12:15], s72 offen lds
Mavv 104,208,120
s_barrier
Mavv 104,212,124
ds_read_b64_tr_b16 v[128:129], v9 offset:0
Mavv 108,216,120
ds_read_b64_tr_b16 v[130:131], v9 offset:256
Mavv 108,220,124
ds_read_b64_tr_b16 v[132:133], v9 offset:16384
Mavv 112,120,224
ds_read_b64_tr_b16 v[134:135], v9 offset:16640
Mavv 112,124,228
ds_read_b64_tr_b16 v[136:137], v10 offset:0
Mavv 116,120,232
ds_read_b64_tr_b16 v[138:139], v10 offset:256
Mavv 116,124,236
ds_read_b64_tr_b16 v[140:141], v10 offset:16384
Mavv 120,120,240
ds_read_b64_tr_b16 v[142:143], v10 offset:16640
Mavv 120,124,244
ds_read_b64_tr_b16 v[144:145], v9 offset:512
Mavv 124,120,248
ds_read_b64_tr_b16 v[146:147], v9 offset:768
Mavv 124,124,252
s_waitcnt lgkmcnt(13)
ds_read_b64_tr_b16 v[148:149], v9 offset:16896
Mavv 128,192,96
s_waitcnt lgkmcnt(13)
ds_read_b128 v[112:115], v7 offset:12288
Mavv 128,196,100
ds_read_b64_tr_b16 v[150:151], v9 offset:17152
Mavv 132,200,96
s_waitcnt lgkmcnt(12)
ds_read_b128 v[116:119], v8 offset:12288
Mavv 132,204,100
ds_read_b64_tr_b16 v[152:153], v10 offset:512
Mavv 136,208,96
ds_read_b64_tr_b16 v[154:155], v10 offset:768
Mavv 136,212,100
s_waitcnt lgkmcnt(12)
ds_read_b64_tr_b16 v[156:157], v10 offset:16896
Mavv 140,216,96
ds_read_b64_tr_b16 v[158:159], v10 offset:17152
Mavv 140,220,100
ds_read_b64_tr_b16 v[160:161], v9 offset:2048
Mavv 144,96,224
s_waitcnt lgkmcnt(12)
ds_read_b64_tr_b16 v[162:163], v9 offset:2304
Mavv 144,100,228
ds_read_b64_tr_b16 v[164:165], v9 offset:18432
Mavv 148,96,232
ds_read_b64_tr_b16 v[166:167], v9 offset:18688
Mavv 148,100,236
s_waitcnt lgkmcnt(12)
ds_read_b64_tr_b16 v[168:169], v10 offset:2048
Mavv 152,96,240
ds_read_b64_tr_b16 v[170:171], v10 offset:2304
Mavv 152,100,244
ds_read_b64_tr_b16 v[172:173], v10 offset:18432
Mavv 156,96,248
s_waitcnt lgkmcnt(12)
ds_read_b64_tr_b16 v[174:175], v10 offset:18688
Mavv 156,100,252
ds_read_b64_tr_b16 v[176:177], v9 offset:2560
Mavv 160,192,104
ds_read_b128 v[120:123], v7 offset:14336
Mavv 160,196,108
s_waitcnt lgkmcnt(12)
ds_read_b64_tr_b16 v[178:179], v9 offset:2816
Mavv 164,200,104
ds_read_b128 v[124:127], v8 offset:14336
Mavv 164,204,108
ds_read_b64_tr_b16 v[180:181], v9 offset:18944
Mavv 168,208,104
s_waitcnt lgkmcnt(12)
ds_read_b64_tr_b16 v[182:183], v9 offset:19200
Mavv 168,212,108
ds_read_b64_tr_b16 v[184:185], v10 offset:2560
Mavv 172,216,104
ds_read_b64_tr_b16 v[186:187], v10 offset:2816
Mavv 172,220,108
s_waitcnt lgkmcnt(12)
ds_read_b64_tr_b16 v[188:189], v10 offset:18944
Mavv 176,104,224
s_add_u32 m0, s66, 47104
s_add_u32 s72, s67, 115584
ds_read_b64_tr_b16 v[190:191], v10 offset:19200
Mavv 176,108,228
buffer_load_dwordx4 v1, s[12:15], s72 offen lds
Mavv 180,104,232
buffer_load_dwordx4 v[32:35], v0, s[48:51], s79 offen sc1
Mavv 180,108,236
Mavv 184,104,240
Mavv 184,108,244
Mavv 188,104,248
buffer_load_dwordx4 v[36:39], v0, s[48:51], s79 offen offset:1024 sc1
Mavv 188,108,252
Mavv 192,192,112
Mavv 192,196,116
Mavv 196,200,112
buffer_load_dwordx4 v[40:43], v0, s[48:51], s79 offen offset:2048 sc1
Mavv 196,204,116
s_waitcnt vmcnt(22) lgkmcnt(0)
Mavv 200,208,112
s_barrier
Mavv 200,212,116
Mavv 204,216,112
ds_read_b128 v[96:99], v5 offset:0
Mavv 204,220,116
buffer_load_dwordx4 v[44:47], v0, s[48:51], s79 offen offset:3072 sc1
Mavv 208,112,224
ds_read_b128 v[100:103], v6 offset:0
Mavv 208,116,228
s_add_u32 m0, s71, 98304
s_add_u32 s72, s68, 0
Mavv 212,112,232
buffer_load_dwordx4 v3, s[16:19], s72 offen lds
Mavv 212,116,236
Mavv 216,112,240
Mavv 216,116,244
Mavv 220,112,248
s_add_u32 m0, s71, 99200
s_add_u32 s72, s68, 0
Mavv 220,116,252
buffer_load_dwordx4 v3, s[16:19], s72 offen offset:128 lds
Mavv 224,192,120
Mavv 224,196,124
Mavv 228,200,120
ds_read_b128 v[104:107], v5 offset:2048
Mavv 228,204,124
Mavv 232,208,120
s_add_u32 m0, s71, 106496
s_add_u32 s72, s68, 262144
ds_read_b128 v[108:111], v6 offset:2048
Mavv 232,212,124
buffer_load_dwordx4 v3, s[16:19], s72 offen lds
Mavv 236,216,120
Mavv 236,220,124
Mavv 240,120,224
Mavv 240,124,228
s_add_u32 m0, s71, 107392
s_add_u32 s72, s68, 262144
Mavv 244,120,232
buffer_load_dwordx4 v3, s[16:19], s72 offen offset:128 lds
Mavv 244,124,236
Mavv 248,120,240
Mavv 248,124,244
Mavv 252,120,248
s_add_u32 m0, s71, 114688
s_add_u32 s72, s68, 524288
Mavv 252,124,252
s_waitcnt lgkmcnt(3)
buffer_load_dwordx4 v3, s[16:19], s72 offen lds
Mavv 0,128,96
s_waitcnt lgkmcnt(2)
ds_read_b128 v[112:115], v5 offset:4096
Mavv 0,132,100
Mavv 4,136,96
ds_read_b128 v[116:119], v6 offset:4096
Mavv 4,140,100
s_add_u32 m0, s71, 115584
s_add_u32 s72, s68, 524288
Mavv 8,144,96
buffer_load_dwordx4 v3, s[16:19], s72 offen offset:128 lds
Mavv 8,148,100
Mavv 12,152,96
Mavv 12,156,100
Mavv 16,96,160
s_add_u32 m0, s71, 122880
s_add_u32 s72, s68, 786432
Mavv 16,100,164
buffer_load_dwordx4 v3, s[16:19], s72 offen lds
Mavv 20,96,168
Mavv 20,100,172
Mavv 24,96,176
Mavv 24,100,180
s_add_u32 m0, s71, 123776
s_add_u32 s72, s68, 786432
Mavv 28,96,184
buffer_load_dwordx4 v3, s[16:19], s72 offen offset:128 lds
Mavv 28,100,188
s_waitcnt lgkmcnt(3)
Mavv 32,128,104
s_waitcnt lgkmcnt(2)
ds_read_b128 v[120:123], v5 offset:6144
Mavv 32,132,108
Mavv 36,136,104
ds_read_b128 v[124:127], v6 offset:6144
Mavv 36,140,108
Mavv 40,144,104
Mavv 40,148,108
Mavv 44,152,104
Mavv 44,156,108
Mavv 48,104,160
Mavv 48,108,164
Mavv 52,104,168
Mavv 52,108,172
Mavv 56,104,176
s_waitcnt vmcnt(11)
v_xor_b32 v50, s74, v33
v_bitop3_b32 v50, v35, s75, v50 bitop3:0xbe
Mavv 56,108,180
s_waitcnt vmcnt(10)
v_bitop3_b32 v50, v37, s74, v50 bitop3:0xbe
v_bitop3_b32 v50, v39, s75, v50 bitop3:0xbe
Mavv 60,104,184
s_waitcnt vmcnt(9)
v_bitop3_b32 v50, v41, s74, v50 bitop3:0xbe
v_bitop3_b32 v50, v43, s75, v50 bitop3:0xbe
Mavv 60,108,188
s_waitcnt vmcnt(8) lgkmcnt(3)
v_bitop3_b32 v50, v45, s74, v50 bitop3:0xbe
v_bitop3_b32 v50, v47, s75, v50 bitop3:0xbe
Mavv 64,128,112
s_waitcnt lgkmcnt(2)
ds_read_b128 v[96:99], v5 offset:8192
v_cmp_ne_u32 vcc, 0, v50
Mavv 64,132,116
s_cbranch_vccnz .Lscan
.Lscan_r:
Mavv 68,136,112
ds_read_b128 v[100:103], v6 offset:8192
v_max_u32 v48, v32, v34
Mavv 68,140,116
v_min_u32 v49, v32, v34
v_med3_u32 v49, v48, v49, v36
Mavv 72,144,112
v_max_u32 v48, v48, v36
v_med3_u32 v49, v48, v49, v38
Mavv 72,148,116
v_max_u32 v48, v48, v38
v_med3_u32 v49, v48, v49, v40
)"
R"(
Mavv 76,152,112
v_max_u32 v48, v48, v40
v_med3_u32 v49, v48, v49, v42
Mavv 76,156,116
v_max_u32 v48, v48, v42
v_med3_u32 v49, v48, v49, v44
Mavv 80,112,160
v_max_u32 v48, v48, v44
v_med3_u32 v49, v48, v49, v46
Mavv 80,116,164
v_max_u32 v48, v48, v46
v_mov_b32 v50, v48
Mavv 84,112,168
Mavv 84,116,172
v_max_u32_dpp v50, v50, v50 quad_perm:[1,0,3,2] row_mask:0xf bank_mask:0xf bound_ctrl:1
Mavv 88,112,176
Mavv 88,116,180
v_max_u32_dpp v50, v50, v50 quad_perm:[2,3,0,1] row_mask:0xf bank_mask:0xf bound_ctrl:1
Mavv 92,112,184
Mavv 92,116,188
s_waitcnt lgkmcnt(3)
v_max_u32_dpp v50, v50, v50 row_half_mirror row_mask:0xf bank_mask:0xf bound_ctrl:1
Mavv 96,128,120
s_waitcnt lgkmcnt(2)
ds_read_b128 v[104:107], v5 offset:10240
Mavv 96,132,124
v_max_u32_dpp v50, v50, v50 row_mirror row_mask:0xf bank_mask:0xf bound_ctrl:1
v_mov_b32 v51, v50
Mavv 100,136,120
ds_read_b128 v[108:111], v6 offset:10240
Mavv 100,140,124
v_permlane16_swap_b32 v50, v51
v_max_u32 v50, v50, v51
Mavv 104,144,120
s_barrier
Mavv 104,148,124
ds_read_b64_tr_b16 v[192:193], v9 offset:32768
v_mov_b32 v51, v50
Mavv 108,152,120
s_cmp_eq_u32 s78, 4096
s_cselect_b32 s73, 0, 0x80000000
ds_read_b64_tr_b16 v[194:195], v9 offset:33024
Mavv 108,156,124
ds_read_b64_tr_b16 v[196:197], v9 offset:49152
v_permlane32_swap_b32 v50, v51
Mavv 112,120,160
ds_read_b64_tr_b16 v[198:199], v9 offset:49408
v_max_u32 v50, v50, v51
Mavv 112,124,164
ds_read_b64_tr_b16 v[200:201], v10 offset:32768
v_cmp_eq_u32 vcc, v50, v48
Mavv 116,120,168
ds_read_b64_tr_b16 v[202:203], v10 offset:33024
v_mov_b32 v52, s74
Mavv 116,124,172
ds_read_b64_tr_b16 v[204:205], v10 offset:49152
v_cndmask_b32 v49, v48, v49, vcc
Mavv 120,120,176
ds_read_b64_tr_b16 v[206:207], v10 offset:49408
v_mov_b32 v53, s75
Mavv 120,124,180
ds_read_b64_tr_b16 v[208:209], v9 offset:33280
v_max_u32_dpp v49, v49, v49 quad_perm:[1,0,3,2] row_mask:0xf bank_mask:0xf bound_ctrl:1
Mavv 124,120,184
ds_read_b64_tr_b16 v[210:211], v9 offset:33536
Mavv 124,124,188
s_waitcnt lgkmcnt(13)
ds_read_b64_tr_b16 v[212:213], v9 offset:49664
v_max_u32_dpp v49, v49, v49 quad_perm:[2,3,0,1] row_mask:0xf bank_mask:0xf bound_ctrl:1
Mavv 128,128,96
s_waitcnt lgkmcnt(13)
ds_read_b128 v[112:115], v5 offset:12288
Mavv 128,132,100
ds_read_b64_tr_b16 v[214:215], v9 offset:49920
v_max_u32_dpp v49, v49, v49 row_half_mirror row_mask:0xf bank_mask:0xf bound_ctrl:1
Mavv 132,136,96
s_waitcnt lgkmcnt(12)
ds_read_b128 v[116:119], v6 offset:12288
Mavv 132,140,100
ds_read_b64_tr_b16 v[216:217], v10 offset:33280
v_max_u32_dpp v49, v49, v49 row_mirror row_mask:0xf bank_mask:0xf bound_ctrl:1
Mavv 136,144,96
ds_read_b64_tr_b16 v[218:219], v10 offset:33536
v_mov_b32 v51, v49
Mavv 136,148,100
s_waitcnt lgkmcnt(12)
ds_read_b64_tr_b16 v[220:221], v10 offset:49664
Mavv 140,152,96
ds_read_b64_tr_b16 v[222:223], v10 offset:49920
v_permlane16_swap_b32 v49, v51
Mavv 140,156,100
ds_read_b64_tr_b16 v[224:225], v9 offset:34816
v_max_u32 v49, v49, v51
Mavv 144,96,160
s_waitcnt lgkmcnt(12)
ds_read_b64_tr_b16 v[226:227], v9 offset:35072
v_mov_b32 v51, v49
Mavv 144,100,164
ds_read_b64_tr_b16 v[228:229], v9 offset:51200
Mavv 148,96,168
ds_read_b64_tr_b16 v[230:231], v9 offset:51456
v_permlane32_swap_b32 v49, v51
Mavv 148,100,172
s_waitcnt lgkmcnt(12)
ds_read_b64_tr_b16 v[232:233], v10 offset:34816
v_max_u32 v49, v49, v51
Mavv 152,96,176
ds_read_b64_tr_b16 v[234:235], v10 offset:35072
v_readfirstlane_b32 s89, v50
Mavv 152,100,180
ds_read_b64_tr_b16 v[236:237], v10 offset:51200
v_readfirstlane_b32 s90, v49
Mavv 156,96,184
s_waitcnt lgkmcnt(12)
ds_read_b64_tr_b16 v[238:239], v10 offset:51456
Mavv 156,100,188
ds_read_b64_tr_b16 v[240:241], v9 offset:35328
Mavv 160,128,104
ds_read_b128 v[120:123], v5 offset:14336
Mavv 160,132,108
s_waitcnt lgkmcnt(12)
ds_read_b64_tr_b16 v[242:243], v9 offset:35584
Mavv 164,136,104
ds_read_b128 v[124:127], v6 offset:14336
Mavv 164,140,108
ds_read_b64_tr_b16 v[244:245], v9 offset:51712
Mavv 168,144,104
s_waitcnt lgkmcnt(12)
ds_read_b64_tr_b16 v[246:247], v9 offset:51968
Mavv 168,148,108
ds_read_b64_tr_b16 v[248:249], v10 offset:35328
Mavv 172,152,104
ds_read_b64_tr_b16 v[250:251], v10 offset:35584
Mavv 172,156,108
s_waitcnt lgkmcnt(12)
ds_read_b64_tr_b16 v[252:253], v10 offset:51712
Mavv 176,104,160
s_add_u32 m0, s69, 65536
s_add_u32 s72, s83, 0
ds_read_b64_tr_b16 v[254:255], v10 offset:51968
Mavv 176,108,164
buffer_load_dwordx4 v11, s[48:51], s72 offen sc1 lds
Mavv 180,104,168
buffer_store_dwordx2 v[52:53], v13, s[48:51], s73 offen sc1
Mavv 180,108,172
s_add_u32 m0, s69, 66560
s_add_u32 s72, s83, 0
Mavv 184,104,176
buffer_load_dwordx4 v12, s[48:51], s72 offen sc1 lds
Mavv 184,108,180
global_load_dwordx4 v[80:83], v0, s[62:63]
Mavv 188,104,184
s_add_u32 m0, s69, 67584
s_add_u32 s72, s83, 34816
Mavv 188,108,188
buffer_load_dwordx4 v11, s[48:51], s72 offen sc1 lds
Mavv 192,128,112
Mavv 192,132,116
s_add_u32 m0, s69, 68608
s_add_u32 s72, s83, 34816
Mavv 196,136,112
buffer_load_dwordx4 v12, s[48:51], s72 offen sc1 lds
Mavv 196,140,116
s_waitcnt lgkmcnt(0)
Mavv 200,144,112
s_barrier
Mavv 200,148,116
Mavv 204,152,112
s_add_u32 m0, s69, 69632
s_add_u32 s72, s82, 0
ds_read_b128 v[96:99], v5 offset:32768
Mavv 204,156,116
buffer_load_dwordx4 v11, s[48:51], s72 offen sc1 lds
Mavv 208,112,160
ds_read_b128 v[100:103], v6 offset:32768
Mavv 208,116,164
s_add_u32 m0, s69, 70656
s_add_u32 s72, s82, 0
Mavv 212,112,168
buffer_load_dwordx4 v12, s[48:51], s72 offen sc1 lds
Mavv 212,116,172
Mavv 216,112,176
s_add_u32 m0, s69, 71680
s_add_u32 s72, s82, 34816
Mavv 216,116,180
buffer_load_dwordx4 v11, s[48:51], s72 offen sc1 lds
Mavv 220,112,184
Mavv 220,116,188
s_add_u32 m0, s69, 72704
s_add_u32 s72, s82, 34816
Mavv 224,128,120
buffer_load_dwordx4 v12, s[48:51], s72 offen sc1 lds
Mavv 224,132,124
Mavv 228,136,120
s_add_u32 m0, s71, 131072
s_add_u32 s72, s68, 1048576
ds_read_b128 v[104:107], v5 offset:34816
Mavv 228,140,124
buffer_load_dwordx4 v3, s[16:19], s72 offen lds
Mavv 232,144,120
ds_read_b128 v[108:111], v6 offset:34816
Mavv 232,148,124
Mavv 236,152,120
Mavv 236,156,124
s_add_u32 m0, s71, 131968
s_add_u32 s72, s68, 1048576
Mavv 240,120,160
buffer_load_dwordx4 v3, s[16:19], s72 offen offset:128 lds
Mavv 240,124,164
Mavv 244,120,168
Mavv 244,124,172
Mavv 248,120,176
s_add_u32 m0, s71, 139264
s_add_u32 s72, s68, 1310720
Mavv 248,124,180
buffer_load_dwordx4 v3, s[16:19], s72 offen lds
Mavv 252,120,184
Mavv 252,124,188
s_waitcnt lgkmcnt(3)
Mavv 0,192,96
s_waitcnt lgkmcnt(2)
ds_read_b128 v[112:115], v5 offset:36864
Mavv 0,196,100
Mavv 4,200,96
s_add_u32 m0, s71, 140160
s_add_u32 s72, s68, 1310720
ds_read_b128 v[116:119], v6 offset:36864
Mavv 4,204,100
buffer_load_dwordx4 v3, s[16:19], s72 offen offset:128 lds
Mavv 8,208,96
Mavv 8,212,100
Mavv 12,216,96
Mavv 12,220,100
s_add_u32 m0, s71, 147456
s_add_u32 s72, s68, 1572864
Mavv 16,96,224
buffer_load_dwordx4 v3, s[16:19], s72 offen lds
Mavv 16,100,228
Mavv 20,96,232
Mavv 20,100,236
Mavv 24,96,240
s_add_u32 m0, s71, 148352
s_add_u32 s72, s68, 1572864
Mavv 24,100,244
buffer_load_dwordx4 v3, s[16:19], s72 offen offset:128 lds
Mavv 28,96,248
Mavv 28,100,252
s_waitcnt lgkmcnt(3)
Mavv 32,192,104
s_waitcnt lgkmcnt(2)
ds_read_b128 v[120:123], v5 offset:38912
Mavv 32,196,108
Mavv 36,200,104
s_add_u32 m0, s71, 155648
s_add_u32 s72, s68, 1835008
ds_read_b128 v[124:127], v6 offset:38912
Mavv 36,204,108
buffer_load_dwordx4 v3, s[16:19], s72 offen lds
Mavv 40,208,104
Mavv 40,212,108
Mavv 44,216,104
Mavv 44,220,108
s_add_u32 m0, s71, 156544
s_add_u32 s72, s68, 1835008
Mavv 48,104,224
buffer_load_dwordx4 v3, s[16:19], s72 offen offset:128 lds
Mavv 48,108,228
v_accvgpr_read_b32 v30, a0
v_accvgpr_read_b32 v31, a1
Mavv 52,104,232
v_mul_f32 v30, 0x3e0293ee, v30
v_mul_f32 v31, 0x3e0293ee, v31
Mavv 52,108,236
v_cvt_pk_bf16_f32 v16, v30, v31
v_accvgpr_read_b32 v30, a2
Mavv 56,104,240
s_add_u32 m0, s69, 0
s_add_u32 s72, s83, 128
v_accvgpr_read_b32 v31, a3
Mavv 56,108,244
buffer_load_dwordx4 v11, s[48:51], s72 offen sc1 lds
Mavv 60,104,248
v_mul_f32 v30, 0x3e0293ee, v30
v_mul_f32 v31, 0x3e0293ee, v31
Mavv 60,108,252
s_waitcnt lgkmcnt(3)
v_cvt_pk_bf16_f32 v17, v30, v31
v_accvgpr_read_b32 v30, a4
Mavv 64,192,112
s_waitcnt lgkmcnt(2)
ds_read_b128 v[96:99], v5 offset:40960
v_accvgpr_read_b32 v31, a5
Mavv 64,196,116
v_mul_f32 v30, 0x3e0293ee, v30
v_mul_f32 v31, 0x3e0293ee, v31
Mavv 68,200,112
s_add_u32 m0, s69, 1024
s_add_u32 s72, s83, 128
ds_read_b128 v[100:103], v6 offset:40960
Mavv 68,204,116
buffer_load_dwordx4 v12, s[48:51], s72 offen sc1 lds
Mavv 72,208,112
v_cvt_pk_bf16_f32 v18, v30, v31
v_accvgpr_read_b32 v30, a6
Mavv 72,212,116
v_accvgpr_read_b32 v31, a7
v_mul_f32 v30, 0x3e0293ee, v30
Mavv 76,216,112
v_mul_f32 v31, 0x3e0293ee, v31
v_cvt_pk_bf16_f32 v19, v30, v31
Mavv 76,220,116
s_add_u32 m0, s69, 2048
s_add_u32 s72, s83, 34944
s_add_u32 s73, s80, 0
Mavv 80,112,224
buffer_load_dwordx4 v11, s[48:51], s72 offen sc1 lds
Mavv 80,116,228
buffer_store_dwordx4 v[16:19], v0, s[48:51], s73 offen sc1
Mavv 84,112,232
v_accvgpr_read_b32 v30, a8
v_accvgpr_read_b32 v31, a9
Mavv 84,116,236
v_mul_f32 v30, 0x3e0293ee, v30
v_mul_f32 v31, 0x3e0293ee, v31
Mavv 88,112,240
s_add_u32 m0, s69, 3072
s_add_u32 s72, s83, 34944
v_cvt_pk_bf16_f32 v20, v30, v31
Mavv 88,116,244
buffer_load_dwordx4 v12, s[48:51], s72 offen sc1 lds
Mavv 92,112,248
v_accvgpr_read_b32 v30, a10
v_accvgpr_read_b32 v31, a11
Mavv 92,116,252
s_waitcnt lgkmcnt(3)
v_mul_f32 v30, 0x3e0293ee, v30
v_mul_f32 v31, 0x3e0293ee, v31
Mavv 96,192,120
s_waitcnt lgkmcnt(2)
ds_read_b128 v[104:107], v5 offset:43008
v_cvt_pk_bf16_f32 v21, v30, v31
Mavv 96,196,124
v_accvgpr_read_b32 v30, a12
v_accvgpr_read_b32 v31, a13
Mavv 100,200,120
s_add_u32 m0, s69, 4096
s_add_u32 s72, s82, 128
ds_read_b128 v[108:111], v6 offset:43008
Mavv 100,204,124
s_waitcnt vmcnt(13)
buffer_load_dwordx4 v11, s[48:51], s72 offen sc1 lds
Mavv 104,208,120
v_mul_f32 v30, 0x3e0293ee, v30
v_mul_f32 v31, 0x3e0293ee, v31
Mavv 104,212,124
ds_read_b128 v[32:35], v24 offset:0
v_cvt_pk_bf16_f32 v22, v30, v31
Mavv 108,216,120
v_accvgpr_read_b32 v30, a14
v_accvgpr_read_b32 v31, a15
Mavv 108,220,124
s_add_u32 m0, s69, 5120
s_add_u32 s72, s82, 128
ds_read_b128 v[36:39], v25 offset:0
Mavv 112,120,224
buffer_load_dwordx4 v12, s[48:51], s72 offen sc1 lds
Mavv 112,124,228
ds_read_b128 v[40:43], v24 offset:2048
v_mul_f32 v30, 0x3e0293ee, v30
Mavv 116,120,232
v_mul_f32 v31, 0x3e0293ee, v31
v_cvt_pk_bf16_f32 v23, v30, v31
Mavv 116,124,236
s_add_u32 s73, s80, 1024
ds_read_b128 v[44:47], v25 offset:2048
Mavv 120,120,240
buffer_store_dwordx4 v[20:23], v0, s[48:51], s73 offen sc1
Mavv 120,124,244
s_add_u32 m0, s69, 6144
s_add_u32 s72, s82, 34944
ds_read_b128 v[48:51], v24 offset:4096
Mavv 124,120,248
buffer_load_dwordx4 v11, s[48:51], s72 offen sc1 lds
Mavv 124,124,252
s_waitcnt lgkmcnt(8)
ds_read_b128 v[52:55], v25 offset:4096
v_accvgpr_read_b32 v30, a32
Mavv 128,192,96
s_waitcnt lgkmcnt(8)
ds_read_b128 v[112:115], v5 offset:45056
v_accvgpr_read_b32 v31, a33
Mavv 128,196,100
ds_read_b128 v[56:59], v24 offset:6144
v_mul_f32 v30, 0x3e0293ee, v30
Mavv 132,200,96
ds_read_b128 v[116:119], v6 offset:45056
v_mul_f32 v31, 0x3e0293ee, v31
Mavv 132,204,100
s_add_u32 m0, s69, 7168
s_add_u32 s72, s82, 34944
ds_read_b128 v[60:63], v25 offset:6144
Mavv 136,208,96
buffer_load_dwordx4 v12, s[48:51], s72 offen sc1 lds
Mavv 136,212,100
v_cvt_pk_bf16_f32 v16, v30, v31
v_accvgpr_read_b32 v30, a34
Mavv 140,216,96
v_accvgpr_read_b32 v31, a35
v_mul_f32 v30, 0x3e0293ee, v30
Mavv 140,220,100
v_mul_f32 v31, 0x3e0293ee, v31
v_cvt_pk_bf16_f32 v17, v30, v31
Mavv 144,96,224
v_accvgpr_read_b32 v30, a36
v_accvgpr_read_b32 v31, a37
Mavv 144,100,228
v_mul_f32 v30, 0x3e0293ee, v30
v_mul_f32 v31, 0x3e0293ee, v31
Mavv 148,96,232
v_cvt_pk_bf16_f32 v18, v30, v31
v_accvgpr_read_b32 v30, a38
Mavv 148,100,236
v_accvgpr_read_b32 v31, a39
v_mul_f32 v30, 0x3e0293ee, v30
Mavv 152,96,240
v_mul_f32 v31, 0x3e0293ee, v31
v_cvt_pk_bf16_f32 v19, v30, v31
Mavv 152,100,244
s_add_u32 s73, s80, 4096
buffer_store_dwordx4 v[16:19], v0, s[48:51], s73 offen sc1
Mavv 156,96,248
v_accvgpr_read_b32 v30, a40
)"
R"(
v_accvgpr_read_b32 v31, a41
Mavv 156,100,252
s_waitcnt lgkmcnt(11)
v_mul_f32 v30, 0x3e0293ee, v30
v_mul_f32 v31, 0x3e0293ee, v31
Mavv 160,192,104
s_waitcnt lgkmcnt(10)
ds_read_b128 v[120:123], v5 offset:47104
v_cvt_pk_bf16_f32 v20, v30, v31
Mavv 160,196,108
v_accvgpr_read_b32 v30, a42
v_accvgpr_read_b32 v31, a43
Mavv 164,200,104
ds_read_b128 v[124:127], v6 offset:47104
v_mul_f32 v30, 0x3e0293ee, v30
Mavv 164,204,108
v_mul_f32 v31, 0x3e0293ee, v31
v_cvt_pk_bf16_f32 v21, v30, v31
Mavv 168,208,104
v_accvgpr_read_b32 v30, a44
v_accvgpr_read_b32 v31, a45
Mavv 168,212,108
v_mul_f32 v30, 0x3e0293ee, v30
v_mul_f32 v31, 0x3e0293ee, v31
Mavv 172,216,104
v_cvt_pk_bf16_f32 v22, v30, v31
v_accvgpr_read_b32 v30, a46
Mavv 172,220,108
v_accvgpr_read_b32 v31, a47
v_mul_f32 v30, 0x3e0293ee, v30
Mavv 176,104,224
v_subrev_u32 v5, 0x10000, v24
v_subrev_u32 v6, 0x10000, v25
Mavv 176,108,228
v_mov_b32 v7, v24
v_mov_b32 v8, v25
Mavv 180,104,232
v_mul_f32 v31, 0x3e0293ee, v31
v_cvt_pk_bf16_f32 v23, v30, v31
Mavv 180,108,236
s_add_u32 s73, s80, 5120
buffer_store_dwordx4 v[20:23], v0, s[48:51], s73 offen sc1
Mavv 184,104,240
v_accvgpr_read_b32 v30, a16
v_accvgpr_read_b32 v31, a17
Mavv 184,108,244
v_cvt_pk_bf16_f32 v16, v30, v31
v_accvgpr_read_b32 v30, a18
Mavv 188,104,248
v_accvgpr_read_b32 v31, a19
v_cvt_pk_bf16_f32 v17, v30, v31
Mavv 188,108,252
s_waitcnt lgkmcnt(5)
v_accvgpr_read_b32 v30, a48
v_accvgpr_read_b32 v31, a49
Mavv 192,192,112
s_waitcnt lgkmcnt(3)
v_cvt_pk_bf16_f32 v18, v30, v31
v_accvgpr_read_b32 v30, a50
Mavv 192,196,116
v_accvgpr_read_b32 v31, a51
v_cvt_pk_bf16_f32 v19, v30, v31
Mavv 196,200,112
s_add_u32 s73, s81, 0
buffer_store_dwordx4 v[16:19], v0, s[48:51], s73 offen sc1
Mavv 196,204,116
s_waitcnt lgkmcnt(0)
v_accvgpr_read_b32 v30, a20
v_accvgpr_read_b32 v31, a21
Mavv 200,208,112
s_barrier
Mavv 200,212,116
v_cvt_pk_bf16_f32 v20, v30, v31
v_accvgpr_read_b32 v30, a22
Mavv 204,216,112
ds_read_b64_tr_b16 v[96:97], v26 offset:0
v_accvgpr_read_b32 v31, a23
Mavv 204,220,116
v_cvt_pk_bf16_f32 v21, v30, v31
v_accvgpr_read_b32 v30, a52
Mavv 208,112,224
ds_read_b64_tr_b16 v[98:99], v26 offset:256
v_accvgpr_read_b32 v31, a53
Mavv 208,116,228
v_cvt_pk_bf16_f32 v22, v30, v31
v_accvgpr_read_b32 v30, a54
Mavv 212,112,232
s_add_u32 m0, s69, 32768
s_add_u32 s72, s83, 256
ds_read_b64_tr_b16 v[100:101], v26 offset:8192
Mavv 212,116,236
buffer_load_dwordx4 v11, s[48:51], s72 offen sc1 lds
Mavv 216,112,240
ds_read_b64_tr_b16 v[102:103], v26 offset:8448
v_accvgpr_read_b32 v31, a55
Mavv 216,116,244
s_add_u32 m0, s69, 33792
s_add_u32 s72, s83, 256
v_cvt_pk_bf16_f32 v23, v30, v31
Mavv 220,112,248
s_add_u32 s73, s81, 2048
buffer_load_dwordx4 v12, s[48:51], s72 offen sc1 lds
Mavv 220,116,252
buffer_store_dwordx4 v[20:23], v0, s[48:51], s73 offen sc1
Mavv 224,192,120
s_add_u32 m0, s69, 34816
s_add_u32 s72, s83, 35072
v_accvgpr_read_b32 v30, a24
Mavv 224,196,124
buffer_load_dwordx4 v11, s[48:51], s72 offen sc1 lds
Mavv 228,200,120
ds_read_b64_tr_b16 v[104:105], v27 offset:0
v_accvgpr_read_b32 v31, a25
Mavv 228,204,124
v_cvt_pk_bf16_f32 v16, v30, v31
v_accvgpr_read_b32 v30, a26
Mavv 232,208,120
s_add_u32 m0, s69, 35840
s_add_u32 s72, s83, 35072
ds_read_b64_tr_b16 v[106:107], v27 offset:256
Mavv 232,212,124
buffer_load_dwordx4 v12, s[48:51], s72 offen sc1 lds
Mavv 236,216,120
ds_read_b64_tr_b16 v[108:109], v27 offset:8192
v_accvgpr_read_b32 v31, a27
Mavv 236,220,124
v_cvt_pk_bf16_f32 v17, v30, v31
v_accvgpr_read_b32 v30, a56
Mavv 240,120,224
s_add_u32 m0, s69, 36864
s_add_u32 s72, s82, 256
ds_read_b64_tr_b16 v[110:111], v27 offset:8448
Mavv 240,124,228
buffer_load_dwordx4 v11, s[48:51], s72 offen sc1 lds
Mavv 244,120,232
v_accvgpr_read_b32 v31, a57
v_cvt_pk_bf16_f32 v18, v30, v31
Mavv 244,124,236
s_add_u32 m0, s69, 37888
s_add_u32 s72, s82, 256
v_accvgpr_read_b32 v30, a58
Mavv 248,120,240
buffer_load_dwordx4 v12, s[48:51], s72 offen sc1 lds
Mavv 248,124,244
v_accvgpr_read_b32 v31, a59
v_cvt_pk_bf16_f32 v19, v30, v31
Mavv 252,120,248
s_add_u32 m0, s69, 38912
s_add_u32 s72, s82, 35072
s_add_u32 s73, s81, 4096
Mavv 252,124,252
s_waitcnt lgkmcnt(6)
buffer_load_dwordx4 v11, s[48:51], s72 offen sc1 lds
Zvvv 128,96,32
s_waitcnt lgkmcnt(4)
ds_read_b64_tr_b16 v[112:113], v26 offset:512
Mvvv 128,100,36
s_add_u32 m0, s69, 39936
s_add_u32 s72, s82, 35072
Zvvv 160,96,40
buffer_load_dwordx4 v12, s[48:51], s72 offen sc1 lds
ds_read_b64_tr_b16 v[114:115], v26 offset:768
Mvvv 160,100,44
buffer_store_dwordx4 v[16:19], v0, s[48:51], s73 offen sc1
Zvvv 192,96,48
ds_read_b64_tr_b16 v[116:117], v26 offset:8704
v_accvgpr_read_b32 v30, a28
Mvvv 192,100,52
v_accvgpr_read_b32 v31, a29
v_cvt_pk_bf16_f32 v20, v30, v31
Zvvv 224,96,56
s_waitcnt vmcnt(13)
ds_read_b64_tr_b16 v[118:119], v26 offset:8960
v_accvgpr_read_b32 v30, a30
Mvvv 224,100,60
s_waitcnt lgkmcnt(6)
ds_read_b128 v[64:67], v5 offset:0
v_accvgpr_read_b32 v31, a31
Zvvv 132,104,32
s_waitcnt lgkmcnt(5)
ds_read_b64_tr_b16 v[120:121], v27 offset:512
v_cvt_pk_bf16_f32 v21, v30, v31
Mvvv 132,108,36
ds_read_b128 v[68:71], v6 offset:0
v_accvgpr_read_b32 v30, a60
Zvvv 164,104,40
ds_read_b64_tr_b16 v[122:123], v27 offset:768
v_accvgpr_read_b32 v31, a61
Mvvv 164,108,44
ds_read_b128 v[72:75], v5 offset:2048
v_cvt_pk_bf16_f32 v22, v30, v31
Zvvv 196,104,48
ds_read_b64_tr_b16 v[124:125], v27 offset:8704
v_accvgpr_read_b32 v30, a62
Mvvv 196,108,52
ds_read_b128 v[76:79], v6 offset:2048
v_accvgpr_read_b32 v31, a63
Zvvv 228,104,56
ds_read_b64_tr_b16 v[126:127], v27 offset:8960
v_cvt_pk_bf16_f32 v23, v30, v31
Mvvv 228,108,60
s_waitcnt lgkmcnt(10)
s_add_u32 s73, s81, 6144
ds_read_b128 v[80:83], v5 offset:4096
Zvvv 136,112,32
s_waitcnt lgkmcnt(9)
ds_read_b64_tr_b16 v[96:97], v26 offset:1024
Mvvv 136,116,36
ds_read_b128 v[84:87], v6 offset:4096
Zvvv 168,112,40
ds_read_b64_tr_b16 v[98:99], v26 offset:1280
Mvvv 168,116,44
ds_read_b128 v[88:91], v5 offset:6144
Zvvv 200,112,48
s_lshl_b32 s63, s61, 1
s_mul_i32 s62, s64, 1114112
ds_read_b64_tr_b16 v[100:101], v26 offset:9216
Mvvv 200,116,52
s_add_u32 s62, s62, s70
s_add_u32 s62, s62, 16777216
ds_read_b128 v[92:95], v6 offset:6144
Zvvv 232,112,56
s_waitcnt lgkmcnt(12)
ds_read_b64_tr_b16 v[102:103], v26 offset:9472
v_lshrrev_b32 v1, 4, v0
Mvvv 232,116,60
s_waitcnt lgkmcnt(8)
buffer_store_dwordx4 v[20:23], v0, s[48:51], s73 offen sc1
Zvvv 140,120,32
s_add_u32 m0, s69, 65536
s_add_u32 s72, s83, 384
ds_read_b64_tr_b16 v[104:105], v27 offset:1024
Mvvv 140,124,36
buffer_load_dwordx4 v11, s[48:51], s72 offen sc1 lds
Zvvv 172,120,40
ds_read_b64_tr_b16 v[106:107], v27 offset:1280
v_accvgpr_read_b32 v30, a64
Mvvv 172,124,44
v_accvgpr_read_b32 v31, a65
v_mul_f32 v30, 0x3e0293ee, v30
Zvvv 204,120,48
s_add_u32 m0, s69, 66560
s_add_u32 s72, s83, 384
ds_read_b64_tr_b16 v[108:109], v27 offset:9216
Mvvv 204,124,52
buffer_load_dwordx4 v12, s[48:51], s72 offen sc1 lds
Zvvv 236,120,56
ds_read_b64_tr_b16 v[110:111], v27 offset:9472
v_mul_f32 v31, 0x3e0293ee, v31
Mvvv 236,124,60
s_waitcnt lgkmcnt(4)
v_cvt_pk_bf16_f32 v16, v30, v31
v_accvgpr_read_b32 v30, a66
Zvvv 144,96,32
s_add_u32 m0, s69, 67584
s_add_u32 s72, s83, 35200
ds_read_b64_tr_b16 v[112:113], v26 offset:1536
Mvvv 144,100,36
buffer_load_dwordx4 v11, s[48:51], s72 offen sc1 lds
Zvvv 176,96,40
ds_read_b64_tr_b16 v[114:115], v26 offset:1792
v_accvgpr_read_b32 v31, a67
Mvvv 176,100,44
v_mul_f32 v30, 0x3e0293ee, v30
v_mul_f32 v31, 0x3e0293ee, v31
Zvvv 208,96,48
s_add_u32 m0, s69, 68608
s_add_u32 s72, s83, 35200
ds_read_b64_tr_b16 v[116:117], v26 offset:9728
Mvvv 208,100,52
buffer_load_dwordx4 v12, s[48:51], s72 offen sc1 lds
Zvvv 240,96,56
ds_read_b64_tr_b16 v[118:119], v26 offset:9984
v_cvt_pk_bf16_f32 v17, v30, v31
Mvvv 240,100,60
s_waitcnt lgkmcnt(4)
v_accvgpr_read_b32 v30, a68
v_accvgpr_read_b32 v31, a69
Zvvv 148,104,32
s_add_u32 m0, s69, 69632
s_add_u32 s72, s82, 384
ds_read_b64_tr_b16 v[120:121], v27 offset:1536
Mvvv 148,108,36
buffer_load_dwordx4 v11, s[48:51], s72 offen sc1 lds
Zvvv 180,104,40
ds_read_b64_tr_b16 v[122:123], v27 offset:1792
v_mul_f32 v30, 0x3e0293ee, v30
Mvvv 180,108,44
v_mul_f32 v31, 0x3e0293ee, v31
v_cvt_pk_bf16_f32 v18, v30, v31
Zvvv 212,104,48
s_add_u32 m0, s69, 70656
s_add_u32 s72, s82, 384
ds_read_b64_tr_b16 v[124:125], v27 offset:9728
Mvvv 212,108,52
buffer_load_dwordx4 v12, s[48:51], s72 offen sc1 lds
Zvvv 244,104,56
s_waitcnt lgkmcnt(5)
ds_read_b64_tr_b16 v[126:127], v27 offset:9984
v_accvgpr_read_b32 v30, a70
Mvvv 244,108,60
s_add_u32 m0, s69, 71680
s_add_u32 s72, s82, 35200
v_accvgpr_read_b32 v31, a71
Zvvv 152,112,32
s_waitcnt lgkmcnt(4)
buffer_load_dwordx4 v11, s[48:51], s72 offen sc1 lds
Mvvv 152,116,36
v_mul_f32 v30, 0x3e0293ee, v30
v_mul_f32 v31, 0x3e0293ee, v31
Zvvv 184,112,40
s_add_u32 m0, s69, 72704
s_add_u32 s72, s82, 35200
v_cvt_pk_bf16_f32 v19, v30, v31
Mvvv 184,116,44
s_add_u32 s73, s80, 8192
buffer_load_dwordx4 v12, s[48:51], s72 offen sc1 lds
Zvvv 216,112,48
buffer_store_dwordx4 v[16:19], v0, s[48:51], s73 offen sc1
Mvvv 216,116,52
s_waitcnt lgkmcnt(0)
v_accvgpr_read_b32 v30, a72
v_accvgpr_read_b32 v31, a73
Zvvv 248,112,56
s_barrier
Mvvv 248,116,60
ds_read_b64_tr_b16 v[96:97], v26 offset:16384
v_mul_f32 v30, 0x3e0293ee, v30
Zvvv 156,120,32
ds_read_b64_tr_b16 v[98:99], v26 offset:16640
v_mul_f32 v31, 0x3e0293ee, v31
Mvvv 156,124,36
ds_read_b64_tr_b16 v[100:101], v26 offset:24576
v_cvt_pk_bf16_f32 v20, v30, v31
Zvvv 188,120,40
ds_read_b64_tr_b16 v[102:103], v26 offset:24832
v_accvgpr_read_b32 v30, a74
Mvvv 188,124,44
v_accvgpr_read_b32 v31, a75
v_mul_f32 v30, 0x3e0293ee, v30
Zvvv 220,120,48
ds_read_b64_tr_b16 v[104:105], v27 offset:16384
v_mul_f32 v31, 0x3e0293ee, v31
Mvvv 220,124,52
ds_read_b64_tr_b16 v[106:107], v27 offset:16640
v_cvt_pk_bf16_f32 v21, v30, v31
Zvvv 252,120,56
ds_read_b64_tr_b16 v[108:109], v27 offset:24576
v_accvgpr_read_b32 v30, a76
Mvvv 252,124,60
s_waitcnt lgkmcnt(5)
ds_read_b64_tr_b16 v[110:111], v27 offset:24832
v_accvgpr_read_b32 v31, a77
Mvvv 128,96,64
s_waitcnt lgkmcnt(4)
s_add_u32 m0, s69, 0
s_add_u32 s72, s83, 512
ds_read_b64_tr_b16 v[112:113], v26 offset:16896
Mvvv 128,100,68
buffer_load_dwordx4 v11, s[48:51], s72 offen sc1 lds
Mvvv 160,96,72
ds_read_b64_tr_b16 v[114:115], v26 offset:17152
v_mul_f32 v30, 0x3e0293ee, v30
Mvvv 160,100,76
v_mul_f32 v31, 0x3e0293ee, v31
v_cvt_pk_bf16_f32 v22, v30, v31
Mvvv 192,96,80
s_add_u32 m0, s69, 1024
s_add_u32 s72, s83, 512
ds_read_b64_tr_b16 v[116:117], v26 offset:25088
Mvvv 192,100,84
buffer_load_dwordx4 v12, s[48:51], s72 offen sc1 lds
Mvvv 224,96,88
s_waitcnt vmcnt(13)
ds_read_b64_tr_b16 v[118:119], v26 offset:25344
v_accvgpr_read_b32 v30, a78
Mvvv 224,100,92
s_waitcnt lgkmcnt(6)
ds_read_b128 v[32:35], v5 offset:32768
v_accvgpr_read_b32 v31, a79
Mvvv 132,104,64
s_waitcnt lgkmcnt(5)
ds_read_b64_tr_b16 v[120:121], v27 offset:16896
v_mul_f32 v30, 0x3e0293ee, v30
Mvvv 132,108,68
ds_read_b128 v[36:39], v6 offset:32768
v_mul_f32 v31, 0x3e0293ee, v31
Mvvv 164,104,72
ds_read_b64_tr_b16 v[122:123], v27 offset:17152
v_cvt_pk_bf16_f32 v23, v30, v31
Mvvv 164,108,76
s_add_u32 s73, s80, 9216
ds_read_b128 v[40:43], v5 offset:34816
v_add_u32 v1, 4, v1
Mvvv 196,104,80
ds_read_b64_tr_b16 v[124:125], v27 offset:25088
v_and_b32 v1, 63, v1
Mvvv 196,108,84
ds_read_b128 v[44:47], v6 offset:34816
v_subrev_u32 v2, s63, v1
Mvvv 228,104,88
ds_read_b64_tr_b16 v[126:127], v27 offset:25344
v_add_u32 v4, -2, v1
Mvvv 228,108,92
s_waitcnt lgkmcnt(10)
ds_read_b128 v[48:51], v5 offset:36864
v_sub_u32 v9, 33, v1
Mvvv 136,112,64
s_waitcnt lgkmcnt(9)
ds_read_b64_tr_b16 v[96:97], v26 offset:17408
v_cmp_eq_u32 vcc, 3, v2
Mvvv 136,116,68
ds_read_b128 v[52:55], v6 offset:36864
Mvvv 168,112,72
ds_read_b64_tr_b16 v[98:99], v26 offset:17664
v_cndmask_b32 v4, v4, v9, vcc
Mvvv 168,116,76
ds_read_b128 v[56:59], v5 offset:38912
v_add_u32 v9, -1, v1
Mvvv 200,112,80
ds_read_b64_tr_b16 v[100:101], v26 offset:25600
v_cmp_eq_u32 vcc, 2, v2
Mvvv 200,116,84
s_waitcnt lgkmcnt(11)
ds_read_b128 v[60:63], v6 offset:38912
Mvvv 232,112,88
s_add_u32 m0, s69, 2048
s_add_u32 s72, s83, 35328
ds_read_b64_tr_b16 v[102:103], v26 offset:25856
Mvvv 232,116,92
s_waitcnt lgkmcnt(12)
)"
R"(
buffer_load_dwordx4 v11, s[48:51], s72 offen sc1 lds
Mvvv 140,120,64
s_waitcnt lgkmcnt(8)
ds_read_b64_tr_b16 v[104:105], v27 offset:17408
v_cndmask_b32 v4, v4, v9, vcc
Mvvv 140,124,68
buffer_store_dwordx4 v[20:23], v0, s[48:51], s73 offen sc1
Mvvv 172,120,72
s_add_u32 m0, s69, 3072
s_add_u32 s72, s83, 35328
ds_read_b64_tr_b16 v[106:107], v27 offset:17664
Mvvv 172,124,76
buffer_load_dwordx4 v12, s[48:51], s72 offen sc1 lds
Mvvv 204,120,80
ds_read_b64_tr_b16 v[108:109], v27 offset:25600
v_accvgpr_read_b32 v30, a96
Mvvv 204,124,84
v_accvgpr_read_b32 v31, a97
v_mul_f32 v30, 0x3e0293ee, v30
Mvvv 236,120,88
s_add_u32 m0, s69, 4096
s_add_u32 s72, s82, 512
ds_read_b64_tr_b16 v[110:111], v27 offset:25856
Mvvv 236,124,92
s_waitcnt lgkmcnt(4)
buffer_load_dwordx4 v11, s[48:51], s72 offen sc1 lds
Mvvv 144,96,64
s_add_u32 m0, s71, 98304
s_add_u32 s72, s68, 2097152
ds_read_b64_tr_b16 v[112:113], v26 offset:17920
Mvvv 144,100,68
buffer_load_dwordx4 v3, s[16:19], s72 offen lds
Mvvv 176,96,72
s_add_u32 m0, s69, 5120
s_add_u32 s72, s82, 512
ds_read_b64_tr_b16 v[114:115], v26 offset:18176
Mvvv 176,100,76
buffer_load_dwordx4 v12, s[48:51], s72 offen sc1 lds
Mvvv 208,96,80
s_add_u32 m0, s71, 99200
s_add_u32 s72, s68, 2097152
ds_read_b64_tr_b16 v[116:117], v26 offset:26112
Mvvv 208,100,84
buffer_load_dwordx4 v3, s[16:19], s72 offen offset:128 lds
Mvvv 240,96,88
s_add_u32 m0, s69, 6144
s_add_u32 s72, s82, 35328
ds_read_b64_tr_b16 v[118:119], v26 offset:26368
Mvvv 240,100,92
s_waitcnt lgkmcnt(4)
buffer_load_dwordx4 v11, s[48:51], s72 offen sc1 lds
Mvvv 148,104,64
s_add_u32 m0, s71, 106496
s_add_u32 s72, s68, 2359296
ds_read_b64_tr_b16 v[120:121], v27 offset:17920
Mvvv 148,108,68
buffer_load_dwordx4 v3, s[16:19], s72 offen lds
Mvvv 180,104,72
s_add_u32 m0, s69, 7168
s_add_u32 s72, s82, 35328
ds_read_b64_tr_b16 v[122:123], v27 offset:18176
Mvvv 180,108,76
buffer_load_dwordx4 v12, s[48:51], s72 offen sc1 lds
Mvvv 212,104,80
s_add_u32 m0, s71, 107392
s_add_u32 s72, s68, 2359296
ds_read_b64_tr_b16 v[124:125], v27 offset:26112
Mvvv 212,108,84
buffer_load_dwordx4 v3, s[16:19], s72 offen offset:128 lds
Mvvv 244,104,88
ds_read_b64_tr_b16 v[126:127], v27 offset:26368
v_mul_f32 v31, 0x3e0293ee, v31
Mvvv 244,108,92
s_waitcnt lgkmcnt(6)
v_cvt_pk_bf16_f32 v16, v30, v31
v_accvgpr_read_b32 v30, a98
Mvvv 152,112,64
s_waitcnt lgkmcnt(4)
v_accvgpr_read_b32 v31, a99
v_mul_f32 v30, 0x3e0293ee, v30
Mvvv 152,116,68
v_mul_f32 v31, 0x3e0293ee, v31
v_cvt_pk_bf16_f32 v17, v30, v31
Mvvv 184,112,72
v_accvgpr_read_b32 v30, a100
v_accvgpr_read_b32 v31, a101
Mvvv 184,116,76
v_mul_f32 v30, 0x3e0293ee, v30
v_mul_f32 v31, 0x3e0293ee, v31
Mvvv 216,112,80
v_cvt_pk_bf16_f32 v18, v30, v31
v_accvgpr_read_b32 v30, a102
Mvvv 216,116,84
s_waitcnt lgkmcnt(0)
v_accvgpr_read_b32 v31, a103
v_mul_f32 v30, 0x3e0293ee, v30
Mvvv 248,112,88
s_barrier
Mvvv 248,116,92
ds_read_b64_tr_b16 v[96:97], v26 offset:32768
v_mul_f32 v31, 0x3e0293ee, v31
Mvvv 156,120,64
ds_read_b64_tr_b16 v[98:99], v26 offset:33024
v_cvt_pk_bf16_f32 v19, v30, v31
Mvvv 156,124,68
s_add_u32 s73, s80, 12288
ds_read_b64_tr_b16 v[100:101], v26 offset:40960
v_sub_u32 v9, 32, v1
Mvvv 188,120,72
ds_read_b64_tr_b16 v[102:103], v26 offset:41216
v_cmp_eq_u32 vcc, 1, v2
Mvvv 188,124,76
buffer_store_dwordx4 v[16:19], v0, s[48:51], s73 offen sc1
Mvvv 220,120,80
ds_read_b64_tr_b16 v[104:105], v27 offset:32768
v_accvgpr_read_b32 v30, a104
Mvvv 220,124,84
ds_read_b64_tr_b16 v[106:107], v27 offset:33024
v_accvgpr_read_b32 v31, a105
Mvvv 252,120,88
ds_read_b64_tr_b16 v[108:109], v27 offset:40960
v_mul_f32 v30, 0x3e0293ee, v30
Mvvv 252,124,92
s_waitcnt lgkmcnt(5)
ds_read_b64_tr_b16 v[110:111], v27 offset:41216
v_mul_f32 v31, 0x3e0293ee, v31
Mvvv 128,96,32
s_waitcnt lgkmcnt(4)
s_add_u32 m0, s69, 32768
s_add_u32 s72, s83, 640
ds_read_b64_tr_b16 v[112:113], v26 offset:33280
Mvvv 128,100,36
buffer_load_dwordx4 v11, s[48:51], s72 offen sc1 lds
Mvvv 160,96,40
ds_read_b64_tr_b16 v[114:115], v26 offset:33536
v_cvt_pk_bf16_f32 v20, v30, v31
Mvvv 160,100,44
v_accvgpr_read_b32 v30, a106
v_accvgpr_read_b32 v31, a107
Mvvv 192,96,48
s_add_u32 m0, s69, 33792
s_add_u32 s72, s83, 640
ds_read_b64_tr_b16 v[116:117], v26 offset:41472
Mvvv 192,100,52
buffer_load_dwordx4 v12, s[48:51], s72 offen sc1 lds
Mvvv 224,96,56
s_waitcnt vmcnt(17)
ds_read_b64_tr_b16 v[118:119], v26 offset:41728
v_mul_f32 v30, 0x3e0293ee, v30
Mvvv 224,100,60
s_waitcnt lgkmcnt(6)
ds_read_b128 v[64:67], v7 offset:0
v_mul_f32 v31, 0x3e0293ee, v31
Mvvv 132,104,32
s_waitcnt lgkmcnt(5)
ds_read_b64_tr_b16 v[120:121], v27 offset:33280
v_cvt_pk_bf16_f32 v21, v30, v31
Mvvv 132,108,36
ds_read_b128 v[68:71], v8 offset:0
v_accvgpr_read_b32 v30, a108
Mvvv 164,104,40
ds_read_b64_tr_b16 v[122:123], v27 offset:33536
v_accvgpr_read_b32 v31, a109
Mvvv 164,108,44
ds_read_b128 v[72:75], v7 offset:2048
v_mul_f32 v30, 0x3e0293ee, v30
Mvvv 196,104,48
ds_read_b64_tr_b16 v[124:125], v27 offset:41472
v_mul_f32 v31, 0x3e0293ee, v31
Mvvv 196,108,52
ds_read_b128 v[76:79], v8 offset:2048
v_cvt_pk_bf16_f32 v22, v30, v31
Mvvv 228,104,56
ds_read_b64_tr_b16 v[126:127], v27 offset:41728
v_accvgpr_read_b32 v30, a110
Mvvv 228,108,60
s_waitcnt lgkmcnt(10)
ds_read_b128 v[80:83], v7 offset:4096
v_accvgpr_read_b32 v31, a111
Mvvv 136,112,32
s_waitcnt lgkmcnt(9)
ds_read_b64_tr_b16 v[96:97], v26 offset:33792
v_mul_f32 v30, 0x3e0293ee, v30
Mvvv 136,116,36
ds_read_b128 v[84:87], v8 offset:4096
v_mul_f32 v31, 0x3e0293ee, v31
Mvvv 168,112,40
ds_read_b64_tr_b16 v[98:99], v26 offset:34048
v_cvt_pk_bf16_f32 v23, v30, v31
Mvvv 168,116,44
s_add_u32 s73, s80, 13312
ds_read_b128 v[88:91], v7 offset:6144
v_cndmask_b32 v4, v4, v9, vcc
Mvvv 200,112,48
ds_read_b64_tr_b16 v[100:101], v26 offset:41984
v_cmp_gt_i32 vcc, 1, v2
Mvvv 200,116,52
s_waitcnt lgkmcnt(11)
ds_read_b128 v[92:95], v8 offset:6144
Mvvv 232,112,56
s_add_u32 m0, s69, 34816
s_add_u32 s72, s83, 35456
ds_read_b64_tr_b16 v[102:103], v26 offset:42240
Mvvv 232,116,60
s_waitcnt lgkmcnt(12)
buffer_load_dwordx4 v11, s[48:51], s72 offen sc1 lds
Mvvv 140,120,32
s_waitcnt lgkmcnt(8)
ds_read_b64_tr_b16 v[104:105], v27 offset:33792
v_cndmask_b32 v4, v4, v1, vcc
Mvvv 140,124,36
buffer_store_dwordx4 v[20:23], v0, s[48:51], s73 offen sc1
Mvvv 172,120,40
s_add_u32 m0, s69, 35840
s_add_u32 s72, s83, 35456
ds_read_b64_tr_b16 v[106:107], v27 offset:34048
Mvvv 172,124,44
buffer_load_dwordx4 v12, s[48:51], s72 offen sc1 lds
Mvvv 204,120,48
ds_read_b64_tr_b16 v[108:109], v27 offset:41984
v_accvgpr_read_b32 v30, a80
Mvvv 204,124,52
v_accvgpr_read_b32 v31, a81
v_cvt_pk_bf16_f32 v16, v30, v31
Mvvv 236,120,56
s_add_u32 m0, s69, 36864
s_add_u32 s72, s82, 640
ds_read_b64_tr_b16 v[110:111], v27 offset:42240
Mvvv 236,124,60
s_waitcnt lgkmcnt(4)
buffer_load_dwordx4 v11, s[48:51], s72 offen sc1 lds
Mvvv 144,96,32
s_add_u32 m0, s71, 114688
s_add_u32 s72, s68, 2621440
ds_read_b64_tr_b16 v[112:113], v26 offset:34304
Mvvv 144,100,36
buffer_load_dwordx4 v3, s[16:19], s72 offen lds
Mvvv 176,96,40
s_add_u32 m0, s69, 37888
s_add_u32 s72, s82, 640
ds_read_b64_tr_b16 v[114:115], v26 offset:34560
Mvvv 176,100,44
buffer_load_dwordx4 v12, s[48:51], s72 offen sc1 lds
Mvvv 208,96,48
s_add_u32 m0, s71, 115584
s_add_u32 s72, s68, 2621440
ds_read_b64_tr_b16 v[116:117], v26 offset:42496
Mvvv 208,100,52
buffer_load_dwordx4 v3, s[16:19], s72 offen offset:128 lds
Mvvv 240,96,56
s_add_u32 m0, s69, 38912
s_add_u32 s72, s82, 35456
ds_read_b64_tr_b16 v[118:119], v26 offset:42752
Mvvv 240,100,60
s_waitcnt lgkmcnt(4)
buffer_load_dwordx4 v11, s[48:51], s72 offen sc1 lds
Mvvv 148,104,32
s_add_u32 m0, s71, 122880
s_add_u32 s72, s68, 2883584
ds_read_b64_tr_b16 v[120:121], v27 offset:34304
Mvvv 148,108,36
buffer_load_dwordx4 v3, s[16:19], s72 offen lds
Mvvv 180,104,40
s_add_u32 m0, s69, 39936
s_add_u32 s72, s82, 35456
ds_read_b64_tr_b16 v[122:123], v27 offset:34560
Mvvv 180,108,44
buffer_load_dwordx4 v12, s[48:51], s72 offen sc1 lds
Mvvv 212,104,48
s_add_u32 m0, s71, 123776
s_add_u32 s72, s68, 2883584
ds_read_b64_tr_b16 v[124:125], v27 offset:42496
Mvvv 212,108,52
buffer_load_dwordx4 v3, s[16:19], s72 offen offset:128 lds
Mvvv 244,104,56
ds_read_b64_tr_b16 v[126:127], v27 offset:42752
v_accvgpr_read_b32 v30, a82
Mvvv 244,108,60
s_waitcnt lgkmcnt(6)
v_accvgpr_read_b32 v31, a83
v_cvt_pk_bf16_f32 v17, v30, v31
Mvvv 152,112,32
s_waitcnt lgkmcnt(4)
v_accvgpr_read_b32 v30, a112
v_accvgpr_read_b32 v31, a113
Mvvv 152,116,36
v_cvt_pk_bf16_f32 v18, v30, v31
v_accvgpr_read_b32 v30, a114
Mvvv 184,112,40
v_accvgpr_read_b32 v31, a115
v_cvt_pk_bf16_f32 v19, v30, v31
Mvvv 184,116,44
s_add_u32 s73, s81, 1024
buffer_store_dwordx4 v[16:19], v0, s[48:51], s73 offen sc1
Mvvv 216,112,48
v_accvgpr_read_b32 v30, a84
v_accvgpr_read_b32 v31, a85
Mvvv 216,116,52
s_waitcnt lgkmcnt(0)
v_cvt_pk_bf16_f32 v20, v30, v31
v_accvgpr_read_b32 v30, a86
Mvvv 248,112,56
s_barrier
Mvvv 248,116,60
ds_read_b64_tr_b16 v[96:97], v26 offset:49152
v_accvgpr_read_b32 v31, a87
Mvvv 156,120,32
ds_read_b64_tr_b16 v[98:99], v26 offset:49408
v_cvt_pk_bf16_f32 v21, v30, v31
Mvvv 156,124,36
ds_read_b64_tr_b16 v[100:101], v26 offset:57344
v_accvgpr_read_b32 v30, a116
Mvvv 188,120,40
ds_read_b64_tr_b16 v[102:103], v26 offset:57600
v_accvgpr_read_b32 v31, a117
Mvvv 188,124,44
v_cvt_pk_bf16_f32 v22, v30, v31
v_accvgpr_read_b32 v30, a118
Mvvv 220,120,48
ds_read_b64_tr_b16 v[104:105], v27 offset:49152
v_accvgpr_read_b32 v31, a119
Mvvv 220,124,52
ds_read_b64_tr_b16 v[106:107], v27 offset:49408
v_cvt_pk_bf16_f32 v23, v30, v31
Mvvv 252,120,56
s_add_u32 s73, s81, 3072
ds_read_b64_tr_b16 v[108:109], v27 offset:57344
v_mul_u32_u24 v4, 34816, v4
Mvvv 252,124,60
s_waitcnt lgkmcnt(5)
s_add_u32 s58, s62, 0
ds_read_b64_tr_b16 v[110:111], v27 offset:57600
v_add_u32 v4, s58, v4
Mvvv 128,96,64
s_waitcnt lgkmcnt(4)
s_add_u32 m0, s69, 65536
s_add_u32 s72, s83, 768
ds_read_b64_tr_b16 v[112:113], v26 offset:49664
Mvvv 128,100,68
s_and_b32 s58, s65, 6
buffer_load_dwordx4 v11, s[48:51], s72 offen sc1 lds
Mvvv 160,96,72
s_lshl_b32 s58, s58, 2
s_mul_i32 s66, s58, 36864
ds_read_b64_tr_b16 v[114:115], v26 offset:49920
Mvvv 160,100,76
s_lshl_b32 s67, s58, 18
buffer_store_dwordx4 v[20:23], v0, s[48:51], s73 offen sc1
Mvvv 192,96,80
s_add_u32 m0, s69, 66560
s_add_u32 s72, s83, 768
ds_read_b64_tr_b16 v[116:117], v26 offset:57856
Mvvv 192,100,84
s_lshl_b32 s58, s61, 2
buffer_load_dwordx4 v12, s[48:51], s72 offen sc1 lds
Mvvv 224,96,88
s_waitcnt vmcnt(19) lgkmcnt(3)
ds_read_b64_tr_b16 v[118:119], v26 offset:58112
v_accvgpr_read_b32 v30, a88
Mvvv 224,100,92
s_lshr_b32 s58, 0x15643270, s58
ds_read_b128 v[32:35], v5 offset:0
v_accvgpr_read_b32 v31, a89
Mvvv 132,104,64
s_and_b32 s58, s58, 7
ds_read_b64_tr_b16 v[120:121], v27 offset:49664
v_cvt_pk_bf16_f32 v16, v30, v31
Mvvv 132,108,68
s_waitcnt lgkmcnt(4)
s_lshl_b32 s58, s58, 8
ds_read_b128 v[36:39], v6 offset:0
v_accvgpr_read_b32 v30, a90
Mvvv 164,104,72
s_add_u32 s67, s67, s58
ds_read_b64_tr_b16 v[122:123], v27 offset:49920
v_accvgpr_read_b32 v31, a91
Mvvv 164,108,76
s_lshl3_add_u32 s67, s70, s67
ds_read_b128 v[40:43], v5 offset:2048
v_cvt_pk_bf16_f32 v17, v30, v31
Mvvv 196,104,80
s_and_b32 s86, s64, 7
ds_read_b64_tr_b16 v[124:125], v27 offset:57856
v_accvgpr_read_b32 v30, a120
Mvvv 196,108,84
s_waitcnt lgkmcnt(6)
s_lshl_b32 s58, s86, 12
ds_read_b128 v[44:47], v6 offset:2048
v_accvgpr_read_b32 v31, a121
Mvvv 228,104,88
s_add_u32 s66, s66, s58
ds_read_b64_tr_b16 v[126:127], v27 offset:58112
v_cvt_pk_bf16_f32 v18, v30, v31
Mvvv 228,108,92
s_add_u32 s66, s66, 54525952
ds_read_b128 v[48:51], v5 offset:4096
v_accvgpr_read_b32 v30, a122
Mvvv 136,112,64
s_lshr_b32 s79, s65, 1
ds_read_b64_tr_b16 v[96:97], v26 offset:50176
v_accvgpr_read_b32 v31, a123
Mvvv 136,116,68
ds_read_b128 v[52:55], v6 offset:4096
v_cvt_pk_bf16_f32 v19, v30, v31
Mvvv 168,112,72
s_waitcnt lgkmcnt(8)
s_add_u32 s73, s81, 5120
ds_read_b64_tr_b16 v[98:99], v26 offset:50432
Mvvv 168,116,76
s_cmp_le_u32 s79, 0
s_cselect_b32 s76, 1, 0
ds_read_b128 v[56:59], v5 offset:6144
Mvvv 200,112,80
s_cmp_le_u32 s79, 1
s_cselect_b32 s77, 2, 1
ds_read_b64_tr_b16 v[100:101], v26 offset:58368
Mvvv 200,116,84
)"
R"(
s_cmp_le_u32 s79, 2
s_cselect_b32 s78, 3, 2
ds_read_b128 v[60:63], v6 offset:6144
Mvvv 232,112,88
s_add_u32 m0, s69, 67584
s_add_u32 s72, s83, 35584
ds_read_b64_tr_b16 v[102:103], v26 offset:58624
Mvvv 232,116,92
s_waitcnt lgkmcnt(8)
buffer_load_dwordx4 v11, s[48:51], s72 offen sc1 lds
Mvvv 140,120,64
s_cmp_le_u32 s79, s57
s_addc_u32 s58, s57, 0
ds_read_b64_tr_b16 v[104:105], v27 offset:50176
Mvvv 140,124,68
buffer_store_dwordx4 v[16:19], v0, s[48:51], s73 offen sc1
Mvvv 172,120,72
s_add_u32 m0, s69, 68608
s_add_u32 s72, s83, 35584
ds_read_b64_tr_b16 v[106:107], v27 offset:50432
Mvvv 172,124,76
buffer_load_dwordx4 v12, s[48:51], s72 offen sc1 lds
Mvvv 204,120,80
ds_read_b64_tr_b16 v[108:109], v27 offset:58368
v_accvgpr_read_b32 v30, a92
Mvvv 204,124,84
v_accvgpr_read_b32 v31, a93
v_cvt_pk_bf16_f32 v20, v30, v31
Mvvv 236,120,88
s_add_u32 m0, s69, 69632
s_add_u32 s72, s82, 768
ds_read_b64_tr_b16 v[110:111], v27 offset:58624
Mvvv 236,124,92
s_waitcnt lgkmcnt(4)
buffer_load_dwordx4 v11, s[48:51], s72 offen sc1 lds
Mvvv 144,96,64
s_add_u32 m0, s71, 131072
s_add_u32 s72, s68, 3145728
ds_read_b64_tr_b16 v[112:113], v26 offset:50688
Mvvv 144,100,68
buffer_load_dwordx4 v3, s[16:19], s72 offen lds
Mvvv 176,96,72
s_add_u32 m0, s69, 70656
s_add_u32 s72, s82, 768
ds_read_b64_tr_b16 v[114:115], v26 offset:50944
Mvvv 176,100,76
buffer_load_dwordx4 v12, s[48:51], s72 offen sc1 lds
Mvvv 208,96,80
s_add_u32 m0, s71, 131968
s_add_u32 s72, s68, 3145728
ds_read_b64_tr_b16 v[116:117], v26 offset:58880
Mvvv 208,100,84
buffer_load_dwordx4 v3, s[16:19], s72 offen offset:128 lds
Mvvv 240,96,88
s_add_u32 m0, s69, 71680
s_add_u32 s72, s82, 35584
ds_read_b64_tr_b16 v[118:119], v26 offset:59136
Mvvv 240,100,92
s_waitcnt lgkmcnt(4)
buffer_load_dwordx4 v11, s[48:51], s72 offen sc1 lds
Mvvv 148,104,64
s_add_u32 m0, s71, 139264
s_add_u32 s72, s68, 3407872
ds_read_b64_tr_b16 v[120:121], v27 offset:50688
Mvvv 148,108,68
buffer_load_dwordx4 v3, s[16:19], s72 offen lds
Mvvv 180,104,72
s_add_u32 m0, s69, 72704
s_add_u32 s72, s82, 35584
ds_read_b64_tr_b16 v[122:123], v27 offset:50944
Mvvv 180,108,76
buffer_load_dwordx4 v12, s[48:51], s72 offen sc1 lds
Mvvv 212,104,80
s_add_u32 m0, s71, 140160
s_add_u32 s72, s68, 3407872
ds_read_b64_tr_b16 v[124:125], v27 offset:58880
Mvvv 212,108,84
buffer_load_dwordx4 v3, s[16:19], s72 offen offset:128 lds
Mvvv 244,104,88
ds_read_b64_tr_b16 v[126:127], v27 offset:59136
v_accvgpr_read_b32 v30, a94
Mvvv 244,108,92
s_waitcnt lgkmcnt(6)
v_accvgpr_read_b32 v31, a95
v_cvt_pk_bf16_f32 v21, v30, v31
Mvvv 152,112,64
s_waitcnt lgkmcnt(4)
v_accvgpr_read_b32 v30, a124
v_accvgpr_read_b32 v31, a125
Mvvv 152,116,68
v_cvt_pk_bf16_f32 v22, v30, v31
v_accvgpr_read_b32 v30, a126
Mvvv 184,112,72
v_accvgpr_read_b32 v31, a127
v_cvt_pk_bf16_f32 v23, v30, v31
Mvvv 184,116,76
s_add_u32 s73, s81, 7168
buffer_store_dwordx4 v[20:23], v0, s[48:51], s73 offen sc1
Mvvv 216,112,80
v_accvgpr_read_b32 v30, a128
v_accvgpr_read_b32 v31, a129
Mvvv 216,116,84
s_waitcnt vmcnt(30) lgkmcnt(0)
v_mul_f32 v30, 0x3e0293ee, v30
v_mul_f32 v31, 0x3e0293ee, v31
Mvvv 248,112,88
s_barrier
Mvvv 248,116,92
ds_read_b64_tr_b16 v[96:97], v26 offset:0
v_cvt_pk_bf16_f32 v16, v30, v31
Mvvv 156,120,64
ds_read_b64_tr_b16 v[98:99], v26 offset:256
v_accvgpr_read_b32 v30, a130
Mvvv 156,124,68
ds_read_b64_tr_b16 v[100:101], v26 offset:8192
v_accvgpr_read_b32 v31, a131
Mvvv 188,120,72
ds_read_b64_tr_b16 v[102:103], v26 offset:8448
v_mul_f32 v30, 0x3e0293ee, v30
Mvvv 188,124,76
v_mul_f32 v31, 0x3e0293ee, v31
v_cvt_pk_bf16_f32 v17, v30, v31
Mvvv 220,120,80
ds_read_b64_tr_b16 v[104:105], v27 offset:0
v_accvgpr_read_b32 v30, a132
Mvvv 220,124,84
ds_read_b64_tr_b16 v[106:107], v27 offset:256
v_accvgpr_read_b32 v31, a133
Mvvv 252,120,88
ds_read_b64_tr_b16 v[108:109], v27 offset:8192
v_mul_f32 v30, 0x3e0293ee, v30
Mvvv 252,124,92
s_waitcnt lgkmcnt(5)
ds_read_b64_tr_b16 v[110:111], v27 offset:8448
v_mul_f32 v31, 0x3e0293ee, v31
Mvvv 128,96,32
s_waitcnt lgkmcnt(4)
s_add_u32 m0, s69, 0
s_add_u32 s72, s83, 896
ds_read_b64_tr_b16 v[112:113], v26 offset:512
Mvvv 128,100,36
buffer_load_dwordx4 v11, s[48:51], s72 offen sc1 lds
Mvvv 160,96,40
ds_read_b64_tr_b16 v[114:115], v26 offset:768
v_cvt_pk_bf16_f32 v18, v30, v31
Mvvv 160,100,44
v_accvgpr_read_b32 v30, a134
v_accvgpr_read_b32 v31, a135
Mvvv 192,96,48
s_add_u32 m0, s69, 1024
s_add_u32 s72, s83, 896
ds_read_b64_tr_b16 v[116:117], v26 offset:8704
Mvvv 192,100,52
buffer_load_dwordx4 v12, s[48:51], s72 offen sc1 lds
Mvvv 224,96,56
s_waitcnt vmcnt(19)
ds_read_b64_tr_b16 v[118:119], v26 offset:8960
v_mul_f32 v30, 0x3e0293ee, v30
Mvvv 224,100,60
s_waitcnt lgkmcnt(6)
ds_read_b128 v[64:67], v5 offset:32768
v_mul_f32 v31, 0x3e0293ee, v31
Mvvv 132,104,32
s_waitcnt lgkmcnt(5)
ds_read_b64_tr_b16 v[120:121], v27 offset:512
v_cvt_pk_bf16_f32 v19, v30, v31
Mvvv 132,108,36
s_add_u32 s73, s80, 34816
ds_read_b128 v[68:71], v6 offset:32768
v_accvgpr_write_b32 a0, 0
Mvvv 164,104,40
s_cmp_eq_u32 s57, 3
s_cselect_b32 s58, s79, s58
ds_read_b64_tr_b16 v[122:123], v27 offset:768
Mvvv 164,108,44
s_mul_i32 s58, s58, 1179648
s_add_u32 s87, s66, s58
ds_read_b128 v[72:75], v5 offset:34816
Mvvv 196,104,48
s_lshl3_add_u32 s91, s61, s65
s_sub_u32 s91, s91, 8
ds_read_b64_tr_b16 v[124:125], v27 offset:8704
Mvvv 196,108,52
ds_read_b128 v[76:79], v6 offset:34816
v_add_u32 v2, s63, v1
Mvvv 228,104,56
ds_read_b64_tr_b16 v[126:127], v27 offset:8960
v_add_u32 v9, -32, v2
Mvvv 228,108,60
s_waitcnt lgkmcnt(10)
ds_read_b128 v[80:83], v5 offset:36864
v_mul_u32_u24 v9, 36864, v9
Mvvv 136,112,32
s_waitcnt lgkmcnt(9)
ds_read_b64_tr_b16 v[96:97], v26 offset:1024
v_add_u32 v9, s87, v9
Mvvv 136,116,36
ds_read_b128 v[84:87], v6 offset:36864
v_cmp_lt_u32 vcc, 31, v2
Mvvv 168,112,40
ds_read_b64_tr_b16 v[98:99], v26 offset:1280
v_accvgpr_write_b32 a1, 0
Mvvv 168,116,44
ds_read_b128 v[88:91], v5 offset:38912
v_cndmask_b32 v4, v4, v9, vcc
Mvvv 200,112,48
ds_read_b64_tr_b16 v[100:101], v26 offset:9216
v_lshrrev_b32 v1, 4, v0
Mvvv 200,116,52
s_waitcnt lgkmcnt(11)
ds_read_b128 v[92:95], v6 offset:38912
v_add_u32 v1, 2, v1
Mvvv 232,112,56
s_add_u32 m0, s69, 2048
s_add_u32 s72, s83, 35712
ds_read_b64_tr_b16 v[102:103], v26 offset:9472
Mvvv 232,116,60
s_waitcnt lgkmcnt(12)
buffer_load_dwordx4 v11, s[48:51], s72 offen sc1 lds
Mvvv 140,120,32
s_waitcnt lgkmcnt(8)
ds_read_b64_tr_b16 v[104:105], v27 offset:1024
v_and_b32 v1, 63, v1
Mvvv 140,124,36
buffer_store_dwordx4 v[16:19], v0, s[48:51], s73 offen sc1
Mvvv 172,120,40
s_add_u32 m0, s69, 3072
s_add_u32 s72, s83, 35712
ds_read_b64_tr_b16 v[106:107], v27 offset:1280
Mvvv 172,124,44
buffer_load_dwordx4 v12, s[48:51], s72 offen sc1 lds
Mvvv 204,120,48
ds_read_b64_tr_b16 v[108:109], v27 offset:9216
v_accvgpr_read_b32 v30, a136
Mvvv 204,124,52
v_accvgpr_read_b32 v31, a137
v_mul_f32 v30, 0x3e0293ee, v30
Mvvv 236,120,56
s_add_u32 m0, s69, 4096
s_add_u32 s72, s82, 896
ds_read_b64_tr_b16 v[110:111], v27 offset:9472
Mvvv 236,124,60
s_waitcnt lgkmcnt(4)
buffer_load_dwordx4 v11, s[48:51], s72 offen sc1 lds
Mvvv 144,96,32
s_add_u32 m0, s71, 147456
s_add_u32 s72, s68, 3670016
ds_read_b64_tr_b16 v[112:113], v26 offset:1536
Mvvv 144,100,36
buffer_load_dwordx4 v3, s[16:19], s72 offen lds
Mvvv 176,96,40
s_add_u32 m0, s69, 5120
s_add_u32 s72, s82, 896
ds_read_b64_tr_b16 v[114:115], v26 offset:1792
Mvvv 176,100,44
buffer_load_dwordx4 v12, s[48:51], s72 offen sc1 lds
Mvvv 208,96,48
s_add_u32 m0, s71, 148352
s_add_u32 s72, s68, 3670016
ds_read_b64_tr_b16 v[116:117], v26 offset:9728
Mvvv 208,100,52
buffer_load_dwordx4 v3, s[16:19], s72 offen offset:128 lds
Mvvv 240,96,56
s_add_u32 m0, s69, 6144
s_add_u32 s72, s82, 35712
ds_read_b64_tr_b16 v[118:119], v26 offset:9984
Mvvv 240,100,60
s_waitcnt lgkmcnt(4)
buffer_load_dwordx4 v11, s[48:51], s72 offen sc1 lds
Mvvv 148,104,32
s_add_u32 m0, s71, 155648
s_add_u32 s72, s68, 3932160
ds_read_b64_tr_b16 v[120:121], v27 offset:1536
Mvvv 148,108,36
buffer_load_dwordx4 v3, s[16:19], s72 offen lds
Mvvv 180,104,40
s_add_u32 m0, s69, 7168
s_add_u32 s72, s82, 35712
ds_read_b64_tr_b16 v[122:123], v27 offset:1792
Mvvv 180,108,44
buffer_load_dwordx4 v12, s[48:51], s72 offen sc1 lds
Mvvv 212,104,48
s_add_u32 m0, s71, 156544
s_add_u32 s72, s68, 3932160
ds_read_b64_tr_b16 v[124:125], v27 offset:9728
Mvvv 212,108,52
buffer_load_dwordx4 v3, s[16:19], s72 offen offset:128 lds
Mvvv 244,104,56
ds_read_b64_tr_b16 v[126:127], v27 offset:9984
v_mul_f32 v31, 0x3e0293ee, v31
Mvvv 244,108,60
s_waitcnt lgkmcnt(6)
v_cvt_pk_bf16_f32 v20, v30, v31
v_accvgpr_read_b32 v30, a138
Mvvv 152,112,32
s_waitcnt lgkmcnt(4)
v_accvgpr_read_b32 v31, a139
v_mul_f32 v30, 0x3e0293ee, v30
Mvvv 152,116,36
v_mul_f32 v31, 0x3e0293ee, v31
v_cvt_pk_bf16_f32 v21, v30, v31
Mvvv 184,112,40
v_accvgpr_read_b32 v30, a140
v_accvgpr_read_b32 v31, a141
Mvvv 184,116,44
v_mul_f32 v30, 0x3e0293ee, v30
v_mul_f32 v31, 0x3e0293ee, v31
Mvvv 216,112,48
v_cvt_pk_bf16_f32 v22, v30, v31
v_accvgpr_read_b32 v30, a142
Mvvv 216,116,52
s_waitcnt vmcnt(29) lgkmcnt(0)
v_accvgpr_read_b32 v31, a143
v_mul_f32 v30, 0x3e0293ee, v30
Mvvv 248,112,56
s_barrier
Mvvv 248,116,60
ds_read_b64_tr_b16 v[96:97], v26 offset:16384
v_mul_f32 v31, 0x3e0293ee, v31
Mvvv 156,120,32
ds_read_b64_tr_b16 v[98:99], v26 offset:16640
v_cvt_pk_bf16_f32 v23, v30, v31
Mvvv 156,124,36
s_add_u32 s73, s80, 35840
ds_read_b64_tr_b16 v[100:101], v26 offset:24576
v_subrev_u32 v2, s63, v1
Mvvv 188,120,40
ds_read_b64_tr_b16 v[102:103], v26 offset:24832
v_add_u32 v14, -2, v1
Mvvv 188,124,44
buffer_store_dwordx4 v[20:23], v0, s[48:51], s73 offen sc1
Mvvv 220,120,48
ds_read_b64_tr_b16 v[104:105], v27 offset:16384
v_accvgpr_read_b32 v30, a160
Mvvv 220,124,52
ds_read_b64_tr_b16 v[106:107], v27 offset:16640
v_accvgpr_read_b32 v31, a161
Mvvv 252,120,56
ds_read_b64_tr_b16 v[108:109], v27 offset:24576
v_mul_f32 v30, 0x3e0293ee, v30
Mvvv 252,124,60
s_waitcnt lgkmcnt(5)
ds_read_b64_tr_b16 v[110:111], v27 offset:24832
v_mul_f32 v31, 0x3e0293ee, v31
Mvvv 128,96,64
s_waitcnt lgkmcnt(4)
s_add_u32 m0, s69, 32768
s_add_u32 s72, s83, 1024
ds_read_b64_tr_b16 v[112:113], v26 offset:16896
Mvvv 128,100,68
buffer_load_dwordx4 v11, s[48:51], s72 offen sc1 lds
Mvvv 160,96,72
ds_read_b64_tr_b16 v[114:115], v26 offset:17152
v_cvt_pk_bf16_f32 v16, v30, v31
Mvvv 160,100,76
v_accvgpr_read_b32 v30, a162
v_accvgpr_read_b32 v31, a163
Mvvv 192,96,80
s_add_u32 m0, s69, 33792
s_add_u32 s72, s83, 1024
ds_read_b64_tr_b16 v[116:117], v26 offset:25088
Mvvv 192,100,84
buffer_load_dwordx4 v12, s[48:51], s72 offen sc1 lds
Mvvv 224,96,88
s_waitcnt vmcnt(18)
ds_read_b64_tr_b16 v[118:119], v26 offset:25344
v_mul_f32 v30, 0x3e0293ee, v30
Mvvv 224,100,92
s_waitcnt lgkmcnt(6)
ds_read_b128 v[32:35], v7 offset:0
v_mul_f32 v31, 0x3e0293ee, v31
Mvvv 132,104,64
s_waitcnt lgkmcnt(5)
ds_read_b64_tr_b16 v[120:121], v27 offset:16896
v_cvt_pk_bf16_f32 v17, v30, v31
Mvvv 132,108,68
ds_read_b128 v[36:39], v8 offset:0
v_accvgpr_read_b32 v30, a164
Mvvv 164,104,72
ds_read_b64_tr_b16 v[122:123], v27 offset:17152
v_accvgpr_read_b32 v31, a165
Mvvv 164,108,76
ds_read_b128 v[40:43], v7 offset:2048
v_mul_f32 v30, 0x3e0293ee, v30
Mvvv 196,104,80
ds_read_b64_tr_b16 v[124:125], v27 offset:25088
v_mul_f32 v31, 0x3e0293ee, v31
Mvvv 196,108,84
ds_read_b128 v[44:47], v8 offset:2048
v_cvt_pk_bf16_f32 v18, v30, v31
Mvvv 228,104,88
ds_read_b64_tr_b16 v[126:127], v27 offset:25344
v_accvgpr_read_b32 v30, a166
Mvvv 228,108,92
s_waitcnt lgkmcnt(10)
ds_read_b128 v[48:51], v7 offset:4096
v_accvgpr_read_b32 v31, a167
Mvvv 136,112,64
s_waitcnt lgkmcnt(9)
ds_read_b64_tr_b16 v[96:97], v26 offset:17408
v_mul_f32 v30, 0x3e0293ee, v30
Mvvv 136,116,68
ds_read_b128 v[52:55], v8 offset:4096
v_mul_f32 v31, 0x3e0293ee, v31
Mvvv 168,112,72
ds_read_b64_tr_b16 v[98:99], v26 offset:17664
v_cvt_pk_bf16_f32 v19, v30, v31
Mvvv 168,116,76
s_add_u32 s73, s80, 38912
ds_read_b128 v[56:59], v7 offset:6144
v_sub_u32 v9, 33, v1
Mvvv 200,112,80
ds_read_b64_tr_b16 v[100:101], v26 offset:25600
v_cmp_eq_u32 vcc, 3, v2
Mvvv 200,116,84
s_waitcnt lgkmcnt(11)
ds_read_b128 v[60:63], v8 offset:6144
v_accvgpr_write_b32 a2, 0
Mvvv 232,112,88
)"
R"(
s_add_u32 m0, s69, 34816
s_add_u32 s72, s83, 35840
ds_read_b64_tr_b16 v[102:103], v26 offset:25856
Mvvv 232,116,92
s_waitcnt lgkmcnt(12)
buffer_load_dwordx4 v11, s[48:51], s72 offen sc1 lds
Mvvv 140,120,64
s_waitcnt lgkmcnt(8)
ds_read_b64_tr_b16 v[104:105], v27 offset:17408
v_cndmask_b32 v14, v14, v9, vcc
Mvvv 140,124,68
buffer_store_dwordx4 v[16:19], v0, s[48:51], s73 offen sc1
Mvvv 172,120,72
s_add_u32 m0, s69, 35840
s_add_u32 s72, s83, 35840
ds_read_b64_tr_b16 v[106:107], v27 offset:17664
Mvvv 172,124,76
buffer_load_dwordx4 v12, s[48:51], s72 offen sc1 lds
Mvvv 204,120,80
ds_read_b64_tr_b16 v[108:109], v27 offset:25600
v_accvgpr_read_b32 v30, a168
Mvvv 204,124,84
v_accvgpr_read_b32 v31, a169
v_mul_f32 v30, 0x3e0293ee, v30
Mvvv 236,120,88
s_add_u32 m0, s69, 36864
s_add_u32 s72, s82, 1024
ds_read_b64_tr_b16 v[110:111], v27 offset:25856
Mvvv 236,124,92
s_waitcnt lgkmcnt(4)
buffer_load_dwordx4 v11, s[48:51], s72 offen sc1 lds
Mvvv 144,96,64
s_add_u32 m0, s71, 98304
s_add_u32 s72, s68, 4194304
ds_read_b64_tr_b16 v[112:113], v26 offset:17920
Mvvv 144,100,68
buffer_load_dwordx4 v3, s[16:19], s72 offen lds
Mvvv 176,96,72
s_add_u32 m0, s69, 37888
s_add_u32 s72, s82, 1024
ds_read_b64_tr_b16 v[114:115], v26 offset:18176
Mvvv 176,100,76
buffer_load_dwordx4 v12, s[48:51], s72 offen sc1 lds
Mvvv 208,96,80
s_add_u32 m0, s71, 99200
s_add_u32 s72, s68, 4194304
ds_read_b64_tr_b16 v[116:117], v26 offset:26112
Mvvv 208,100,84
buffer_load_dwordx4 v3, s[16:19], s72 offen offset:128 lds
Mvvv 240,96,88
s_add_u32 m0, s69, 38912
s_add_u32 s72, s82, 35840
ds_read_b64_tr_b16 v[118:119], v26 offset:26368
Mvvv 240,100,92
s_waitcnt lgkmcnt(4)
buffer_load_dwordx4 v11, s[48:51], s72 offen sc1 lds
Mvvv 148,104,64
s_add_u32 m0, s71, 106496
s_add_u32 s72, s68, 4456448
ds_read_b64_tr_b16 v[120:121], v27 offset:17920
Mvvv 148,108,68
buffer_load_dwordx4 v3, s[16:19], s72 offen lds
Mvvv 180,104,72
s_add_u32 m0, s69, 39936
s_add_u32 s72, s82, 35840
ds_read_b64_tr_b16 v[122:123], v27 offset:18176
Mvvv 180,108,76
buffer_load_dwordx4 v12, s[48:51], s72 offen sc1 lds
Mvvv 212,104,80
s_add_u32 m0, s71, 107392
s_add_u32 s72, s68, 4456448
ds_read_b64_tr_b16 v[124:125], v27 offset:26112
Mvvv 212,108,84
buffer_load_dwordx4 v3, s[16:19], s72 offen offset:128 lds
Mvvv 244,104,88
ds_read_b64_tr_b16 v[126:127], v27 offset:26368
v_mul_f32 v31, 0x3e0293ee, v31
Mvvv 244,108,92
s_waitcnt lgkmcnt(6)
v_cvt_pk_bf16_f32 v20, v30, v31
v_accvgpr_read_b32 v30, a170
Mvvv 152,112,64
s_waitcnt lgkmcnt(4)
v_accvgpr_read_b32 v31, a171
v_mul_f32 v30, 0x3e0293ee, v30
Mvvv 152,116,68
v_mul_f32 v31, 0x3e0293ee, v31
v_cvt_pk_bf16_f32 v21, v30, v31
Mvvv 184,112,72
v_accvgpr_read_b32 v30, a172
v_accvgpr_read_b32 v31, a173
Mvvv 184,116,76
v_mul_f32 v30, 0x3e0293ee, v30
v_mul_f32 v31, 0x3e0293ee, v31
Mvvv 216,112,80
v_cvt_pk_bf16_f32 v22, v30, v31
v_accvgpr_read_b32 v30, a174
Mvvv 216,116,84
s_waitcnt vmcnt(28) lgkmcnt(0)
v_accvgpr_read_b32 v31, a175
v_mul_f32 v30, 0x3e0293ee, v30
Mvvv 248,112,88
s_barrier
Mvvv 248,116,92
ds_read_b64_tr_b16 v[96:97], v26 offset:32768
v_mul_f32 v31, 0x3e0293ee, v31
Mvvv 156,120,64
ds_read_b64_tr_b16 v[98:99], v26 offset:33024
v_cvt_pk_bf16_f32 v23, v30, v31
Mvvv 156,124,68
s_add_u32 s73, s80, 39936
ds_read_b64_tr_b16 v[100:101], v26 offset:40960
v_add_u32 v9, -1, v1
Mvvv 188,120,72
ds_read_b64_tr_b16 v[102:103], v26 offset:41216
v_cmp_eq_u32 vcc, 2, v2
Mvvv 188,124,76
buffer_store_dwordx4 v[20:23], v0, s[48:51], s73 offen sc1
Mvvv 220,120,80
ds_read_b64_tr_b16 v[104:105], v27 offset:32768
v_accvgpr_read_b32 v30, a144
Mvvv 220,124,84
ds_read_b64_tr_b16 v[106:107], v27 offset:33024
v_accvgpr_read_b32 v31, a145
Mvvv 252,120,88
ds_read_b64_tr_b16 v[108:109], v27 offset:40960
v_cvt_pk_bf16_f32 v16, v30, v31
Mvvv 252,124,92
s_waitcnt lgkmcnt(5)
ds_read_b64_tr_b16 v[110:111], v27 offset:41216
v_accvgpr_read_b32 v30, a146
Mvvv 128,96,32
s_waitcnt lgkmcnt(4)
s_add_u32 m0, s69, 65536
s_add_u32 s72, s83, 1152
ds_read_b64_tr_b16 v[112:113], v26 offset:33280
Mvvv 128,100,36
buffer_load_dwordx4 v11, s[48:51], s72 offen sc1 lds
Mvvv 160,96,40
ds_read_b64_tr_b16 v[114:115], v26 offset:33536
v_accvgpr_read_b32 v31, a147
Mvvv 160,100,44
v_cvt_pk_bf16_f32 v17, v30, v31
v_accvgpr_read_b32 v30, a176
Mvvv 192,96,48
s_add_u32 m0, s69, 66560
s_add_u32 s72, s83, 1152
ds_read_b64_tr_b16 v[116:117], v26 offset:41472
Mvvv 192,100,52
buffer_load_dwordx4 v12, s[48:51], s72 offen sc1 lds
Mvvv 224,96,56
s_waitcnt vmcnt(18)
ds_read_b64_tr_b16 v[118:119], v26 offset:41728
v_accvgpr_read_b32 v31, a177
Mvvv 224,100,60
s_waitcnt lgkmcnt(6)
ds_read_b128 v[64:67], v5 offset:0
v_cvt_pk_bf16_f32 v18, v30, v31
Mvvv 132,104,32
s_waitcnt lgkmcnt(5)
ds_read_b64_tr_b16 v[120:121], v27 offset:33280
v_accvgpr_read_b32 v30, a178
Mvvv 132,108,36
ds_read_b128 v[68:71], v6 offset:0
v_accvgpr_read_b32 v31, a179
Mvvv 164,104,40
ds_read_b64_tr_b16 v[122:123], v27 offset:33536
v_cvt_pk_bf16_f32 v19, v30, v31
Mvvv 164,108,44
s_add_u32 s73, s81, 34816
ds_read_b128 v[72:75], v5 offset:2048
v_cndmask_b32 v14, v14, v9, vcc
Mvvv 196,104,48
ds_read_b64_tr_b16 v[124:125], v27 offset:41472
v_sub_u32 v9, 32, v1
Mvvv 196,108,52
ds_read_b128 v[76:79], v6 offset:2048
v_cmp_eq_u32 vcc, 1, v2
Mvvv 228,104,56
ds_read_b64_tr_b16 v[126:127], v27 offset:41728
v_accvgpr_write_b32 a3, 0
Mvvv 228,108,60
s_waitcnt lgkmcnt(10)
ds_read_b128 v[80:83], v5 offset:4096
v_cndmask_b32 v14, v14, v9, vcc
Mvvv 136,112,32
s_waitcnt lgkmcnt(9)
ds_read_b64_tr_b16 v[96:97], v26 offset:33792
v_cmp_gt_i32 vcc, 1, v2
Mvvv 136,116,36
ds_read_b128 v[84:87], v6 offset:4096
v_accvgpr_write_b32 a4, 0
Mvvv 168,112,40
ds_read_b64_tr_b16 v[98:99], v26 offset:34048
v_cndmask_b32 v14, v14, v1, vcc
Mvvv 168,116,44
ds_read_b128 v[88:91], v5 offset:6144
v_mul_u32_u24 v14, 34816, v14
Mvvv 200,112,48
s_add_u32 s58, s62, 16384
ds_read_b64_tr_b16 v[100:101], v26 offset:41984
v_add_u32 v14, s58, v14
Mvvv 200,116,52
s_waitcnt lgkmcnt(11)
ds_read_b128 v[92:95], v6 offset:6144
v_add_u32 v2, s63, v1
Mvvv 232,112,56
s_add_u32 m0, s69, 67584
s_add_u32 s72, s83, 35968
ds_read_b64_tr_b16 v[102:103], v26 offset:42240
Mvvv 232,116,60
s_waitcnt lgkmcnt(12)
buffer_load_dwordx4 v11, s[48:51], s72 offen sc1 lds
Mvvv 140,120,32
s_waitcnt lgkmcnt(8)
ds_read_b64_tr_b16 v[104:105], v27 offset:33792
v_add_u32 v9, -32, v2
Mvvv 140,124,36
buffer_store_dwordx4 v[16:19], v0, s[48:51], s73 offen sc1
Mvvv 172,120,40
s_add_u32 m0, s69, 68608
s_add_u32 s72, s83, 35968
ds_read_b64_tr_b16 v[106:107], v27 offset:34048
Mvvv 172,124,44
buffer_load_dwordx4 v12, s[48:51], s72 offen sc1 lds
Mvvv 204,120,48
ds_read_b64_tr_b16 v[108:109], v27 offset:41984
v_accvgpr_read_b32 v30, a148
Mvvv 204,124,52
v_accvgpr_read_b32 v31, a149
v_cvt_pk_bf16_f32 v20, v30, v31
Mvvv 236,120,56
s_add_u32 m0, s69, 69632
s_add_u32 s72, s82, 1152
ds_read_b64_tr_b16 v[110:111], v27 offset:42240
Mvvv 236,124,60
s_waitcnt lgkmcnt(4)
buffer_load_dwordx4 v11, s[48:51], s72 offen sc1 lds
Mvvv 144,96,32
s_add_u32 m0, s71, 114688
s_add_u32 s72, s68, 4718592
ds_read_b64_tr_b16 v[112:113], v26 offset:34304
Mvvv 144,100,36
buffer_load_dwordx4 v3, s[16:19], s72 offen lds
Mvvv 176,96,40
s_add_u32 m0, s69, 70656
s_add_u32 s72, s82, 1152
ds_read_b64_tr_b16 v[114:115], v26 offset:34560
Mvvv 176,100,44
buffer_load_dwordx4 v12, s[48:51], s72 offen sc1 lds
Mvvv 208,96,48
s_add_u32 m0, s71, 115584
s_add_u32 s72, s68, 4718592
ds_read_b64_tr_b16 v[116:117], v26 offset:42496
Mvvv 208,100,52
buffer_load_dwordx4 v3, s[16:19], s72 offen offset:128 lds
Mvvv 240,96,56
s_add_u32 m0, s69, 71680
s_add_u32 s72, s82, 35968
ds_read_b64_tr_b16 v[118:119], v26 offset:42752
Mvvv 240,100,60
s_waitcnt lgkmcnt(4)
buffer_load_dwordx4 v11, s[48:51], s72 offen sc1 lds
Mvvv 148,104,32
s_add_u32 m0, s71, 122880
s_add_u32 s72, s68, 4980736
ds_read_b64_tr_b16 v[120:121], v27 offset:34304
Mvvv 148,108,36
buffer_load_dwordx4 v3, s[16:19], s72 offen lds
Mvvv 180,104,40
s_add_u32 m0, s69, 72704
s_add_u32 s72, s82, 35968
ds_read_b64_tr_b16 v[122:123], v27 offset:34560
Mvvv 180,108,44
buffer_load_dwordx4 v12, s[48:51], s72 offen sc1 lds
Mvvv 212,104,48
s_add_u32 m0, s71, 123776
s_add_u32 s72, s68, 4980736
ds_read_b64_tr_b16 v[124:125], v27 offset:42496
Mvvv 212,108,52
buffer_load_dwordx4 v3, s[16:19], s72 offen offset:128 lds
Mvvv 244,104,56
ds_read_b64_tr_b16 v[126:127], v27 offset:42752
v_accvgpr_read_b32 v30, a150
Mvvv 244,108,60
s_waitcnt lgkmcnt(6)
v_accvgpr_read_b32 v31, a151
v_cvt_pk_bf16_f32 v21, v30, v31
Mvvv 152,112,32
s_waitcnt lgkmcnt(4)
v_accvgpr_read_b32 v30, a180
v_accvgpr_read_b32 v31, a181
Mvvv 152,116,36
v_cvt_pk_bf16_f32 v22, v30, v31
v_accvgpr_read_b32 v30, a182
Mvvv 184,112,40
v_accvgpr_read_b32 v31, a183
v_cvt_pk_bf16_f32 v23, v30, v31
Mvvv 184,116,44
s_add_u32 s73, s81, 36864
buffer_store_dwordx4 v[20:23], v0, s[48:51], s73 offen sc1
Mvvv 216,112,48
v_accvgpr_read_b32 v30, a152
v_accvgpr_read_b32 v31, a153
Mvvv 216,116,52
s_waitcnt vmcnt(29) lgkmcnt(0)
v_cvt_pk_bf16_f32 v16, v30, v31
v_accvgpr_read_b32 v30, a154
Mvvv 248,112,56
s_barrier
Mvvv 248,116,60
ds_read_b64_tr_b16 v[96:97], v26 offset:49152
v_accvgpr_read_b32 v31, a155
Mvvv 156,120,32
ds_read_b64_tr_b16 v[98:99], v26 offset:49408
v_cvt_pk_bf16_f32 v17, v30, v31
Mvvv 156,124,36
ds_read_b64_tr_b16 v[100:101], v26 offset:57344
v_accvgpr_read_b32 v30, a184
Mvvv 188,120,40
ds_read_b64_tr_b16 v[102:103], v26 offset:57600
v_accvgpr_read_b32 v31, a185
Mvvv 188,124,44
v_cvt_pk_bf16_f32 v18, v30, v31
v_accvgpr_read_b32 v30, a186
Mvvv 220,120,48
ds_read_b64_tr_b16 v[104:105], v27 offset:49152
v_accvgpr_read_b32 v31, a187
Mvvv 220,124,52
ds_read_b64_tr_b16 v[106:107], v27 offset:49408
v_cvt_pk_bf16_f32 v19, v30, v31
Mvvv 252,120,56
s_add_u32 s73, s81, 38912
ds_read_b64_tr_b16 v[108:109], v27 offset:57344
v_mul_u32_u24 v9, 131072, v9
Mvvv 252,124,60
s_waitcnt lgkmcnt(5)
s_add_u32 s58, s67, 262144
ds_read_b64_tr_b16 v[110:111], v27 offset:57600
v_add_u32 v9, s58, v9
Mvvv 128,96,64
s_waitcnt lgkmcnt(4)
s_add_u32 m0, s69, 0
s_add_u32 s72, s83, 1280
ds_read_b64_tr_b16 v[112:113], v26 offset:49664
Mvvv 128,100,68
buffer_load_dwordx4 v11, s[48:51], s72 offen sc1 lds
Mvvv 160,96,72
ds_read_b64_tr_b16 v[114:115], v26 offset:49920
v_cmp_lt_u32 vcc, 31, v2
Mvvv 160,100,76
buffer_store_dwordx4 v[16:19], v0, s[48:51], s73 offen sc1
Mvvv 192,96,80
s_add_u32 m0, s69, 1024
s_add_u32 s72, s83, 1280
ds_read_b64_tr_b16 v[116:117], v26 offset:57856
Mvvv 192,100,84
buffer_load_dwordx4 v12, s[48:51], s72 offen sc1 lds
Mvvv 224,96,88
s_waitcnt vmcnt(19)
ds_read_b64_tr_b16 v[118:119], v26 offset:58112
v_accvgpr_read_b32 v30, a156
Mvvv 224,100,92
s_waitcnt lgkmcnt(6)
ds_read_b128 v[32:35], v5 offset:32768
v_accvgpr_read_b32 v31, a157
Mvvv 132,104,64
s_waitcnt lgkmcnt(5)
ds_read_b64_tr_b16 v[120:121], v27 offset:49664
v_cvt_pk_bf16_f32 v20, v30, v31
Mvvv 132,108,68
ds_read_b128 v[36:39], v6 offset:32768
v_accvgpr_read_b32 v30, a158
Mvvv 164,104,72
ds_read_b64_tr_b16 v[122:123], v27 offset:49920
v_accvgpr_read_b32 v31, a159
Mvvv 164,108,76
ds_read_b128 v[40:43], v5 offset:34816
v_cvt_pk_bf16_f32 v21, v30, v31
Mvvv 196,104,80
ds_read_b64_tr_b16 v[124:125], v27 offset:57856
v_accvgpr_read_b32 v30, a188
Mvvv 196,108,84
ds_read_b128 v[44:47], v6 offset:34816
v_accvgpr_read_b32 v31, a189
Mvvv 228,104,88
ds_read_b64_tr_b16 v[126:127], v27 offset:58112
v_cvt_pk_bf16_f32 v22, v30, v31
Mvvv 228,108,92
s_waitcnt lgkmcnt(10)
ds_read_b128 v[48:51], v5 offset:36864
v_accvgpr_read_b32 v30, a190
Mvvv 136,112,64
s_waitcnt lgkmcnt(9)
ds_read_b64_tr_b16 v[96:97], v26 offset:50176
v_accvgpr_read_b32 v31, a191
Mvvv 136,116,68
s_waitcnt lgkmcnt(6)
ds_read_b128 v[52:55], v6 offset:36864
v_cvt_pk_bf16_f32 v23, v30, v31
Mvvv 168,112,72
s_add_u32 s73, s81, 40960
ds_read_b64_tr_b16 v[98:99], v26 offset:50432
v_cndmask_b32 v14, v14, v9, vcc
Mvvv 168,116,76
s_and_b32 s92, s63, 2
s_lshl_b32 s92, s92, 14
ds_read_b128 v[56:59], v5 offset:38912
Mvvv 200,112,80
s_mov_b64 s[12:13], s[48:49]
s_mov_b64 s[14:15], s[50:51]
ds_read_b64_tr_b16 v[100:101], v26 offset:58368
Mvvv 200,116,84
s_mov_b32 s24, 0
s_mov_b32 s25, 0
ds_read_b128 v[60:63], v6 offset:38912
)"
R"(
Mvvv 232,112,88
s_add_u32 m0, s69, 2048
s_add_u32 s72, s83, 36096
ds_read_b64_tr_b16 v[102:103], v26 offset:58624
Mvvv 232,116,92
s_mov_b32 s26, 0
buffer_load_dwordx4 v11, s[48:51], s72 offen sc1 lds
Mvvv 140,120,64
s_waitcnt lgkmcnt(8)
ds_read_b64_tr_b16 v[104:105], v27 offset:50176
v_accvgpr_write_b32 a5, 0
Mvvv 140,124,68
buffer_store_dwordx4 v[20:23], v0, s[48:51], s73 offen sc1
Mvvv 172,120,72
s_add_u32 m0, s69, 3072
s_add_u32 s72, s83, 36096
ds_read_b64_tr_b16 v[106:107], v27 offset:50432
Mvvv 172,124,76
buffer_load_dwordx4 v12, s[48:51], s72 offen sc1 lds
Mvvv 204,120,80
ds_read_b64_tr_b16 v[108:109], v27 offset:58368
v_accvgpr_read_b32 v30, a192
Mvvv 204,124,84
v_accvgpr_read_b32 v31, a193
v_mul_f32 v30, 0x3e0293ee, v30
Mvvv 236,120,88
s_add_u32 m0, s69, 4096
s_add_u32 s72, s82, 1280
ds_read_b64_tr_b16 v[110:111], v27 offset:58624
Mvvv 236,124,92
s_waitcnt lgkmcnt(4)
buffer_load_dwordx4 v11, s[48:51], s72 offen sc1 lds
Mvvv 144,96,64
s_add_u32 m0, s71, 131072
s_add_u32 s72, s68, 5242880
ds_read_b64_tr_b16 v[112:113], v26 offset:50688
Mvvv 144,100,68
buffer_load_dwordx4 v3, s[16:19], s72 offen lds
Mvvv 176,96,72
s_add_u32 m0, s69, 5120
s_add_u32 s72, s82, 1280
ds_read_b64_tr_b16 v[114:115], v26 offset:50944
Mvvv 176,100,76
buffer_load_dwordx4 v12, s[48:51], s72 offen sc1 lds
Mvvv 208,96,80
s_add_u32 m0, s71, 131968
s_add_u32 s72, s68, 5242880
ds_read_b64_tr_b16 v[116:117], v26 offset:58880
Mvvv 208,100,84
buffer_load_dwordx4 v3, s[16:19], s72 offen offset:128 lds
Mvvv 240,96,88
s_add_u32 m0, s69, 6144
s_add_u32 s72, s82, 36096
ds_read_b64_tr_b16 v[118:119], v26 offset:59136
Mvvv 240,100,92
s_waitcnt lgkmcnt(4)
buffer_load_dwordx4 v11, s[48:51], s72 offen sc1 lds
Mvvv 148,104,64
s_add_u32 m0, s71, 139264
s_add_u32 s72, s68, 5505024
ds_read_b64_tr_b16 v[120:121], v27 offset:50688
Mvvv 148,108,68
buffer_load_dwordx4 v3, s[16:19], s72 offen lds
Mvvv 180,104,72
s_add_u32 m0, s69, 7168
s_add_u32 s72, s82, 36096
ds_read_b64_tr_b16 v[122:123], v27 offset:50944
Mvvv 180,108,76
buffer_load_dwordx4 v12, s[48:51], s72 offen sc1 lds
Mvvv 212,104,80
s_add_u32 m0, s71, 140160
s_add_u32 s72, s68, 5505024
ds_read_b64_tr_b16 v[124:125], v27 offset:58880
Mvvv 212,108,84
buffer_load_dwordx4 v3, s[16:19], s72 offen offset:128 lds
Mvvv 244,104,88
ds_read_b64_tr_b16 v[126:127], v27 offset:59136
v_mul_f32 v31, 0x3e0293ee, v31
Mvvv 244,108,92
s_waitcnt lgkmcnt(6)
v_cvt_pk_bf16_f32 v16, v30, v31
v_accvgpr_read_b32 v30, a194
Mvvv 152,112,64
s_waitcnt lgkmcnt(4)
v_accvgpr_read_b32 v31, a195
v_mul_f32 v30, 0x3e0293ee, v30
Mvvv 152,116,68
v_mul_f32 v31, 0x3e0293ee, v31
v_cvt_pk_bf16_f32 v17, v30, v31
Mvvv 184,112,72
v_accvgpr_read_b32 v30, a196
v_accvgpr_read_b32 v31, a197
Mvvv 184,116,76
v_mul_f32 v30, 0x3e0293ee, v30
v_mul_f32 v31, 0x3e0293ee, v31
Mvvv 216,112,80
v_cvt_pk_bf16_f32 v18, v30, v31
v_accvgpr_read_b32 v30, a198
Mvvv 216,116,84
s_waitcnt vmcnt(29) lgkmcnt(0)
v_accvgpr_read_b32 v31, a199
v_mul_f32 v30, 0x3e0293ee, v30
Mvvv 248,112,88
s_barrier
Mvvv 248,116,92
ds_read_b64_tr_b16 v[96:97], v26 offset:0
v_mul_f32 v31, 0x3e0293ee, v31
Mvvv 156,120,64
ds_read_b64_tr_b16 v[98:99], v26 offset:256
v_cvt_pk_bf16_f32 v19, v30, v31
Mvvv 156,124,68
s_add_u32 s73, s80, 43008
ds_read_b64_tr_b16 v[100:101], v26 offset:8192
v_accvgpr_write_b32 a6, 0
Mvvv 188,120,72
ds_read_b64_tr_b16 v[102:103], v26 offset:8448
v_accvgpr_write_b32 a7, 0
Mvvv 188,124,76
buffer_store_dwordx4 v[16:19], v0, s[48:51], s73 offen sc1
Mvvv 220,120,80
ds_read_b64_tr_b16 v[104:105], v27 offset:0
v_accvgpr_read_b32 v30, a200
Mvvv 220,124,84
ds_read_b64_tr_b16 v[106:107], v27 offset:256
v_accvgpr_read_b32 v31, a201
Mvvv 252,120,88
ds_read_b64_tr_b16 v[108:109], v27 offset:8192
v_mul_f32 v30, 0x3e0293ee, v30
Mvvv 252,124,92
s_waitcnt lgkmcnt(5)
ds_read_b64_tr_b16 v[110:111], v27 offset:8448
v_mul_f32 v31, 0x3e0293ee, v31
Mvvv 128,96,32
s_waitcnt lgkmcnt(4)
s_add_u32 m0, s69, 32768
s_add_u32 s72, s83, 1408
ds_read_b64_tr_b16 v[112:113], v26 offset:512
Mvvv 128,100,36
buffer_load_dwordx4 v11, s[48:51], s72 offen sc1 lds
Mvvv 160,96,40
ds_read_b64_tr_b16 v[114:115], v26 offset:768
v_cvt_pk_bf16_f32 v20, v30, v31
Mvvv 160,100,44
v_accvgpr_read_b32 v30, a202
v_accvgpr_read_b32 v31, a203
Mvvv 192,96,48
s_add_u32 m0, s69, 33792
s_add_u32 s72, s83, 1408
ds_read_b64_tr_b16 v[116:117], v26 offset:8704
Mvvv 192,100,52
buffer_load_dwordx4 v12, s[48:51], s72 offen sc1 lds
Mvvv 224,96,56
s_waitcnt vmcnt(19)
ds_read_b64_tr_b16 v[118:119], v26 offset:8960
v_mul_f32 v30, 0x3e0293ee, v30
Mvvv 224,100,60
s_waitcnt lgkmcnt(6)
ds_read_b128 v[64:67], v7 offset:0
v_mul_f32 v31, 0x3e0293ee, v31
Mvvv 132,104,32
s_waitcnt lgkmcnt(5)
ds_read_b64_tr_b16 v[120:121], v27 offset:512
v_cvt_pk_bf16_f32 v21, v30, v31
Mvvv 132,108,36
ds_read_b128 v[68:71], v8 offset:0
v_accvgpr_read_b32 v30, a204
Mvvv 164,104,40
ds_read_b64_tr_b16 v[122:123], v27 offset:768
v_accvgpr_read_b32 v31, a205
Mvvv 164,108,44
ds_read_b128 v[72:75], v7 offset:2048
v_mul_f32 v30, 0x3e0293ee, v30
Mvvv 196,104,48
ds_read_b64_tr_b16 v[124:125], v27 offset:8704
v_mul_f32 v31, 0x3e0293ee, v31
Mvvv 196,108,52
ds_read_b128 v[76:79], v8 offset:2048
v_cvt_pk_bf16_f32 v22, v30, v31
Mvvv 228,104,56
ds_read_b64_tr_b16 v[126:127], v27 offset:8960
v_accvgpr_read_b32 v30, a206
Mvvv 228,108,60
s_waitcnt lgkmcnt(10)
ds_read_b128 v[80:83], v7 offset:4096
v_accvgpr_read_b32 v31, a207
Mvvv 136,112,32
s_waitcnt lgkmcnt(9)
ds_read_b64_tr_b16 v[96:97], v26 offset:1024
v_mul_f32 v30, 0x3e0293ee, v30
Mvvv 136,116,36
ds_read_b128 v[84:87], v8 offset:4096
v_mul_f32 v31, 0x3e0293ee, v31
Mvvv 168,112,40
ds_read_b64_tr_b16 v[98:99], v26 offset:1280
v_cvt_pk_bf16_f32 v23, v30, v31
Mvvv 168,116,44
s_add_u32 s73, s80, 44032
ds_read_b128 v[88:91], v7 offset:6144
v_accvgpr_write_b32 a8, 0
Mvvv 200,112,48
ds_read_b64_tr_b16 v[100:101], v26 offset:9216
v_accvgpr_write_b32 a9, 0
Mvvv 200,116,52
s_waitcnt lgkmcnt(11)
ds_read_b128 v[92:95], v8 offset:6144
v_accvgpr_write_b32 a10, 0
Mvvv 232,112,56
s_add_u32 m0, s69, 34816
s_add_u32 s72, s83, 36224
ds_read_b64_tr_b16 v[102:103], v26 offset:9472
Mvvv 232,116,60
s_waitcnt lgkmcnt(12)
buffer_load_dwordx4 v11, s[48:51], s72 offen sc1 lds
Mvvv 140,120,32
s_waitcnt lgkmcnt(8)
ds_read_b64_tr_b16 v[104:105], v27 offset:1024
v_accvgpr_write_b32 a11, 0
Mvvv 140,124,36
buffer_store_dwordx4 v[20:23], v0, s[48:51], s73 offen sc1
Mvvv 172,120,40
s_add_u32 m0, s69, 35840
s_add_u32 s72, s83, 36224
ds_read_b64_tr_b16 v[106:107], v27 offset:1280
Mvvv 172,124,44
buffer_load_dwordx4 v12, s[48:51], s72 offen sc1 lds
Mvvv 204,120,48
ds_read_b64_tr_b16 v[108:109], v27 offset:9216
v_accvgpr_read_b32 v30, a224
Mvvv 204,124,52
v_accvgpr_read_b32 v31, a225
v_mul_f32 v30, 0x3e0293ee, v30
Mvvv 236,120,56
s_add_u32 m0, s69, 36864
s_add_u32 s72, s82, 1408
ds_read_b64_tr_b16 v[110:111], v27 offset:9472
Mvvv 236,124,60
s_waitcnt lgkmcnt(4)
buffer_load_dwordx4 v11, s[48:51], s72 offen sc1 lds
Mvvv 144,96,32
s_add_u32 m0, s71, 147456
s_add_u32 s72, s68, 5767168
ds_read_b64_tr_b16 v[112:113], v26 offset:1536
Mvvv 144,100,36
buffer_load_dwordx4 v3, s[16:19], s72 offen lds
Mvvv 176,96,40
s_add_u32 m0, s69, 37888
s_add_u32 s72, s82, 1408
ds_read_b64_tr_b16 v[114:115], v26 offset:1792
Mvvv 176,100,44
buffer_load_dwordx4 v12, s[48:51], s72 offen sc1 lds
Mvvv 208,96,48
s_add_u32 m0, s71, 148352
s_add_u32 s72, s68, 5767168
ds_read_b64_tr_b16 v[116:117], v26 offset:9728
Mvvv 208,100,52
buffer_load_dwordx4 v3, s[16:19], s72 offen offset:128 lds
Mvvv 240,96,56
s_add_u32 m0, s69, 38912
s_add_u32 s72, s82, 36224
ds_read_b64_tr_b16 v[118:119], v26 offset:9984
Mvvv 240,100,60
s_waitcnt lgkmcnt(4)
buffer_load_dwordx4 v11, s[48:51], s72 offen sc1 lds
Mvvv 148,104,32
s_add_u32 m0, s71, 155648
s_add_u32 s72, s68, 6029312
ds_read_b64_tr_b16 v[120:121], v27 offset:1536
Mvvv 148,108,36
buffer_load_dwordx4 v3, s[16:19], s72 offen lds
Mvvv 180,104,40
s_add_u32 m0, s69, 39936
s_add_u32 s72, s82, 36224
ds_read_b64_tr_b16 v[122:123], v27 offset:1792
Mvvv 180,108,44
buffer_load_dwordx4 v12, s[48:51], s72 offen sc1 lds
Mvvv 212,104,48
s_add_u32 m0, s71, 156544
s_add_u32 s72, s68, 6029312
ds_read_b64_tr_b16 v[124:125], v27 offset:9728
Mvvv 212,108,52
buffer_load_dwordx4 v3, s[16:19], s72 offen offset:128 lds
Mvvv 244,104,56
ds_read_b64_tr_b16 v[126:127], v27 offset:9984
v_mul_f32 v31, 0x3e0293ee, v31
Mvvv 244,108,60
s_waitcnt lgkmcnt(6)
v_cvt_pk_bf16_f32 v16, v30, v31
v_accvgpr_read_b32 v30, a226
Mvvv 152,112,32
s_waitcnt lgkmcnt(4)
v_accvgpr_read_b32 v31, a227
v_mul_f32 v30, 0x3e0293ee, v30
Mvvv 152,116,36
v_mul_f32 v31, 0x3e0293ee, v31
v_cvt_pk_bf16_f32 v17, v30, v31
Mvvv 184,112,40
v_accvgpr_read_b32 v30, a228
v_accvgpr_read_b32 v31, a229
Mvvv 184,116,44
v_mul_f32 v30, 0x3e0293ee, v30
v_mul_f32 v31, 0x3e0293ee, v31
Mvvv 216,112,48
v_cvt_pk_bf16_f32 v18, v30, v31
v_accvgpr_read_b32 v30, a230
Mvvv 216,116,52
s_waitcnt vmcnt(29) lgkmcnt(0)
v_accvgpr_read_b32 v31, a231
v_mul_f32 v30, 0x3e0293ee, v30
Mvvv 248,112,56
s_barrier
Mvvv 248,116,60
ds_read_b64_tr_b16 v[96:97], v26 offset:16384
v_mul_f32 v31, 0x3e0293ee, v31
Mvvv 156,120,32
ds_read_b64_tr_b16 v[98:99], v26 offset:16640
v_cvt_pk_bf16_f32 v19, v30, v31
Mvvv 156,124,36
s_add_u32 s73, s80, 47104
ds_read_b64_tr_b16 v[100:101], v26 offset:24576
v_accvgpr_write_b32 a12, 0
Mvvv 188,120,40
ds_read_b64_tr_b16 v[102:103], v26 offset:24832
v_accvgpr_write_b32 a13, 0
Mvvv 188,124,44
buffer_store_dwordx4 v[16:19], v0, s[48:51], s73 offen sc1
Mvvv 220,120,48
ds_read_b64_tr_b16 v[104:105], v27 offset:16384
v_accvgpr_read_b32 v30, a232
Mvvv 220,124,52
ds_read_b64_tr_b16 v[106:107], v27 offset:16640
v_accvgpr_read_b32 v31, a233
Mvvv 252,120,56
ds_read_b64_tr_b16 v[108:109], v27 offset:24576
v_mul_f32 v30, 0x3e0293ee, v30
Mvvv 252,124,60
s_waitcnt lgkmcnt(5)
ds_read_b64_tr_b16 v[110:111], v27 offset:24832
v_mul_f32 v31, 0x3e0293ee, v31
Mvvv 128,96,64
s_waitcnt lgkmcnt(4)
s_add_u32 m0, s69, 65536
s_add_u32 s72, s83, 1536
ds_read_b64_tr_b16 v[112:113], v26 offset:16896
Mvvv 128,100,68
buffer_load_dwordx4 v11, s[48:51], s72 offen sc1 lds
Mvvv 160,96,72
ds_read_b64_tr_b16 v[114:115], v26 offset:17152
v_cvt_pk_bf16_f32 v20, v30, v31
Mvvv 160,100,76
v_accvgpr_read_b32 v30, a234
v_accvgpr_read_b32 v31, a235
Mvvv 192,96,80
s_add_u32 m0, s69, 66560
s_add_u32 s72, s83, 1536
ds_read_b64_tr_b16 v[116:117], v26 offset:25088
Mvvv 192,100,84
buffer_load_dwordx4 v12, s[48:51], s72 offen sc1 lds
Mvvv 224,96,88
s_waitcnt vmcnt(18)
ds_read_b64_tr_b16 v[118:119], v26 offset:25344
v_mul_f32 v30, 0x3e0293ee, v30
Mvvv 224,100,92
s_waitcnt lgkmcnt(6)
ds_read_b128 v[32:35], v5 offset:0
v_mul_f32 v31, 0x3e0293ee, v31
Mvvv 132,104,64
s_waitcnt lgkmcnt(5)
ds_read_b64_tr_b16 v[120:121], v27 offset:16896
v_cvt_pk_bf16_f32 v21, v30, v31
Mvvv 132,108,68
ds_read_b128 v[36:39], v6 offset:0
v_accvgpr_read_b32 v30, a236
Mvvv 164,104,72
ds_read_b64_tr_b16 v[122:123], v27 offset:17152
v_accvgpr_read_b32 v31, a237
Mvvv 164,108,76
ds_read_b128 v[40:43], v5 offset:2048
v_mul_f32 v30, 0x3e0293ee, v30
Mvvv 196,104,80
ds_read_b64_tr_b16 v[124:125], v27 offset:25088
v_mul_f32 v31, 0x3e0293ee, v31
Mvvv 196,108,84
ds_read_b128 v[44:47], v6 offset:2048
v_cvt_pk_bf16_f32 v22, v30, v31
Mvvv 228,104,88
ds_read_b64_tr_b16 v[126:127], v27 offset:25344
v_accvgpr_read_b32 v30, a238
Mvvv 228,108,92
s_waitcnt lgkmcnt(10)
ds_read_b128 v[48:51], v5 offset:4096
v_accvgpr_read_b32 v31, a239
Mvvv 136,112,64
s_waitcnt lgkmcnt(9)
ds_read_b64_tr_b16 v[96:97], v26 offset:17408
v_mul_f32 v30, 0x3e0293ee, v30
Mvvv 136,116,68
ds_read_b128 v[52:55], v6 offset:4096
v_mul_f32 v31, 0x3e0293ee, v31
Mvvv 168,112,72
ds_read_b64_tr_b16 v[98:99], v26 offset:17664
v_cvt_pk_bf16_f32 v23, v30, v31
Mvvv 168,116,76
s_add_u32 s73, s80, 48128
ds_read_b128 v[56:59], v5 offset:6144
v_accvgpr_write_b32 a14, 0
Mvvv 200,112,80
ds_read_b64_tr_b16 v[100:101], v26 offset:25600
v_accvgpr_write_b32 a15, 0
Mvvv 200,116,84
s_waitcnt lgkmcnt(11)
ds_read_b128 v[60:63], v6 offset:6144
v_accvgpr_write_b32 a16, 0
Mvvv 232,112,88
s_add_u32 m0, s69, 67584
s_add_u32 s72, s83, 36352
ds_read_b64_tr_b16 v[102:103], v26 offset:25856
)"
R"(
Mvvv 232,116,92
s_waitcnt lgkmcnt(12)
buffer_load_dwordx4 v11, s[48:51], s72 offen sc1 lds
Mvvv 140,120,64
s_waitcnt lgkmcnt(8)
ds_read_b64_tr_b16 v[104:105], v27 offset:17408
v_accvgpr_write_b32 a17, 0
Mvvv 140,124,68
buffer_store_dwordx4 v[20:23], v0, s[48:51], s73 offen sc1
Mvvv 172,120,72
s_add_u32 m0, s69, 68608
s_add_u32 s72, s83, 36352
ds_read_b64_tr_b16 v[106:107], v27 offset:17664
Mvvv 172,124,76
buffer_load_dwordx4 v12, s[48:51], s72 offen sc1 lds
Mvvv 204,120,80
ds_read_b64_tr_b16 v[108:109], v27 offset:25600
v_accvgpr_read_b32 v30, a208
Mvvv 204,124,84
v_accvgpr_read_b32 v31, a209
v_cvt_pk_bf16_f32 v16, v30, v31
Mvvv 236,120,88
s_add_u32 m0, s69, 69632
s_add_u32 s72, s82, 1536
ds_read_b64_tr_b16 v[110:111], v27 offset:25856
Mvvv 236,124,92
s_waitcnt lgkmcnt(4)
buffer_load_dwordx4 v11, s[48:51], s72 offen sc1 lds
Mvvv 144,96,64
s_add_u32 m0, s71, 98304
s_add_u32 s72, s68, 6291456
ds_read_b64_tr_b16 v[112:113], v26 offset:17920
Mvvv 144,100,68
buffer_load_dwordx4 v3, s[16:19], s72 offen lds
Mvvv 176,96,72
s_add_u32 m0, s69, 70656
s_add_u32 s72, s82, 1536
ds_read_b64_tr_b16 v[114:115], v26 offset:18176
Mvvv 176,100,76
buffer_load_dwordx4 v12, s[48:51], s72 offen sc1 lds
Mvvv 208,96,80
s_add_u32 m0, s71, 99200
s_add_u32 s72, s68, 6291456
ds_read_b64_tr_b16 v[116:117], v26 offset:26112
Mvvv 208,100,84
buffer_load_dwordx4 v3, s[16:19], s72 offen offset:128 lds
Mvvv 240,96,88
s_add_u32 m0, s69, 71680
s_add_u32 s72, s82, 36352
ds_read_b64_tr_b16 v[118:119], v26 offset:26368
Mvvv 240,100,92
s_waitcnt lgkmcnt(4)
buffer_load_dwordx4 v11, s[48:51], s72 offen sc1 lds
Mvvv 148,104,64
s_add_u32 m0, s71, 106496
s_add_u32 s72, s68, 6553600
ds_read_b64_tr_b16 v[120:121], v27 offset:17920
Mvvv 148,108,68
buffer_load_dwordx4 v3, s[16:19], s72 offen lds
Mvvv 180,104,72
s_add_u32 m0, s69, 72704
s_add_u32 s72, s82, 36352
ds_read_b64_tr_b16 v[122:123], v27 offset:18176
Mvvv 180,108,76
buffer_load_dwordx4 v12, s[48:51], s72 offen sc1 lds
Mvvv 212,104,80
s_add_u32 m0, s71, 107392
s_add_u32 s72, s68, 6553600
ds_read_b64_tr_b16 v[124:125], v27 offset:26112
Mvvv 212,108,84
buffer_load_dwordx4 v3, s[16:19], s72 offen offset:128 lds
Mvvv 244,104,88
ds_read_b64_tr_b16 v[126:127], v27 offset:26368
v_accvgpr_read_b32 v30, a210
Mvvv 244,108,92
s_waitcnt lgkmcnt(6)
v_accvgpr_read_b32 v31, a211
v_cvt_pk_bf16_f32 v17, v30, v31
Mvvv 152,112,64
s_waitcnt lgkmcnt(4)
v_accvgpr_read_b32 v30, a240
v_accvgpr_read_b32 v31, a241
Mvvv 152,116,68
v_cvt_pk_bf16_f32 v18, v30, v31
v_accvgpr_read_b32 v30, a242
Mvvv 184,112,72
v_accvgpr_read_b32 v31, a243
v_cvt_pk_bf16_f32 v19, v30, v31
Mvvv 184,116,76
s_add_u32 s73, s81, 35840
buffer_store_dwordx4 v[16:19], v0, s[48:51], s73 offen sc1
Mvvv 216,112,80
v_accvgpr_read_b32 v30, a212
v_accvgpr_read_b32 v31, a213
Mvvv 216,116,84
s_waitcnt vmcnt(29) lgkmcnt(0)
v_cvt_pk_bf16_f32 v20, v30, v31
v_accvgpr_read_b32 v30, a214
Mvvv 248,112,88
s_barrier
Mvvv 248,116,92
ds_read_b64_tr_b16 v[96:97], v26 offset:32768
v_accvgpr_read_b32 v31, a215
Mvvv 156,120,64
ds_read_b64_tr_b16 v[98:99], v26 offset:33024
v_cvt_pk_bf16_f32 v21, v30, v31
Mvvv 156,124,68
ds_read_b64_tr_b16 v[100:101], v26 offset:40960
v_accvgpr_read_b32 v30, a244
Mvvv 188,120,72
ds_read_b64_tr_b16 v[102:103], v26 offset:41216
v_accvgpr_read_b32 v31, a245
Mvvv 188,124,76
v_cvt_pk_bf16_f32 v22, v30, v31
v_accvgpr_read_b32 v30, a246
Mvvv 220,120,80
ds_read_b64_tr_b16 v[104:105], v27 offset:32768
v_accvgpr_read_b32 v31, a247
Mvvv 220,124,84
ds_read_b64_tr_b16 v[106:107], v27 offset:33024
v_cvt_pk_bf16_f32 v23, v30, v31
Mvvv 252,120,88
s_add_u32 s73, s81, 37888
ds_read_b64_tr_b16 v[108:109], v27 offset:40960
v_accvgpr_write_b32 a18, 0
Mvvv 252,124,92
s_waitcnt lgkmcnt(5)
ds_read_b64_tr_b16 v[110:111], v27 offset:41216
v_accvgpr_write_b32 a19, 0
Mvvv 128,96,32
s_waitcnt lgkmcnt(4)
s_add_u32 m0, s69, 0
s_add_u32 s72, s83, 1664
ds_read_b64_tr_b16 v[112:113], v26 offset:33280
Mvvv 128,100,36
buffer_load_dwordx4 v11, s[48:51], s72 offen sc1 lds
Mvvv 160,96,40
ds_read_b64_tr_b16 v[114:115], v26 offset:33536
v_accvgpr_write_b32 a20, 0
Mvvv 160,100,44
buffer_store_dwordx4 v[20:23], v0, s[48:51], s73 offen sc1
Mvvv 192,96,48
s_add_u32 m0, s69, 1024
s_add_u32 s72, s83, 1664
ds_read_b64_tr_b16 v[116:117], v26 offset:41472
Mvvv 192,100,52
buffer_load_dwordx4 v12, s[48:51], s72 offen sc1 lds
Mvvv 224,96,56
s_waitcnt vmcnt(19)
ds_read_b64_tr_b16 v[118:119], v26 offset:41728
v_accvgpr_read_b32 v30, a216
Mvvv 224,100,60
s_waitcnt lgkmcnt(6)
ds_read_b128 v[64:67], v5 offset:32768
v_accvgpr_read_b32 v31, a217
Mvvv 132,104,32
s_waitcnt lgkmcnt(5)
ds_read_b64_tr_b16 v[120:121], v27 offset:33280
v_cvt_pk_bf16_f32 v16, v30, v31
Mvvv 132,108,36
ds_read_b128 v[68:71], v6 offset:32768
v_accvgpr_read_b32 v30, a218
Mvvv 164,104,40
ds_read_b64_tr_b16 v[122:123], v27 offset:33536
v_accvgpr_read_b32 v31, a219
Mvvv 164,108,44
ds_read_b128 v[72:75], v5 offset:34816
v_cvt_pk_bf16_f32 v17, v30, v31
Mvvv 196,104,48
ds_read_b64_tr_b16 v[124:125], v27 offset:41472
v_accvgpr_read_b32 v30, a248
Mvvv 196,108,52
ds_read_b128 v[76:79], v6 offset:34816
v_accvgpr_read_b32 v31, a249
Mvvv 228,104,56
ds_read_b64_tr_b16 v[126:127], v27 offset:41728
v_cvt_pk_bf16_f32 v18, v30, v31
Mvvv 228,108,60
s_waitcnt lgkmcnt(10)
ds_read_b128 v[80:83], v5 offset:36864
v_accvgpr_read_b32 v30, a250
Mvvv 136,112,32
s_waitcnt lgkmcnt(9)
ds_read_b64_tr_b16 v[96:97], v26 offset:33792
v_accvgpr_read_b32 v31, a251
Mvvv 136,116,36
ds_read_b128 v[84:87], v6 offset:36864
v_cvt_pk_bf16_f32 v19, v30, v31
Mvvv 168,112,40
s_add_u32 s73, s81, 39936
ds_read_b64_tr_b16 v[98:99], v26 offset:34048
v_accvgpr_write_b32 a21, 0
Mvvv 168,116,44
ds_read_b128 v[88:91], v5 offset:38912
v_accvgpr_write_b32 a22, 0
Mvvv 200,112,48
ds_read_b64_tr_b16 v[100:101], v26 offset:41984
v_accvgpr_write_b32 a23, 0
Mvvv 200,116,52
s_waitcnt lgkmcnt(11)
ds_read_b128 v[92:95], v6 offset:38912
v_accvgpr_write_b32 a24, 0
Mvvv 232,112,56
s_add_u32 m0, s69, 2048
s_add_u32 s72, s83, 36480
ds_read_b64_tr_b16 v[102:103], v26 offset:42240
Mvvv 232,116,60
s_waitcnt lgkmcnt(12)
buffer_load_dwordx4 v11, s[48:51], s72 offen sc1 lds
Mvvv 140,120,32
s_waitcnt lgkmcnt(8)
ds_read_b64_tr_b16 v[104:105], v27 offset:33792
v_accvgpr_write_b32 a25, 0
Mvvv 140,124,36
buffer_store_dwordx4 v[16:19], v0, s[48:51], s73 offen sc1
Mvvv 172,120,40
s_add_u32 m0, s69, 3072
s_add_u32 s72, s83, 36480
ds_read_b64_tr_b16 v[106:107], v27 offset:34048
Mvvv 172,124,44
buffer_load_dwordx4 v12, s[48:51], s72 offen sc1 lds
Mvvv 204,120,48
ds_read_b64_tr_b16 v[108:109], v27 offset:41984
v_accvgpr_read_b32 v30, a220
Mvvv 204,124,52
v_accvgpr_read_b32 v31, a221
v_cvt_pk_bf16_f32 v20, v30, v31
Mvvv 236,120,56
s_add_u32 m0, s69, 4096
s_add_u32 s72, s82, 1664
ds_read_b64_tr_b16 v[110:111], v27 offset:42240
Mvvv 236,124,60
s_waitcnt lgkmcnt(4)
buffer_load_dwordx4 v11, s[48:51], s72 offen sc1 lds
Mvvv 144,96,32
s_add_u32 m0, s71, 114688
s_add_u32 s72, s68, 6815744
ds_read_b64_tr_b16 v[112:113], v26 offset:34304
Mvvv 144,100,36
buffer_load_dwordx4 v3, s[16:19], s72 offen lds
Mvvv 176,96,40
s_add_u32 m0, s69, 5120
s_add_u32 s72, s82, 1664
ds_read_b64_tr_b16 v[114:115], v26 offset:34560
Mvvv 176,100,44
buffer_load_dwordx4 v12, s[48:51], s72 offen sc1 lds
Mvvv 208,96,48
s_add_u32 m0, s71, 115584
s_add_u32 s72, s68, 6815744
ds_read_b64_tr_b16 v[116:117], v26 offset:42496
Mvvv 208,100,52
buffer_load_dwordx4 v3, s[16:19], s72 offen offset:128 lds
Mvvv 240,96,56
s_add_u32 m0, s69, 6144
s_add_u32 s72, s82, 36480
ds_read_b64_tr_b16 v[118:119], v26 offset:42752
Mvvv 240,100,60
s_waitcnt lgkmcnt(4)
buffer_load_dwordx4 v11, s[48:51], s72 offen sc1 lds
Mvvv 148,104,32
s_add_u32 m0, s71, 122880
s_add_u32 s72, s68, 7077888
ds_read_b64_tr_b16 v[120:121], v27 offset:34304
Mvvv 148,108,36
buffer_load_dwordx4 v3, s[16:19], s72 offen lds
Mvvv 180,104,40
s_add_u32 m0, s69, 7168
s_add_u32 s72, s82, 36480
ds_read_b64_tr_b16 v[122:123], v27 offset:34560
Mvvv 180,108,44
buffer_load_dwordx4 v12, s[48:51], s72 offen sc1 lds
Mvvv 212,104,48
s_add_u32 m0, s71, 123776
s_add_u32 s72, s68, 7077888
ds_read_b64_tr_b16 v[124:125], v27 offset:42496
Mvvv 212,108,52
buffer_load_dwordx4 v3, s[16:19], s72 offen offset:128 lds
Mvvv 244,104,56
ds_read_b64_tr_b16 v[126:127], v27 offset:42752
v_accvgpr_read_b32 v30, a222
Mvvv 244,108,60
s_waitcnt lgkmcnt(6)
v_accvgpr_read_b32 v31, a223
v_cvt_pk_bf16_f32 v21, v30, v31
Mvvv 152,112,32
s_waitcnt lgkmcnt(4)
v_accvgpr_read_b32 v30, a252
v_accvgpr_read_b32 v31, a253
Mvvv 152,116,36
v_cvt_pk_bf16_f32 v22, v30, v31
v_accvgpr_read_b32 v30, a254
Mvvv 184,112,40
v_accvgpr_read_b32 v31, a255
v_cvt_pk_bf16_f32 v23, v30, v31
Mvvv 184,116,44
s_add_u32 s73, s81, 41984
buffer_store_dwordx4 v[20:23], v0, s[48:51], s73 offen sc1
Mvvv 216,112,48
s_bitcmp1_b32 0x60, s61
s_cselect_b64 s[20:21], s[28:29], s[44:45]
s_cselect_b64 s[22:23], s[30:31], s[46:47]
Mvvv 216,116,52
s_waitcnt vmcnt(30) lgkmcnt(0)
s_lshl2_add_u32 s58, s64, s57
s_mul_i32 s58, s58, 3072
Mvvv 248,112,56
s_barrier
s_lshl_b32 s59, s61, 2
Mvvv 248,116,60
s_lshr_b32 s59, 0x8a000000, s59
ds_read_b64_tr_b16 v[96:97], v26 offset:49152
v_mov_b32 v16, s74
Mvvv 156,120,32
s_and_b32 s59, s59, 15
ds_read_b64_tr_b16 v[98:99], v26 offset:49408
v_mov_b32 v17, s75
Mvvv 156,124,36
s_sub_u32 s59, s59, 1
s_mul_i32 s59, s59, 0x60000
ds_read_b64_tr_b16 v[100:101], v26 offset:57344
Mvvv 188,120,40
s_add_u32 s58, s58, s59
ds_read_b64_tr_b16 v[102:103], v26 offset:57600
v_accvgpr_write_b32 a26, 0
Mvvv 188,124,44
s_cmp_ge_u32 s61, 4
s_cselect_b32 s93, s58, 0x80000000
s_lshl_b32 s58, s61, 5
Mvvv 220,120,48
s_add_u32 s58, s58, s64
s_mul_i32 s94, s58, 36864
ds_read_b64_tr_b16 v[104:105], v27 offset:49152
Mvvv 220,124,52
s_add_u32 s94, s94, s69
s_add_u32 s94, s94, 54525952
ds_read_b64_tr_b16 v[106:107], v27 offset:49408
Mvvv 252,120,56
s_xor_b32 s58, s58, 0x1e0
s_mul_i32 s95, s58, 36864
ds_read_b64_tr_b16 v[108:109], v27 offset:57344
Mvvv 252,124,60
s_waitcnt lgkmcnt(5)
s_sub_u32 s95, s95, s69
s_add_u32 s95, s95, 54550528
ds_read_b64_tr_b16 v[110:111], v27 offset:57600
Mvvv 128,96,64
s_waitcnt lgkmcnt(4)
s_add_u32 m0, s69, 32768
s_add_u32 s72, s83, 1792
ds_read_b64_tr_b16 v[112:113], v26 offset:49664
Mvvv 128,100,68
s_lshl2_add_u32 s58, s57, s79
buffer_load_dwordx4 v11, s[48:51], s72 offen sc1 lds
Mvvv 160,96,72
s_lshl_b32 s58, s58, 10
s_lshr_b32 s59, s86, 1
ds_read_b64_tr_b16 v[114:115], v26 offset:49920
Mvvv 160,100,76
s_lshl_b32 s59, s59, 8
s_add_u32 s58, s58, s59
s_and_b32 s59, s86, 1
Mvvv 192,96,80
s_add_u32 m0, s69, 33792
s_add_u32 s72, s83, 1792
ds_read_b64_tr_b16 v[116:117], v26 offset:57856
Mvvv 192,100,84
s_lshl3_add_u32 s58, s59, s58
buffer_load_dwordx4 v12, s[48:51], s72 offen sc1 lds
Mvvv 224,96,88
s_waitcnt vmcnt(19) lgkmcnt(3)
s_lshl4_add_u32 s58, s61, s58
ds_read_b64_tr_b16 v[118:119], v26 offset:58112
Mvvv 224,100,92
s_add_u32 s96, s58, 32768
s_and_b32 s58, s57, 2
ds_read_b128 v[32:35], v7 offset:0
Mvvv 132,104,64
s_lshl_b32 s97, s58, 13
s_lshl2_add_u32 s98, s61, s58
ds_read_b64_tr_b16 v[120:121], v27 offset:49664
Mvvv 132,108,68
s_bitcmp1_b32 s57, 0
s_cselect_b64 s[4:5], -1, 0
ds_read_b128 v[36:39], v8 offset:0
Mvvv 164,104,72
s_getpc_b64 s[6:7]
.Lgb:
ds_read_b64_tr_b16 v[122:123], v27 offset:49920
v_accvgpr_write_b32 a27, 0
Mvvv 164,108,76
s_add_u32 s6, s6, .Lsb-.Lgb
s_addc_u32 s7, s7, 0
ds_read_b128 v[40:43], v7 offset:2048
Mvvv 196,104,80
s_mov_b32 s32, 0x91001000
s_mov_b32 s33, 0
ds_read_b64_tr_b16 v[124:125], v27 offset:57856
Mvvv 196,108,84
s_lshl2_add_u32 s58, s61, 5
s_bitset1_b64 s[32:33], s58
ds_read_b128 v[44:47], v8 offset:2048
Mvvv 228,104,88
s_waitcnt lgkmcnt(7)
ds_read_b64_tr_b16 v[126:127], v27 offset:58112
v_lshrrev_b32 v1, 1, v0
Mvvv 228,108,92
s_sub_u32 s58, 15, s61
s_lshr_b32 s58, s58, 1
ds_read_b128 v[48:51], v7 offset:4096
Mvvv 136,112,64
s_lshl_b32 s58, s58, 5
s_add_u32 s58, s58, 24
ds_read_b64_tr_b16 v[96:97], v26 offset:50176
Mvvv 136,116,68
ds_read_b128 v[52:55], v8 offset:4096
v_min_u32 v1, s58, v1
Mvvv 168,112,72
ds_read_b64_tr_b16 v[98:99], v26 offset:50432
v_bfe_u32 v25, v0, 6, 3
Mvvv 168,116,76
ds_read_b128 v[56:59], v7 offset:6144
)"
R"(
v_mul_u32_u24 v25, 7168, v25
Mvvv 200,112,80
ds_read_b64_tr_b16 v[100:101], v26 offset:58368
v_lshrrev_b32 v24, 9, v0
Mvvv 200,116,84
s_waitcnt lgkmcnt(11)
ds_read_b128 v[60:63], v8 offset:6144
v_lshl_add_u32 v25, v24, 6, v25
Mvvv 232,112,88
s_add_u32 m0, s69, 34816
s_add_u32 s72, s83, 36608
ds_read_b64_tr_b16 v[102:103], v26 offset:58624
Mvvv 232,116,92
s_waitcnt lgkmcnt(12)
buffer_load_dwordx4 v11, s[48:51], s72 offen sc1 lds
Mvvv 140,120,64
s_waitcnt lgkmcnt(8)
ds_read_b64_tr_b16 v[104:105], v27 offset:50176
v_and_b32 v24, 48, v0
Mvvv 140,124,68
v_add_u32 v25, v25, v24
v_bfe_u32 v24, v0, 6, 2
Mvvv 172,120,72
s_add_u32 m0, s69, 35840
s_add_u32 s72, s83, 36608
ds_read_b64_tr_b16 v[106:107], v27 offset:50432
Mvvv 172,124,76
buffer_load_dwordx4 v12, s[48:51], s72 offen sc1 lds
Mvvv 204,120,80
ds_read_b64_tr_b16 v[108:109], v27 offset:58368
v_lshlrev_b32 v24, 6, v24
Mvvv 204,124,84
v_bfe_u32 v2, v0, 4, 2
v_lshl_add_u32 v24, v2, 3, v24
Mvvv 236,120,88
s_add_u32 m0, s69, 36864
s_add_u32 s72, s82, 1792
ds_read_b64_tr_b16 v[110:111], v27 offset:58624
Mvvv 236,124,92
s_waitcnt lgkmcnt(4)
buffer_load_dwordx4 v11, s[48:51], s72 offen sc1 lds
Mvvv 144,96,64
s_add_u32 m0, s71, 131072
s_add_u32 s72, s68, 7340032
ds_read_b64_tr_b16 v[112:113], v26 offset:50688
Mvvv 144,100,68
buffer_load_dwordx4 v3, s[16:19], s72 offen lds
Mvvv 176,96,72
s_add_u32 m0, s69, 37888
s_add_u32 s72, s82, 1792
ds_read_b64_tr_b16 v[114:115], v26 offset:50944
Mvvv 176,100,76
buffer_load_dwordx4 v12, s[48:51], s72 offen sc1 lds
Mvvv 208,96,80
s_add_u32 m0, s71, 131968
s_add_u32 s72, s68, 7340032
ds_read_b64_tr_b16 v[116:117], v26 offset:58880
Mvvv 208,100,84
buffer_load_dwordx4 v3, s[16:19], s72 offen offset:128 lds
Mvvv 240,96,88
s_add_u32 m0, s69, 38912
s_add_u32 s72, s82, 36608
ds_read_b64_tr_b16 v[118:119], v26 offset:59136
Mvvv 240,100,92
s_waitcnt lgkmcnt(4)
buffer_load_dwordx4 v11, s[48:51], s72 offen sc1 lds
Mvvv 148,104,64
s_add_u32 m0, s71, 139264
s_add_u32 s72, s68, 7602176
ds_read_b64_tr_b16 v[120:121], v27 offset:50688
Mvvv 148,108,68
buffer_load_dwordx4 v3, s[16:19], s72 offen lds
Mvvv 180,104,72
s_add_u32 m0, s69, 39936
s_add_u32 s72, s82, 36608
ds_read_b64_tr_b16 v[122:123], v27 offset:50944
Mvvv 180,108,76
buffer_load_dwordx4 v12, s[48:51], s72 offen sc1 lds
Mvvv 212,104,80
s_add_u32 m0, s71, 140160
s_add_u32 s72, s68, 7602176
ds_read_b64_tr_b16 v[124:125], v27 offset:58880
Mvvv 212,108,84
buffer_load_dwordx4 v3, s[16:19], s72 offen offset:128 lds
Mvvv 244,104,88
ds_read_b64_tr_b16 v[126:127], v27 offset:59136
v_lshrrev_b32 v2, 8, v0
Mvvv 244,108,92
s_waitcnt lgkmcnt(4)
v_lshl_add_u32 v24, v2, 10, v24
v_accvgpr_write_b32 a28, 0
Mvvv 152,112,64
s_add_u32 s59, s69, 131072
v_add_u32 v24, s59, v24
v_accvgpr_write_b32 a29, 0
Mvvv 152,116,68
v_accvgpr_write_b32 a30, 0
v_accvgpr_write_b32 a31, 0
Mvvv 184,112,72
v_accvgpr_write_b32 a32, 0
v_accvgpr_write_b32 a33, 0
Mvvv 184,116,76
v_accvgpr_write_b32 a34, 0
v_accvgpr_write_b32 a35, 0
Mvvv 216,112,80
v_accvgpr_write_b32 a36, 0
v_accvgpr_write_b32 a37, 0
Mvvv 216,116,84
s_waitcnt vmcnt(28) lgkmcnt(0)
v_accvgpr_write_b32 a38, 0
v_accvgpr_write_b32 a39, 0
Mvvv 248,112,88
s_barrier
Mvvv 248,116,92
ds_read_b64_tr_b16 v[96:97], v26 offset:0
v_accvgpr_write_b32 a40, 0
Mvvv 156,120,64
ds_read_b64_tr_b16 v[98:99], v26 offset:256
v_accvgpr_write_b32 a41, 0
Mvvv 156,124,68
ds_read_b64_tr_b16 v[100:101], v26 offset:8192
v_accvgpr_write_b32 a42, 0
Mvvv 188,120,72
ds_read_b64_tr_b16 v[102:103], v26 offset:8448
v_accvgpr_write_b32 a43, 0
Mvvv 188,124,76
v_accvgpr_write_b32 a44, 0
v_accvgpr_write_b32 a45, 0
Mvvv 220,120,80
ds_read_b64_tr_b16 v[104:105], v27 offset:0
v_accvgpr_write_b32 a46, 0
Mvvv 220,124,84
ds_read_b64_tr_b16 v[106:107], v27 offset:256
v_accvgpr_write_b32 a47, 0
Mvvv 252,120,88
ds_read_b64_tr_b16 v[108:109], v27 offset:8192
v_accvgpr_write_b32 a48, 0
Mvvv 252,124,92
s_waitcnt lgkmcnt(5)
ds_read_b64_tr_b16 v[110:111], v27 offset:8448
v_accvgpr_write_b32 a49, 0
Mvvv 128,96,32
s_waitcnt lgkmcnt(4)
s_add_u32 m0, s69, 65536
s_add_u32 s72, s83, 1920
ds_read_b64_tr_b16 v[112:113], v26 offset:512
Mvvv 128,100,36
buffer_load_dwordx4 v11, s[48:51], s72 offen sc1 lds
Mvvv 160,96,40
ds_read_b64_tr_b16 v[114:115], v26 offset:768
v_accvgpr_write_b32 a50, 0
Mvvv 160,100,44
v_accvgpr_write_b32 a51, 0
v_accvgpr_write_b32 a52, 0
Mvvv 192,96,48
s_add_u32 m0, s69, 66560
s_add_u32 s72, s83, 1920
ds_read_b64_tr_b16 v[116:117], v26 offset:8704
Mvvv 192,100,52
buffer_load_dwordx4 v12, s[48:51], s72 offen sc1 lds
Mvvv 224,96,56
s_waitcnt vmcnt(16)
ds_read_b64_tr_b16 v[118:119], v26 offset:8960
v_accvgpr_write_b32 a53, 0
Mvvv 224,100,60
s_waitcnt lgkmcnt(6)
ds_read_b128 v[64:67], v5 offset:0
v_accvgpr_write_b32 a54, 0
Mvvv 132,104,32
s_waitcnt lgkmcnt(5)
ds_read_b64_tr_b16 v[120:121], v27 offset:512
v_accvgpr_write_b32 a55, 0
Mvvv 132,108,36
ds_read_b128 v[68:71], v6 offset:0
v_accvgpr_write_b32 a56, 0
Mvvv 164,104,40
ds_read_b64_tr_b16 v[122:123], v27 offset:768
v_accvgpr_write_b32 a57, 0
Mvvv 164,108,44
ds_read_b128 v[72:75], v5 offset:2048
v_accvgpr_write_b32 a58, 0
Mvvv 196,104,48
ds_read_b64_tr_b16 v[124:125], v27 offset:8704
v_accvgpr_write_b32 a59, 0
Mvvv 196,108,52
ds_read_b128 v[76:79], v6 offset:2048
v_accvgpr_write_b32 a60, 0
Mvvv 228,104,56
ds_read_b64_tr_b16 v[126:127], v27 offset:8960
v_accvgpr_write_b32 a61, 0
Mvvv 228,108,60
s_waitcnt lgkmcnt(10)
ds_read_b128 v[80:83], v5 offset:4096
v_accvgpr_write_b32 a62, 0
Mvvv 136,112,32
s_waitcnt lgkmcnt(9)
ds_read_b64_tr_b16 v[96:97], v26 offset:1024
v_accvgpr_write_b32 a63, 0
Mvvv 136,116,36
ds_read_b128 v[84:87], v6 offset:4096
v_accvgpr_write_b32 a64, 0
Mvvv 168,112,40
ds_read_b64_tr_b16 v[98:99], v26 offset:1280
v_accvgpr_write_b32 a65, 0
Mvvv 168,116,44
ds_read_b128 v[88:91], v5 offset:6144
v_accvgpr_write_b32 a66, 0
Mvvv 200,112,48
ds_read_b64_tr_b16 v[100:101], v26 offset:9216
v_accvgpr_write_b32 a67, 0
Mvvv 200,116,52
s_waitcnt lgkmcnt(11)
ds_read_b128 v[92:95], v6 offset:6144
v_accvgpr_write_b32 a68, 0
Mvvv 232,112,56
s_add_u32 m0, s69, 67584
s_add_u32 s72, s83, 36736
ds_read_b64_tr_b16 v[102:103], v26 offset:9472
Mvvv 232,116,60
s_waitcnt lgkmcnt(12)
buffer_load_dwordx4 v11, s[48:51], s72 offen sc1 lds
Mvvv 140,120,32
s_waitcnt lgkmcnt(8)
ds_read_b64_tr_b16 v[104:105], v27 offset:1024
v_accvgpr_write_b32 a69, 0
Mvvv 140,124,36
v_accvgpr_write_b32 a70, 0
v_accvgpr_write_b32 a71, 0
Mvvv 172,120,40
s_add_u32 m0, s69, 68608
s_add_u32 s72, s83, 36736
ds_read_b64_tr_b16 v[106:107], v27 offset:1280
Mvvv 172,124,44
buffer_load_dwordx4 v12, s[48:51], s72 offen sc1 lds
Mvvv 204,120,48
s_waitcnt vmcnt(16)
ds_read_b64_tr_b16 v[108:109], v27 offset:9216
v_accvgpr_write_b32 a72, 0
Mvvv 204,124,52
buffer_store_dwordx2 v[16:17], v13, s[48:51], s85 offen sc1
Mvvv 236,120,56
s_add_u32 m0, s69, 69632
s_add_u32 s72, s82, 1920
ds_read_b64_tr_b16 v[110:111], v27 offset:9472
Mvvv 236,124,60
s_waitcnt lgkmcnt(4)
buffer_load_dwordx4 v11, s[48:51], s72 offen sc1 lds
Mvvv 144,96,32
s_add_u32 m0, s71, 147456
s_add_u32 s72, s68, 7864320
ds_read_b64_tr_b16 v[112:113], v26 offset:1536
Mvvv 144,100,36
buffer_load_dwordx4 v3, s[16:19], s72 offen lds
Mvvv 176,96,40
s_add_u32 m0, s69, 70656
s_add_u32 s72, s82, 1920
ds_read_b64_tr_b16 v[114:115], v26 offset:1792
Mvvv 176,100,44
buffer_load_dwordx4 v12, s[48:51], s72 offen sc1 lds
Mvvv 208,96,48
s_add_u32 m0, s71, 148352
s_add_u32 s72, s68, 7864320
ds_read_b64_tr_b16 v[116:117], v26 offset:9728
Mvvv 208,100,52
buffer_load_dwordx4 v3, s[16:19], s72 offen offset:128 lds
Mvvv 240,96,56
s_add_u32 m0, s69, 71680
s_add_u32 s72, s82, 36736
ds_read_b64_tr_b16 v[118:119], v26 offset:9984
Mvvv 240,100,60
s_waitcnt lgkmcnt(4)
buffer_load_dwordx4 v11, s[48:51], s72 offen sc1 lds
Mvvv 148,104,32
s_add_u32 m0, s71, 155648
s_add_u32 s72, s68, 8126464
ds_read_b64_tr_b16 v[120:121], v27 offset:1536
Mvvv 148,108,36
buffer_load_dwordx4 v3, s[16:19], s72 offen lds
Mvvv 180,104,40
s_add_u32 m0, s69, 72704
s_add_u32 s72, s82, 36736
ds_read_b64_tr_b16 v[122:123], v27 offset:1792
Mvvv 180,108,44
buffer_load_dwordx4 v12, s[48:51], s72 offen sc1 lds
Mvvv 212,104,48
s_add_u32 m0, s71, 156544
s_add_u32 s72, s68, 8126464
ds_read_b64_tr_b16 v[124:125], v27 offset:9728
Mvvv 212,108,52
buffer_load_dwordx4 v3, s[16:19], s72 offen offset:128 lds
Mvvv 244,104,56
ds_read_b64_tr_b16 v[126:127], v27 offset:9984
v_accvgpr_write_b32 a73, 0
Mvvv 244,108,60
s_waitcnt lgkmcnt(6)
v_accvgpr_write_b32 a74, 0
v_accvgpr_write_b32 a75, 0
Mvvv 152,112,32
s_waitcnt lgkmcnt(4)
v_accvgpr_write_b32 a76, 0
v_accvgpr_write_b32 a77, 0
Mvvv 152,116,36
v_accvgpr_write_b32 a78, 0
v_accvgpr_write_b32 a79, 0
Mvvv 184,112,40
v_accvgpr_write_b32 a80, 0
v_accvgpr_write_b32 a81, 0
Mvvv 184,116,44
v_accvgpr_write_b32 a82, 0
v_accvgpr_write_b32 a83, 0
Mvvv 216,112,48
v_accvgpr_write_b32 a84, 0
v_accvgpr_write_b32 a85, 0
Mvvv 216,116,52
s_waitcnt lgkmcnt(0)
v_accvgpr_write_b32 a86, 0
v_accvgpr_write_b32 a87, 0
Mvvv 248,112,56
s_barrier
Mvvv 248,116,60
ds_read_b64_tr_b16 v[96:97], v26 offset:16384
v_accvgpr_write_b32 a88, 0
Mvvv 156,120,32
ds_read_b64_tr_b16 v[98:99], v26 offset:16640
v_accvgpr_write_b32 a89, 0
Mvvv 156,124,36
ds_read_b64_tr_b16 v[100:101], v26 offset:24576
v_accvgpr_write_b32 a90, 0
Mvvv 188,120,40
ds_read_b64_tr_b16 v[102:103], v26 offset:24832
v_accvgpr_write_b32 a91, 0
Mvvv 188,124,44
v_accvgpr_write_b32 a92, 0
v_accvgpr_write_b32 a93, 0
Mvvv 220,120,48
ds_read_b64_tr_b16 v[104:105], v27 offset:16384
v_accvgpr_write_b32 a94, 0
Mvvv 220,124,52
ds_read_b64_tr_b16 v[106:107], v27 offset:16640
v_accvgpr_write_b32 a95, 0
Mvvv 252,120,56
ds_read_b64_tr_b16 v[108:109], v27 offset:24576
v_accvgpr_write_b32 a96, 0
Mvvv 252,124,60
s_waitcnt lgkmcnt(5)
ds_read_b64_tr_b16 v[110:111], v27 offset:24832
v_accvgpr_write_b32 a97, 0
Mvvv 128,96,64
s_waitcnt lgkmcnt(4)
ds_read_b64_tr_b16 v[112:113], v26 offset:16896
v_accvgpr_write_b32 a98, 0
Mvvv 128,100,68
v_accvgpr_write_b32 a99, 0
v_accvgpr_write_b32 a100, 0
Mvvv 160,96,72
ds_read_b64_tr_b16 v[114:115], v26 offset:17152
v_accvgpr_write_b32 a101, 0
Mvvv 160,100,76
v_accvgpr_write_b32 a102, 0
v_accvgpr_write_b32 a103, 0
Mvvv 192,96,80
s_lshl2_add_u32 s82, s60, s57
s_lshl_b32 s82, s82, 10
ds_read_b64_tr_b16 v[116:117], v26 offset:25088
Mvvv 192,100,84
s_mov_b32 s83, .Lhot_end-.Lstart
s_min_u32 s82, s82, s83
v_accvgpr_write_b32 a104, 0
Mvvv 224,96,88
s_waitcnt vmcnt(14) lgkmcnt(3)
s_getpc_b64 s[16:17]
.Lcp3:
ds_read_b64_tr_b16 v[118:119], v26 offset:25344
Mvvv 224,100,92
s_sub_u32 s16, s16, .Lcp3-.Lstart
s_subb_u32 s17, s17, 0
ds_read_b128 v[32:35], v5 offset:32768
Mvvv 132,104,64
s_add_u32 s16, s16, s82
s_addc_u32 s17, s17, 0
ds_read_b64_tr_b16 v[120:121], v27 offset:16896
Mvvv 132,108,68
s_and_b32 s16, s16, -16
ds_read_b128 v[36:39], v6 offset:32768
v_accvgpr_write_b32 a105, 0
Mvvv 164,104,72
ds_read_b64_tr_b16 v[122:123], v27 offset:17152
v_accvgpr_write_b32 a106, 0
Mvvv 164,108,76
ds_read_b128 v[40:43], v5 offset:34816
v_accvgpr_write_b32 a107, 0
Mvvv 196,104,80
ds_read_b64_tr_b16 v[124:125], v27 offset:25088
v_accvgpr_write_b32 a108, 0
Mvvv 196,108,84
ds_read_b128 v[44:47], v6 offset:34816
v_accvgpr_write_b32 a109, 0
Mvvv 228,104,88
ds_read_b64_tr_b16 v[126:127], v27 offset:25344
v_accvgpr_write_b32 a110, 0
Mvvv 228,108,92
s_waitcnt lgkmcnt(10)
ds_read_b128 v[48:51], v5 offset:36864
v_accvgpr_write_b32 a111, 0
Mvvv 136,112,64
s_waitcnt lgkmcnt(9)
ds_read_b64_tr_b16 v[96:97], v26 offset:17408
v_accvgpr_write_b32 a112, 0
Mvvv 136,116,68
ds_read_b128 v[52:55], v6 offset:36864
v_accvgpr_write_b32 a113, 0
Mvvv 168,112,72
ds_read_b64_tr_b16 v[98:99], v26 offset:17664
v_accvgpr_write_b32 a114, 0
Mvvv 168,116,76
ds_read_b128 v[56:59], v5 offset:38912
v_accvgpr_write_b32 a115, 0
Mvvv 200,112,80
ds_read_b64_tr_b16 v[100:101], v26 offset:25600
v_accvgpr_write_b32 a116, 0
Mvvv 200,116,84
ds_read_b128 v[60:63], v6 offset:38912
v_accvgpr_write_b32 a117, 0
Mvvv 232,112,88
s_waitcnt lgkmcnt(12)
ds_read_b64_tr_b16 v[102:103], v26 offset:25856
v_accvgpr_write_b32 a118, 0
Mvvv 232,116,92
s_waitcnt lgkmcnt(12)
global_load_dwordx4 v[28:31], v0, s[16:17]
)"
R"(
Mvvv 140,120,64
s_waitcnt lgkmcnt(8)
ds_read_b64_tr_b16 v[104:105], v27 offset:17408
v_accvgpr_write_b32 a119, 0
Mvvv 140,124,68
v_accvgpr_write_b32 a120, 0
v_accvgpr_write_b32 a121, 0
Mvvv 172,120,72
ds_read_b64_tr_b16 v[106:107], v27 offset:17664
v_accvgpr_write_b32 a122, 0
Mvvv 172,124,76
v_accvgpr_write_b32 a123, 0
v_accvgpr_write_b32 a124, 0
Mvvv 204,120,80
ds_read_b64_tr_b16 v[108:109], v27 offset:25600
v_accvgpr_write_b32 a125, 0
Mvvv 204,124,84
v_accvgpr_write_b32 a126, 0
v_accvgpr_write_b32 a127, 0
Mvvv 236,120,88
s_lshl3_add_u32 s88, s57, 0x18000
ds_read_b64_tr_b16 v[110:111], v27 offset:25856
v_mov_b32 v9, s88
Mvvv 236,124,92
s_waitcnt lgkmcnt(8)
v_mov_b32 v22, s89
v_mov_b32 v23, s90
Mvvv 144,96,64
s_waitcnt lgkmcnt(4)
ds_read_b64_tr_b16 v[112:113], v26 offset:17920
Mvvv 144,100,68
ds_write_b64 v9, v[22:23]
Mvvv 176,96,72
ds_read_b64_tr_b16 v[114:115], v26 offset:18176
v_bfe_u32 v9, v0, 4, 3
Mvvv 176,100,76
v_lshlrev_b32 v9, 2, v9
v_add_u32 v9, 0x18000, v9
Mvvv 208,96,80
ds_read_b64_tr_b16 v[116:117], v26 offset:26112
Mvvv 208,100,84
Mvvv 240,96,88
ds_read_b64_tr_b16 v[118:119], v26 offset:26368
Mvvv 240,100,92
s_waitcnt lgkmcnt(7)
Mvvv 148,104,64
s_waitcnt lgkmcnt(5)
ds_read_b64_tr_b16 v[120:121], v27 offset:17920
Mvvv 148,108,68
Mvvv 180,104,72
ds_read_b64_tr_b16 v[122:123], v27 offset:18176
Mvvv 180,108,76
Mvvv 212,104,80
ds_read_b64_tr_b16 v[124:125], v27 offset:26112
Mvvv 212,108,84
Mvvv 244,104,88
ds_read_b64_tr_b16 v[126:127], v27 offset:26368
Mvvv 244,108,92
s_waitcnt lgkmcnt(6)
Mvvv 152,112,64
s_waitcnt lgkmcnt(4)
Mvvv 152,116,68
Mvvv 184,112,72
Mvvv 184,116,76
Mvvv 216,112,80
Mvvv 216,116,84
s_waitcnt vmcnt(14) lgkmcnt(0)
Mvvv 248,112,88
s_barrier
Mvvv 248,116,92
ds_read_b64_tr_b16 v[96:97], v26 offset:32768
Mvvv 156,120,64
ds_read_b64_tr_b16 v[98:99], v26 offset:33024
Mvvv 156,124,68
ds_read_b64_tr_b16 v[100:101], v26 offset:40960
Mvvv 188,120,72
ds_read_b64_tr_b16 v[102:103], v26 offset:41216
Mvvv 188,124,76
ds_read_b32 v22, v9
Mvvv 220,120,80
ds_read_b64_tr_b16 v[104:105], v27 offset:32768
Mvvv 220,124,84
ds_read_b64_tr_b16 v[106:107], v27 offset:33024
Mvvv 252,120,88
ds_read_b64_tr_b16 v[108:109], v27 offset:40960
Mvvv 252,124,92
s_waitcnt lgkmcnt(6)
ds_read_b64_tr_b16 v[110:111], v27 offset:41216
Mvvv 128,96,32
s_waitcnt lgkmcnt(5)
s_mov_b32 s35, 0
s_or_b32 s81, s92, 0x10000
ds_read_b64_tr_b16 v[112:113], v26 offset:33280
Mvvv 128,100,36
buffer_load_dwordx2 v[20:21], v1, s[48:51], s84 offen sc1
Mvvv 160,96,40
s_waitcnt lgkmcnt(5)
ds_read_b64_tr_b16 v[114:115], v26 offset:33536
v_add_u32 v2, s81, v0
Mvvv 160,100,44
s_cmp_eq_u32 s97, 0
s_cselect_b32 s85, 0x30000, s70
v_mov_b32 v10, v22
Mvvv 192,96,48
s_cmp_eq_u32 s98, 2
s_cselect_b32 s80, s97, 0
ds_read_b64_tr_b16 v[116:117], v26 offset:41472
Mvvv 192,100,52
s_add_u32 s80, s80, s92
s_add_u32 s80, s80, 0x4000
v_max_u32_dpp v10, v10, v10 quad_perm:[1,0,3,2] row_mask:0xf bank_mask:0xf bound_ctrl:1
Mvvv 224,96,56
s_waitcnt vmcnt(3)
s_and_b32 s80, s80, 0xc000
ds_read_b64_tr_b16 v[118:119], v26 offset:41728
Mvvv 224,100,60
s_waitcnt lgkmcnt(6)
ds_read_b128 v[64:67], v7 offset:0
v_max_u32_dpp v10, v10, v10 quad_perm:[2,3,0,1] row_mask:0xf bank_mask:0xf bound_ctrl:1
Mvvv 132,104,32
s_waitcnt lgkmcnt(5)
ds_read_b64_tr_b16 v[120:121], v27 offset:33280
Mvvv 132,108,36
ds_read_b128 v[68:71], v8 offset:0
v_max_u32_dpp v10, v10, v10 row_half_mirror row_mask:0xf bank_mask:0xf bound_ctrl:1
Mvvv 164,104,40
ds_read_b64_tr_b16 v[122:123], v27 offset:33536
v_cmp_eq_u32 vcc, v10, v22
Mvvv 164,108,44
ds_read_b128 v[72:75], v7 offset:2048
Mvvv 196,104,48
ds_read_b64_tr_b16 v[124:125], v27 offset:41472
v_cndmask_b32 v22, v22, 0, vcc
Mvvv 196,108,52
ds_read_b128 v[76:79], v8 offset:2048
Mvvv 228,104,56
ds_read_b64_tr_b16 v[126:127], v27 offset:41728
v_max_u32_dpp v22, v22, v22 quad_perm:[1,0,3,2] row_mask:0xf bank_mask:0xf bound_ctrl:1
Mvvv 228,108,60
s_waitcnt lgkmcnt(10)
ds_read_b128 v[80:83], v7 offset:4096
Mvvv 136,112,32
s_waitcnt lgkmcnt(9)
ds_read_b64_tr_b16 v[96:97], v26 offset:33792
v_max_u32_dpp v22, v22, v22 quad_perm:[2,3,0,1] row_mask:0xf bank_mask:0xf bound_ctrl:1
Mvvv 136,116,36
ds_read_b128 v[84:87], v8 offset:4096
Mvvv 168,112,40
ds_read_b64_tr_b16 v[98:99], v26 offset:34048
v_max_u32_dpp v22, v22, v22 row_half_mirror row_mask:0xf bank_mask:0xf bound_ctrl:1
Mvvv 168,116,44
ds_read_b128 v[88:91], v7 offset:6144
v_readfirstlane_b32 s89, v10
Mvvv 200,112,48
ds_read_b64_tr_b16 v[100:101], v26 offset:41984
v_readfirstlane_b32 s90, v22
Mvvv 200,116,52
ds_read_b128 v[92:95], v8 offset:6144
Mvvv 232,112,56
s_waitcnt lgkmcnt(12)
ds_read_b64_tr_b16 v[102:103], v26 offset:42240
Mvvv 232,116,60
s_waitcnt lgkmcnt(12)
Mvvv 140,120,32
s_waitcnt lgkmcnt(8)
ds_read_b64_tr_b16 v[104:105], v27 offset:33792
Mvvv 140,124,36
Mvvv 172,120,40
ds_read_b64_tr_b16 v[106:107], v27 offset:34048
Mvvv 172,124,44
Mvvv 204,120,48
ds_read_b64_tr_b16 v[108:109], v27 offset:41984
Mvvv 204,124,52
Mvvv 236,120,56
ds_read_b64_tr_b16 v[110:111], v27 offset:42240
Mvvv 236,124,60
s_waitcnt lgkmcnt(8)
Mvvv 144,96,32
s_waitcnt lgkmcnt(4)
ds_read_b64_tr_b16 v[112:113], v26 offset:34304
Mvvv 144,100,36
Mvvv 176,96,40
ds_read_b64_tr_b16 v[114:115], v26 offset:34560
Mvvv 176,100,44
Mvvv 208,96,48
ds_read_b64_tr_b16 v[116:117], v26 offset:42496
Mvvv 208,100,52
Mvvv 240,96,56
ds_read_b64_tr_b16 v[118:119], v26 offset:42752
Mvvv 240,100,60
s_waitcnt lgkmcnt(6)
Mvvv 148,104,32
s_waitcnt lgkmcnt(4)
ds_read_b64_tr_b16 v[120:121], v27 offset:34304
Mvvv 148,108,36
Mvvv 180,104,40
ds_read_b64_tr_b16 v[122:123], v27 offset:34560
Mvvv 180,108,44
Mvvv 212,104,48
ds_read_b64_tr_b16 v[124:125], v27 offset:42496
Mvvv 212,108,52
Mvvv 244,104,56
ds_read_b64_tr_b16 v[126:127], v27 offset:42752
Mvvv 244,108,60
s_waitcnt lgkmcnt(6)
Mvvv 152,112,32
s_waitcnt vmcnt(0) lgkmcnt(4)
v_xor_b32 v20, s74, v20
v_bitop3_b32 v20, v21, s75, v20 bitop3:0xbe
Mvvv 152,116,36
v_cmp_ne_u32 vcc, 0, v20
Mvvv 184,112,40
s_cbranch_vccnz .Lkvf
.Lkvf_r:
Mvvv 184,116,44
v_mov_b32 v9, s89
v_mov_b32 v10, s90
Mvvv 216,112,48
s_mov_b32 s18, 0xaaaaaaaa
s_mov_b32 s19, 0xaaaaaaaa
v_cndmask_b32 v9, v9, v10, s[18:19]
Mvvv 216,116,52
s_waitcnt lgkmcnt(0)
v_not_b32 v9, v9
v_and_b32 v9, 0x7ff, v9
Mvvv 248,112,56
s_barrier
Mvvv 248,116,60
ds_read_b64_tr_b16 v[96:97], v26 offset:49152
v_mul_u32_u24 v9, 2176, v9
Mvvv 156,120,32
ds_read_b64_tr_b16 v[98:99], v26 offset:49408
v_readlane_b32 s34, v4, 60
Mvvv 156,124,36
s_add_u32 s58, s92, 0
s_and_b32 s58, s58, 0xc000
ds_read_b64_tr_b16 v[100:101], v26 offset:57344
Mvvv 188,120,40
s_add_u32 s58, s58, s70
s_add_u32 m0, s58, 0
ds_read_b64_tr_b16 v[102:103], v26 offset:57600
Mvvv 188,124,44
v_lshrrev_b32 v10, 8, v0
v_lshl_add_u32 v9, v10, 4, v9
Mvvv 220,120,48
s_lshr_b32 s59, s60, 3
s_lshl2_add_u32 s59, s59, s57
ds_read_b64_tr_b16 v[104:105], v27 offset:49152
Mvvv 220,124,52
s_lshl_b32 s59, s59, 7
s_add_u32 s59, s59, 4194304
ds_read_b64_tr_b16 v[106:107], v27 offset:49408
Mvvv 252,120,56
ds_read_b64_tr_b16 v[108:109], v27 offset:57344
Mvvv 252,124,60
s_waitcnt lgkmcnt(5)
ds_read_b64_tr_b16 v[110:111], v27 offset:57600
Mvvv 128,96,64
s_waitcnt lgkmcnt(4)
ds_read_b64_tr_b16 v[112:113], v26 offset:49664
Mvvv 128,100,68
buffer_load_dwordx4 v0, s[48:51], s34 offen sc1 lds
Mvvv 160,96,72
ds_read_b64_tr_b16 v[114:115], v26 offset:49920
Mvvv 160,100,76
buffer_load_dwordx4 v0, s[48:51], s34 offen offset:1024 sc1 lds
Mvvv 192,96,80
ds_read_b64_tr_b16 v[116:117], v26 offset:57856
Mvvv 192,100,84
buffer_load_dwordx4 v0, s[48:51], s34 offen offset:2048 sc1 lds
Mvvv 224,96,88
ds_read_b64_tr_b16 v[118:119], v26 offset:58112
Mvvv 224,100,92
s_waitcnt lgkmcnt(6)
buffer_load_dwordx4 v0, s[48:51], s34 offen offset:3072 sc1 lds
Mvvv 132,104,64
s_waitcnt lgkmcnt(4)
ds_read_b64_tr_b16 v[120:121], v27 offset:49664
v_readlane_b32 s34, v4, 61
Mvvv 132,108,68
s_add_u32 s58, s92, 16384
s_and_b32 s58, s58, 0xc000
s_add_u32 s58, s58, s70
Mvvv 164,104,72
s_add_u32 m0, s58, 0
ds_read_b64_tr_b16 v[122:123], v27 offset:49920
Mvvv 164,108,76
buffer_load_dwordx4 v[16:19], v9, s[48:51], s59 offen sc1
Mvvv 196,104,80
ds_read_b64_tr_b16 v[124:125], v27 offset:57856
Mvvv 196,108,84
buffer_load_dwordx4 v0, s[48:51], s34 offen sc1 lds
Mvvv 228,104,88
ds_read_b64_tr_b16 v[126:127], v27 offset:58112
Mvvv 228,108,92
s_waitcnt lgkmcnt(6)
buffer_load_dwordx4 v0, s[48:51], s34 offen offset:1024 sc1 lds
Mvvv 136,112,64
s_waitcnt lgkmcnt(4)
ds_read_b64_tr_b16 v[96:97], v26 offset:50176
Mvvv 136,116,68
buffer_load_dwordx4 v0, s[48:51], s34 offen offset:2048 sc1 lds
Mvvv 168,112,72
ds_read_b64_tr_b16 v[98:99], v26 offset:50432
v_cvt_pk_bf16_f32 v128, v128, v129
Mvvv 168,116,76
v_cvt_pk_bf16_f32 v129, v130, v131
v_cvt_pk_bf16_f32 v130, v132, v133
Mvvv 200,112,80
ds_read_b64_tr_b16 v[100:101], v26 offset:58368
v_cvt_pk_bf16_f32 v131, v134, v135
Mvvv 200,116,84
v_cvt_pk_bf16_f32 v160, v160, v161
v_cvt_pk_bf16_f32 v161, v162, v163
Mvvv 232,112,88
ds_read_b64_tr_b16 v[102:103], v26 offset:58624
v_cvt_pk_bf16_f32 v162, v164, v165
Mvvv 232,116,92
s_waitcnt lgkmcnt(6)
v_cvt_pk_bf16_f32 v163, v166, v167
v_cvt_pk_bf16_f32 v192, v192, v193
Mvvv 140,120,64
s_waitcnt lgkmcnt(4)
ds_read_b64_tr_b16 v[104:105], v27 offset:50176
v_cvt_pk_bf16_f32 v193, v194, v195
Mvvv 140,124,68
v_cvt_pk_bf16_f32 v194, v196, v197
v_cvt_pk_bf16_f32 v195, v198, v199
Mvvv 172,120,72
ds_read_b64_tr_b16 v[106:107], v27 offset:50432
v_cvt_pk_bf16_f32 v224, v224, v225
Mvvv 172,124,76
v_cvt_pk_bf16_f32 v225, v226, v227
v_cvt_pk_bf16_f32 v226, v228, v229
Mvvv 204,120,80
ds_read_b64_tr_b16 v[108:109], v27 offset:58368
v_cvt_pk_bf16_f32 v227, v230, v231
Mvvv 204,124,84
buffer_load_dwordx4 v0, s[48:51], s34 offen offset:3072 sc1 lds
Mvvv 236,120,88
s_waitcnt lgkmcnt(5)
ds_read_b64_tr_b16 v[110:111], v27 offset:58624
v_readlane_b32 s34, v4, 62
Mvvv 236,124,92
s_add_u32 s58, s92, 32768
s_and_b32 s58, s58, 0xc000
s_add_u32 s58, s58, s70
Mvvv 144,96,64
s_waitcnt lgkmcnt(4)
s_add_u32 m0, s58, 0
ds_read_b64_tr_b16 v[112:113], v26 offset:50688
Mvvv 144,100,68
s_waitcnt lgkmcnt(3)
buffer_load_dwordx4 v[20:23], v9, s[48:51], s59 offen offset:64 sc1
Mvvv 176,96,72
s_mov_b32 s99, 1
ds_read_b64_tr_b16 v[114:115], v26 offset:50944
v_cvt_pk_bf16_f32 v136, v136, v137
Mvvv 176,100,76
s_lshr_b32 s59, s60, 3
v_cvt_pk_bf16_f32 v137, v138, v139
v_cvt_pk_bf16_f32 v138, v140, v141
Mvvv 208,96,80
s_lshl2_add_u32 s59, s59, s57
ds_read_b64_tr_b16 v[116:117], v26 offset:58880
v_cvt_pk_bf16_f32 v139, v142, v143
Mvvv 208,100,84
s_waitcnt lgkmcnt(3)
s_mul_i32 s59, s59, 458752
v_cvt_pk_bf16_f32 v168, v168, v169
v_cvt_pk_bf16_f32 v169, v170, v171
Mvvv 240,96,88
s_lshl_b32 s68, s91, 7
ds_read_b64_tr_b16 v[118:119], v26 offset:59136
v_cvt_pk_bf16_f32 v170, v172, v173
Mvvv 240,100,92
s_add_u32 s68, s68, s59
v_cvt_pk_bf16_f32 v171, v174, v175
v_cvt_pk_bf16_f32 v200, v200, v201
Mvvv 148,104,64
s_add_u32 s71, s69, 131072
ds_read_b64_tr_b16 v[120:121], v27 offset:50688
v_cvt_pk_bf16_f32 v201, v202, v203
Mvvv 148,108,68
s_add_u32 s73, s99, 8
v_cvt_pk_bf16_f32 v202, v204, v205
v_cvt_pk_bf16_f32 v203, v206, v207
Mvvv 180,104,72
s_mov_b32 s62, 196608
ds_read_b64_tr_b16 v[122:123], v27 offset:50944
v_cvt_pk_bf16_f32 v232, v232, v233
Mvvv 180,108,76
s_mov_b32 s63, 57344
v_cvt_pk_bf16_f32 v233, v234, v235
v_cvt_pk_bf16_f32 v234, v236, v237
Mvvv 212,104,80
s_mov_b32 s64, 1024
ds_read_b64_tr_b16 v[124:125], v27 offset:58880
v_cvt_pk_bf16_f32 v235, v238, v239
Mvvv 212,108,84
buffer_load_dwordx4 v0, s[48:51], s34 offen sc1 lds
Mvvv 244,104,88
s_cmp_eq_u32 s61, 0
s_cselect_b32 s73, 0, s73
ds_read_b64_tr_b16 v[126:127], v27 offset:59136
Mvvv 244,108,92
s_waitcnt lgkmcnt(6)
buffer_load_dwordx4 v0, s[48:51], s34 offen offset:1024 sc1 lds
Mvvv 152,112,64
s_waitcnt lgkmcnt(4)
buffer_load_dwordx4 v0, s[48:51], s34 offen offset:2048 sc1 lds
Mvvv 152,116,68
buffer_load_dwordx4 v0, s[48:51], s34 offen offset:3072 sc1 lds
Mvvv 184,112,72
v_cvt_pk_bf16_f32 v144, v144, v145
v_cvt_pk_bf16_f32 v145, v146, v147
Mvvv 184,116,76
v_cvt_pk_bf16_f32 v146, v148, v149
v_cvt_pk_bf16_f32 v147, v150, v151
Mvvv 216,112,80
v_cvt_pk_bf16_f32 v176, v176, v177
)"
R"(
v_cvt_pk_bf16_f32 v177, v178, v179
Mvvv 216,116,84
s_waitcnt lgkmcnt(0)
v_cvt_pk_bf16_f32 v178, v180, v181
v_cvt_pk_bf16_f32 v179, v182, v183
Mvvv 248,112,88
s_barrier
v_cvt_pk_bf16_f32 v208, v208, v209
v_cvt_pk_bf16_f32 v209, v210, v211
Mvvv 248,116,92
v_cvt_pk_bf16_f32 v210, v212, v213
v_cvt_pk_bf16_f32 v211, v214, v215
Mvvv 156,120,64
v_cvt_pk_bf16_f32 v240, v240, v241
v_cvt_pk_bf16_f32 v241, v242, v243
Mvvv 156,124,68
v_cvt_pk_bf16_f32 v242, v244, v245
v_cvt_pk_bf16_f32 v243, v246, v247
Mvvv 188,120,72
Mvvv 188,124,76
Mvvv 220,120,80
Mvvv 220,124,84
Mvvv 252,120,88
Mvvv 252,124,92
v_cvt_pk_bf16_f32 v152, v152, v153
v_cvt_pk_bf16_f32 v153, v154, v155
v_cvt_pk_bf16_f32 v154, v156, v157
v_cvt_pk_bf16_f32 v155, v158, v159
v_cvt_pk_bf16_f32 v184, v184, v185
v_cvt_pk_bf16_f32 v185, v186, v187
v_cvt_pk_bf16_f32 v186, v188, v189
v_cvt_pk_bf16_f32 v187, v190, v191
v_cvt_pk_bf16_f32 v216, v216, v217
v_cvt_pk_bf16_f32 v217, v218, v219
v_cvt_pk_bf16_f32 v218, v220, v221
v_cvt_pk_bf16_f32 v219, v222, v223
v_cvt_pk_bf16_f32 v248, v248, v249
v_cvt_pk_bf16_f32 v249, v250, v251
v_cvt_pk_bf16_f32 v250, v252, v253
v_cvt_pk_bf16_f32 v251, v254, v255
v_readlane_b32 s34, v14, 62
s_add_u32 s58, s92, 0
s_and_b32 s58, s58, 0xc000
s_add_u32 s58, s58, s70
s_add_u32 m0, s58, 65536
s_nop 0
buffer_load_dwordx4 v0, s[48:51], s34 offen sc1 lds
buffer_load_dwordx4 v0, s[48:51], s34 offen offset:1024 sc1 lds
buffer_load_dwordx4 v0, s[48:51], s34 offen offset:2048 sc1 lds
buffer_load_dwordx4 v0, s[48:51], s34 offen offset:3072 sc1 lds
v_mov_b32 v12, v4
v_mov_b32 v3, v0
v_add_u32 v1, s92, v0
s_waitcnt vmcnt(14)
s_barrier
ds_read_b128 a[128:131], v1 offset:0
ds_read_b128 a[132:135], v1 offset:1024
ds_read_b128 a[136:139], v1 offset:2048
ds_read_b128 a[140:143], v1 offset:3072
ds_read_b128 a[144:147], v1 offset:4096
ds_read_b128 a[148:151], v1 offset:5120
ds_read_b128 a[152:155], v1 offset:6144
ds_read_b128 a[156:159], v1 offset:7168
s_waitcnt lgkmcnt(7)
Zvav 32,128,128
s_waitcnt lgkmcnt(6)
ds_read_b128 a[160:163], v1 offset:8192
v_and_b32 v26, 48, v0
Mvav 32,132,136
s_waitcnt lgkmcnt(6)
ds_read_b128 a[164:167], v1 offset:9216
v_mov_b32 v27, 0x3f803f80
Mvav 32,136,144
s_waitcnt lgkmcnt(6)
ds_read_b128 a[168:171], v1 offset:10240
v_cmp_eq_u32 vcc, 0, v26
Mvav 32,140,152
s_waitcnt lgkmcnt(6)
ds_read_b128 a[172:175], v1 offset:11264
v_mov_b32 v112, 0
Zvav 48,144,128
s_waitcnt lgkmcnt(6)
ds_read_b128 a[176:179], v1 offset:12288
v_cndmask_b32 v96, 0, v27, vcc
Mvav 48,148,136
s_waitcnt lgkmcnt(6)
ds_read_b128 a[180:183], v1 offset:13312
v_cndmask_b32 v97, 0, v27, vcc
Mvav 48,152,144
s_waitcnt lgkmcnt(6)
ds_read_b128 a[184:187], v1 offset:14336
v_cndmask_b32 v98, 0, v27, vcc
Mvav 48,156,152
ds_read_b128 a[188:191], v1 offset:15360
v_cndmask_b32 v99, 0, v27, vcc
Zvav 36,128,160
v_cmp_eq_u32 vcc, 16, v26
v_mov_b32 v113, 0
Mvav 36,132,168
v_mov_b32 v114, 0
v_mov_b32 v115, 0
Mvav 36,136,176
v_cndmask_b32 v100, 0, v27, vcc
v_cndmask_b32 v101, 0, v27, vcc
Mvav 36,140,184
v_cndmask_b32 v102, 0, v27, vcc
v_cndmask_b32 v103, 0, v27, vcc
Zvav 52,144,160
v_cmp_eq_u32 vcc, 32, v26
Mvav 52,148,168
v_max3_f32 v4, v32, v33, v34
v_max3_f32 v4, v4, v35, v48
Mvav 52,152,176
s_waitcnt lgkmcnt(0)
v_max3_f32 v4, v4, v49, v50
v_max_f32 v4, v4, v51
Mvav 52,156,184
s_barrier
Zvav 40,128,192
v_mov_b32 v64, v4
v_cndmask_b32 v104, 0, v27, vcc
Mvav 40,132,200
v_cndmask_b32 v105, 0, v27, vcc
v_cndmask_b32 v106, 0, v27, vcc
Mvav 40,136,208
v_permlane16_swap_b32 v4, v64
v_max_f32 v4, v4, v64
Mvav 40,140,216
v_mov_b32 v64, v4
v_cndmask_b32 v107, 0, v27, vcc
Zvav 56,144,192
v_cmp_eq_u32 vcc, 48, v26
Mvav 56,148,200
v_permlane32_swap_b32 v4, v64
v_max_f32 v4, v4, v64
Mvav 56,152,208
v_sub_f32 v7, 0, v4
v_sub_f32 v6, 0, v4
Mvav 56,156,216
v_sub_f32 v5, 0, v4
v_sub_f32 v4, 0, v4
Zvav 44,128,224
v_add_f32 v32, v32, v4
v_add_f32 v33, v33, v4
Mvav 44,132,232
v_exp_f32 v120, v32
Mvav 44,136,240
v_exp_f32 v121, v33
Mvav 44,140,248
v_add_f32 v34, v34, v4
v_add_f32 v35, v35, v4
Zvav 60,144,224
v_exp_f32 v122, v34
Mvav 60,148,232
v_exp_f32 v123, v35
Mvav 60,152,240
v_cvt_pk_bf16_f32 v196, v120, v121
v_cvt_pk_bf16_f32 v197, v122, v123
Mvav 60,156,248
v_add_f32 v48, v48, v4
v_add_f32 v49, v49, v4
v_add_f32 v50, v50, v4
v_add_f32 v51, v51, v4
v_exp_f32 v80, v48
v_exp_f32 v81, v49
v_exp_f32 v82, v50
v_readlane_b32 s34, v14, 63
s_add_u32 s58, s92, 16384
s_and_b32 s58, s58, 0xc000
s_add_u32 s58, s58, s70
s_add_u32 m0, s58, 65536
s_nop 0
buffer_load_dwordx4 v0, s[48:51], s34 offen sc1 lds
v_exp_f32 v83, v51
v_cvt_pk_bf16_f32 v198, v80, v81
v_cvt_pk_bf16_f32 v199, v82, v83
v_max3_f32 v8, v36, v37, v38
v_max3_f32 v8, v8, v39, v52
buffer_load_dwordx4 v0, s[48:51], s34 offen offset:1024 sc1 lds
v_max3_f32 v8, v8, v53, v54
v_max_f32 v8, v8, v55
v_mov_b32 v65, v8
s_nop 1
v_permlane16_swap_b32 v8, v65
v_max_f32 v8, v8, v65
buffer_load_dwordx4 v0, s[48:51], s34 offen offset:2048 sc1 lds
v_mov_b32 v65, v8
s_nop 1
v_permlane32_swap_b32 v8, v65
v_max_f32 v8, v8, v65
v_sub_f32 v11, 0, v8
v_sub_f32 v10, 0, v8
buffer_load_dwordx4 v0, s[48:51], s34 offen offset:3072 sc1 lds
v_sub_f32 v9, 0, v8
v_sub_f32 v8, 0, v8
v_add_f32 v36, v36, v8
v_add_f32 v37, v37, v8
v_add_f32 v38, v38, v8
v_readlane_b32 s34, v12, 63
s_add_u32 s58, s92, 49152
s_and_b32 s58, s58, 0xc000
s_add_u32 s58, s58, s70
s_add_u32 m0, s58, 0
s_nop 0
buffer_load_dwordx4 v0, s[48:51], s34 offen sc1 lds
v_add_f32 v39, v39, v8
v_exp_f32 v68, v36
v_exp_f32 v69, v37
v_exp_f32 v70, v38
v_exp_f32 v71, v39
buffer_load_dwordx4 v0, s[48:51], s34 offen offset:1024 sc1 lds
v_cvt_pk_bf16_f32 v204, v68, v69
v_cvt_pk_bf16_f32 v205, v70, v71
v_add_f32 v52, v52, v8
v_add_f32 v53, v53, v8
v_add_f32 v54, v54, v8
buffer_load_dwordx4 v0, s[48:51], s34 offen offset:2048 sc1 lds
v_add_f32 v55, v55, v8
v_exp_f32 v84, v52
v_exp_f32 v85, v53
v_exp_f32 v86, v54
v_exp_f32 v87, v55
buffer_load_dwordx4 v0, s[48:51], s34 offen offset:3072 sc1 lds
v_cvt_pk_bf16_f32 v206, v84, v85
v_cvt_pk_bf16_f32 v207, v86, v87
v_cndmask_b32 v108, 0, v27, vcc
v_cndmask_b32 v109, 0, v27, vcc
v_cndmask_b32 v110, 0, v27, vcc
v_readlane_b32 s34, v12, s35
v_readlane_b32 s82, v14, s35
s_or_b32 m0, s70, s92
s_nop 2
buffer_load_dwordx4 v0, s[48:51], s34 offen sc1 lds
v_cndmask_b32 v111, 0, v27, vcc
s_lshl2_add_u32 s59, s61, s57
s_cmp_eq_u32 s59, 0
s_cbranch_scc1 .Lm0
.Lm0_r:
v_max3_f32 v132, v40, v41, v42
v_max3_f32 v140, v44, v45, v46
v_max3_f32 v132, v132, v43, v56
v_max3_f32 v140, v140, v47, v60
v_max3_f32 v132, v132, v57, v58
buffer_load_dwordx4 v0, s[48:51], s34 offen offset:1024 sc1 lds
v_max3_f32 v140, v140, v61, v62
v_max_f32 v132, v132, v59
v_max_f32 v140, v140, v63
v_mov_b32 v66, v132
v_mov_b32 v67, v140
buffer_load_dwordx4 v0, s[48:51], s34 offen offset:2048 sc1 lds
v_permlane16_swap_b32 v132, v66
v_permlane16_swap_b32 v140, v67
v_max_f32 v132, v132, v66
v_max_f32 v140, v140, v67
v_mov_b32 v66, v132
buffer_load_dwordx4 v0, s[48:51], s34 offen offset:3072 sc1 lds
v_mov_b32 v67, v140
v_permlane32_swap_b32 v132, v66
s_nop 0
v_permlane32_swap_b32 v140, v67
v_max_f32 v132, v132, v66
v_max_f32 v140, v140, v67
s_xor_b32 s58, s92, 0x18000
s_or_b32 m0, s70, s58
s_nop 0
buffer_load_dwordx4 v3, s[12:15], s82 offen sc1 lds
v_sub_f32 v135, 0, v132
v_sub_f32 v143, 0, v140
v_sub_f32 v134, 0, v132
v_sub_f32 v142, 0, v140
v_sub_f32 v133, 0, v132
s_add_u32 s72, s82, s24
buffer_load_dwordx4 v3, s[12:15], s72 offen offset:1024 sc1 lds
v_sub_f32 v141, 0, v140
v_sub_f32 v132, 0, v132
v_sub_f32 v140, 0, v140
v_add_f32 v40, v40, v132
v_add_f32 v44, v44, v140
s_add_u32 s72, s82, s25
buffer_load_dwordx4 v3, s[12:15], s72 offen offset:2048 sc1 lds
v_add_f32 v41, v41, v132
v_add_f32 v45, v45, v140
v_add_f32 v42, v42, v132
v_add_f32 v46, v46, v140
v_add_f32 v43, v43, v132
s_add_u32 s72, s82, s26
buffer_load_dwordx4 v3, s[12:15], s72 offen offset:3072 sc1 lds
v_add_f32 v47, v47, v140
v_exp_f32 v72, v40
v_exp_f32 v76, v44
v_exp_f32 v73, v41
v_exp_f32 v77, v45
s_add_u32 s93, s93, 0x60000
buffer_load_dwordx4 v[28:31], v0, s[20:23], s93 offen
v_exp_f32 v74, v42
v_exp_f32 v78, v46
v_exp_f32 v75, v43
v_exp_f32 v79, v47
v_cvt_pk_bf16_f32 v212, v72, v73
buffer_load_dwordx4 v[28:31], v0, s[20:23], s93 offen offset:1024
v_cvt_pk_bf16_f32 v220, v76, v77
v_cvt_pk_bf16_f32 v213, v74, v75
v_cvt_pk_bf16_f32 v221, v78, v79
v_add_f32 v56, v56, v132
v_add_f32 v60, v60, v140
buffer_load_dwordx4 v[28:31], v0, s[20:23], s93 offen offset:2048
v_add_f32 v57, v57, v132
v_add_f32 v61, v61, v140
v_add_f32 v58, v58, v132
v_add_f32 v62, v62, v140
v_add_f32 v59, v59, v132
v_add_f32 v63, v63, v140
v_exp_f32 v88, v56
v_exp_f32 v92, v60
v_exp_f32 v89, v57
v_exp_f32 v93, v61
v_exp_f32 v90, v58
v_exp_f32 v94, v62
v_exp_f32 v91, v59
v_exp_f32 v95, v63
v_cvt_pk_bf16_f32 v214, v88, v89
v_cvt_pk_bf16_f32 v222, v92, v93
v_cvt_pk_bf16_f32 v215, v90, v91
v_cvt_pk_bf16_f32 v223, v94, v95
s_waitcnt lgkmcnt(0)
s_waitcnt vmcnt(19)
s_cmp_eq_u32 s98, 0
s_cbranch_scc1 .Lm1
s_branch .Lloop1
.p2align 6
.Lloop0:
Cvav 32,128,128,4
s_waitcnt vmcnt(28)
s_barrier
Mvav 32,132,136
v_readlane_b32 s34, v12, s35
v_readlane_b32 s82, v14, s35
Mvav 32,136,144
Mvav 32,140,152
Cvvv 112,96,228,116
Mvvv 112,100,236
s_or_b32 m0, s70, s92
Mvvv 112,104,244
buffer_load_dwordx4 v0, s[48:51], s34 offen sc1 lds
Mvvv 112,108,252
buffer_load_dwordx4 v0, s[48:51], s34 offen offset:1024 sc1 lds
Cvav 36,128,160,8
Mvav 36,132,168
buffer_load_dwordx4 v0, s[48:51], s34 offen offset:2048 sc1 lds
Mvav 36,136,176
v_exp_f32 v120, v32
Mvav 36,140,184
Cvav 40,128,192,132
v_exp_f32 v121, v33
Mvav 40,132,200
v_add_f32 v26, v112, v113
v_add_f32 v27, v114, v115
Mvav 40,136,208
v_exp_f32 v122, v34
Mvav 40,140,216
v_add_f32 v26, v26, v27
v_cmp_nge_f32 vcc, 0x53800000, v26
Cvav 44,128,224,140
v_exp_f32 v123, v35
Mvav 44,132,232
buffer_load_dwordx4 v0, s[48:51], s34 offen offset:3072 sc1 lds
Mvav 44,136,240
v_exp_f32 v124, v36
Mvav 44,140,248
s_xor_b32 s58, s92, 0x18000
v_cvt_pk_bf16_f32 v196, v120, v121
v_cvt_pk_bf16_f32 v197, v122, v123
Cvav 48,144,128,4
s_or_b32 m0, s70, s58
v_exp_f32 v125, v37
Mvav 48,148,136
s_or_b32 s81, s92, 0x10000
buffer_load_dwordx4 v3, s[12:15], s82 offen sc1 lds
Mvav 48,152,144
v_exp_f32 v126, v38
Mvav 48,156,152
v_cvt_pk_bf16_f32 v204, v124, v125
Cvav 52,144,160,8
s_add_u32 s72, s82, s24
v_exp_f32 v127, v39
Mvav 52,148,168
buffer_load_dwordx4 v3, s[12:15], s72 offen offset:1024 sc1 lds
Mvav 52,152,176
v_exp_f32 v120, v40
Mvav 52,156,184
v_cvt_pk_bf16_f32 v205, v126, v127
Cvav 56,144,192,132
s_add_u32 s72, s82, s25
v_exp_f32 v121, v41
Mvav 56,148,200
buffer_load_dwordx4 v3, s[12:15], s72 offen offset:2048 sc1 lds
Mvav 56,152,208
v_exp_f32 v122, v42
Mvav 56,156,216
v_cvt_pk_bf16_f32 v212, v120, v121
v_add_u32 v2, s81, v0
Cvav 60,144,224,140
s_add_u32 s72, s82, s26
v_exp_f32 v123, v43
Mvav 60,148,232
buffer_load_dwordx4 v3, s[12:15], s72 offen offset:3072 sc1 lds
Mvav 60,152,240
v_exp_f32 v124, v44
Mvav 60,156,248
s_cbranch_vccnz .Lab0
.Lab0_r:
Maav 0,196,228
v_exp_f32 v125, v45
Maav 4,196,236
s_cmp_lt_u32 s99, s73
s_cselect_b32 m0, s71, 0x30000
v_cvt_pk_bf16_f32 v213, v122, v123
Maav 64,196,244
v_exp_f32 v126, v46
Maav 68,196,252
buffer_load_dwordx4 v25, s[36:39], s68 offen nt lds
Maav 8,204,228
v_exp_f32 v127, v47
Maav 12,204,236
s_or_b32 m0, m0, s62
v_cvt_pk_bf16_f32 v220, v124, v125
v_cvt_pk_bf16_f32 v221, v126, v127
Maav 72,204,244
s_add_u32 s72, s68, 128
v_exp_f32 v120, v48
Maav 76,204,252
buffer_load_dwordx4 v25, s[36:39], s72 offen lds
Maav 16,212,228
v_exp_f32 v121, v49
Maav 20,212,236
v_cvt_pk_bf16_f32 v198, v120, v121
Maav 80,212,244
s_add_u32 s93, s93, 0x60000
v_exp_f32 v122, v50
Maav 84,212,252
buffer_load_dwordx4 v[28:31], v0, s[20:23], s93 offen
Maav 24,220,228
v_exp_f32 v123, v51
Maav 28,220,236
v_cvt_pk_bf16_f32 v199, v122, v123
Maav 88,220,244
s_add_u32 s59, s99, 2
v_exp_f32 v124, v52
Maav 92,220,252
buffer_load_dwordx4 v[28:31], v0, s[20:23], s93 offen offset:1024
Maav 32,228,228
v_exp_f32 v125, v53
Maav 36,228,236
s_cmp_eq_u32 s59, s98
s_cselect_b32 s59, s97, 0
v_cvt_pk_bf16_f32 v206, v124, v125
Maav 96,228,244
s_add_u32 s80, s92, 0x4000
)"
R"(
v_exp_f32 v126, v54
Maav 100,228,252
s_add_u32 s80, s80, s59
buffer_load_dwordx4 v[28:31], v0, s[20:23], s93 offen offset:2048
Maav 40,236,228
s_and_b32 s80, s80, 0xc000
v_exp_f32 v127, v55
Maav 44,236,236
s_add_u32 s99, s99, 1
s_add_u32 s68, s68, s63
v_cvt_pk_bf16_f32 v207, v126, v127
Maav 104,236,244
s_add_u32 s71, s71, s64
v_exp_f32 v120, v56
Maav 108,236,252
v_exp_f32 v121, v57
Maav 48,244,228
v_exp_f32 v122, v58
Maav 52,244,236
v_exp_f32 v123, v59
Maav 112,244,244
v_exp_f32 v124, v60
Maav 116,244,252
v_exp_f32 v125, v61
Maav 56,252,228
v_exp_f32 v126, v62
Maav 60,252,236
v_exp_f32 v127, v63
Maav 120,252,244
s_waitcnt lgkmcnt(0)
v_cvt_pk_bf16_f32 v214, v120, v121
v_cvt_pk_bf16_f32 v215, v122, v123
Maav 124,252,252
v_cvt_pk_bf16_f32 v222, v124, v125
v_cvt_pk_bf16_f32 v223, v126, v127
.Lloop1:
Cvav 64,160,128,4
s_waitcnt vmcnt(28)
s_barrier
Mvav 64,164,136
ds_read_b128 a[192:195], v2 offset:0
v_add_u32 v1, s80, v0
Mvav 64,168,144
ds_read_b128 a[200:203], v2 offset:2048
Mvav 64,172,152
ds_read_b128 a[208:211], v2 offset:4096
Cvvv 116,96,196,112
ds_read_b128 a[216:219], v2 offset:6144
Mvvv 116,100,204
ds_read_b128 a[224:227], v2 offset:8192
Mvvv 116,104,212
ds_read_b128 a[232:235], v2 offset:10240
Mvvv 116,108,220
ds_read_b128 a[240:243], v2 offset:12288
Cvav 68,160,160,8
v_exp_f32 v120, v64
Mvav 68,164,168
ds_read_b128 a[248:251], v2 offset:14336
Mvav 68,168,176
v_exp_f32 v121, v65
Mvav 68,172,184
ds_read_b128 a[196:199], v2 offset:1024
v_cvt_pk_bf16_f32 v228, v120, v121
Cvav 72,160,192,132
v_exp_f32 v122, v66
Mvav 72,164,200
ds_read_b128 a[204:207], v2 offset:3072
v_add_f32 v26, v116, v117
Mvav 72,168,208
v_exp_f32 v123, v67
Mvav 72,172,216
ds_read_b128 a[212:215], v2 offset:5120
v_add_f32 v27, v118, v119
Cvav 76,160,224,140
v_exp_f32 v124, v68
Mvav 76,164,232
ds_read_b128 a[220:223], v2 offset:7168
v_add_f32 v26, v26, v27
Mvav 76,168,240
v_exp_f32 v125, v69
Mvav 76,172,248
ds_read_b128 a[228:231], v2 offset:9216
v_cmp_nge_f32 vcc, 0x53800000, v26
Cvav 80,176,128,4
s_or_b32 s81, s92, 0x10000
v_exp_f32 v126, v70
Mvav 80,180,136
ds_read_b128 a[236:239], v2 offset:11264
v_cvt_pk_bf16_f32 v229, v122, v123
Mvav 80,184,144
v_exp_f32 v127, v71
Mvav 80,188,152
s_cmp_lt_u32 s99, s73
s_cselect_b32 m0, s71, 0x30000
ds_read_b128 a[244:247], v2 offset:13312
Cvav 84,176,160,8
s_waitcnt lgkmcnt(12)
v_exp_f32 v120, v72
Mvav 84,180,168
ds_read_b128 a[252:255], v2 offset:15360
buffer_load_dwordx4 v25, s[36:39], s68 offen nt lds
Mvav 84,184,176
v_exp_f32 v121, v73
Mvav 84,188,184
ds_read_b128 a[128:131], v1 offset:0
v_cvt_pk_bf16_f32 v236, v124, v125
Cvav 88,176,192,132
s_waitcnt lgkmcnt(11)
v_exp_f32 v122, v74
Mvav 88,180,200
s_or_b32 m0, m0, s62
ds_read_b128 a[132:135], v1 offset:1024
v_cvt_pk_bf16_f32 v237, v126, v127
Mvav 88,184,208
s_add_u32 s72, s68, 128
v_exp_f32 v123, v75
Mvav 88,188,216
ds_read_b128 a[136:139], v1 offset:2048
buffer_load_dwordx4 v25, s[36:39], s72 offen lds
Cvav 92,176,224,140
v_exp_f32 v124, v76
Mvav 92,180,232
ds_read_b128 a[140:143], v1 offset:3072
v_cvt_pk_bf16_f32 v244, v120, v121
Mvav 92,184,240
v_exp_f32 v125, v77
Mvav 92,188,248
s_cbranch_vccnz .Lab1
.Lab1_r:
Maav 0,192,196
v_exp_f32 v126, v78
Maav 4,192,204
ds_read_b128 a[144:147], v1 offset:4096
v_cvt_pk_bf16_f32 v245, v122, v123
Maav 64,192,212
v_exp_f32 v127, v79
Maav 68,192,220
s_waitcnt lgkmcnt(12)
ds_read_b128 a[148:151], v1 offset:5120
v_cvt_pk_bf16_f32 v252, v124, v125
Maav 8,200,196
v_exp_f32 v120, v80
Maav 12,200,204
ds_read_b128 a[152:155], v1 offset:6144
v_cvt_pk_bf16_f32 v253, v126, v127
Maav 72,200,212
v_exp_f32 v121, v81
Maav 76,200,220
ds_read_b128 a[156:159], v1 offset:7168
v_cvt_pk_bf16_f32 v230, v120, v121
Maav 16,208,196
v_exp_f32 v122, v82
Maav 20,208,204
s_waitcnt lgkmcnt(12)
ds_read_b128 a[160:163], v1 offset:8192
v_add_u32 v2, s81, v0
Maav 80,208,212
v_exp_f32 v123, v83
Maav 84,208,220
ds_read_b128 a[164:167], v1 offset:9216
v_cvt_pk_bf16_f32 v231, v122, v123
Maav 24,216,196
v_exp_f32 v124, v84
Maav 28,216,204
s_waitcnt lgkmcnt(11)
ds_read_b128 a[168:171], v1 offset:10240
Maav 88,216,212
s_add_u32 s35, s35, 1
v_exp_f32 v125, v85
Maav 92,216,220
s_add_u32 s92, s92, 0x4000
ds_read_b128 a[172:175], v1 offset:11264
v_cvt_pk_bf16_f32 v238, v124, v125
Maav 32,224,196
s_and_b32 s92, s92, 0xc000
v_exp_f32 v126, v86
Maav 36,224,204
s_add_u32 s80, s92, 0x4000
s_and_b32 s80, s80, 0xc000
ds_read_b128 a[176:179], v1 offset:12288
Maav 96,224,212
s_add_u32 s99, s99, 1
v_exp_f32 v127, v87
Maav 100,224,220
s_add_u32 s68, s68, s63
ds_read_b128 a[180:183], v1 offset:13312
v_cvt_pk_bf16_f32 v239, v126, v127
Maav 40,232,196
s_add_u32 s71, s71, s64
v_exp_f32 v120, v88
Maav 44,232,204
s_waitcnt lgkmcnt(12)
ds_read_b128 a[184:187], v1 offset:14336
Maav 104,232,212
ds_read_b128 a[188:191], v1 offset:15360
Maav 108,232,220
v_exp_f32 v121, v89
Maav 48,240,196
v_exp_f32 v122, v90
Maav 52,240,204
v_exp_f32 v123, v91
Maav 112,240,212
v_exp_f32 v124, v92
Maav 116,240,220
v_exp_f32 v125, v93
Maav 56,248,196
v_exp_f32 v126, v94
Maav 60,248,204
v_exp_f32 v127, v95
Maav 120,248,212
s_waitcnt lgkmcnt(0)
v_cvt_pk_bf16_f32 v246, v120, v121
v_cvt_pk_bf16_f32 v247, v122, v123
Maav 124,248,220
v_cvt_pk_bf16_f32 v254, v124, v125
v_cvt_pk_bf16_f32 v255, v126, v127
s_bitcmp1_b64 s[32:33], s99
s_cbranch_scc1 .Lhab1
.Lhab1_r:
s_cmp_lt_u32 s99, s98
s_cbranch_scc1 .Lloop0
Cvav 32,128,128,4
s_waitcnt vmcnt(20)
s_barrier
Mvav 32,132,136
v_bfe_u32 v172, v0, 4, 4
v_lshrrev_b32 v173, 8, v0
Mvav 32,136,144
v_lshlrev_b32 v173, 2, v173
v_sub_u32 v173, v172, v173
Mvav 32,140,152
v_mov_b32 v174, 0xff800000
v_cndmask_b32 v172, v173, 64, s[4:5]
Cvvv 112,96,228,116
v_cmp_gt_i32 vcc, 0, v172
v_cndmask_b32 v156, v174, v132, s[4:5]
Mvvv 112,100,236
v_readlane_b32 s34, v12, s35
v_readlane_b32 s82, v14, s35
Mvvv 112,104,244
v_cndmask_b32 v148, v132, v174, vcc
v_cndmask_b32 v164, v140, v174, vcc
Mvvv 112,108,252
v_cmp_gt_i32 vcc, 1, v172
v_cndmask_b32 v157, v174, v132, s[4:5]
Cvav 36,128,160,8
Mvav 36,132,168
v_cndmask_b32 v149, v132, v174, vcc
v_cndmask_b32 v165, v140, v174, vcc
Mvav 36,136,176
v_exp_f32 v120, v32
Mvav 36,140,184
v_cmp_gt_i32 vcc, 2, v172
v_cndmask_b32 v158, v174, v132, s[4:5]
Cvav 44,128,224,140
v_exp_f32 v121, v33
Mvav 44,132,232
v_cndmask_b32 v150, v132, v174, vcc
v_cndmask_b32 v166, v140, v174, vcc
v_cvt_pk_bf16_f32 v196, v120, v121
Mvav 44,136,240
v_exp_f32 v122, v34
Mvav 44,140,248
v_cmp_gt_i32 vcc, 3, v172
v_cndmask_b32 v159, v174, v132, s[4:5]
Cvav 48,144,128,4
v_exp_f32 v123, v35
Mvav 48,148,136
v_cndmask_b32 v151, v132, v174, vcc
v_cndmask_b32 v167, v140, v174, vcc
Mvav 48,152,144
v_exp_f32 v124, v36
Mvav 48,156,152
v_add_f32 v26, v112, v113
v_add_f32 v27, v114, v115
Cvav 52,144,160,8
s_add_u32 s81, s92, s97
v_exp_f32 v125, v37
Mvav 52,148,168
s_and_b32 s81, s81, 0xc000
v_add_f32 v26, v26, v27
v_cmp_nge_f32 vcc, 0x53800000, v26
Mvav 52,152,176
s_or_b32 m0, s70, s92
v_exp_f32 v126, v38
Mvav 52,156,184
s_or_b32 s81, s81, 0x10000
buffer_load_dwordx4 v0, s[48:51], s34 offen sc1 lds
Cvav 40,128,192,148
v_exp_f32 v127, v39
Mvav 40,132,200
buffer_load_dwordx4 v0, s[48:51], s34 offen offset:1024 sc1 lds
Mvav 40,136,208
v_exp_f32 v120, v44
Mvav 40,140,216
v_cvt_pk_bf16_f32 v197, v122, v123
v_cvt_pk_bf16_f32 v204, v124, v125
Cvav 56,144,192,156
v_exp_f32 v121, v45
Mvav 56,148,200
buffer_load_dwordx4 v0, s[48:51], s34 offen offset:2048 sc1 lds
Mvav 56,152,208
v_exp_f32 v122, v46
Mvav 56,156,216
buffer_load_dwordx4 v0, s[48:51], s34 offen offset:3072 sc1 lds
Cvav 60,144,224,164
v_exp_f32 v123, v47
Mvav 60,148,232
v_cvt_pk_bf16_f32 v205, v126, v127
v_cvt_pk_bf16_f32 v220, v120, v121
Mvav 60,152,240
v_exp_f32 v124, v48
Mvav 60,156,248
s_xor_b32 s58, s92, 0x18000
s_cbranch_vccnz .Ld0
.Ld0_r:
Maav 0,196,228
s_or_b32 m0, s70, s58
v_exp_f32 v125, v49
v_bfe_u32 v64, v0, 4, 4
v_lshrrev_b32 v65, 8, v0
Maav 4,196,236
buffer_load_dwordx4 v3, s[12:15], s82 offen sc1 lds
Maav 64,196,244
s_add_u32 s72, s82, s24
v_exp_f32 v126, v50
v_lshlrev_b32 v65, 2, v65
v_sub_u32 v65, v64, v65
Maav 68,196,252
buffer_load_dwordx4 v3, s[12:15], s72 offen offset:1024 sc1 lds
Maav 8,204,228
v_exp_f32 v127, v51
v_mov_b32 v66, 0xff800000
v_cndmask_b32 v65, -16, v65, s[4:5]
Maav 12,204,236
v_cvt_pk_bf16_f32 v221, v122, v123
v_cvt_pk_bf16_f32 v198, v124, v125
v_cmp_gt_i32 vcc, 0, v65
v_cndmask_b32 v180, v66, v140, s[4:5]
Maav 72,204,244
s_add_u32 s72, s82, s25
v_exp_f32 v120, v52
v_cndmask_b32 v172, v132, v66, vcc
v_cndmask_b32 v188, v140, v66, vcc
Maav 76,204,252
buffer_load_dwordx4 v3, s[12:15], s72 offen offset:2048 sc1 lds
Maav 16,212,228
s_add_u32 s72, s82, s26
v_exp_f32 v121, v53
v_cmp_gt_i32 vcc, 1, v65
v_cndmask_b32 v181, v66, v140, s[4:5]
Maav 20,212,236
buffer_load_dwordx4 v3, s[12:15], s72 offen offset:3072 sc1 lds
Maav 80,212,244
v_exp_f32 v122, v54
v_cndmask_b32 v173, v132, v66, vcc
v_cndmask_b32 v189, v140, v66, vcc
Maav 84,212,252
s_cmp_lt_u32 s99, s73
s_cselect_b32 m0, s71, 0x30000
v_cvt_pk_bf16_f32 v199, v126, v127
v_cmp_gt_i32 vcc, 2, v65
v_cndmask_b32 v182, v66, v140, s[4:5]
Maav 24,220,228
v_exp_f32 v123, v55
v_cndmask_b32 v174, v132, v66, vcc
v_cndmask_b32 v190, v140, v66, vcc
Maav 28,220,236
buffer_load_dwordx4 v25, s[36:39], s68 offen nt lds
Maav 88,220,244
s_or_b32 m0, m0, s62
s_add_u32 s72, s68, 128
v_exp_f32 v124, v40
v_cmp_gt_i32 vcc, 3, v65
v_cndmask_b32 v183, v66, v140, s[4:5]
Maav 92,220,252
s_add_u32 s80, s92, 0x4000
buffer_load_dwordx4 v25, s[36:39], s72 offen lds
Maav 32,228,228
s_add_u32 s80, s80, s97
v_exp_f32 v125, v41
Maav 36,228,236
s_and_b32 s80, s80, 0xc000
v_cvt_pk_bf16_f32 v206, v120, v121
v_cvt_pk_bf16_f32 v207, v122, v123
Maav 96,228,244
s_add_u32 s99, s99, 1
v_exp_f32 v126, v42
Maav 100,228,252
s_add_u32 s68, s68, s63
v_cvt_pk_bf16_f32 v212, v124, v125
v_add_u32 v2, s81, v0
Maav 40,236,228
s_add_u32 s71, s71, s64
v_exp_f32 v127, v43
Maav 44,236,236
v_cvt_pk_bf16_f32 v213, v126, v127
v_cndmask_b32 v175, v132, v66, vcc
v_cndmask_b32 v191, v140, v66, vcc
Maav 104,236,244
v_exp_f32 v120, v56
Maav 108,236,252
v_exp_f32 v121, v57
Maav 48,244,228
v_exp_f32 v122, v58
Maav 52,244,236
v_exp_f32 v123, v59
Maav 112,244,244
v_exp_f32 v124, v60
Maav 116,244,252
v_exp_f32 v125, v61
Maav 56,252,228
v_exp_f32 v126, v62
Maav 60,252,236
v_exp_f32 v127, v63
Maav 120,252,244
s_waitcnt lgkmcnt(0)
v_cvt_pk_bf16_f32 v214, v120, v121
v_cvt_pk_bf16_f32 v215, v122, v123
Maav 124,252,252
v_cvt_pk_bf16_f32 v222, v124, v125
v_cvt_pk_bf16_f32 v223, v126, v127
s_bitcmp1_b64 s[32:33], s99
s_cbranch_scc1 .Lhd0
.Lhd0_r:
.Ldiag1:
Cvav 64,160,128,4
s_waitcnt vmcnt(15)
s_barrier
Mvav 64,164,136
s_lshr_b32 s58, s97, 14
ds_read_b128 a[192:195], v2 offset:0
v_add_u32 v1, s80, v0
Mvav 64,168,144
s_add_u32 s35, s35, s58
s_add_u32 s92, s92, s97
ds_read_b128 a[200:203], v2 offset:2048
v_mov_b32 v88, 0xff800000
v_mov_b32 v89, 0xff800000
Mvav 64,172,152
s_and_b32 s92, s92, 0xc000
ds_read_b128 a[208:211], v2 offset:4096
v_mov_b32 v90, 0xff800000
v_mov_b32 v91, 0xff800000
Cvvv 116,96,196,112
ds_read_b128 a[216:219], v2 offset:6144
v_readlane_b32 s34, v12, s35
v_mov_b32 v246, 0
v_mov_b32 v247, 0
Mvvv 116,100,204
ds_read_b128 a[224:227], v2 offset:8192
v_readlane_b32 s82, v14, s35
Mvvv 116,104,212
ds_read_b128 a[232:235], v2 offset:10240
Mvvv 116,108,220
ds_read_b128 a[240:243], v2 offset:12288
Cvav 68,160,160,8
v_exp_f32 v120, v64
Mvav 68,164,168
ds_read_b128 a[248:251], v2 offset:14336
Mvav 68,168,176
s_or_b32 m0, s85, s92
v_exp_f32 v121, v65
Mvav 68,172,184
ds_read_b128 a[196:199], v2 offset:1024
buffer_load_dwordx4 v0, s[48:51], s34 offen sc1 lds
Cvav 72,160,192,172
v_exp_f32 v122, v66
Mvav 72,164,200
ds_read_b128 a[204:207], v2 offset:3072
v_add_f32 v26, v116, v117
Mvav 72,168,208
v_exp_f32 v123, v67
Mvav 72,172,216
ds_read_b128 a[212:215], v2 offset:5120
v_add_f32 v27, v118, v119
v_cvt_pk_bf16_f32 v229, v122, v123
Cvav 76,160,224,180
v_exp_f32 v124, v68
Mvav 76,164,232
ds_read_b128 a[220:223], v2 offset:7168
v_add_f32 v26, v26, v27
Mvav 76,168,240
v_exp_f32 v125, v69
Mvav 76,172,248
ds_read_b128 a[228:231], v2 offset:9216
v_cmp_nge_f32 vcc, 0x53800000, v26
Cvav 80,176,128,4
)"
R"(
s_or_b32 s81, s92, 0x10000
v_exp_f32 v126, v70
Mvav 80,180,136
ds_read_b128 a[236:239], v2 offset:11264
buffer_load_dwordx4 v0, s[48:51], s34 offen offset:1024 sc1 lds
Mvav 80,184,144
v_exp_f32 v127, v71
Mvav 80,188,152
ds_read_b128 a[244:247], v2 offset:13312
v_cvt_pk_bf16_f32 v228, v120, v121
v_cvt_pk_bf16_f32 v237, v126, v127
Cvav 84,176,160,8
s_waitcnt lgkmcnt(12)
v_exp_f32 v120, v72
Mvav 84,180,168
ds_read_b128 a[252:255], v2 offset:15360
buffer_load_dwordx4 v0, s[48:51], s34 offen offset:2048 sc1 lds
Mvav 84,184,176
v_exp_f32 v121, v73
Mvav 84,188,184
ds_read_b128 a[128:131], v1 offset:0
buffer_load_dwordx4 v0, s[48:51], s34 offen offset:3072 sc1 lds
Cvav 92,176,224,188
v_exp_f32 v122, v74
Mvav 92,180,232
ds_read_b128 a[132:135], v1 offset:1024
v_cvt_pk_bf16_f32 v236, v124, v125
Mvav 92,184,240
s_waitcnt lgkmcnt(3)
v_exp_f32 v123, v75
Mvav 92,188,248
s_xor_b32 s58, s92, 0x18000
s_cbranch_vccnz .Ld1
.Ld1_r:
Maav 0,192,196
s_or_b32 m0, s85, s58
v_exp_f32 v124, v76
v_bfe_u32 v32, v0, 4, 4
Maav 4,192,204
ds_read_b128 a[136:139], v1 offset:2048
buffer_load_dwordx4 v3, s[12:15], s82 offen sc1 lds
Maav 64,192,212
s_add_u32 s72, s82, s24
v_exp_f32 v125, v77
v_lshrrev_b32 v33, 8, v0
v_lshlrev_b32 v33, 2, v33
Maav 68,192,220
ds_read_b128 a[140:143], v1 offset:3072
buffer_load_dwordx4 v3, s[12:15], s72 offen offset:1024 sc1 lds
Maav 8,200,196
s_waitcnt lgkmcnt(2)
v_exp_f32 v126, v78
v_cvt_pk_bf16_f32 v252, v124, v125
v_sub_u32 v33, v32, v33
v_mov_b32 v34, 0xff800000
Maav 12,200,204
ds_read_b128 a[144:147], v1 offset:4096
v_cvt_pk_bf16_f32 v244, v120, v121
v_cndmask_b32 v32, 64, v33, s[4:5]
v_cndmask_b32 v33, v33, -16, s[4:5]
Maav 72,200,212
s_add_u32 s72, s82, s25
v_exp_f32 v127, v79
v_cmp_gt_i32 vcc, 0, v32
v_cndmask_b32 v156, v4, v34, s[4:5]
Maav 76,200,220
ds_read_b128 a[148:151], v1 offset:5120
buffer_load_dwordx4 v3, s[12:15], s72 offen offset:2048 sc1 lds
Maav 16,208,196
s_add_u32 s72, s82, s26
v_exp_f32 v120, v80
v_cndmask_b32 v148, v4, v34, vcc
v_cndmask_b32 v164, v8, v34, vcc
Maav 20,208,204
ds_read_b128 a[152:155], v1 offset:6144
buffer_load_dwordx4 v3, s[12:15], s72 offen offset:3072 sc1 lds
Maav 80,208,212
v_exp_f32 v121, v81
v_cmp_gt_i32 vcc, 1, v32
v_cndmask_b32 v157, v4, v34, s[4:5]
Maav 84,208,220
s_cmp_lt_u32 s99, s73
s_cselect_b32 m0, s71, 0x30000
ds_read_b128 a[156:159], v1 offset:7168
v_cvt_pk_bf16_f32 v245, v122, v123
v_cndmask_b32 v149, v4, v34, vcc
v_cndmask_b32 v165, v8, v34, vcc
Maav 24,216,196
v_exp_f32 v122, v82
v_cmp_gt_i32 vcc, 2, v32
v_cndmask_b32 v158, v4, v34, s[4:5]
Maav 28,216,204
ds_read_b128 a[160:163], v1 offset:8192
buffer_load_dwordx4 v25, s[36:39], s68 offen nt lds
Maav 88,216,212
s_or_b32 m0, m0, s62
s_add_u32 s72, s68, 128
v_exp_f32 v123, v83
v_cndmask_b32 v150, v4, v34, vcc
v_cndmask_b32 v166, v8, v34, vcc
Maav 92,216,220
ds_read_b128 a[164:167], v1 offset:9216
buffer_load_dwordx4 v25, s[36:39], s72 offen lds
Maav 32,224,196
v_exp_f32 v124, v84
v_cmp_gt_i32 vcc, 3, v32
v_cndmask_b32 v159, v4, v34, s[4:5]
Maav 36,224,204
ds_read_b128 a[168:171], v1 offset:10240
v_cvt_pk_bf16_f32 v253, v126, v127
v_cndmask_b32 v151, v4, v34, vcc
v_cndmask_b32 v167, v8, v34, vcc
Maav 96,224,212
v_exp_f32 v125, v85
v_cmp_gt_i32 vcc, 0, v33
v_cndmask_b32 v180, v8, v34, s[4:5]
Maav 100,224,220
ds_read_b128 a[172:175], v1 offset:11264
v_cvt_pk_bf16_f32 v230, v120, v121
v_cndmask_b32 v172, v4, v34, vcc
v_cndmask_b32 v188, v8, v34, vcc
Maav 40,232,196
v_exp_f32 v126, v86
v_cmp_gt_i32 vcc, 1, v33
v_cndmask_b32 v181, v8, v34, s[4:5]
Maav 44,232,204
ds_read_b128 a[176:179], v1 offset:12288
v_cvt_pk_bf16_f32 v231, v122, v123
v_cndmask_b32 v173, v4, v34, vcc
v_cndmask_b32 v189, v8, v34, vcc
Maav 104,232,212
v_exp_f32 v127, v87
v_cmp_gt_i32 vcc, 2, v33
v_cndmask_b32 v182, v8, v34, s[4:5]
Maav 108,232,220
ds_read_b128 a[180:183], v1 offset:13312
v_cvt_pk_bf16_f32 v238, v124, v125
v_cndmask_b32 v174, v4, v34, vcc
v_cndmask_b32 v190, v8, v34, vcc
Maav 48,240,196
v_exp_f32 v120, v92
v_cmp_gt_i32 vcc, 3, v33
v_cndmask_b32 v183, v8, v34, s[4:5]
Maav 52,240,204
ds_read_b128 a[184:187], v1 offset:14336
v_cvt_pk_bf16_f32 v239, v126, v127
v_cndmask_b32 v175, v4, v34, vcc
v_cndmask_b32 v191, v8, v34, vcc
Maav 112,240,212
s_add_u32 s35, s35, 1
ds_read_b128 a[188:191], v1 offset:15360
v_add_u32 v2, s81, v0
Maav 116,240,220
s_add_u32 s92, s92, 0x4000
v_exp_f32 v121, v93
Maav 56,248,196
s_and_b32 s92, s92, 0xc000
v_exp_f32 v122, v94
Maav 60,248,204
s_add_u32 s80, s92, 0x4000
v_exp_f32 v123, v95
Maav 120,248,212
s_and_b32 s80, s80, 0xc000
s_waitcnt lgkmcnt(0)
v_cvt_pk_bf16_f32 v254, v120, v121
v_cvt_pk_bf16_f32 v255, v122, v123
Maav 124,248,220
s_add_u32 s99, s99, 1
s_add_u32 s68, s68, s63
s_add_u32 s71, s71, s64
s_bitcmp1_b64 s[32:33], s99
s_cbranch_scc1 .Lhd1
.Lhd1_r:
Cvav 32,128,128,148
s_waitcnt vmcnt(20)
s_barrier
Mvav 32,132,136
s_lshl3_add_u32 s18, s79, s86
v_add_u32 v1, s80, v0
v_readlane_b32 s34, v12, s35
Mvav 32,136,144
s_and_b32 s19, s67, 0x700
s_lshr_b32 s59, s70, 6
v_readlane_b32 s82, v14, s35
v_lshrrev_b32 v225, 6, v0
v_mov_b32 v56, 0xff800000
v_mov_b32 v57, 0xff800000
Mvav 32,140,152
s_add_u32 s19, s19, s59
s_mul_i32 s18, s18, 34816
s_add_u32 s18, s18, s19
v_and_b32 v226, 48, v0
v_mov_b32 v58, 0xff800000
v_mov_b32 v59, 0xff800000
Cvvv 112,96,228,116
v_mul_u32_u24 v225, 2176, v225
v_mov_b32 v214, 0
v_mov_b32 v215, 0
Mvvv 112,100,236
s_mov_b32 s85, 0x80000000
s_mov_b32 s97, 0x80000000
v_add_u32 v225, v225, v226
Mvvv 112,104,244
s_or_b32 m0, s70, s92
Mvvv 112,108,252
buffer_load_dwordx4 v0, s[48:51], s34 offen sc1 lds
Cvav 36,128,160,8
buffer_load_dwordx4 v0, s[48:51], s34 offen offset:1024 sc1 lds
Mvav 36,132,168
buffer_load_dwordx4 v0, s[48:51], s34 offen offset:2048 sc1 lds
Mvav 36,136,176
v_exp_f32 v120, v32
Mvav 36,140,184
Cvav 40,144,128,156
v_exp_f32 v121, v33
Mvav 40,148,136
v_add_f32 v26, v112, v113
v_add_f32 v27, v114, v115
Mvav 40,152,144
v_exp_f32 v122, v34
Mvav 40,156,152
v_add_f32 v26, v26, v27
v_cmp_nge_f32 vcc, 0x53800000, v26
Cvav 44,144,160,164
v_exp_f32 v123, v35
Mvav 44,148,168
buffer_load_dwordx4 v0, s[48:51], s34 offen offset:3072 sc1 lds
Mvav 44,152,176
s_add_u32 s19, s18, 4194304
v_exp_f32 v124, v36
Mvav 44,156,184
buffer_load_dwordx4 v[192:195], v225, s[48:51], s19 offen sc1
Cvav 48,160,128,172
s_or_b32 s81, s92, 0x10000
v_exp_f32 v125, v37
Mvav 48,164,136
s_xor_b32 s58, s92, 0x18000
v_cvt_pk_bf16_f32 v196, v120, v121
v_cvt_pk_bf16_f32 v197, v122, v123
Mvav 48,168,144
s_or_b32 m0, s70, s58
v_exp_f32 v126, v38
Mvav 48,172,152
buffer_load_dwordx4 v3, s[12:15], s82 offen sc1 lds
Cvav 52,160,160,180
s_add_u32 s72, s82, s24
v_exp_f32 v127, v39
Mvav 52,164,168
ds_read_b128 a[128:131], v1 offset:0
buffer_load_dwordx4 v3, s[12:15], s72 offen offset:1024 sc1 lds
Mvav 52,168,176
v_exp_f32 v120, v40
Mvav 52,172,184
ds_read_b128 a[132:135], v1 offset:1024
v_cvt_pk_bf16_f32 v204, v124, v125
Cvav 60,176,160,188
s_add_u32 s19, s18, 5308416
v_exp_f32 v121, v41
Mvav 60,180,168
ds_read_b128 a[136:139], v1 offset:2048
buffer_load_dwordx4 v[200:203], v225, s[48:51], s19 offen sc1
Mvav 60,184,176
v_exp_f32 v122, v42
v_cvt_pk_bf16_f32 v198, v120, v121
Mvav 60,188,184
s_cbranch_vccnz .Ly
.Ly_r:
Maav 0,196,228
s_add_u32 s72, s82, s25
v_exp_f32 v123, v43
Maav 4,196,236
ds_read_b128 a[140:143], v1 offset:3072
buffer_load_dwordx4 v3, s[12:15], s72 offen offset:2048 sc1 lds
Maav 64,196,244
s_add_u32 s72, s82, s26
v_exp_f32 v124, v44
Maav 68,196,252
ds_read_b128 a[144:147], v1 offset:4096
buffer_load_dwordx4 v3, s[12:15], s72 offen offset:3072 sc1 lds
Maav 8,204,228
v_exp_f32 v125, v45
Maav 12,204,236
s_cmp_lt_u32 s99, s73
s_cselect_b32 m0, s71, 0x30000
ds_read_b128 a[148:151], v1 offset:5120
v_cvt_pk_bf16_f32 v205, v126, v127
Maav 72,204,244
v_exp_f32 v126, v46
Maav 76,204,252
ds_read_b128 a[152:155], v1 offset:6144
buffer_load_dwordx4 v25, s[36:39], s68 offen nt lds
Maav 16,212,228
s_add_u32 s19, s18, 6422528
v_exp_f32 v127, v47
Maav 20,212,236
ds_read_b128 a[156:159], v1 offset:7168
buffer_load_dwordx4 v[208:211], v225, s[48:51], s19 offen sc1
Maav 80,212,244
v_exp_f32 v120, v48
Maav 84,212,252
s_or_b32 m0, m0, s62
ds_read_b128 a[160:163], v1 offset:8192
v_cvt_pk_bf16_f32 v199, v122, v123
Maav 24,220,228
s_add_u32 s72, s68, 128
v_exp_f32 v121, v49
Maav 28,220,236
ds_read_b128 a[164:167], v1 offset:9216
buffer_load_dwordx4 v25, s[36:39], s72 offen lds
Maav 88,220,244
s_add_u32 s19, s18, 7536640
v_exp_f32 v122, v50
Maav 92,220,252
ds_read_b128 a[168:171], v1 offset:10240
buffer_load_dwordx4 v[216:219], v225, s[48:51], s19 offen sc1
Maav 32,228,228
s_add_u32 s35, s35, 1
v_exp_f32 v123, v51
Maav 36,228,236
s_add_u32 s92, s92, 0x4000
ds_read_b128 a[172:175], v1 offset:11264
v_cvt_pk_bf16_f32 v206, v124, v125
Maav 96,228,244
s_and_b32 s92, s92, 0xc000
v_exp_f32 v124, v52
Maav 100,228,252
s_add_u32 s80, s92, 0x4000
ds_read_b128 a[176:179], v1 offset:12288
v_cvt_pk_bf16_f32 v207, v126, v127
Maav 40,236,228
s_and_b32 s80, s80, 0xc000
v_exp_f32 v125, v53
Maav 44,236,236
s_add_u32 s99, s99, 1
ds_read_b128 a[180:183], v1 offset:13312
v_cvt_pk_bf16_f32 v212, v120, v121
Maav 104,236,244
s_add_u32 s68, s68, s63
v_exp_f32 v126, v54
Maav 108,236,252
s_add_u32 s71, s71, s64
ds_read_b128 a[184:187], v1 offset:14336
v_cvt_pk_bf16_f32 v213, v122, v123
Maav 48,244,228
v_exp_f32 v127, v55
Maav 52,244,236
s_waitcnt lgkmcnt(12)
ds_read_b128 a[188:191], v1 offset:15360
v_cvt_pk_bf16_f32 v220, v124, v125
Maav 112,244,244
v_exp_f32 v120, v60
Maav 116,244,252
v_exp_f32 v121, v61
Maav 56,252,228
v_exp_f32 v122, v62
Maav 60,252,236
v_exp_f32 v123, v63
Maav 120,252,244
s_waitcnt lgkmcnt(0)
v_cvt_pk_bf16_f32 v221, v126, v127
v_cvt_pk_bf16_f32 v222, v120, v121
Maav 124,252,252
v_cvt_pk_bf16_f32 v223, v122, v123
v_add_u32 v2, s81, v0
s_bitcmp1_b64 s[32:33], s99
s_cbranch_scc1 .Lhy
.Lhy_r:
s_cmp_ge_u32 s99, 33
s_cbranch_scc1 .Lend
.Laa:
Cvav 64,128,128,4
s_waitcnt vmcnt(20)
s_barrier
Mvav 64,132,136
ds_read_b128 a[192:195], v2 offset:0
v_add_u32 v1, s80, v0
Mvav 64,136,144
ds_read_b128 a[196:199], v2 offset:1024
v_readlane_b32 s34, v12, s35
Mvav 64,140,152
ds_read_b128 a[200:203], v2 offset:2048
v_readlane_b32 s82, v14, s35
Cvvv 116,96,196,112
ds_read_b128 a[204:207], v2 offset:3072
Mvvv 116,100,204
ds_read_b128 a[208:211], v2 offset:4096
Mvvv 116,96,212
ds_read_b128 a[212:215], v2 offset:5120
Mvvv 116,100,220
ds_read_b128 a[216:219], v2 offset:6144
Cvav 68,128,160,8
s_or_b32 m0, s70, s92
v_exp_f32 v120, v64
Mvav 68,132,168
ds_read_b128 a[220:223], v2 offset:7168
buffer_load_dwordx4 v0, s[48:51], s34 offen sc1 lds
Mvav 68,136,176
v_exp_f32 v121, v65
Mvav 68,140,184
ds_read_b128 a[224:227], v2 offset:8192
v_cvt_pk_bf16_f32 v228, v120, v121
Cvav 72,144,128,4
v_exp_f32 v122, v66
Mvav 72,148,136
ds_read_b128 a[228:231], v2 offset:9216
v_add_f32 v26, v116, v117
Mvav 72,152,144
v_exp_f32 v123, v67
Mvav 72,156,152
ds_read_b128 a[232:235], v2 offset:10240
v_cmp_nge_f32 vcc, 0x53800000, v26
Cvav 76,144,160,8
v_exp_f32 v124, v68
Mvav 76,148,168
ds_read_b128 a[236:239], v2 offset:11264
buffer_load_dwordx4 v0, s[48:51], s34 offen offset:1024 sc1 lds
Mvav 76,152,176
v_exp_f32 v125, v69
Mvav 76,156,184
ds_read_b128 a[240:243], v2 offset:12288
v_cvt_pk_bf16_f32 v229, v122, v123
Cvav 80,160,128,4
s_or_b32 s81, s92, 0x10000
v_exp_f32 v126, v70
Mvav 80,164,136
ds_read_b128 a[244:247], v2 offset:13312
buffer_load_dwordx4 v0, s[48:51], s34 offen offset:2048 sc1 lds
Mvav 80,168,144
v_exp_f32 v127, v71
Mvav 80,172,152
ds_read_b128 a[248:251], v2 offset:14336
v_cvt_pk_bf16_f32 v236, v124, v125
Cvav 84,160,160,8
s_waitcnt lgkmcnt(12)
v_exp_f32 v120, v72
Mvav 84,164,168
ds_read_b128 a[252:255], v2 offset:15360
buffer_load_dwordx4 v0, s[48:51], s34 offen offset:3072 sc1 lds
Mvav 84,168,176
v_exp_f32 v121, v73
Mvav 84,172,184
s_xor_b32 s58, s92, 0x18000
ds_read_b128 a[128:131], v1 offset:0
v_cvt_pk_bf16_f32 v237, v126, v127
Cvav 88,176,128,4
s_or_b32 m0, s70, s58
v_exp_f32 v122, v74
Mvav 88,180,136
ds_read_b128 a[132:135], v1 offset:1024
buffer_load_dwordx4 v3, s[12:15], s82 offen sc1 lds
Mvav 88,184,144
v_exp_f32 v123, v75
Mvav 88,188,152
)"
R"(
s_waitcnt lgkmcnt(12)
ds_read_b128 a[136:139], v1 offset:2048
v_cvt_pk_bf16_f32 v230, v120, v121
Cvav 92,176,160,8
s_add_u32 s72, s82, s24
v_exp_f32 v124, v76
Mvav 92,180,168
ds_read_b128 a[140:143], v1 offset:3072
buffer_load_dwordx4 v3, s[12:15], s72 offen offset:1024 sc1 lds
Mvav 92,184,176
v_exp_f32 v125, v77
Mvav 92,188,184
s_cbranch_vccnz .Laa1
.Laa1_r:
Maav 0,192,196
v_exp_f32 v126, v78
Maav 0,196,212
s_waitcnt lgkmcnt(11)
ds_read_b128 a[144:147], v1 offset:4096
v_cvt_pk_bf16_f32 v231, v122, v123
Maav 4,192,204
s_add_u32 s72, s82, s25
v_exp_f32 v127, v79
Maav 4,196,220
ds_read_b128 a[148:151], v1 offset:5120
buffer_load_dwordx4 v3, s[12:15], s72 offen offset:2048 sc1 lds
Maav 8,200,196
v_exp_f32 v120, v80
Maav 8,204,212
ds_read_b128 a[152:155], v1 offset:6144
v_cvt_pk_bf16_f32 v238, v124, v125
Maav 12,200,204
s_add_u32 s72, s82, s26
v_exp_f32 v121, v81
Maav 12,204,220
ds_read_b128 a[156:159], v1 offset:7168
buffer_load_dwordx4 v3, s[12:15], s72 offen offset:3072 sc1 lds
Maav 16,208,196
s_waitcnt lgkmcnt(12)
v_exp_f32 v122, v82
Maav 16,212,212
s_cmp_lt_u32 s99, s73
s_cselect_b32 m0, s71, 0x30000
ds_read_b128 a[160:163], v1 offset:8192
v_add_u32 v2, s81, v0
Maav 20,208,204
v_exp_f32 v123, v83
Maav 20,212,220
ds_read_b128 a[164:167], v1 offset:9216
buffer_load_dwordx4 v25, s[36:39], s68 offen nt lds
Maav 24,216,196
s_waitcnt lgkmcnt(8)
v_exp_f32 v124, v84
Maav 24,220,212
s_or_b32 m0, m0, s62
ds_read_b128 a[168:171], v1 offset:10240
v_cvt_pk_bf16_f32 v239, v126, v127
Maav 28,216,204
s_add_u32 s72, s68, 128
v_exp_f32 v125, v85
Maav 28,220,220
ds_read_b128 a[172:175], v1 offset:11264
buffer_load_dwordx4 v25, s[36:39], s72 offen lds
Maav 32,224,196
s_add_u32 s35, s35, 1
v_exp_f32 v126, v86
Maav 32,228,212
s_add_u32 s92, s92, 0x4000
ds_read_b128 a[176:179], v1 offset:12288
v_cvt_pk_bf16_f32 v244, v120, v121
Maav 36,224,204
s_and_b32 s92, s92, 0xc000
v_exp_f32 v127, v87
Maav 36,228,220
s_add_u32 s80, s92, 0x4000
ds_read_b128 a[180:183], v1 offset:13312
v_cvt_pk_bf16_f32 v245, v122, v123
Maav 40,232,196
s_and_b32 s80, s80, 0xc000
v_exp_f32 v120, v88
Maav 40,236,212
s_add_u32 s99, s99, 1
ds_read_b128 a[184:187], v1 offset:14336
v_cvt_pk_bf16_f32 v252, v124, v125
Maav 44,232,204
s_add_u32 s68, s68, s63
ds_read_b128 a[188:191], v1 offset:15360
v_cvt_pk_bf16_f32 v253, v126, v127
Maav 44,236,220
s_add_u32 s71, s71, s64
v_exp_f32 v121, v89
Maav 48,240,196
v_exp_f32 v122, v90
Maav 48,244,212
v_exp_f32 v123, v91
Maav 52,240,204
v_exp_f32 v124, v92
Maav 52,244,220
v_exp_f32 v125, v93
Maav 56,248,196
v_exp_f32 v126, v94
Maav 56,252,212
v_exp_f32 v127, v95
Maav 60,248,204
s_waitcnt lgkmcnt(0)
v_cvt_pk_bf16_f32 v246, v120, v121
v_cvt_pk_bf16_f32 v247, v122, v123
Maav 60,252,220
v_cvt_pk_bf16_f32 v254, v124, v125
v_cvt_pk_bf16_f32 v255, v126, v127
s_bitcmp1_b64 s[32:33], s99
s_cbranch_scc1 .Lhaa1
.Lhaa1_r:
Cvav 32,128,128,4
s_waitcnt vmcnt(20)
s_barrier
Mvav 32,132,136
ds_read_b128 a[192:195], v2 offset:0
v_add_u32 v1, s80, v0
Mvav 32,136,144
ds_read_b128 a[196:199], v2 offset:1024
v_readlane_b32 s34, v12, s35
Mvav 32,140,152
ds_read_b128 a[200:203], v2 offset:2048
v_readlane_b32 s82, v14, s35
Cvvv 112,96,228,116
ds_read_b128 a[204:207], v2 offset:3072
Mvvv 112,100,236
ds_read_b128 a[208:211], v2 offset:4096
Mvvv 112,96,244
ds_read_b128 a[212:215], v2 offset:5120
Mvvv 112,100,252
ds_read_b128 a[216:219], v2 offset:6144
Cvav 36,128,160,8
s_or_b32 m0, s70, s92
v_exp_f32 v120, v32
Mvav 36,132,168
ds_read_b128 a[220:223], v2 offset:7168
buffer_load_dwordx4 v0, s[48:51], s34 offen sc1 lds
Mvav 36,136,176
v_exp_f32 v121, v33
Mvav 36,140,184
ds_read_b128 a[224:227], v2 offset:8192
v_cvt_pk_bf16_f32 v196, v120, v121
Cvav 40,144,128,4
v_exp_f32 v122, v34
Mvav 40,148,136
ds_read_b128 a[228:231], v2 offset:9216
v_add_f32 v26, v112, v113
Mvav 40,152,144
v_exp_f32 v123, v35
Mvav 40,156,152
ds_read_b128 a[232:235], v2 offset:10240
v_cmp_nge_f32 vcc, 0x53800000, v26
Cvav 44,144,160,8
v_exp_f32 v124, v36
Mvav 44,148,168
ds_read_b128 a[236:239], v2 offset:11264
buffer_load_dwordx4 v0, s[48:51], s34 offen offset:1024 sc1 lds
Mvav 44,152,176
v_exp_f32 v125, v37
Mvav 44,156,184
ds_read_b128 a[240:243], v2 offset:12288
v_cvt_pk_bf16_f32 v197, v122, v123
Cvav 48,160,128,4
s_or_b32 s81, s92, 0x10000
v_exp_f32 v126, v38
Mvav 48,164,136
ds_read_b128 a[244:247], v2 offset:13312
buffer_load_dwordx4 v0, s[48:51], s34 offen offset:2048 sc1 lds
Mvav 48,168,144
v_exp_f32 v127, v39
Mvav 48,172,152
ds_read_b128 a[248:251], v2 offset:14336
v_cvt_pk_bf16_f32 v204, v124, v125
Cvav 52,160,160,8
s_waitcnt lgkmcnt(12)
v_exp_f32 v120, v40
Mvav 52,164,168
ds_read_b128 a[252:255], v2 offset:15360
buffer_load_dwordx4 v0, s[48:51], s34 offen offset:3072 sc1 lds
Mvav 52,168,176
v_exp_f32 v121, v41
Mvav 52,172,184
s_xor_b32 s58, s92, 0x18000
ds_read_b128 a[128:131], v1 offset:0
v_cvt_pk_bf16_f32 v205, v126, v127
Cvav 56,176,128,4
s_or_b32 m0, s70, s58
v_exp_f32 v122, v42
Mvav 56,180,136
ds_read_b128 a[132:135], v1 offset:1024
buffer_load_dwordx4 v3, s[12:15], s82 offen sc1 lds
Mvav 56,184,144
v_exp_f32 v123, v43
Mvav 56,188,152
s_waitcnt lgkmcnt(12)
ds_read_b128 a[136:139], v1 offset:2048
v_cvt_pk_bf16_f32 v198, v120, v121
Cvav 60,176,160,8
s_add_u32 s72, s82, s24
v_exp_f32 v124, v44
Mvav 60,180,168
ds_read_b128 a[140:143], v1 offset:3072
buffer_load_dwordx4 v3, s[12:15], s72 offen offset:1024 sc1 lds
Mvav 60,184,176
v_exp_f32 v125, v45
Mvav 60,188,184
s_cbranch_vccnz .Laa0
.Laa0_r:
Maav 0,192,228
s_waitcnt lgkmcnt(11)
v_exp_f32 v126, v46
Maav 0,196,244
ds_read_b128 a[144:147], v1 offset:4096
buffer_store_dwordx2 v[132:133], v13, s[48:51], s85 offen sc1
v_cvt_pk_bf16_f32 v199, v122, v123
Maav 4,192,236
s_add_u32 s72, s82, s25
v_exp_f32 v127, v47
Maav 4,196,252
ds_read_b128 a[148:151], v1 offset:5120
buffer_load_dwordx4 v3, s[12:15], s72 offen offset:2048 sc1 lds
Maav 8,200,228
s_mov_b32 s85, s97
v_exp_f32 v120, v48
Maav 8,204,244
s_mov_b32 s97, 0x80000000
ds_read_b128 a[152:155], v1 offset:6144
v_cvt_pk_bf16_f32 v206, v124, v125
Maav 12,200,236
s_add_u32 s72, s82, s26
v_exp_f32 v121, v49
Maav 12,204,252
ds_read_b128 a[156:159], v1 offset:7168
buffer_load_dwordx4 v3, s[12:15], s72 offen offset:3072 sc1 lds
Maav 16,208,228
s_waitcnt lgkmcnt(12)
v_exp_f32 v122, v50
Maav 16,212,244
s_cmp_lt_u32 s99, s73
s_cselect_b32 m0, s71, 0x30000
ds_read_b128 a[160:163], v1 offset:8192
v_add_u32 v2, s81, v0
Maav 20,208,236
v_exp_f32 v123, v51
Maav 20,212,252
ds_read_b128 a[164:167], v1 offset:9216
buffer_load_dwordx4 v25, s[36:39], s68 offen nt lds
Maav 24,216,228
s_waitcnt lgkmcnt(8)
v_exp_f32 v124, v52
Maav 24,220,244
s_or_b32 m0, m0, s62
ds_read_b128 a[168:171], v1 offset:10240
v_cvt_pk_bf16_f32 v207, v126, v127
Maav 28,216,236
s_add_u32 s72, s68, 128
v_exp_f32 v125, v53
Maav 28,220,252
ds_read_b128 a[172:175], v1 offset:11264
buffer_load_dwordx4 v25, s[36:39], s72 offen lds
Maav 32,224,228
s_add_u32 s35, s35, 1
v_exp_f32 v126, v54
Maav 32,228,244
s_add_u32 s92, s92, 0x4000
ds_read_b128 a[176:179], v1 offset:12288
v_cvt_pk_bf16_f32 v212, v120, v121
Maav 36,224,236
s_and_b32 s92, s92, 0xc000
v_exp_f32 v127, v55
Maav 36,228,252
s_add_u32 s80, s92, 0x4000
ds_read_b128 a[180:183], v1 offset:13312
v_cvt_pk_bf16_f32 v213, v122, v123
Maav 40,232,228
s_and_b32 s80, s80, 0xc000
v_exp_f32 v120, v56
Maav 40,236,244
s_add_u32 s99, s99, 1
ds_read_b128 a[184:187], v1 offset:14336
v_cvt_pk_bf16_f32 v220, v124, v125
Maav 44,232,236
s_add_u32 s68, s68, s63
ds_read_b128 a[188:191], v1 offset:15360
v_cvt_pk_bf16_f32 v221, v126, v127
Maav 44,236,252
s_add_u32 s71, s71, s64
v_exp_f32 v121, v57
Maav 48,240,228
v_exp_f32 v122, v58
Maav 48,244,244
v_exp_f32 v123, v59
Maav 52,240,236
v_exp_f32 v124, v60
Maav 52,244,252
v_exp_f32 v125, v61
Maav 56,248,228
v_exp_f32 v126, v62
Maav 56,252,244
v_exp_f32 v127, v63
Maav 60,248,236
s_waitcnt lgkmcnt(0)
v_cvt_pk_bf16_f32 v214, v120, v121
v_cvt_pk_bf16_f32 v215, v122, v123
Maav 60,252,252
v_cvt_pk_bf16_f32 v222, v124, v125
v_cvt_pk_bf16_f32 v223, v126, v127
s_bitcmp1_b64 s[32:33], s99
s_cbranch_scc1 .Lhaa0
.Lhaa0_r:
s_cmp_lt_u32 s99, 33
s_cbranch_scc1 .Laa
.Lend:
v_lshrrev_b32 v26, 9, v0
v_bfe_u32 v27, v0, 4, 5
v_lshlrev_b32 v27, 4, v27
v_lshl_add_u32 v26, v26, 11, v27
s_lshr_b32 s58, s70, 3
s_add_u32 s58, s58, 0x20000
v_add_u32 v3, s58, v26
ds_read_b64_tr_b16 a[64:65], v3 offset:0
ds_read_b64_tr_b16 a[66:67], v3 offset:4096
ds_read_b64_tr_b16 a[72:73], v3 offset:8192
ds_read_b64_tr_b16 a[74:75], v3 offset:12288
ds_read_b64_tr_b16 a[80:81], v3 offset:16384
ds_read_b64_tr_b16 a[82:83], v3 offset:20480
ds_read_b64_tr_b16 a[88:89], v3 offset:24576
ds_read_b64_tr_b16 a[90:91], v3 offset:28672
Cvvv 116,96,196,112
s_waitcnt lgkmcnt(0)
s_waitcnt vmcnt(20)
s_barrier
Mvvv 116,100,204
s_add_u32 m0, s70, 0
ds_read_b64_tr_b16 a[68:69], v3 offset:8
ds_read_b64_tr_b16 a[70:71], v3 offset:4104
Mvvv 116,96,212
s_add_u32 s72, s67, 524288
ds_read_b64_tr_b16 a[76:77], v3 offset:8200
ds_read_b64_tr_b16 a[78:79], v3 offset:12296
buffer_load_dwordx4 v25, s[28:31], s72 offen lds
Mvvv 116,100,220
s_add_u32 m0, s70, 896
ds_read_b64_tr_b16 a[84:85], v3 offset:16392
ds_read_b64_tr_b16 a[86:87], v3 offset:20488
Zvaa 64,64,128
s_add_u32 s72, s67, 524288
ds_read_b64_tr_b16 a[92:93], v3 offset:24584
ds_read_b64_tr_b16 a[94:95], v3 offset:28680
buffer_load_dwordx4 v25, s[28:31], s72 offen offset:128 lds
Mvaa 64,72,132
s_add_u32 m0, s70, 2048
ds_read_b128 a[192:195], v2 offset:0
ds_read_b128 a[196:199], v2 offset:1024
Mvaa 64,80,136
s_add_u32 s72, s67, 540672
ds_read_b128 a[200:203], v2 offset:2048
ds_read_b128 a[204:207], v2 offset:3072
buffer_load_dwordx4 v25, s[28:31], s72 offen lds
Mvaa 64,88,140
s_waitcnt lgkmcnt(10)
s_add_u32 m0, s70, 2944
ds_read_b128 a[208:211], v2 offset:4096
v_subrev_u32 v1, 0x10000, v3
Zvaa 68,68,128
s_waitcnt lgkmcnt(9)
s_add_u32 s72, s67, 540672
ds_read_b128 a[212:215], v2 offset:5120
buffer_load_dwordx4 v25, s[28:31], s72 offen offset:128 lds
v_subrev_u32 v10, 0x20000, v3
Mvaa 68,76,132
s_waitcnt lgkmcnt(8)
s_add_u32 m0, s70, 98304
ds_read_b128 a[216:219], v2 offset:6144
buffer_load_dwordx4 v[16:19], v0, s[48:51], s83 offen sc1
v_add_f32 v26, v116, v117
Mvaa 68,84,136
s_waitcnt lgkmcnt(7)
s_add_u32 s72, s67, 655360
ds_read_b128 a[220:223], v2 offset:7168
buffer_load_dwordx4 v25, s[28:31], s72 offen lds
v_cmp_nge_f32 vcc, 0x53800000, v26
Mvaa 68,92,140
s_add_u32 m0, s70, 99200
ds_read_b128 a[224:227], v2 offset:8192
v_add_u32 v12, 0x10000, v0
Zvaa 72,64,144
s_add_u32 s72, s67, 655360
ds_read_b128 a[228:231], v2 offset:9216
buffer_load_dwordx4 v25, s[28:31], s72 offen offset:128 lds
v_add_u32 v14, 0x20000, v0
Mvaa 72,72,148
s_waitcnt lgkmcnt(7)
s_add_u32 m0, s70, 100352
s_xor_b32 s38, s96, 0x30f0
ds_read_b128 a[232:235], v2 offset:10240
Mvaa 72,80,152
s_add_u32 s72, s67, 671744
ds_read_b128 a[236:239], v2 offset:11264
buffer_load_dwordx4 v25, s[28:31], s72 offen lds
Mvaa 72,88,156
s_add_u32 m0, s70, 101248
s_lshl_b32 s21, s91, 14
ds_read_b128 a[240:243], v2 offset:12288
Zvaa 76,68,144
s_add_u32 s72, s67, 671744
ds_read_b128 a[244:247], v2 offset:13312
buffer_load_dwordx4 v25, s[28:31], s72 offen offset:128 lds
Mvaa 76,76,148
s_lshl_b32 s59, s57, 7
s_add_u32 s21, s21, s59
ds_read_b128 a[248:251], v2 offset:14336
Mvaa 76,84,152
s_add_u32 s21, s21, 1048576
s_mul_i32 s58, s57, 36864
ds_read_b128 a[252:255], v2 offset:15360
Mvaa 76,92,156
s_cbranch_vccnz .Ldr
.Ldr_r:
s_add_u32 s66, s66, s58
Maav 0,192,196
s_mul_i32 s58, s76, 1179648
s_add_u32 s12, s66, s58
s_mul_i32 s58, s77, 1179648
Maav 0,196,212
s_add_u32 s13, s66, s58
s_mul_i32 s58, s78, 1179648
s_add_u32 s14, s66, s58
v_rcp_f32 v32, v116
Maav 4,192,204
s_mul_i32 s58, s79, 1179648
s_add_u32 s15, s66, s58
s_lshl3_add_u32 s58, s86, s61
v_rcp_f32 v33, v117
Maav 4,196,220
s_lshl_b32 s58, s58, 19
s_lshr_b32 s59, s70, 1
s_add_u32 s58, s58, s59
s_add_u32 s84, s95, 4096
Maav 8,200,196
s_waitcnt vmcnt(9) lgkmcnt(0)
s_add_u32 m0, s70, 114688
s_add_u32 s58, s58, 75497472
Maav 8,204,212
s_barrier
s_add_u32 s72, s67, 786432
Maav 12,200,204
s_lshl_b32 s59, s79, 13
buffer_load_dwordx4 v25, s[28:31], s72 offen lds
)"
R"(
Maav 12,204,220
s_add_u32 m0, s70, 115584
s_add_u32 s22, s58, s59
s_lshl_b32 s59, s79, 17
Maav 16,208,196
s_add_u32 s72, s67, 786432
buffer_load_dwordx4 v25, s[28:31], s72 offen offset:128 lds
Maav 16,212,212
s_add_u32 m0, s70, 116736
s_add_u32 s23, s58, s59
s_and_b32 s59, s61, 1
Maav 20,208,204
s_add_u32 s72, s67, 802816
buffer_load_dwordx4 v25, s[28:31], s72 offen lds
Maav 20,212,220
s_add_u32 m0, s70, 117632
s_lshl_b32 s32, s59, 12
s_lshr_b32 s59, s70, 2
Maav 24,216,196
s_add_u32 s72, s67, 802816
buffer_load_dwordx4 v25, s[28:31], s72 offen offset:128 lds
v_accvgpr_read_b32 v34, a0
v_accvgpr_read_b32 v35, a1
v_mul_f32 v34, v34, v32
v_mul_f32 v35, v35, v32
Maav 24,220,212
s_add_u32 m0, s70, 131072
s_sub_u32 s32, s32, s59
s_lshl3_add_u32 s58, s86, s61
v_cvt_pk_bf16_f32 v40, v34, v35
v_accvgpr_read_b32 v34, a2
v_accvgpr_read_b32 v35, a3
v_mul_f32 v34, v34, v32
v_mul_f32 v35, v35, v32
v_cvt_pk_bf16_f32 v41, v34, v35
Maav 28,216,204
s_add_u32 s72, s67, 917504
buffer_load_dwordx4 v25, s[28:31], s72 offen lds
v_accvgpr_read_b32 v34, a8
v_accvgpr_read_b32 v35, a9
v_mul_f32 v34, v34, v32
v_mul_f32 v35, v35, v32
Maav 28,220,220
s_add_u32 m0, s70, 131968
s_lshl2_add_u32 s58, s58, s57
s_lshl_b32 s58, s58, 7
v_cvt_pk_bf16_f32 v42, v34, v35
v_accvgpr_read_b32 v34, a10
v_accvgpr_read_b32 v35, a11
v_mul_f32 v34, v34, v32
v_mul_f32 v35, v35, v32
v_cvt_pk_bf16_f32 v43, v34, v35
Maav 32,224,196
s_add_u32 s72, s67, 917504
buffer_load_dwordx4 v25, s[28:31], s72 offen offset:128 lds
v_accvgpr_read_b32 v34, a4
v_accvgpr_read_b32 v35, a5
v_mul_f32 v34, v34, v33
v_mul_f32 v35, v35, v33
Maav 32,228,212
s_add_u32 m0, s70, 133120
s_add_u32 s33, s58, 49152
s_lshl3_add_u32 s34, s79, s33
v_cvt_pk_bf16_f32 v44, v34, v35
v_accvgpr_read_b32 v34, a6
v_accvgpr_read_b32 v35, a7
v_mul_f32 v34, v34, v33
v_mul_f32 v35, v35, v33
v_cvt_pk_bf16_f32 v45, v34, v35
Maav 36,224,204
s_add_u32 s72, s67, 933888
buffer_load_dwordx4 v25, s[28:31], s72 offen lds
v_accvgpr_read_b32 v34, a12
v_accvgpr_read_b32 v35, a13
v_mul_f32 v34, v34, v33
v_mul_f32 v35, v35, v33
Maav 36,228,220
s_add_u32 m0, s70, 134016
s_and_b32 s24, s74, 0xff
s_bfe_u32 s25, s74, 0x80008
v_cvt_pk_bf16_f32 v46, v34, v35
v_accvgpr_read_b32 v34, a14
v_accvgpr_read_b32 v35, a15
v_mul_f32 v34, v34, v33
v_mul_f32 v35, v35, v33
v_cvt_pk_bf16_f32 v47, v34, v35
Maav 40,232,196
s_add_u32 s72, s67, 933888
buffer_load_dwordx4 v25, s[28:31], s72 offen offset:128 lds
v_accvgpr_read_b32 v34, a16
v_accvgpr_read_b32 v35, a17
v_mul_f32 v34, v34, v32
v_mul_f32 v35, v35, v32
Maav 40,236,212
s_add_u32 m0, s70, 147456
s_bfe_u32 s26, s74, 0x80010
s_lshr_b32 s27, s74, 24
buffer_store_dwordx4 v[40:43], v0, s[48:51], s95 offen sc1
v_cvt_pk_bf16_f32 v48, v34, v35
v_accvgpr_read_b32 v34, a18
v_accvgpr_read_b32 v35, a19
v_mul_f32 v34, v34, v32
Maav 44,232,204
s_add_u32 s72, s67, 1048576
buffer_load_dwordx4 v25, s[28:31], s72 offen lds
v_mul_f32 v35, v35, v32
v_cvt_pk_bf16_f32 v49, v34, v35
v_accvgpr_read_b32 v34, a24
v_accvgpr_read_b32 v35, a25
Maav 44,236,220
s_add_u32 m0, s70, 148352
s_lshr_b32 s58, s60, 3
s_lshl2_add_u32 s58, s58, s57
buffer_store_dwordx4 v[44:47], v0, s[48:51], s84 offen sc1
v_mul_f32 v34, v34, v32
v_mul_f32 v35, v35, v32
v_cvt_pk_bf16_f32 v50, v34, v35
v_accvgpr_read_b32 v34, a26
Maav 48,240,196
s_add_u32 s72, s67, 1048576
buffer_load_dwordx4 v25, s[28:31], s72 offen offset:128 lds
v_accvgpr_read_b32 v35, a27
v_mul_f32 v34, v34, v32
v_mul_f32 v35, v35, v32
v_cvt_pk_bf16_f32 v51, v34, v35
Maav 48,244,212
s_add_u32 m0, s70, 149504
s_lshl_b32 s59, s58, 7
s_lshl_b32 s20, s91, 17
buffer_store_dwordx4 v[48:51], v0, s[48:51], s95 offen offset:1024 sc1
v_accvgpr_read_b32 v34, a20
v_accvgpr_read_b32 v35, a21
v_mul_f32 v34, v34, v33
v_mul_f32 v35, v35, v33
Maav 52,240,204
s_add_u32 s72, s67, 1064960
ds_read_b128 v[128:131], v0 offset:16384
buffer_load_dwordx4 v25, s[28:31], s72 offen lds
v_cvt_pk_bf16_f32 v52, v34, v35
v_accvgpr_read_b32 v34, a22
v_accvgpr_read_b32 v35, a23
Maav 52,244,220
s_add_u32 m0, s70, 150400
s_add_u32 s20, s20, s59
ds_read_b128 v[136:139], v0 offset:17408
v_mul_f32 v34, v34, v33
v_mul_f32 v35, v35, v33
v_cvt_pk_bf16_f32 v53, v34, v35
v_accvgpr_read_b32 v34, a28
v_accvgpr_read_b32 v35, a29
Maav 56,248,196
s_add_u32 s72, s67, 1064960
ds_read_b128 v[144:147], v0 offset:18432
buffer_load_dwordx4 v25, s[28:31], s72 offen offset:128 lds
v_mul_f32 v34, v34, v33
v_mul_f32 v35, v35, v33
v_cvt_pk_bf16_f32 v54, v34, v35
Maav 56,252,212
s_mul_i32 s35, s58, 14336
s_lshl_b32 s59, s91, 7
ds_read_b128 v[152:155], v0 offset:19456
v_accvgpr_read_b32 v34, a30
v_accvgpr_read_b32 v35, a31
v_mul_f32 v34, v34, v33
v_mul_f32 v35, v35, v33
v_cvt_pk_bf16_f32 v55, v34, v35
Maav 60,248,204
s_add_u32 s35, s35, s59
s_add_u32 s35, s35, 2097152
ds_read_b64_tr_b16 a[96:97], v1 offset:0
buffer_store_dwordx4 v[52:55], v0, s[48:51], s84 offen offset:1024 sc1
v_accvgpr_read_b32 v34, a32
v_accvgpr_read_b32 v35, a33
v_mul_f32 v34, v34, v32
Maav 60,252,220
s_cmp_eq_u32 s61, 0
s_cselect_b32 s20, 0x80000000, s20
ds_read_b64_tr_b16 a[98:99], v1 offset:4096
v_mul_f32 v35, v35, v32
v_cvt_pk_bf16_f32 v228, v34, v35
v_accvgpr_read_b32 v34, a34
v_accvgpr_read_b32 v35, a35
v_mul_f32 v34, v34, v32
Zvaa 80,64,160
s_cmp_eq_u32 s61, 0
s_cselect_b32 s35, 0x80000000, s35
ds_read_b64_tr_b16 a[104:105], v1 offset:8192
v_mul_f32 v35, v35, v32
v_cvt_pk_bf16_f32 v229, v34, v35
v_accvgpr_read_b32 v34, a40
v_accvgpr_read_b32 v35, a41
v_mul_f32 v34, v34, v32
Mvaa 80,72,164
s_cmp_eq_u32 s61, 0
s_cselect_b64 s[36:37], 0, -1
ds_read_b64_tr_b16 a[106:107], v1 offset:12288
v_mul_f32 v35, v35, v32
v_cvt_pk_bf16_f32 v230, v34, v35
v_accvgpr_read_b32 v34, a42
v_accvgpr_read_b32 v35, a43
v_mul_f32 v34, v34, v32
Mvaa 80,80,168
s_cmp_eq_u32 s61, 0
s_cselect_b32 s21, 0x80000000, s21
ds_read_b64_tr_b16 a[112:113], v1 offset:16384
v_mul_f32 v35, v35, v32
v_cvt_pk_bf16_f32 v231, v34, v35
v_accvgpr_read_b32 v34, a36
v_accvgpr_read_b32 v35, a37
v_mul_f32 v34, v34, v33
Mvaa 80,88,172
s_waitcnt lgkmcnt(6)
ds_read_b64_tr_b16 a[114:115], v1 offset:20480
buffer_store_dwordx4 v[228:231], v0, s[48:51], s95 offen offset:2048 sc1
v_mul_f32 v35, v35, v33
v_cvt_pk_bf16_f32 v236, v34, v35
v_accvgpr_read_b32 v34, a38
Zvaa 84,68,160
ds_read_b64_tr_b16 a[120:121], v1 offset:24576
v_accvgpr_read_b32 v35, a39
v_mul_f32 v34, v34, v33
v_mul_f32 v35, v35, v33
v_cvt_pk_bf16_f32 v237, v34, v35
v_accvgpr_read_b32 v34, a44
Mvaa 84,76,164
ds_read_b64_tr_b16 a[122:123], v1 offset:28672
v_accvgpr_read_b32 v35, a45
v_mul_f32 v34, v34, v33
v_mul_f32 v35, v35, v33
v_cvt_pk_bf16_f32 v238, v34, v35
v_accvgpr_read_b32 v34, a46
Mvaa 84,84,168
s_waitcnt lgkmcnt(6)
ds_read_b64_tr_b16 a[100:101], v1 offset:8
v_accvgpr_read_b32 v35, a47
v_mul_f32 v34, v34, v33
v_mul_f32 v35, v35, v33
v_cvt_pk_bf16_f32 v239, v34, v35
v_accvgpr_read_b32 v34, a48
Mvaa 84,92,172
ds_read_b64_tr_b16 a[102:103], v1 offset:4104
buffer_store_dwordx4 v[236:239], v0, s[48:51], s84 offen offset:2048 sc1
v_accvgpr_read_b32 v35, a49
v_mul_f32 v34, v34, v32
v_mul_f32 v35, v35, v32
Zvaa 88,64,176
ds_read_b64_tr_b16 a[108:109], v1 offset:8200
v_cvt_pk_bf16_f32 v244, v34, v35
v_accvgpr_read_b32 v34, a50
v_accvgpr_read_b32 v35, a51
v_mul_f32 v34, v34, v32
v_mul_f32 v35, v35, v32
Mvaa 88,72,180
s_waitcnt lgkmcnt(6)
ds_read_b64_tr_b16 a[110:111], v1 offset:12296
v_cvt_pk_bf16_f32 v245, v34, v35
v_accvgpr_read_b32 v34, a56
v_accvgpr_read_b32 v35, a57
v_mul_f32 v34, v34, v32
v_mul_f32 v35, v35, v32
Mvaa 88,80,184
ds_read_b64_tr_b16 a[116:117], v1 offset:16392
v_cvt_pk_bf16_f32 v246, v34, v35
v_accvgpr_read_b32 v34, a58
v_accvgpr_read_b32 v35, a59
v_mul_f32 v34, v34, v32
v_mul_f32 v35, v35, v32
Mvaa 88,88,188
ds_read_b64_tr_b16 a[118:119], v1 offset:20488
v_cvt_pk_bf16_f32 v247, v34, v35
v_accvgpr_read_b32 v34, a52
v_accvgpr_read_b32 v35, a53
v_mul_f32 v34, v34, v33
v_mul_f32 v35, v35, v33
Zvaa 92,68,176
ds_read_b64_tr_b16 a[124:125], v1 offset:24584
buffer_store_dwordx4 v[244:247], v0, s[48:51], s95 offen offset:3072 sc1
v_cvt_pk_bf16_f32 v252, v34, v35
v_accvgpr_read_b32 v34, a54
v_accvgpr_read_b32 v35, a55
Mvaa 92,76,180
ds_read_b64_tr_b16 a[126:127], v1 offset:28680
v_mul_f32 v34, v34, v33
v_mul_f32 v35, v35, v33
v_cvt_pk_bf16_f32 v253, v34, v35
v_accvgpr_read_b32 v34, a60
v_accvgpr_read_b32 v35, a61
Mvaa 92,84,184
ds_read_b128 v[160:163], v0 offset:20480
v_mul_f32 v34, v34, v33
v_mul_f32 v35, v35, v33
v_cvt_pk_bf16_f32 v254, v34, v35
v_accvgpr_read_b32 v34, a62
v_accvgpr_read_b32 v35, a63
Mvaa 92,92,188
ds_read_b128 v[168:171], v0 offset:21504
v_mul_f32 v34, v34, v33
v_mul_f32 v35, v35, v33
v_cvt_pk_bf16_f32 v255, v34, v35
Mvav 64,96,128
ds_read_b128 v[176:179], v0 offset:22528
buffer_store_dwordx4 v[252:255], v0, s[48:51], s84 offen offset:3072 sc1
Mvav 64,104,136
s_waitcnt lgkmcnt(13)
ds_read_b128 v[184:187], v0 offset:23552
Mvav 64,112,144
s_waitcnt lgkmcnt(12)
ds_read_b128 v[132:135], v0 offset:24576
Mvav 64,120,152
s_waitcnt lgkmcnt(7)
ds_read_b128 v[140:143], v0 offset:25600
Mvav 68,100,128
s_waitcnt lgkmcnt(6)
s_add_u32 s62, s70, 32768
ds_read_b128 v[148:151], v0 offset:26624
v_lshrrev_b32 v11, 8, v0
Mvav 68,108,136
s_waitcnt lgkmcnt(6)
s_lshl3_add_u32 s64, s79, s86
ds_read_b128 v[156:159], v0 offset:27648
v_lshlrev_b32 v11, 12, v11
Mvav 68,116,144
s_waitcnt lgkmcnt(6)
s_and_b32 s39, s67, 0x700
ds_read_b128 v[164:167], v0 offset:28672
v_and_b32 v56, 128, v0
Mvav 68,124,152
s_lshr_b32 s63, s70, 6
ds_read_b128 v[172:175], v0 offset:29696
v_lshl_add_u32 v11, v56, 2, v11
Mvav 72,96,160
s_add_u32 m0, s70, 65536
ds_read_b128 v[180:183], v0 offset:30720
ds_read_b128 v[188:191], v0 offset:31744
Mvav 72,104,168
s_waitcnt vmcnt(20) lgkmcnt(7)
s_barrier
s_add_u32 s72, s67, 1179648
Mvav 72,112,176
s_waitcnt lgkmcnt(6)
ds_read_b128 v[128:131], v0 offset:32768
buffer_load_dwordx4 v25, s[28:31], s72 offen lds
Mvav 72,120,184
s_waitcnt lgkmcnt(6)
s_add_u32 m0, s70, 66432
ds_read_b128 v[136:139], v0 offset:33792
v_mov_b32 v20, s74
Mvav 76,100,160
s_waitcnt lgkmcnt(6)
s_add_u32 s72, s67, 1179648
ds_read_b128 v[144:147], v0 offset:34816
v_mov_b32 v21, s75
Mvav 76,108,168
ds_read_b128 v[152:155], v0 offset:35840
buffer_load_dwordx4 v25, s[28:31], s72 offen offset:128 lds
Mvav 76,116,176
s_add_u32 m0, s70, 67584
ds_read_b64_tr_b16 a[128:129], v10 offset:0
v_mov_b32 v22, 0x80000000
Mvav 76,124,184
s_waitcnt lgkmcnt(6)
s_add_u32 s72, s67, 1196032
ds_read_b64_tr_b16 a[130:131], v10 offset:4096
v_writelane_b32 v22, s96, 0
Mvav 80,96,132
ds_read_b64_tr_b16 a[136:137], v10 offset:8192
buffer_load_dwordx4 v25, s[28:31], s72 offen lds
Mvav 80,104,140
s_add_u32 m0, s70, 68480
ds_read_b64_tr_b16 a[138:139], v10 offset:12288
v_writelane_b32 v22, s38, 1
Mvav 80,112,148
s_waitcnt lgkmcnt(6)
s_add_u32 s72, s67, 1196032
s_mov_b32 s18, 0x700070
ds_read_b64_tr_b16 a[144:145], v1 offset:32768
Mvav 80,120,156
ds_read_b64_tr_b16 a[146:147], v1 offset:36864
buffer_load_dwordx4 v25, s[28:31], s72 offen offset:128 lds
Mvav 84,100,132
s_mov_b32 s19, 0x700070
ds_read_b64_tr_b16 a[152:153], v1 offset:40960
v_and_b32 v56, 112, v0
Mvav 84,108,140
s_waitcnt lgkmcnt(6)
s_add_u32 m0, s70, 81920
ds_read_b64_tr_b16 a[154:155], v1 offset:45056
v_xor_b32 v16, s74, v16
Mvav 84,116,148
s_add_u32 s72, s87, 147456
ds_read_b64_tr_b16 a[132:133], v10 offset:8
v_bitop3_b32 v16, v17, s75, v16 bitop3:0xbe
Mvav 84,124,156
ds_read_b64_tr_b16 a[134:135], v10 offset:4104
buffer_load_dwordx4 v0, s[48:51], s72 offen sc1 lds
Mvav 88,96,164
s_waitcnt lgkmcnt(6)
s_add_u32 s39, s39, s63
ds_read_b64_tr_b16 a[140:141], v10 offset:8200
v_bitop3_b32 v16, v18, s74, v16 bitop3:0xbe
Mvav 88,104,172
s_lshl_b32 s63, s64, 15
ds_read_b64_tr_b16 a[142:143], v10 offset:12296
v_bitop3_b32 v16, v19, s75, v16 bitop3:0xbe
Mvav 88,112,180
ds_read_b64_tr_b16 a[148:149], v1 offset:32776
buffer_load_dwordx4 v0, s[48:51], s72 offen offset:1024 sc1 lds
Mvav 88,120,188
s_add_u32 s63, s63, s39
ds_read_b64_tr_b16 a[150:151], v1 offset:36872
v_cmp_ne_u32 vcc, 0, v16
Mvav 92,100,164
s_and_b64 vcc, vcc, s[18:19]
ds_read_b64_tr_b16 a[156:157], v1 offset:40968
v_add_u32 v11, v56, v11
Mvav 92,108,172
)"
R"(
ds_read_b64_tr_b16 a[158:159], v1 offset:45064
buffer_load_dwordx4 v0, s[48:51], s72 offen offset:2048 sc1 lds
Mvav 92,116,180
s_cbranch_vccnz .Lcf2
.Lcf2_r:
ds_read_b128 v[160:163], v0 offset:36864
Mvav 92,124,188
s_lshr_b32 s68, s39, 6
ds_read_b128 v[168:171], v0 offset:37888
v_bfe_u32 v60, v0, 6, 2
Mvav 64,128,128
s_waitcnt lgkmcnt(11)
ds_read_b128 v[176:179], v0 offset:38912
v_lshlrev_b32 v60, 6, v60
Mvav 64,136,136
ds_read_b128 v[184:187], v0 offset:39936
buffer_load_dwordx4 v0, s[48:51], s72 offen offset:3072 sc1 lds
Mvav 64,144,144
s_waitcnt lgkmcnt(12)
ds_read_b128 v[132:135], v0 offset:40960
v_bfe_u32 v58, v0, 4, 2
Mvav 64,152,152
s_waitcnt lgkmcnt(11)
ds_read_b128 v[140:143], v0 offset:41984
v_lshl_add_u32 v60, v58, 3, v60
Mvav 68,132,128
s_waitcnt lgkmcnt(10)
ds_read_b128 v[148:151], v0 offset:43008
v_lshrrev_b32 v58, 8, v0
Mvav 68,140,136
s_waitcnt lgkmcnt(9)
ds_read_b128 v[156:159], v0 offset:44032
v_lshl_add_u32 v60, v58, 10, v60
Mvav 68,148,144
s_waitcnt lgkmcnt(8)
ds_read_b128 v[164:167], v0 offset:45056
v_add_u32 v60, s62, v60
Mvav 68,156,152
s_waitcnt lgkmcnt(7)
ds_read_b128 v[172:175], v0 offset:46080
v_add_u32 v61, 81920, v60
Mvav 72,128,160
s_add_u32 m0, s70, 0
ds_read_b128 v[180:183], v0 offset:47104
ds_read_b128 v[188:191], v0 offset:48128
Mvav 72,136,168
s_waitcnt vmcnt(20) lgkmcnt(8)
s_barrier
s_add_u32 s72, s12, 4718592
Mvav 72,144,176
ds_read_b128 v[128:131], v0 offset:49152
buffer_load_dwordx4 v0, s[48:51], s72 offen sc1 lds
Mvav 72,152,184
ds_read_b128 v[136:139], v0 offset:50176
v_lshrrev_b32 v5, 6, v0
Mvav 76,132,160
ds_read_b128 v[144:147], v0 offset:51200
v_and_b32 v6, 48, v0
Mvav 76,140,168
ds_read_b128 v[152:155], v0 offset:52224
buffer_load_dwordx4 v0, s[48:51], s72 offen offset:1024 sc1 lds
Mvav 76,148,176
ds_read_b64_tr_b16 a[160:161], v1 offset:49152
v_lshl_add_u32 v4, v5, 11, v6
Mvav 76,156,184
s_waitcnt lgkmcnt(11)
ds_read_b64_tr_b16 a[162:163], v1 offset:53248
v_add_u32 v5, v5, v6
Mvav 80,128,132
ds_read_b64_tr_b16 a[168:169], v1 offset:57344
buffer_load_dwordx4 v0, s[48:51], s72 offen offset:2048 sc1 lds
Mvav 80,136,140
s_waitcnt lgkmcnt(12)
ds_read_b64_tr_b16 a[170:171], v1 offset:61440
v_lshlrev_b32 v5, 2, v5
Mvav 80,144,148
s_waitcnt lgkmcnt(9)
ds_read_b64_tr_b16 a[176:177], v3 offset:0
v_bfe_u32 v7, v0, 4, 3
Mvav 80,152,156
ds_read_b64_tr_b16 a[178:179], v3 offset:4096
buffer_load_dwordx4 v0, s[48:51], s72 offen offset:3072 sc1 lds
Mvav 84,132,132
s_add_u32 m0, s70, 16384
ds_read_b64_tr_b16 a[184:185], v3 offset:8192
v_lshrrev_b32 v9, 8, v0
Mvav 84,140,140
s_add_u32 s72, s67, 1310720
ds_read_b64_tr_b16 a[186:187], v3 offset:12288
v_lshl_add_u32 v7, v7, 2, v9
Mvav 84,148,148
ds_read_b64_tr_b16 a[164:165], v1 offset:49160
buffer_load_dwordx4 v25, s[28:31], s72 offen lds
Mvav 84,156,156
ds_read_b64_tr_b16 a[166:167], v1 offset:53256
v_lshlrev_b32 v7, 1, v7
Mvav 88,128,164
s_waitcnt lgkmcnt(12)
s_add_u32 m0, s70, 17280
ds_read_b64_tr_b16 a[172:173], v1 offset:57352
Mvav 88,136,172
s_add_u32 s72, s67, 1310720
ds_read_b64_tr_b16 a[174:175], v1 offset:61448
Mvav 88,144,180
ds_read_b64_tr_b16 a[180:181], v3 offset:8
buffer_load_dwordx4 v25, s[28:31], s72 offen offset:128 lds
Mvav 88,152,188
s_waitcnt lgkmcnt(12)
s_add_u32 m0, s70, 18432
ds_read_b64_tr_b16 a[182:183], v3 offset:4104
Mvav 92,132,164
s_add_u32 s72, s67, 1327104
ds_read_b64_tr_b16 a[188:189], v3 offset:8200
Mvav 92,140,172
ds_read_b64_tr_b16 a[190:191], v3 offset:12296
buffer_load_dwordx4 v25, s[28:31], s72 offen lds
Mvav 92,148,180
s_waitcnt lgkmcnt(12)
s_add_u32 m0, s70, 19328
ds_read_b128 v[160:163], v0 offset:53248
Mvav 92,156,188
s_add_u32 s72, s67, 1327104
ds_read_b128 v[168:171], v0 offset:54272
Mvav 64,160,128
ds_read_b128 v[176:179], v0 offset:55296
buffer_load_dwordx4 v25, s[28:31], s72 offen offset:128 lds
Mvav 64,168,136
s_waitcnt lgkmcnt(12)
s_add_u32 m0, s70, 98304
ds_read_b128 v[184:187], v0 offset:56320
Mvav 64,176,144
s_waitcnt lgkmcnt(10)
s_add_u32 s72, s67, 1441792
ds_read_b128 v[132:135], v0 offset:57344
Mvav 64,184,152
ds_read_b128 v[140:143], v0 offset:58368
buffer_load_dwordx4 v25, s[28:31], s72 offen lds
Mvav 68,164,128
s_waitcnt lgkmcnt(10)
ds_read_b128 v[148:151], v0 offset:59392
Mvav 68,172,136
s_waitcnt lgkmcnt(9)
s_add_u32 m0, s70, 99200
ds_read_b128 v[156:159], v0 offset:60416
Mvav 68,180,144
s_waitcnt lgkmcnt(6)
s_add_u32 s72, s67, 1441792
ds_read_b128 v[164:167], v0 offset:61440
Mvav 68,188,152
ds_read_b128 v[172:175], v0 offset:62464
buffer_load_dwordx4 v25, s[28:31], s72 offen offset:128 lds
Mvav 72,160,160
s_add_u32 m0, s70, 100352
ds_read_b128 v[180:183], v0 offset:63488
ds_read_b128 v[188:191], v0 offset:64512
Mvav 72,168,168
s_waitcnt vmcnt(10) lgkmcnt(8)
s_barrier
s_add_u32 s72, s67, 1458176
Mvav 72,176,176
ds_read_b128 v[128:131], v12 offset:16384
buffer_load_dwordx4 v25, s[28:31], s72 offen lds
Mvav 72,184,184
s_add_u32 m0, s70, 101248
ds_read_b128 v[136:139], v12 offset:17408
buffer_store_dwordx2 v[20:21], v22, s[48:51], 0 offen sc1
Mvav 76,164,160
s_add_u32 s72, s67, 1458176
ds_read_b128 v[144:147], v12 offset:18432
Mvav 76,172,168
ds_read_b128 v[152:155], v12 offset:19456
buffer_load_dwordx4 v25, s[28:31], s72 offen offset:128 lds
Mvav 76,180,176
s_add_u32 m0, s70, 32768
ds_read_b64_tr_b16 a[192:193], v3 offset:16384
Mvav 76,188,184
s_waitcnt lgkmcnt(11)
s_add_u32 s72, s87, 184320
ds_read_b64_tr_b16 a[194:195], v3 offset:20480
Mvav 80,160,132
ds_read_b64_tr_b16 a[200:201], v3 offset:24576
buffer_load_dwordx4 v0, s[48:51], s72 offen sc1 lds
Mvav 80,168,140
s_waitcnt lgkmcnt(12)
ds_read_b64_tr_b16 a[202:203], v3 offset:28672
Mvav 80,176,148
s_waitcnt lgkmcnt(12)
ds_read_b64_tr_b16 a[208:209], v1 offset:0
Mvav 80,184,156
ds_read_b64_tr_b16 a[210:211], v1 offset:4096
buffer_load_dwordx4 v0, s[48:51], s72 offen offset:1024 sc1 lds
Mvav 84,164,132
ds_read_b64_tr_b16 a[216:217], v1 offset:8192
Mvav 84,172,140
s_waitcnt lgkmcnt(12)
ds_read_b64_tr_b16 a[218:219], v1 offset:12288
Mvav 84,180,148
ds_read_b64_tr_b16 a[196:197], v3 offset:16392
Mvav 84,188,156
ds_read_b64_tr_b16 a[198:199], v3 offset:20488
buffer_load_dwordx4 v0, s[48:51], s72 offen offset:2048 sc1 lds
Mvav 88,160,164
s_waitcnt lgkmcnt(12)
ds_read_b64_tr_b16 a[204:205], v3 offset:24584
Mvav 88,168,172
ds_read_b64_tr_b16 a[206:207], v3 offset:28680
Mvav 88,176,180
ds_read_b64_tr_b16 a[212:213], v1 offset:8
buffer_load_dwordx4 v0, s[48:51], s72 offen offset:3072 sc1 lds
Mvav 88,184,188
s_waitcnt lgkmcnt(12)
s_add_u32 m0, s70, 114688
ds_read_b64_tr_b16 a[214:215], v1 offset:4104
Mvav 92,164,164
s_add_u32 s72, s67, 1572864
ds_read_b64_tr_b16 a[220:221], v1 offset:8200
Mvav 92,172,172
ds_read_b64_tr_b16 a[222:223], v1 offset:12296
buffer_load_dwordx4 v25, s[28:31], s72 offen lds
Mvav 92,180,180
s_waitcnt lgkmcnt(12)
s_add_u32 m0, s70, 115584
ds_read_b128 v[160:163], v12 offset:20480
Mvav 92,188,188
s_add_u32 s72, s67, 1572864
ds_read_b128 v[168:171], v12 offset:21504
Mvav 64,192,128
ds_read_b128 v[176:179], v12 offset:22528
buffer_load_dwordx4 v25, s[28:31], s72 offen offset:128 lds
Mvav 64,200,136
s_waitcnt lgkmcnt(12)
ds_read_b128 v[184:187], v12 offset:23552
Mvav 64,208,144
s_waitcnt lgkmcnt(12)
s_add_u32 m0, s70, 116736
ds_read_b128 v[132:135], v12 offset:24576
Mvav 64,216,152
s_waitcnt lgkmcnt(9)
s_add_u32 s72, s67, 1589248
ds_read_b128 v[140:143], v12 offset:25600
Mvav 68,196,128
ds_read_b128 v[148:151], v12 offset:26624
buffer_load_dwordx4 v25, s[28:31], s72 offen lds
Mvav 68,204,136
s_waitcnt lgkmcnt(9)
s_add_u32 m0, s70, 117632
ds_read_b128 v[156:159], v12 offset:27648
Mvav 68,212,144
s_waitcnt lgkmcnt(7)
s_add_u32 s72, s67, 1589248
ds_read_b128 v[164:167], v12 offset:28672
Mvav 68,220,152
ds_read_b128 v[172:175], v12 offset:29696
buffer_load_dwordx4 v25, s[28:31], s72 offen offset:128 lds
Mvav 72,192,160
s_waitcnt lgkmcnt(8)
ds_read_b128 v[180:183], v12 offset:30720
ds_read_b128 v[188:191], v12 offset:31744
Mvav 72,200,168
s_waitcnt vmcnt(17) lgkmcnt(9)
s_barrier
s_add_u32 m0, s70, 131072
Mvav 72,208,176
s_waitcnt lgkmcnt(8)
s_add_u32 s72, s67, 1703936
ds_read_b128 v[128:131], v0 offset:0
Mvav 72,216,184
buffer_load_dwordx4 v25, s[28:31], s72 offen lds
Mvav 76,196,160
s_add_u32 m0, s70, 131968
ds_read_b128 v[136:139], v0 offset:1024
Mvav 76,204,168
s_add_u32 s72, s67, 1703936
buffer_load_dwordx4 v25, s[28:31], s72 offen offset:128 lds
Mvav 76,212,176
ds_read_b128 v[144:147], v0 offset:2048
Mvav 76,220,184
s_waitcnt lgkmcnt(10)
s_add_u32 m0, s70, 133120
Mvav 80,192,132
s_waitcnt lgkmcnt(9)
s_add_u32 s72, s67, 1720320
ds_read_b128 v[152:155], v0 offset:3072
Mvav 80,200,140
s_waitcnt lgkmcnt(9)
buffer_load_dwordx4 v25, s[28:31], s72 offen lds
Mvav 80,208,148
s_waitcnt lgkmcnt(8)
ds_read_b128 v[160:163], v0 offset:4096
Mvav 80,216,156
s_add_u32 m0, s70, 134016
Mvav 84,196,132
s_add_u32 s72, s67, 1720320
ds_read_b128 v[168:171], v0 offset:5120
Mvav 84,204,140
buffer_load_dwordx4 v25, s[28:31], s72 offen offset:128 lds
Mvav 84,212,148
s_waitcnt lgkmcnt(9)
s_add_u32 m0, s70, 49152
ds_read_b128 v[176:179], v0 offset:6144
Mvav 84,220,156
s_add_u32 s72, s87, 221184
buffer_load_dwordx4 v0, s[48:51], s72 offen sc1 lds
Mvav 88,192,164
s_waitcnt lgkmcnt(9)
ds_read_b128 v[184:187], v0 offset:7168
Mvav 88,200,172
s_waitcnt lgkmcnt(9)
Mvav 88,208,180
s_waitcnt lgkmcnt(8)
ds_read_b128 v[132:135], v0 offset:8192
Mvav 88,216,188
buffer_load_dwordx4 v0, s[48:51], s72 offen offset:1024 sc1 lds
Mvav 92,196,164
ds_read_b128 v[140:143], v0 offset:9216
Mvav 92,204,172
buffer_load_dwordx4 v0, s[48:51], s72 offen offset:2048 sc1 lds
Mvav 92,212,180
ds_read_b128 v[148:151], v0 offset:10240
Mvav 92,220,188
s_waitcnt lgkmcnt(10)
Zvav 96,64,128
s_waitcnt lgkmcnt(9)
ds_read_b128 v[156:159], v0 offset:11264
Mvav 96,72,136
s_waitcnt lgkmcnt(9)
buffer_load_dwordx4 v0, s[48:51], s72 offen offset:3072 sc1 lds
Mvav 96,80,144
s_waitcnt lgkmcnt(8)
ds_read_b128 v[164:167], v0 offset:12288
Mvav 96,88,152
s_add_u32 m0, s70, 65536
Zvav 100,68,128
s_add_u32 s72, s13, 4718592
ds_read_b128 v[172:175], v0 offset:13312
Mvav 100,76,136
buffer_load_dwordx4 v0, s[48:51], s72 offen sc1 lds
Mvav 100,84,144
ds_read_b128 v[180:183], v0 offset:14336
Mvav 100,92,152
s_waitcnt lgkmcnt(10)
Mvav 96,96,160
s_waitcnt lgkmcnt(9)
ds_read_b128 v[188:191], v0 offset:15360
Mvav 96,104,168
s_waitcnt vmcnt(13) lgkmcnt(8)
s_barrier
Mvav 96,112,176
ds_read_b128 v[128:131], v0 offset:32768
buffer_load_dwordx4 v0, s[48:51], s72 offen offset:1024 sc1 lds
Mvav 96,120,184
ds_read_b128 v[136:139], v0 offset:33792
Mvav 100,100,160
ds_read_b128 v[144:147], v0 offset:34816
Mvav 100,108,168
ds_read_b128 v[152:155], v0 offset:35840
buffer_load_dwordx4 v0, s[48:51], s72 offen offset:2048 sc1 lds
Mvav 100,116,176
ds_read_b64_tr_b16 a[224:225], v10 offset:16384
Mvav 100,124,184
s_waitcnt lgkmcnt(12)
ds_read_b64_tr_b16 a[226:227], v10 offset:20480
Mvav 96,128,132
s_waitcnt lgkmcnt(11)
ds_read_b64_tr_b16 a[232:233], v10 offset:24576
Mvav 96,136,140
ds_read_b64_tr_b16 a[234:235], v10 offset:28672
buffer_load_dwordx4 v0, s[48:51], s72 offen offset:3072 sc1 lds
Mvav 96,144,148
s_waitcnt lgkmcnt(12)
ds_read_b64_tr_b16 a[240:241], v1 offset:32768
Mvav 96,152,156
s_add_u32 m0, s70, 147456
ds_read_b64_tr_b16 a[242:243], v1 offset:36864
Mvav 100,132,132
s_waitcnt lgkmcnt(11)
s_add_u32 s72, s67, 1835008
ds_read_b64_tr_b16 a[248:249], v1 offset:40960
Mvav 100,140,140
ds_read_b64_tr_b16 a[250:251], v1 offset:45056
buffer_load_dwordx4 v25, s[28:31], s72 offen lds
Mvav 100,148,148
ds_read_b64_tr_b16 a[228:229], v10 offset:16392
Mvav 100,156,156
s_add_u32 m0, s70, 148352
ds_read_b64_tr_b16 a[230:231], v10 offset:20488
Mvav 96,160,164
s_waitcnt lgkmcnt(12)
s_add_u32 s72, s67, 1835008
ds_read_b64_tr_b16 a[236:237], v10 offset:24584
Mvav 96,168,172
ds_read_b64_tr_b16 a[238:239], v10 offset:28680
buffer_load_dwordx4 v25, s[28:31], s72 offen offset:128 lds
Mvav 96,176,180
ds_read_b64_tr_b16 a[244:245], v1 offset:32776
Mvav 96,184,188
s_waitcnt lgkmcnt(12)
s_add_u32 m0, s70, 149504
ds_read_b64_tr_b16 a[246:247], v1 offset:36872
Mvav 100,164,164
s_add_u32 s72, s67, 1851392
ds_read_b64_tr_b16 a[252:253], v1 offset:40968
)"
R"(
Mvav 100,172,172
ds_read_b64_tr_b16 a[254:255], v1 offset:45064
buffer_load_dwordx4 v25, s[28:31], s72 offen lds
Mvav 100,180,180
s_waitcnt lgkmcnt(12)
ds_read_b128 v[160:163], v0 offset:36864
Mvav 100,188,188
s_add_u32 m0, s70, 150400
ds_read_b128 v[168:171], v0 offset:37888
Mvav 64,224,128
s_waitcnt lgkmcnt(11)
s_add_u32 s72, s67, 1851392
ds_read_b128 v[176:179], v0 offset:38912
Mvav 64,232,136
ds_read_b128 v[184:187], v0 offset:39936
buffer_load_dwordx4 v25, s[28:31], s72 offen offset:128 lds
Mvav 64,240,144
s_waitcnt lgkmcnt(12)
ds_read_b128 v[132:135], v0 offset:40960
Mvav 64,248,152
s_waitcnt lgkmcnt(11)
s_add_u32 m0, s70, 81920
ds_read_b128 v[140:143], v0 offset:41984
Mvav 68,228,128
s_waitcnt lgkmcnt(8)
s_add_u32 s72, s67, 1966080
ds_read_b128 v[148:151], v0 offset:43008
Mvav 68,236,136
ds_read_b128 v[156:159], v0 offset:44032
buffer_load_dwordx4 v25, s[28:31], s72 offen lds
Mvav 68,244,144
s_waitcnt lgkmcnt(8)
ds_read_b128 v[164:167], v0 offset:45056
Mvav 68,252,152
s_waitcnt lgkmcnt(7)
ds_read_b128 v[172:175], v0 offset:46080
Mvav 72,224,160
s_add_u32 m0, s70, 82816
ds_read_b128 v[180:183], v0 offset:47104
ds_read_b128 v[188:191], v0 offset:48128
Mvav 72,232,168
s_waitcnt vmcnt(9) lgkmcnt(8)
s_barrier
s_add_u32 s72, s67, 1966080
Mvav 72,240,176
ds_read_b128 v[128:131], v0 offset:49152
buffer_load_dwordx4 v25, s[28:31], s72 offen offset:128 lds
Mvav 72,248,184
s_add_u32 m0, s70, 83968
ds_read_b128 v[136:139], v0 offset:50176
Mvav 76,228,160
s_add_u32 s72, s67, 1982464
ds_read_b128 v[144:147], v0 offset:51200
Mvav 76,236,168
ds_read_b128 v[152:155], v0 offset:52224
buffer_load_dwordx4 v25, s[28:31], s72 offen lds
Mvav 76,244,176
ds_read_b64_tr_b16 a[0:1], v1 offset:49152
Mvav 76,252,184
s_waitcnt lgkmcnt(12)
s_add_u32 m0, s70, 84864
ds_read_b64_tr_b16 a[2:3], v1 offset:53248
Mvav 80,224,132
s_waitcnt lgkmcnt(11)
s_add_u32 s72, s67, 1982464
ds_read_b64_tr_b16 a[8:9], v1 offset:57344
Mvav 80,232,140
ds_read_b64_tr_b16 a[10:11], v1 offset:61440
buffer_load_dwordx4 v25, s[28:31], s72 offen offset:128 lds
Mvav 80,240,148
s_waitcnt lgkmcnt(12)
ds_read_b64_tr_b16 a[16:17], v3 offset:0
Mvav 80,248,156
s_add_u32 m0, s70, 0
ds_read_b64_tr_b16 a[18:19], v3 offset:4096
Mvav 84,228,132
s_waitcnt lgkmcnt(11)
s_add_u32 s72, s87, 258048
ds_read_b64_tr_b16 a[24:25], v3 offset:8192
Mvav 84,236,140
ds_read_b64_tr_b16 a[26:27], v3 offset:12288
buffer_load_dwordx4 v0, s[48:51], s72 offen sc1 lds
Mvav 84,244,148
ds_read_b64_tr_b16 a[4:5], v1 offset:49160
Mvav 84,252,156
ds_read_b64_tr_b16 a[6:7], v1 offset:53256
Mvav 88,224,164
s_waitcnt lgkmcnt(12)
ds_read_b64_tr_b16 a[12:13], v1 offset:57352
Mvav 88,232,172
ds_read_b64_tr_b16 a[14:15], v1 offset:61448
buffer_load_dwordx4 v0, s[48:51], s72 offen offset:1024 sc1 lds
Mvav 88,240,180
ds_read_b64_tr_b16 a[20:21], v3 offset:8
Mvav 88,248,188
s_waitcnt lgkmcnt(12)
ds_read_b64_tr_b16 a[22:23], v3 offset:4104
Mvav 92,228,164
ds_read_b64_tr_b16 a[28:29], v3 offset:8200
Mvav 92,236,172
ds_read_b64_tr_b16 a[30:31], v3 offset:12296
buffer_load_dwordx4 v0, s[48:51], s72 offen offset:2048 sc1 lds
Mvav 92,244,180
s_waitcnt lgkmcnt(12)
ds_read_b128 v[160:163], v0 offset:53248
Mvav 92,252,188
ds_read_b128 v[168:171], v0 offset:54272
Mvav 64,0,128
s_waitcnt lgkmcnt(11)
ds_read_b128 v[176:179], v0 offset:55296
Mvav 64,8,136
ds_read_b128 v[184:187], v0 offset:56320
buffer_load_dwordx4 v0, s[48:51], s72 offen offset:3072 sc1 lds
Mvav 64,16,144
s_waitcnt lgkmcnt(12)
ds_read_b128 v[132:135], v0 offset:57344
Mvav 64,24,152
s_waitcnt lgkmcnt(11)
s_add_u32 m0, s70, 16384
ds_read_b128 v[140:143], v0 offset:58368
Mvav 68,4,128
s_waitcnt lgkmcnt(8)
s_add_u32 s72, s12, 4866048
ds_read_b128 v[148:151], v0 offset:59392
Mvav 68,12,136
ds_read_b128 v[156:159], v0 offset:60416
buffer_load_dwordx4 v0, s[48:51], s72 offen sc1 lds
Mvav 68,20,144
s_waitcnt lgkmcnt(8)
ds_read_b128 v[164:167], v0 offset:61440
Mvav 68,28,152
s_waitcnt lgkmcnt(8)
ds_read_b128 v[172:175], v0 offset:62464
Mvav 72,0,160
s_waitcnt lgkmcnt(8)
ds_read_b128 v[180:183], v0 offset:63488
ds_read_b128 v[188:191], v0 offset:64512
Mvav 72,8,168
s_waitcnt vmcnt(13) lgkmcnt(8)
s_barrier
Mvav 72,16,176
ds_read_b128 v[128:131], v12 offset:0
buffer_load_dwordx4 v0, s[48:51], s72 offen offset:1024 sc1 lds
Mvav 72,24,184
Mvav 76,4,160
ds_read_b128 v[136:139], v12 offset:1024
Mvav 76,12,168
buffer_load_dwordx4 v0, s[48:51], s72 offen offset:2048 sc1 lds
Mvav 76,20,176
ds_read_b128 v[144:147], v12 offset:2048
Mvav 76,28,184
s_waitcnt lgkmcnt(10)
Mvav 80,0,132
s_waitcnt lgkmcnt(9)
ds_read_b128 v[152:155], v12 offset:3072
Mvav 80,8,140
s_waitcnt lgkmcnt(8)
buffer_load_dwordx4 v0, s[48:51], s72 offen offset:3072 sc1 lds
Mvav 80,16,148
ds_read_b128 v[160:163], v12 offset:4096
buffer_load_dwordx4 v[16:19], v0, s[48:51], s83 offen sc1
Mvav 80,24,156
s_add_u32 m0, s70, 98304
Mvav 84,4,132
s_add_u32 s72, s13, 4866048
ds_read_b128 v[168:171], v12 offset:5120
Mvav 84,12,140
buffer_load_dwordx4 v0, s[48:51], s72 offen sc1 lds
Mvav 84,20,148
ds_read_b128 v[176:179], v12 offset:6144
Mvav 84,28,156
s_waitcnt lgkmcnt(10)
Mvav 88,0,164
s_waitcnt lgkmcnt(9)
ds_read_b128 v[184:187], v12 offset:7168
Mvav 88,8,172
s_waitcnt lgkmcnt(9)
buffer_load_dwordx4 v0, s[48:51], s72 offen offset:1024 sc1 lds
Mvav 88,16,180
s_waitcnt lgkmcnt(8)
ds_read_b128 v[132:135], v12 offset:8192
Mvav 88,24,188
Mvav 92,4,164
ds_read_b128 v[140:143], v12 offset:9216
Mvav 92,12,172
buffer_load_dwordx4 v0, s[48:51], s72 offen offset:2048 sc1 lds
Mvav 92,20,180
ds_read_b128 v[148:151], v12 offset:10240
Mvav 92,28,188
s_waitcnt lgkmcnt(10)
Zvav 104,64,128
s_waitcnt lgkmcnt(9)
ds_read_b128 v[156:159], v12 offset:11264
Mvav 104,72,136
s_waitcnt lgkmcnt(9)
buffer_load_dwordx4 v0, s[48:51], s72 offen offset:3072 sc1 lds
Mvav 104,80,144
s_waitcnt lgkmcnt(8)
ds_read_b128 v[164:167], v12 offset:12288
Mvav 104,88,152
s_add_u32 m0, s70, 32768
buffer_load_dwordx4 v[40:43], v11, s[48:51], s21 offen sc1
Zvav 108,68,128
s_add_u32 s72, s20, 0
ds_read_b128 v[172:175], v12 offset:13312
Mvav 108,76,136
buffer_load_dwordx4 v25, s[44:47], s72 offen lds
Mvav 108,84,144
ds_read_b128 v[180:183], v12 offset:14336
Mvav 108,92,152
s_waitcnt lgkmcnt(10)
Mvav 104,96,160
s_waitcnt lgkmcnt(9)
s_add_u32 m0, s70, 33792
ds_read_b128 v[188:191], v12 offset:15360
Mvav 104,104,168
s_waitcnt vmcnt(11) lgkmcnt(8)
s_barrier
s_add_u32 s72, s20, 16384
Mvav 104,112,176
ds_read_b128 v[128:131], v0 offset:0
buffer_load_dwordx4 v25, s[44:47], s72 offen lds
Mvav 104,120,184
s_add_u32 m0, s70, 34816
ds_read_b128 v[136:139], v0 offset:1024
Mvav 108,100,160
s_add_u32 s72, s20, 32768
ds_read_b128 v[144:147], v0 offset:2048
Mvav 108,108,168
ds_read_b128 v[152:155], v0 offset:3072
buffer_load_dwordx4 v25, s[44:47], s72 offen lds
Mvav 108,116,176
ds_read_b64_tr_b16 a[32:33], v3 offset:16384
Mvav 108,124,184
s_waitcnt lgkmcnt(12)
s_add_u32 m0, s70, 35840
ds_read_b64_tr_b16 a[34:35], v3 offset:20480
Mvav 104,128,132
s_waitcnt lgkmcnt(11)
s_add_u32 s72, s20, 49152
ds_read_b64_tr_b16 a[40:41], v3 offset:24576
Mvav 104,136,140
ds_read_b64_tr_b16 a[42:43], v3 offset:28672
buffer_load_dwordx4 v25, s[44:47], s72 offen lds
Mvav 104,144,148
s_waitcnt vmcnt(9) lgkmcnt(9)
ds_read_b64_tr_b16 a[48:49], v1 offset:16384
v_xor_b32 v16, s74, v16
Mvav 104,152,156
s_add_u32 m0, s70, 114688
ds_read_b64_tr_b16 a[50:51], v1 offset:20480
v_bitop3_b32 v16, v17, s75, v16 bitop3:0xbe
Mvav 108,132,132
s_add_u32 s72, s20, 65536
ds_read_b64_tr_b16 a[56:57], v1 offset:24576
v_bitop3_b32 v16, v18, s74, v16 bitop3:0xbe
Mvav 108,140,140
ds_read_b64_tr_b16 a[58:59], v1 offset:28672
buffer_load_dwordx4 v25, s[44:47], s72 offen lds
Mvav 108,148,148
s_waitcnt lgkmcnt(10)
ds_read_b64_tr_b16 a[36:37], v3 offset:16392
v_bitop3_b32 v16, v19, s75, v16 bitop3:0xbe
Mvav 108,156,156
s_add_u32 m0, s70, 115712
ds_read_b64_tr_b16 a[38:39], v3 offset:20488
v_cmp_ne_u32 vcc, 0, v16
Mvav 104,160,164
s_add_u32 s72, s20, 81920
s_cbranch_vccnz .Lcf3
.Lcf3_r:
ds_read_b64_tr_b16 a[44:45], v3 offset:24584
Mvav 104,168,172
ds_read_b64_tr_b16 a[46:47], v3 offset:28680
buffer_load_dwordx4 v25, s[44:47], s72 offen lds
Mvav 104,176,180
ds_read_b64_tr_b16 a[52:53], v1 offset:16392
Mvav 104,184,188
s_waitcnt lgkmcnt(12)
s_add_u32 m0, s70, 116736
ds_read_b64_tr_b16 a[54:55], v1 offset:20488
Mvav 108,164,164
s_add_u32 s72, s20, 98304
ds_read_b64_tr_b16 a[60:61], v1 offset:24584
Mvav 108,172,172
ds_read_b64_tr_b16 a[62:63], v1 offset:28680
buffer_load_dwordx4 v25, s[44:47], s72 offen lds
Mvav 108,180,180
s_waitcnt lgkmcnt(12)
ds_read_b128 v[160:163], v0 offset:4096
Mvav 108,188,188
s_add_u32 m0, s70, 117760
ds_read_b128 v[168:171], v0 offset:5120
Mvav 64,32,128
s_waitcnt lgkmcnt(11)
s_add_u32 s72, s20, 114688
ds_read_b128 v[176:179], v0 offset:6144
Mvav 64,40,136
ds_read_b128 v[184:187], v0 offset:7168
buffer_load_dwordx4 v25, s[44:47], s72 offen lds
Mvav 64,48,144
s_waitcnt lgkmcnt(6)
ds_read_b128 v[132:135], v0 offset:8192
v_lshlrev_b32 v16, 16, v216
Mvav 64,56,152
s_add_u32 m0, s70, 131072
ds_read_b128 v[140:143], v0 offset:9216
v_and_b32 v17, 0xffff0000, v216
Mvav 68,36,128
s_add_u32 s72, s14, 4718592
ds_read_b128 v[148:151], v0 offset:10240
v_lshlrev_b32 v18, 16, v217
Mvav 68,44,136
ds_read_b128 v[156:159], v0 offset:11264
buffer_load_dwordx4 v0, s[48:51], s72 offen sc1 lds
Mvav 68,52,144
s_waitcnt lgkmcnt(8)
ds_read_b128 v[164:167], v0 offset:12288
v_and_b32 v19, 0xffff0000, v217
Mvav 68,60,152
s_waitcnt lgkmcnt(8)
ds_read_b128 v[172:175], v0 offset:13312
v_lshlrev_b32 v20, 16, v218
Mvav 72,32,160
s_waitcnt lgkmcnt(6)
ds_read_b128 v[180:183], v0 offset:14336
ds_read_b128 v[188:191], v0 offset:15360
Mvav 72,40,168
s_barrier
Mvav 72,48,176
ds_read_b128 v[128:131], v0 offset:16384
buffer_load_dwordx4 v0, s[48:51], s72 offen offset:1024 sc1 lds
Mvav 72,56,184
v_and_b32 v21, 0xffff0000, v218
v_lshlrev_b32 v22, 16, v219
Mvav 76,36,160
ds_read_b128 v[136:139], v0 offset:17408
v_and_b32 v23, 0xffff0000, v219
Mvav 76,44,168
buffer_load_dwordx4 v0, s[48:51], s72 offen offset:2048 sc1 lds
Mvav 76,52,176
ds_read_b128 v[144:147], v0 offset:18432
Mvav 76,60,184
s_waitcnt lgkmcnt(10)
buffer_load_dwordx4 v[44:47], v11, s[48:51], s21 offen offset:1024 sc1
Mvav 80,32,132
s_waitcnt lgkmcnt(9)
ds_read_b128 v[152:155], v0 offset:19456
Mvav 80,40,140
s_waitcnt lgkmcnt(9)
buffer_load_dwordx4 v0, s[48:51], s72 offen offset:3072 sc1 lds
Mvav 80,48,148
s_waitcnt lgkmcnt(8)
ds_read_b128 v[160:163], v0 offset:20480
Mvav 80,56,156
s_add_u32 m0, s70, 49152
Mvav 84,36,132
s_add_u32 s72, s14, 4866048
ds_read_b128 v[168:171], v0 offset:21504
Mvav 84,44,140
buffer_load_dwordx4 v0, s[48:51], s72 offen sc1 lds
Mvav 84,52,148
ds_read_b128 v[176:179], v0 offset:22528
Mvav 84,60,156
s_waitcnt lgkmcnt(10)
Mvav 88,32,164
s_waitcnt lgkmcnt(9)
ds_read_b128 v[184:187], v0 offset:23552
Mvav 88,40,172
s_waitcnt lgkmcnt(9)
buffer_load_dwordx4 v0, s[48:51], s72 offen offset:1024 sc1 lds
Mvav 88,48,180
s_waitcnt lgkmcnt(8)
ds_read_b128 v[132:135], v0 offset:24576
Mvav 88,56,188
buffer_load_dwordx4 v[48:51], v11, s[48:51], s21 offen offset:2048 sc1
Mvav 92,36,164
ds_read_b128 v[140:143], v0 offset:25600
Mvav 92,44,172
buffer_load_dwordx4 v0, s[48:51], s72 offen offset:2048 sc1 lds
Mvav 92,52,180
ds_read_b128 v[148:151], v0 offset:26624
Mvav 92,60,188
s_waitcnt lgkmcnt(10)
Mvav 96,192,128
s_waitcnt lgkmcnt(7)
ds_read_b128 v[156:159], v0 offset:27648
v_mov_b32 v62, s74
Mvav 96,200,136
s_lshl_b32 s58, s76, 17
buffer_load_dwordx4 v0, s[48:51], s72 offen offset:3072 sc1 lds
Mvav 96,208,144
s_add_u32 s58, s58, s22
ds_read_b128 v[164:167], v0 offset:28672
v_mov_b32 v2, 0xffffff00
Mvav 96,216,152
s_add_u32 s58, s58, 0
v_cvt_pk_bf16_f32 v64, v64, v65
v_cvt_pk_bf16_f32 v65, v66, v67
Mvav 100,196,128
s_add_u32 s58, s58, s32
ds_read_b128 v[172:175], v0 offset:29696
v_cvt_pk_bf16_f32 v66, v68, v69
Mvav 100,204,136
s_add_u32 m0, s70, 65536
buffer_load_dwordx4 v[52:55], v11, s[48:51], s21 offen offset:3072 sc1
Mvav 100,212,144
s_add_u32 s72, s12, 9437184
ds_read_b128 v[180:183], v0 offset:30720
v_cvt_pk_bf16_f32 v67, v70, v71
Mvav 100,220,152
)"
R"(
s_waitcnt lgkmcnt(10)
buffer_load_dwordx4 v0, s[48:51], s72 offen sc1 lds
Mvav 96,224,160
s_waitcnt lgkmcnt(9)
ds_read_b128 v[188:191], v0 offset:31744
v_cvt_pk_bf16_f32 v72, v72, v73
Mvav 96,232,168
s_waitcnt vmcnt(21) lgkmcnt(9)
s_barrier
Mvav 96,240,176
s_waitcnt lgkmcnt(8)
ds_read_b128 v[128:131], v12 offset:32768
v_cvt_pk_bf16_f32 v73, v74, v75
Mvav 96,248,184
buffer_load_dwordx4 v0, s[48:51], s72 offen offset:1024 sc1 lds
Mvav 100,228,160
ds_read_b128 v[136:139], v12 offset:33792
v_cvt_pk_bf16_f32 v74, v76, v77
Mvav 100,236,168
v_cvt_pk_bf16_f32 v75, v78, v79
v_cvt_pk_bf16_f32 v80, v80, v81
Mvav 100,244,176
ds_read_b128 v[144:147], v12 offset:34816
v_cvt_pk_bf16_f32 v81, v82, v83
Mvav 100,252,184
s_waitcnt lgkmcnt(10)
buffer_store_dwordx4 v[64:67], v0, s[48:51], s58 offen sc1
Mvav 96,0,132
s_waitcnt lgkmcnt(7)
ds_read_b128 v[152:155], v12 offset:35840
v_cvt_pk_bf16_f32 v82, v84, v85
Mvav 96,8,140
s_lshl_b32 s58, s77, 17
buffer_load_dwordx4 v0, s[48:51], s72 offen offset:2048 sc1 lds
Mvav 96,16,148
s_add_u32 s58, s58, s22
ds_read_b128 v[160:163], v12 offset:36864
v_cvt_pk_bf16_f32 v83, v86, v87
Mvav 96,24,156
s_waitcnt vmcnt(23)
s_add_u32 s58, s58, 0
v_mov_b32 v63, s75
v_xor_b32 v40, s24, v40
Mvav 100,4,132
s_add_u32 s58, s58, s32
ds_read_b128 v[168:171], v12 offset:37888
v_xor_b32 v41, s25, v41
Mvav 100,12,140
buffer_load_dwordx4 v0, s[48:51], s72 offen offset:3072 sc1 lds
Mvav 100,20,148
ds_read_b128 v[176:179], v12 offset:38912
v_xor_b32 v42, s26, v42
Mvav 100,28,156
s_waitcnt lgkmcnt(10)
v_xor_b32 v43, s27, v43
v_or3_b32 v56, v40, v41, v42
Mvav 96,32,164
s_waitcnt lgkmcnt(7)
ds_read_b128 v[184:187], v12 offset:39936
v_lshlrev_b32 v64, 16, v192
Mvav 96,40,172
s_add_u32 m0, s70, 81920
buffer_store_dwordx4 v[72:75], v0, s[48:51], s58 offen sc1
Mvav 96,48,180
s_add_u32 s72, s12, 9584640
ds_read_b128 v[132:135], v12 offset:40960
v_mul_f32 v24, 0xbfb8aa3b, v64
Mvav 96,56,188
s_lshl_b32 s58, s78, 17
buffer_load_dwordx4 v0, s[48:51], s72 offen sc1 lds
Mvav 100,36,164
s_add_u32 s58, s58, s22
ds_read_b128 v[140:143], v12 offset:41984
v_and_b32 v65, 0xffff0000, v192
Mvav 100,44,172
s_add_u32 s58, s58, 0
v_exp_f32 v24, v24
Mvav 100,52,180
s_add_u32 s58, s58, s32
ds_read_b128 v[148:151], v12 offset:43008
v_add_f32 v24, 1.0, v24
Mvav 100,60,188
s_waitcnt lgkmcnt(10)
buffer_load_dwordx4 v0, s[48:51], s72 offen offset:1024 sc1 lds
Mvav 104,192,128
s_waitcnt lgkmcnt(9)
ds_read_b128 v[156:159], v12 offset:44032
v_mul_f32 v25, 0xbfb8aa3b, v65
Mvav 104,200,136
s_waitcnt lgkmcnt(9)
v_rcp_f32 v24, v24
Mvav 104,208,144
s_waitcnt lgkmcnt(8)
ds_read_b128 v[164:167], v12 offset:45056
v_mul_f32 v64, v64, v24
Mvav 104,216,152
buffer_store_dwordx4 v[80:83], v0, s[48:51], s58 offen sc1
Mvav 108,196,128
ds_read_b128 v[172:175], v12 offset:46080
v_lshlrev_b32 v66, 16, v193
Mvav 108,204,136
s_waitcnt vmcnt(20)
buffer_load_dwordx4 v0, s[48:51], s72 offen offset:2048 sc1 lds
Mvav 108,212,144
ds_read_b128 v[180:183], v12 offset:47104
v_mul_f32 v26, 0xbfb8aa3b, v66
Mvav 108,220,152
s_waitcnt lgkmcnt(9)
ds_read_b64_tr_b16 v[196:197], v60 offset:0
v_and_b32 v67, 0xffff0000, v193
Mvav 104,224,160
s_lshl_b32 s58, s76, 17
ds_read_b128 v[188:191], v12 offset:48128
v_cvt_pk_bf16_f32 v96, v96, v97
Mvav 104,232,168
s_waitcnt vmcnt(11) lgkmcnt(8)
s_barrier
Mvav 104,240,176
ds_read_b128 v[128:131], v14 offset:0
buffer_load_dwordx4 v0, s[48:51], s72 offen offset:3072 sc1 lds
Mvav 104,248,184
s_add_u32 s58, s58, s22
ds_read_b64_tr_b16 v[198:199], v60 offset:256
v_cvt_pk_bf16_f32 v97, v98, v99
Mvav 108,228,160
s_add_u32 s58, s58, 32768
ds_read_b128 v[136:139], v14 offset:1024
v_cvt_pk_bf16_f32 v98, v100, v101
Mvav 108,236,168
s_add_u32 m0, s70, 147456
ds_read_b64_tr_b16 v[204:205], v61 offset:0
v_cvt_pk_bf16_f32 v99, v102, v103
Mvav 108,244,176
s_add_u32 s72, s13, 9437184
ds_read_b128 v[144:147], v14 offset:2048
v_mul_f32 v27, 0xbfb8aa3b, v67
Mvav 108,252,184
s_add_u32 s58, s58, s32
buffer_load_dwordx4 v0, s[48:51], s72 offen sc1 lds
Mvav 104,0,132
s_waitcnt lgkmcnt(12)
ds_read_b128 v[152:155], v14 offset:3072
v_lshlrev_b32 v68, 16, v194
Mvav 104,8,140
s_waitcnt lgkmcnt(12)
buffer_store_dwordx4 v[96:99], v0, s[48:51], s58 offen sc1
Mvav 104,16,148
s_waitcnt lgkmcnt(11)
ds_read_b128 v[160:163], v14 offset:4096
v_mul_f32 v28, 0xbfb8aa3b, v68
Mvav 104,24,156
buffer_load_dwordx4 v0, s[48:51], s72 offen offset:1024 sc1 lds
Mvav 108,4,132
ds_read_b128 v[168:171], v14 offset:5120
v_and_b32 v69, 0xffff0000, v194
Mvav 108,12,140
ds_read_b64_tr_b16 v[206:207], v61 offset:256
v_xor_b32 v44, s24, v44
Mvav 108,20,148
ds_read_b128 v[176:179], v14 offset:6144
v_xor_b32 v45, s25, v45
Mvav 108,28,156
s_waitcnt lgkmcnt(12)
ds_read_b64_tr_b16 v[212:213], v60 offset:32
v_xor_b32 v46, s26, v46
Mvav 104,32,164
ds_read_b128 v[184:187], v14 offset:7168
v_xor_b32 v47, s27, v47
Mvav 104,40,172
buffer_load_dwordx4 v0, s[48:51], s72 offen offset:2048 sc1 lds
Mvav 104,48,180
s_waitcnt lgkmcnt(12)
ds_read_b128 v[132:135], v14 offset:8192
v_or3_b32 v56, v56, v43, v44
Mvav 104,56,188
ds_read_b64_tr_b16 v[214:215], v60 offset:288
v_or3_b32 v56, v56, v45, v46
Mvav 108,36,164
ds_read_b128 v[140:143], v14 offset:9216
v_mul_f32 v29, 0xbfb8aa3b, v69
Mvav 108,44,172
buffer_load_dwordx4 v0, s[48:51], s72 offen offset:3072 sc1 lds
Mvav 108,52,180
s_waitcnt lgkmcnt(12)
ds_read_b128 v[148:151], v14 offset:10240
v_lshlrev_b32 v70, 16, v195
Mvav 108,60,188
ds_read_b64_tr_b16 v[220:221], v61 offset:32
v_mul_f32 v30, 0xbfb8aa3b, v70
Zvav 96,64,128
s_waitcnt lgkmcnt(11)
ds_read_b128 v[156:159], v14 offset:11264
v_and_b32 v71, 0xffff0000, v195
Mvav 96,72,136
s_add_u32 m0, s70, 0
ds_read_b64_tr_b16 v[222:223], v61 offset:288
v_xor_b32 v48, s24, v48
Mvav 96,80,144
s_add_u32 s72, s13, 9584640
ds_read_b128 v[164:167], v14 offset:12288
v_xor_b32 v49, s25, v49
Mvav 96,88,152
buffer_load_dwordx4 v0, s[48:51], s72 offen sc1 lds
Zvav 100,68,128
ds_read_b128 v[172:175], v14 offset:13312
v_xor_b32 v50, s26, v50
Mvav 100,76,136
s_waitcnt lgkmcnt(12)
ds_read_b64_tr_b16 v[228:229], v60 offset:512
v_xor_b32 v51, s27, v51
Mvav 100,84,144
ds_read_b128 v[180:183], v14 offset:14336
v_or3_b32 v56, v56, v47, v48
Mvav 100,92,152
s_waitcnt lgkmcnt(11)
buffer_load_dwordx4 v0, s[48:51], s72 offen offset:1024 sc1 lds
Mvav 96,96,160
s_lshl_b32 s58, s77, 17
ds_read_b128 v[188:191], v14 offset:15360
v_cvt_pk_bf16_f32 v104, v104, v105
Mvav 96,104,168
s_add_u32 s58, s58, s22
ds_read_b64_tr_b16 v[230:231], v60 offset:768
v_cvt_pk_bf16_f32 v105, v106, v107
Mvav 96,112,176
s_add_u32 s58, s58, 32768
ds_read_b128 v[128:131], v0 offset:49152
v_cvt_pk_bf16_f32 v106, v108, v109
Mvav 96,120,184
s_add_u32 s58, s58, s32
ds_read_b128 v[136:139], v0 offset:50176
v_cvt_pk_bf16_f32 v107, v110, v111
Mvav 100,100,160
buffer_load_dwordx4 v0, s[48:51], s72 offen offset:2048 sc1 lds
Mvav 100,108,168
s_waitcnt vmcnt(19) lgkmcnt(12)
ds_read_b128 v[144:147], v0 offset:51200
v_xor_b32 v52, s24, v52
Mvav 100,116,176
ds_read_b128 v[152:155], v0 offset:52224
v_xor_b32 v53, s25, v53
Mvav 100,124,184
buffer_store_dwordx4 v[104:107], v0, s[48:51], s58 offen sc1
Mvav 96,128,132
ds_read_b128 v[160:163], v0 offset:53248
v_xor_b32 v54, s26, v54
Mvav 96,136,140
s_waitcnt lgkmcnt(12)
ds_read_b128 v[168:171], v0 offset:54272
v_xor_b32 v55, s27, v55
Mvav 96,144,148
buffer_load_dwordx4 v0, s[48:51], s72 offen offset:3072 sc1 lds
Mvav 96,152,156
s_waitcnt lgkmcnt(10)
ds_read_b128 v[176:179], v0 offset:55296
v_or3_b32 v56, v56, v49, v50
Mvav 100,132,132
s_add_u32 m0, s70, 16384
ds_read_b128 v[184:187], v0 offset:56320
v_or3_b32 v56, v56, v51, v52
Mvav 100,140,140
s_add_u32 s72, s14, 9437184
ds_read_b64_tr_b16 v[236:237], v61 offset:512
v_or3_b32 v56, v56, v53, v54
Mvav 100,148,148
ds_read_b128 v[132:135], v0 offset:57344
buffer_load_dwordx4 v0, s[48:51], s72 offen sc1 lds
Mvav 100,156,156
ds_read_b128 v[140:143], v0 offset:58368
v_or_b32 v56, v56, v55
Mvav 96,160,164
s_waitcnt lgkmcnt(12)
ds_read_b64_tr_b16 v[238:239], v61 offset:768
v_and_b32 v56, 0xff, v56
Mvav 96,168,172
ds_read_b128 v[148:151], v0 offset:59392
v_cmp_ne_u32 vcc, 0, v56
Mvav 96,176,180
s_and_b64 vcc, vcc, s[36:37]
ds_read_b128 v[156:159], v0 offset:60416
v_mul_f32 v31, 0xbfb8aa3b, v71
Mvav 96,184,188
buffer_load_dwordx4 v0, s[48:51], s72 offen offset:1024 sc1 lds
Mvav 100,164,164
s_waitcnt lgkmcnt(12)
ds_read_b128 v[164:167], v0 offset:61440
v_lshlrev_b32 v72, 16, v200
Mvav 100,172,172
ds_read_b128 v[172:175], v0 offset:62464
v_mul_f32 v24, 0xbfb8aa3b, v72
Mvav 100,180,180
s_cbranch_vccnz .Lgu
.Lgu_r:
s_lshl_b32 s59, s57, 5
Mvav 100,188,188
s_add_u32 s59, s59, 32768
ds_read_b128 v[180:183], v0 offset:63488
v_add_f32 v40, v40, v44
Mvav 96,192,128
s_waitcnt lgkmcnt(12)
ds_read_b128 v[188:191], v0 offset:64512
v_add_f32 v41, v41, v45
Mvav 96,200,136
buffer_load_dwordx4 v0, s[48:51], s72 offen offset:2048 sc1 lds
Mvav 96,208,144
ds_read_b64_tr_b16 v[244:245], v60 offset:544
v_add_f32 v42, v42, v46
Mvav 96,216,152
ds_read_b64_tr_b16 v[246:247], v60 offset:800
v_add_f32 v43, v43, v47
Mvav 100,196,128
s_waitcnt vmcnt(15) lgkmcnt(12)
ds_read_b64_tr_b16 v[252:253], v61 offset:544
v_add_f32 v48, v48, v52
Mvav 100,204,136
buffer_load_dwordx4 v0, s[48:51], s72 offen offset:3072 sc1 lds
Mvav 100,212,144
ds_read_b64_tr_b16 v[254:255], v61 offset:800
v_add_f32 v49, v49, v53
Mvav 100,220,152
buffer_store_dwordx2 v[62:63], v13, s[48:51], s34 offen sc1
Mvav 96,224,160
v_exp_f32 v25, v25
Mvav 96,232,168
s_waitcnt vmcnt(15) lgkmcnt(0)
s_barrier
s_add_u32 m0, s70, 98304
Mvav 96,240,176
s_add_u32 s72, s14, 9584640
ds_read_b128 v[128:131], v12 offset:0
v_add_f32 v50, v50, v54
Mvav 96,248,184
buffer_load_dwordx4 v0, s[48:51], s72 offen sc1 lds
Mvav 100,228,160
ds_read_b128 v[136:139], v12 offset:1024
v_add_f32 v51, v51, v55
Mvav 100,236,168
v_exp_f32 v26, v26
Mvav 100,244,176
ds_read_b128 v[144:147], v12 offset:2048
v_add_f32 v40, v40, v48
Mvav 100,252,184
buffer_load_dwordx4 v0, s[48:51], s72 offen offset:1024 sc1 lds
Mvav 96,0,132
ds_read_b128 v[152:155], v12 offset:3072
v_add_f32 v41, v41, v49
Mvav 96,8,140
v_exp_f32 v27, v27
Mvav 96,16,148
ds_read_b128 v[160:163], v12 offset:4096
v_add_f32 v42, v42, v50
Mvav 96,24,156
v_exp_f32 v28, v28
Mvav 100,4,132
ds_read_b128 v[168:171], v12 offset:5120
v_add_f32 v43, v43, v51
Mvav 100,12,140
buffer_load_dwordx4 v0, s[48:51], s72 offen offset:2048 sc1 lds
Mvav 100,20,148
ds_read_b128 v[176:179], v12 offset:6144
v_permlane16_swap_b32 v40, v41
Mvav 100,28,156
v_exp_f32 v29, v29
Mvav 96,32,164
ds_read_b128 v[184:187], v12 offset:7168
v_permlane16_swap_b32 v42, v43
Mvav 96,40,172
buffer_load_dwordx4 v0, s[48:51], s72 offen offset:3072 sc1 lds
Mvav 96,48,180
ds_read_b128 v[132:135], v12 offset:8192
v_add_f32 v40, v40, v41
Mvav 96,56,188
v_exp_f32 v30, v30
Mvav 100,36,164
ds_read_b128 v[140:143], v12 offset:9216
v_add_f32 v42, v42, v43
Mvav 100,44,172
v_exp_f32 v31, v31
Mvav 100,52,180
ds_read_b128 v[148:151], v12 offset:10240
v_permlane32_swap_b32 v40, v42
Mvav 100,60,188
s_waitcnt lgkmcnt(10)
v_exp_f32 v24, v24
Zvav 104,64,128
s_waitcnt lgkmcnt(9)
ds_read_b128 v[156:159], v12 offset:11264
v_add_f32 v40, v40, v42
Mvav 104,72,136
s_waitcnt lgkmcnt(9)
v_cvt_pk_bf16_f32 v40, 0, v40
v_mul_f32 v42, 0xbfb8aa3b, v40
Mvav 104,80,144
s_waitcnt lgkmcnt(8)
ds_read_b128 v[164:167], v12 offset:12288
v_bfe_u32 v56, v0, 4, 3
Mvav 104,88,152
v_exp_f32 v42, v42
Zvav 108,68,128
ds_read_b128 v[172:175], v12 offset:13312
v_mov_b32_dpp v41, v40 row_ror:8 row_mask:0xf bank_mask:0xf bound_ctrl:1
Mvav 108,76,136
v_add_f32 v42, 1.0, v42
v_lshrrev_b32 v57, 9, v0
Mvav 108,84,144
ds_read_b128 v[180:183], v12 offset:14336
v_lshl_add_u32 v56, v56, 1, v57
Mvav 108,92,152
s_waitcnt lgkmcnt(7)
v_rcp_f32 v42, v42
Mvav 104,96,160
s_lshl_b32 s58, s78, 17
ds_read_b128 v[188:191], v12 offset:15360
v_cvt_pk_bf16_f32 v96, v96, v97
Mvav 104,104,168
s_add_u32 s58, s58, s22
v_cvt_pk_bf16_f32 v97, v98, v99
v_cvt_pk_bf16_f32 v98, v100, v101
Mvav 104,112,176
s_add_u32 s58, s58, 32768
ds_read_b128 v[128:131], v12 offset:16384
v_cvt_pk_bf16_f32 v99, v102, v103
Mvav 104,120,184
s_add_u32 s58, s58, s32
)"
R"(
ds_read_b128 v[136:139], v12 offset:17408
v_mul_f32 v40, v40, v42
Mvav 108,100,160
buffer_store_dwordx4 v[96:99], v0, s[48:51], s58 offen sc1
Mvav 108,108,168
ds_read_b128 v[144:147], v12 offset:18432
v_cvt_pk_bf16_f32 v40, 0, v40
Mvav 108,116,176
ds_read_b128 v[152:155], v12 offset:19456
v_mul_f32 v40, v40, v41
Mvav 108,124,184
s_waitcnt lgkmcnt(11)
v_cvt_pk_bf16_f32 v40, v40, v40
v_bfe_u32 v57, v0, 8, 1
Mvav 104,128,132
s_waitcnt lgkmcnt(8)
ds_read_b128 v[160:163], v12 offset:20480
v_lshl_add_u32 v56, v57, 6, v56
Mvav 104,136,140
s_add_u32 m0, s70, 131072
ds_read_b128 v[168:171], v12 offset:21504
v_and_b32 v57, 128, v0
Mvav 104,144,148
s_add_u32 s72, s12, 14155776
v_add_u32 v56, v56, v57
v_lshl_add_u32 v56, v56, 1, s59
Mvav 104,152,156
ds_read_b128 v[176:179], v12 offset:22528
buffer_load_dwordx4 v0, s[48:51], s72 offen sc1 lds
Mvav 108,132,132
ds_read_b128 v[184:187], v12 offset:23552
v_lshrrev_b32 v57, 8, v0
Mvav 108,140,140
ds_write_b16 v56, v40
Mvav 108,148,148
ds_read_b128 v[132:135], v12 offset:24576
v_and_b32 v56, 16, v0
Mvav 108,156,156
s_waitcnt lgkmcnt(13)
ds_read_b128 v[140:143], v12 offset:25600
v_lshlrev_b32 v57, 4, v57
Mvav 104,160,164
s_waitcnt lgkmcnt(13)
buffer_load_dwordx4 v0, s[48:51], s72 offen offset:1024 sc1 lds
Mvav 104,168,172
s_waitcnt lgkmcnt(12)
ds_read_b128 v[148:151], v12 offset:26624
v_lshl_add_u32 v56, v56, 3, v57
Mvav 104,176,180
s_waitcnt lgkmcnt(12)
ds_read_b128 v[156:159], v12 offset:27648
v_add_u32 v56, 32768, v56
Mvav 104,184,188
v_add_f32 v25, 1.0, v25
v_add_f32 v26, 1.0, v26
Mvav 108,164,164
ds_read_b128 v[164:167], v12 offset:28672
v_add_f32 v27, 1.0, v27
Mvav 108,172,172
ds_read_b128 v[172:175], v12 offset:29696
v_add_f32 v28, 1.0, v28
Mvav 108,180,180
buffer_load_dwordx4 v0, s[48:51], s72 offen offset:2048 sc1 lds
Mvav 108,188,188
s_waitcnt lgkmcnt(12)
ds_read_b128 v[180:183], v12 offset:30720
v_add_f32 v29, 1.0, v29
Mvav 104,192,128
ds_read_b128 v[188:191], v12 offset:31744
v_add_f32 v30, 1.0, v30
Mvav 104,200,136
v_rcp_f32 v25, v25
Mvav 104,208,144
s_waitcnt lgkmcnt(13)
buffer_load_dwordx4 v0, s[48:51], s72 offen offset:3072 sc1 lds
Mvav 104,216,152
v_rcp_f32 v26, v26
Mvav 108,196,128
v_rcp_f32 v27, v27
Mvav 108,204,136
v_rcp_f32 v28, v28
Mvav 108,212,144
v_rcp_f32 v29, v29
Mvav 108,220,152
s_waitcnt lgkmcnt(12)
v_rcp_f32 v30, v30
Mvav 104,224,160
s_waitcnt lgkmcnt(11)
v_mul_f32 v65, v65, v25
v_mul_f32 v66, v66, v26
Mvav 104,232,168
s_waitcnt vmcnt(14) lgkmcnt(0)
s_barrier
s_add_u32 m0, s70, 49152
Mvav 104,240,176
s_add_u32 s72, s12, 14303232
ds_read_b128 v[128:131], v14 offset:16384
v_lshrrev_b32 v57, 9, v0
Mvav 104,248,184
s_mov_b32 vcc_lo, 0xffff0000
buffer_load_dwordx4 v0, s[48:51], s72 offen sc1 lds
Mvav 108,228,160
s_mov_b32 vcc_hi, 0xffff0000
ds_read_b128 v[136:139], v14 offset:17408
v_mul_u32_u24 v57, 7040, v57
Mvav 108,236,168
ds_read_b128 v[32:35], v56 offset:0
v_lshrrev_b32 v58, 2, v0
Mvav 108,244,176
ds_read_b128 v[144:147], v14 offset:18432
v_add_u32 v57, v57, v58
Mvav 108,252,184
buffer_load_dwordx4 v0, s[48:51], s72 offen offset:1024 sc1 lds
Mvav 104,0,132
ds_read_b128 v[152:155], v14 offset:19456
v_mul_f32 v67, v67, v27
Mvav 104,8,140
ds_read_b128 v[36:39], v56 offset:64
v_bfe_u32 v56, v0, 4, 2
Mvav 104,16,148
ds_read_b128 v[160:163], v14 offset:20480
v_lshlrev_b32 v56, 3, v56
Mvav 104,24,156
v_lshrrev_b32 v56, v56, s74
v_and_b32 v56, 0xff, v56
Mvav 108,4,132
ds_read_b128 v[168:171], v14 offset:21504
v_mul_f32 v68, v68, v28
Mvav 108,12,140
s_waitcnt lgkmcnt(5)
buffer_load_dwordx4 v0, s[48:51], s72 offen offset:2048 sc1 lds
Zvvv 40,32,196
Mvav 108,20,148
ds_read_b128 v[176:179], v14 offset:22528
v_mul_f32 v69, v69, v29
Zvvv 44,32,212
Mvav 108,28,156
v_mul_f32 v70, v70, v30
v_add_f32 v31, 1.0, v31
Zvvv 48,32,228
Mvav 104,32,164
ds_read_b128 v[184:187], v14 offset:23552
v_add_f32 v24, 1.0, v24
Zvvv 52,32,244
Mvav 104,40,172
s_waitcnt lgkmcnt(4)
buffer_load_dwordx4 v0, s[48:51], s72 offen offset:3072 sc1 lds
Mvvv 40,36,204
Mvav 104,48,180
ds_read_b128 v[132:135], v14 offset:24576
v_and_b32 v73, 0xffff0000, v200
Mvvv 44,36,220
Mvav 104,56,188
v_rcp_f32 v31, v31
Mvvv 48,36,236
Mvav 108,36,164
ds_read_b128 v[140:143], v14 offset:25600
v_mul_f32 v71, v71, v31
Mvvv 52,36,252
Mvav 108,44,172
v_rcp_f32 v24, v24
Mvav 108,52,180
ds_read_b128 v[148:151], v14 offset:26624
v_mul_f32 v72, v72, v24
Mvav 108,60,188
v_mul_f32 v25, 0xbfb8aa3b, v73
v_lshlrev_b32 v74, 16, v201
Zvav 96,64,128
ds_read_b128 v[156:159], v14 offset:27648
v_mul_f32 v26, 0xbfb8aa3b, v74
Mvav 96,72,136
v_exp_f32 v25, v25
Mvav 96,80,144
ds_read_b128 v[164:167], v14 offset:28672
v_add_f32 v25, 1.0, v25
Mvav 96,88,152
v_rcp_f32 v25, v25
Zvav 100,68,128
ds_read_b128 v[172:175], v14 offset:29696
v_cndmask_b32 v40, v40, v44, vcc
Mvav 100,76,136
v_exp_f32 v26, v26
Mvav 100,84,144
ds_read_b128 v[180:183], v14 offset:30720
v_cndmask_b32 v41, v41, v45, vcc
Mvav 100,92,152
s_waitcnt lgkmcnt(7)
v_cndmask_b32 v48, v48, v52, vcc
v_cndmask_b32 v49, v49, v53, vcc
Mvav 96,96,160
s_lshl_b32 s58, s76, 17
ds_read_b128 v[188:191], v14 offset:31744
v_cvt_pk_bf16_f32 v104, v104, v105
Mvav 96,104,168
s_add_u32 s58, s58, s22
v_cvt_pk_bf16_f32 v105, v106, v107
v_cvt_pk_bf16_f32 v106, v108, v109
Mvav 96,112,176
s_add_u32 s58, s58, 65536
ds_read_b128 v[128:131], v0 offset:0
v_cvt_pk_bf16_f32 v107, v110, v111
Mvav 96,120,184
s_add_u32 s58, s58, s32
buffer_store_dwordx4 v[104:107], v0, s[48:51], s58 offen sc1
Mvav 100,100,160
s_mov_b64 vcc, 0xffffffff
ds_read_b128 v[136:139], v0 offset:1024
v_cndmask_b32 v58, v48, v40, vcc
Mvav 100,108,168
v_cndmask_b32 v59, v49, v41, vcc
v_and_or_b32 v58, v58, v2, v56
Mvav 100,116,176
ds_read_b128 v[144:147], v0 offset:2048
v_and_or_b32 v59, v59, v2, v56
Mvav 100,124,184
s_waitcnt lgkmcnt(7)
buffer_store_dword v58, v57, s[48:51], s35 offen sc1
Mvav 96,128,132
s_add_u32 s35, s35, 229376
ds_read_b128 v[152:155], v0 offset:3072
v_mul_f32 v73, v73, v25
Mvav 96,136,140
s_add_u32 m0, s70, 65536
buffer_store_dword v59, v57, s[48:51], s35 offen sc1
Mvav 96,144,148
s_add_u32 s72, s13, 14155776
ds_read_b128 v[160:163], v0 offset:4096
v_add_f32 v26, 1.0, v26
Mvav 96,152,156
buffer_load_dwordx4 v0, s[48:51], s72 offen sc1 lds
Mvav 100,132,132
ds_read_b128 v[168:171], v0 offset:5120
v_and_b32 v75, 0xffff0000, v201
Mvav 100,140,140
v_rcp_f32 v26, v26
Mvav 100,148,148
ds_read_b128 v[176:179], v0 offset:6144
v_mul_f32 v74, v74, v26
Mvav 100,156,156
s_waitcnt lgkmcnt(10)
v_mul_f32 v27, 0xbfb8aa3b, v75
v_lshlrev_b32 v76, 16, v202
Mvav 96,160,164
s_waitcnt lgkmcnt(9)
ds_read_b128 v[184:187], v0 offset:7168
v_mul_f32 v28, 0xbfb8aa3b, v76
Mvav 96,168,172
s_waitcnt lgkmcnt(9)
buffer_load_dwordx4 v0, s[48:51], s72 offen offset:1024 sc1 lds
Mvav 96,176,180
s_waitcnt lgkmcnt(8)
ds_read_b128 v[132:135], v0 offset:8192
v_and_b32 v77, 0xffff0000, v202
Mvav 96,184,188
v_exp_f32 v27, v27
Mvav 100,164,164
ds_read_b128 v[140:143], v0 offset:9216
v_add_f32 v27, 1.0, v27
Mvav 100,172,172
buffer_load_dwordx4 v0, s[48:51], s72 offen offset:2048 sc1 lds
Mvav 100,180,180
ds_read_b128 v[148:151], v0 offset:10240
v_mul_f32 v29, 0xbfb8aa3b, v77
Mvav 100,188,188
s_waitcnt lgkmcnt(10)
v_rcp_f32 v27, v27
Mvav 96,192,128
s_waitcnt vmcnt(14) lgkmcnt(8)
ds_read_b128 v[156:159], v0 offset:11264
v_mul_f32 v75, v75, v27
Mvav 96,200,136
s_add_u32 s39, s63, 0
buffer_store_dwordx2 v[62:63], v13, s[48:51], s34 offen offset:32 sc1
Mvav 96,208,144
s_waitcnt lgkmcnt(8)
ds_read_b128 v[164:167], v0 offset:12288
v_lshlrev_b32 v78, 16, v203
Mvav 96,216,152
buffer_load_dwordx4 v0, s[48:51], s72 offen offset:3072 sc1 lds
Mvav 100,196,128
ds_read_b128 v[172:175], v0 offset:13312
v_mul_f32 v30, 0xbfb8aa3b, v78
Mvav 100,204,136
v_exp_f32 v28, v28
Mvav 100,212,144
ds_read_b128 v[180:183], v0 offset:14336
v_add_f32 v28, 1.0, v28
Mvav 100,220,152
s_waitcnt lgkmcnt(10)
v_rcp_f32 v28, v28
Mvav 96,224,160
s_waitcnt lgkmcnt(7)
ds_read_b128 v[188:191], v0 offset:15360
v_mul_f32 v76, v76, v28
Mvav 96,232,168
s_barrier
s_add_u32 m0, s70, 32768
Mvav 96,240,176
s_add_u32 s72, s13, 14303232
ds_read_b128 v[128:131], v0 offset:16384
v_and_b32 v79, 0xffff0000, v203
Mvav 96,248,184
buffer_load_dwordx4 v0, s[48:51], s72 offen sc1 lds
Mvav 100,228,160
ds_read_b128 v[136:139], v0 offset:17408
v_mul_f32 v31, 0xbfb8aa3b, v79
Mvav 100,236,168
v_exp_f32 v29, v29
Mvav 100,244,176
ds_read_b128 v[144:147], v0 offset:18432
v_add_f32 v29, 1.0, v29
Mvav 100,252,184
s_waitcnt lgkmcnt(10)
buffer_load_dwordx4 v0, s[48:51], s72 offen offset:1024 sc1 lds
Mvav 96,0,132
s_waitcnt lgkmcnt(9)
ds_read_b128 v[152:155], v0 offset:19456
v_lshlrev_b32 v80, 16, v208
Mvav 96,8,140
s_waitcnt lgkmcnt(9)
v_rcp_f32 v29, v29
Mvav 96,16,148
s_waitcnt lgkmcnt(8)
ds_read_b128 v[160:163], v0 offset:20480
v_mul_f32 v77, v77, v29
Mvav 96,24,156
v_exp_f32 v30, v30
Mvav 100,4,132
ds_read_b128 v[168:171], v0 offset:21504
v_add_f32 v30, 1.0, v30
Mvav 100,12,140
buffer_load_dwordx4 v0, s[48:51], s72 offen offset:2048 sc1 lds
Mvav 100,20,148
ds_read_b128 v[176:179], v0 offset:22528
v_mul_f32 v24, 0xbfb8aa3b, v80
Mvav 100,28,156
s_waitcnt lgkmcnt(10)
v_rcp_f32 v30, v30
Mvav 96,32,164
s_waitcnt lgkmcnt(9)
ds_read_b128 v[184:187], v0 offset:23552
v_mul_f32 v78, v78, v30
Mvav 96,40,172
s_waitcnt lgkmcnt(9)
buffer_load_dwordx4 v0, s[48:51], s72 offen offset:3072 sc1 lds
Mvav 96,48,180
s_waitcnt lgkmcnt(8)
ds_read_b128 v[132:135], v0 offset:24576
v_and_b32 v81, 0xffff0000, v208
Mvav 96,56,188
v_exp_f32 v31, v31
Mvav 100,36,164
ds_read_b128 v[140:143], v0 offset:25600
v_add_f32 v31, 1.0, v31
Mvav 100,44,172
v_rcp_f32 v31, v31
Mvav 100,52,180
ds_read_b128 v[148:151], v0 offset:26624
v_mul_f32 v79, v79, v31
Mvav 100,60,188
s_waitcnt lgkmcnt(10)
v_exp_f32 v24, v24
Zvav 104,64,128
s_waitcnt lgkmcnt(9)
ds_read_b128 v[156:159], v0 offset:27648
v_add_f32 v24, 1.0, v24
Mvav 104,72,136
s_waitcnt lgkmcnt(9)
v_rcp_f32 v24, v24
Mvav 104,80,144
s_waitcnt lgkmcnt(8)
ds_read_b128 v[164:167], v0 offset:28672
v_mul_f32 v80, v80, v24
Mvav 104,88,152
buffer_load_dwordx4 v[224:227], v4, s[8:11], s39 offen
Zvav 108,68,128
s_add_u32 s39, s63, 1048576
ds_read_b128 v[172:175], v0 offset:29696
v_mul_f32 v25, 0xbfb8aa3b, v81
Mvav 108,76,136
v_exp_f32 v25, v25
Mvav 108,84,144
ds_read_b128 v[180:183], v0 offset:30720
v_add_f32 v25, 1.0, v25
Mvav 108,92,152
s_waitcnt lgkmcnt(7)
v_rcp_f32 v25, v25
Mvav 104,96,160
s_lshl_b32 s58, s77, 17
ds_read_b128 v[188:191], v0 offset:31744
v_cvt_pk_bf16_f32 v96, v96, v97
Mvav 104,104,168
s_add_u32 s58, s58, s22
v_cvt_pk_bf16_f32 v97, v98, v99
v_cvt_pk_bf16_f32 v98, v100, v101
Mvav 104,112,176
s_add_u32 s58, s58, 65536
ds_read_b128 v[128:131], v12 offset:32768
v_cvt_pk_bf16_f32 v99, v102, v103
Mvav 104,120,184
s_add_u32 s58, s58, s32
v_mul_f32 v81, v81, v25
v_lshlrev_b32 v82, 16, v209
Mvav 108,100,160
ds_read_b128 v[136:139], v12 offset:33792
v_mul_f32 v26, 0xbfb8aa3b, v82
Mvav 108,108,168
buffer_store_dwordx4 v[96:99], v0, s[48:51], s58 offen sc1
Mvav 108,116,176
ds_read_b128 v[144:147], v12 offset:34816
v_and_b32 v83, 0xffff0000, v209
Mvav 108,124,184
s_waitcnt lgkmcnt(10)
v_exp_f32 v26, v26
Mvav 104,128,132
s_waitcnt lgkmcnt(7)
ds_read_b128 v[152:155], v12 offset:35840
v_add_f32 v26, 1.0, v26
Mvav 104,136,140
s_add_u32 m0, s70, 81920
v_rcp_f32 v26, v26
Mvav 104,144,148
s_add_u32 s72, s14, 14155776
ds_read_b128 v[160:163], v12 offset:36864
v_mul_f32 v82, v82, v26
Mvav 104,152,156
buffer_load_dwordx4 v0, s[48:51], s72 offen sc1 lds
Mvav 108,132,132
ds_read_b128 v[168:171], v12 offset:37888
v_mul_f32 v27, 0xbfb8aa3b, v83
Mvav 108,140,140
v_exp_f32 v27, v27
Mvav 108,148,148
ds_read_b128 v[176:179], v12 offset:38912
v_add_f32 v27, 1.0, v27
Mvav 108,156,156
s_waitcnt lgkmcnt(10)
v_rcp_f32 v27, v27
Mvav 104,160,164
s_waitcnt lgkmcnt(9)
ds_read_b128 v[184:187], v12 offset:39936
v_mul_f32 v83, v83, v27
Mvav 104,168,172
s_waitcnt lgkmcnt(9)
buffer_load_dwordx4 v0, s[48:51], s72 offen offset:1024 sc1 lds
Mvav 104,176,180
s_waitcnt lgkmcnt(8)
ds_read_b128 v[132:135], v12 offset:40960
v_lshlrev_b32 v84, 16, v210
Mvav 104,184,188
buffer_load_dwordx4 v[232:235], v4, s[8:11], s39 offen
Mvav 108,164,164
)"
R"(
s_add_u32 s39, s63, 2097152
ds_read_b128 v[140:143], v12 offset:41984
v_bfe_u32 v8, v0, 4, 4
Mvav 108,172,172
buffer_load_dwordx4 v0, s[48:51], s72 offen offset:2048 sc1 lds
Mvav 108,180,180
ds_read_b128 v[148:151], v12 offset:43008
v_lshlrev_b32 v8, 3, v8
Mvav 108,188,188
s_waitcnt lgkmcnt(10)
buffer_load_dwordx2 v[58:59], v8, s[48:51], s33 offen sc1
Mvav 104,192,128
s_waitcnt lgkmcnt(9)
ds_read_b128 v[156:159], v12 offset:44032
v_mul_f32 v28, 0xbfb8aa3b, v84
Mvav 104,200,136
s_waitcnt lgkmcnt(9)
v_exp_f32 v28, v28
Mvav 104,208,144
s_waitcnt lgkmcnt(8)
ds_read_b128 v[164:167], v12 offset:45056
v_add_f32 v28, 1.0, v28
Mvav 104,216,152
buffer_load_dwordx4 v0, s[48:51], s72 offen offset:3072 sc1 lds
Mvav 108,196,128
ds_read_b128 v[172:175], v12 offset:46080
v_and_b32 v85, 0xffff0000, v210
Mvav 108,204,136
s_add_u32 m0, s70, 114688
v_rcp_f32 v28, v28
Mvav 108,212,144
s_add_u32 s72, s14, 14303232
ds_read_b128 v[180:183], v12 offset:47104
v_mul_f32 v84, v84, v28
Mvav 108,220,152
s_waitcnt lgkmcnt(10)
buffer_load_dwordx4 v0, s[48:51], s72 offen sc1 lds
Mvav 104,224,160
s_waitcnt lgkmcnt(9)
ds_read_b128 v[188:191], v12 offset:48128
v_mul_f32 v29, 0xbfb8aa3b, v85
Mvav 104,232,168
s_waitcnt vmcnt(21) lgkmcnt(9)
s_barrier
Mvav 104,240,176
s_waitcnt lgkmcnt(8)
ds_read_b128 v[128:131], v14 offset:0
v_lshlrev_b32 v86, 16, v211
Mvav 104,248,184
v_exp_f32 v29, v29
Mvav 108,228,160
ds_read_b128 v[136:139], v14 offset:1024
v_add_f32 v29, 1.0, v29
Mvav 108,236,168
buffer_load_dwordx4 v0, s[48:51], s72 offen offset:1024 sc1 lds
Mvav 108,244,176
ds_read_b128 v[144:147], v14 offset:2048
v_mul_f32 v30, 0xbfb8aa3b, v86
Mvav 108,252,184
s_waitcnt lgkmcnt(10)
v_rcp_f32 v29, v29
Mvav 104,0,132
s_waitcnt lgkmcnt(9)
ds_read_b128 v[152:155], v14 offset:3072
v_mul_f32 v85, v85, v29
Mvav 104,8,140
s_waitcnt lgkmcnt(9)
buffer_load_dwordx4 v0, s[48:51], s72 offen offset:2048 sc1 lds
Mvav 104,16,148
s_waitcnt lgkmcnt(8)
ds_read_b128 v[160:163], v14 offset:4096
v_and_b32 v87, 0xffff0000, v211
Mvav 104,24,156
v_exp_f32 v30, v30
Mvav 108,4,132
ds_read_b128 v[168:171], v14 offset:5120
v_add_f32 v30, 1.0, v30
Mvav 108,12,140
v_rcp_f32 v30, v30
Mvav 108,20,148
ds_read_b128 v[176:179], v14 offset:6144
v_mul_f32 v86, v86, v30
Mvav 108,28,156
s_waitcnt lgkmcnt(10)
buffer_load_dwordx4 v0, s[48:51], s72 offen offset:3072 sc1 lds
Mvav 104,32,164
s_waitcnt lgkmcnt(9)
ds_read_b128 v[184:187], v14 offset:7168
v_mul_f32 v31, 0xbfb8aa3b, v87
Mvav 104,40,172
s_waitcnt lgkmcnt(9)
v_exp_f32 v31, v31
Mvav 104,48,180
s_waitcnt lgkmcnt(8)
ds_read_b128 v[132:135], v14 offset:8192
v_add_f32 v31, 1.0, v31
Mvav 104,56,188
v_rcp_f32 v31, v31
Mvav 108,36,164
ds_read_b128 v[140:143], v14 offset:9216
v_mul_f32 v87, v87, v31
Mvav 108,44,172
buffer_load_dwordx4 v[240:243], v4, s[8:11], s39 offen
Mvav 108,52,180
s_add_u32 s39, s63, 3145728
ds_read_b128 v[148:151], v14 offset:10240
v_mul_f32 v24, 0xbfb8aa3b, v16
Mvav 108,60,188
s_waitcnt lgkmcnt(10)
v_exp_f32 v24, v24
Zvav 96,64,128
s_waitcnt lgkmcnt(9)
ds_read_b128 v[156:159], v14 offset:11264
v_add_f32 v24, 1.0, v24
Mvav 96,72,136
s_waitcnt lgkmcnt(9)
v_rcp_f32 v24, v24
Mvav 96,80,144
s_waitcnt lgkmcnt(8)
ds_read_b128 v[164:167], v14 offset:12288
v_mul_f32 v16, v16, v24
Mvav 96,88,152
v_mul_f32 v25, 0xbfb8aa3b, v17
v_mul_f32 v26, 0xbfb8aa3b, v18
Zvav 100,68,128
ds_read_b128 v[172:175], v14 offset:13312
v_mul_f32 v27, 0xbfb8aa3b, v19
Mvav 100,76,136
v_exp_f32 v25, v25
Mvav 100,84,144
ds_read_b128 v[180:183], v14 offset:14336
v_add_f32 v25, 1.0, v25
Mvav 100,92,152
s_waitcnt lgkmcnt(7)
v_rcp_f32 v25, v25
Mvav 96,96,160
s_lshl_b32 s58, s78, 17
ds_read_b128 v[188:191], v14 offset:15360
v_cvt_pk_bf16_f32 v104, v104, v105
Mvav 96,104,168
s_add_u32 s58, s58, s22
v_exp_f32 v26, v26
Mvav 96,112,176
s_add_u32 s58, s58, 65536
ds_read_b128 v[128:131], v0 offset:49152
v_cvt_pk_bf16_f32 v105, v106, v107
Mvav 96,120,184
s_add_u32 s58, s58, s32
v_exp_f32 v27, v27
Mvav 100,100,160
ds_read_b128 v[136:139], v0 offset:50176
v_cvt_pk_bf16_f32 v106, v108, v109
Mvav 100,108,168
buffer_load_dwordx4 v[248:251], v4, s[8:11], s39 offen
Mvav 100,116,176
ds_read_b128 v[144:147], v0 offset:51200
v_cvt_pk_bf16_f32 v107, v110, v111
Mvav 100,124,184
s_waitcnt lgkmcnt(10)
v_mul_f32 v17, v17, v25
v_add_f32 v26, 1.0, v26
Mvav 96,128,132
s_waitcnt lgkmcnt(7)
ds_read_b128 v[152:155], v0 offset:52224
v_add_f32 v27, 1.0, v27
Mvav 96,136,140
s_add_u32 m0, s70, 147456
v_rcp_f32 v26, v26
Mvav 96,144,148
s_add_u32 s72, s15, 4718592
ds_read_b128 v[160:163], v0 offset:53248
v_mul_f32 v18, v18, v26
Mvav 96,152,156
buffer_load_dwordx4 v0, s[48:51], s72 offen sc1 lds
Mvav 100,132,132
ds_read_b128 v[168:171], v0 offset:54272
v_mul_f32 v28, 0xbfb8aa3b, v20
Mvav 100,140,140
v_rcp_f32 v27, v27
Mvav 100,148,148
ds_read_b128 v[176:179], v0 offset:55296
v_mul_f32 v19, v19, v27
Mvav 100,156,156
s_waitcnt lgkmcnt(10)
buffer_store_dwordx4 v[104:107], v0, s[48:51], s58 offen sc1
Mvav 96,160,164
s_waitcnt lgkmcnt(9)
ds_read_b128 v[184:187], v0 offset:56320
v_mul_f32 v29, 0xbfb8aa3b, v21
Mvav 96,168,172
s_waitcnt lgkmcnt(9)
buffer_load_dwordx4 v0, s[48:51], s72 offen offset:1024 sc1 lds
Mvav 96,176,180
s_waitcnt lgkmcnt(8)
ds_read_b128 v[132:135], v0 offset:57344
v_mul_f32 v30, 0xbfb8aa3b, v22
Mvav 96,184,188
v_exp_f32 v28, v28
Mvav 100,164,164
ds_read_b128 v[140:143], v0 offset:58368
v_add_f32 v28, 1.0, v28
Mvav 100,172,172
buffer_load_dwordx4 v0, s[48:51], s72 offen offset:2048 sc1 lds
Mvav 100,180,180
ds_read_b128 v[148:151], v0 offset:59392
v_mul_f32 v31, 0xbfb8aa3b, v23
Mvav 100,188,188
s_waitcnt lgkmcnt(10)
v_rcp_f32 v28, v28
Mvav 96,192,128
s_waitcnt lgkmcnt(9)
ds_read_b128 v[156:159], v0 offset:60416
v_mul_f32 v20, v20, v28
Mvav 96,200,136
s_waitcnt lgkmcnt(9)
v_exp_f32 v29, v29
Mvav 96,208,144
s_waitcnt lgkmcnt(8)
ds_read_b128 v[164:167], v0 offset:61440
v_add_f32 v29, 1.0, v29
Mvav 96,216,152
buffer_load_dwordx4 v0, s[48:51], s72 offen offset:3072 sc1 lds
Mvav 100,196,128
ds_read_b128 v[172:175], v0 offset:62464
Mvav 100,204,136
s_add_u32 m0, s70, 0
v_rcp_f32 v29, v29
Mvav 100,212,144
s_waitcnt vmcnt(12)
s_add_u32 s72, s15, 4866048
ds_read_b128 v[180:183], v0 offset:63488
v_xor_b32 v58, s74, v58
Mvav 100,220,152
s_waitcnt lgkmcnt(10)
buffer_load_dwordx4 v0, s[48:51], s72 offen sc1 lds
Mvav 96,224,160
s_waitcnt lgkmcnt(8)
ds_read_b128 v[188:191], v0 offset:64512
v_bitop3_b32 v58, v59, s75, v58 bitop3:0xbe
Mvav 96,232,168
s_barrier
Mvav 96,240,176
s_waitcnt lgkmcnt(8)
ds_read_b128 v[128:131], v12 offset:0
v_cmp_ne_u32 vcc, 0, v58
Mvav 96,248,184
s_and_b64 vcc, vcc, 0xff
s_cbranch_vccnz .Lpf1
.Lpf1_r:
Mvav 100,228,160
s_lshl_b32 s62, s76, 13
ds_read_b128 v[136:139], v12 offset:1024
v_mul_f32 v21, v21, v29
Mvav 100,236,168
s_add_u32 s62, s62, s23
buffer_load_dwordx4 v0, s[48:51], s72 offen offset:1024 sc1 lds
Mvav 100,244,176
s_waitcnt lgkmcnt(9)
s_add_u32 s62, s62, s32
ds_read_b128 v[144:147], v12 offset:2048
Mvav 100,252,184
s_add_u32 s62, s62, 32768
buffer_load_dwordx4 v[44:47], v0, s[48:51], s62 offen sc1
Mvav 96,0,132
s_waitcnt lgkmcnt(8)
s_lshl_b32 s62, s77, 13
ds_read_b128 v[152:155], v12 offset:3072
Mvav 96,8,140
s_add_u32 s62, s62, s23
buffer_load_dwordx4 v0, s[48:51], s72 offen offset:2048 sc1 lds
Mvav 96,16,148
s_waitcnt lgkmcnt(8)
s_add_u32 s62, s62, s32
ds_read_b128 v[160:163], v12 offset:4096
Mvav 96,24,156
s_add_u32 s62, s62, 32768
v_exp_f32 v30, v30
Mvav 100,4,132
ds_read_b128 v[168:171], v12 offset:5120
v_add_f32 v30, 1.0, v30
Mvav 100,12,140
v_rcp_f32 v30, v30
Mvav 100,20,148
ds_read_b128 v[176:179], v12 offset:6144
v_mul_f32 v22, v22, v30
Mvav 100,28,156
s_waitcnt lgkmcnt(10)
buffer_load_dwordx4 v0, s[48:51], s72 offen offset:3072 sc1 lds
Mvav 96,32,164
s_waitcnt lgkmcnt(9)
ds_read_b128 v[184:187], v12 offset:7168
Mvav 96,40,172
s_waitcnt lgkmcnt(9)
buffer_load_dwordx4 v[48:51], v0, s[48:51], s62 offen sc1
Mvav 96,48,180
s_waitcnt lgkmcnt(8)
s_lshl_b32 s62, s78, 13
ds_read_b128 v[132:135], v12 offset:8192
Mvav 96,56,188
s_add_u32 s62, s62, s23
v_exp_f32 v31, v31
Mvav 100,36,164
s_add_u32 s62, s62, s32
ds_read_b128 v[140:143], v12 offset:9216
v_add_f32 v31, 1.0, v31
Mvav 100,44,172
s_add_u32 s62, s62, 32768
v_rcp_f32 v31, v31
Mvav 100,52,180
ds_read_b128 v[148:151], v12 offset:10240
v_mul_f32 v23, v23, v31
Mvav 100,60,188
s_waitcnt lgkmcnt(10)
Zvav 104,64,128
s_waitcnt lgkmcnt(9)
s_not_b32 s59, s90
ds_read_b128 v[156:159], v12 offset:11264
Mvav 104,72,136
s_waitcnt lgkmcnt(8)
s_bfe_u32 s59, s59, 0x50004
Mvav 104,80,144
s_cmp_eq_u32 s59, s64
s_cselect_b32 s73, 2, 0
ds_read_b128 v[164:167], v12 offset:12288
Mvav 104,88,152
s_not_b32 s59, s89
buffer_load_dwordx4 v[52:55], v0, s[48:51], s62 offen sc1
Zvav 108,68,128
s_lshl_b32 s62, s76, 13
ds_read_b128 v[172:175], v12 offset:13312
Mvav 108,76,136
s_add_u32 s62, s62, s23
Mvav 108,84,144
s_add_u32 s62, s62, s32
ds_read_b128 v[180:183], v12 offset:14336
Mvav 108,92,152
s_waitcnt lgkmcnt(7)
s_add_u32 s62, s62, 0
Mvav 104,96,160
s_lshl_b32 s58, s76, 17
ds_read_b128 v[188:191], v12 offset:15360
v_and_or_b32 v96, v96, v2, s24
Mvav 104,104,168
s_add_u32 s58, s58, s22
v_and_or_b32 v97, v97, v2, s25
v_and_or_b32 v98, v98, v2, s26
Mvav 104,112,176
s_add_u32 s58, s58, 98304
ds_read_b128 v[128:131], v0 offset:32768
v_and_or_b32 v99, v99, v2, s27
Mvav 104,120,184
s_bfe_u32 s59, s59, 0x50004
v_and_or_b32 v100, v100, v2, s24
v_and_or_b32 v101, v101, v2, s25
Mvav 108,100,160
s_cmp_eq_u32 s59, s64
s_addc_u32 s73, s73, 0
ds_read_b128 v[136:139], v0 offset:33792
Mvav 108,108,168
buffer_store_dwordx4 v[96:99], v0, s[48:51], s58 offen sc1
Mvav 108,116,176
s_waitcnt lgkmcnt(9)
ds_read_b128 v[144:147], v0 offset:34816
v_and_or_b32 v102, v102, v2, s26
Mvav 108,124,184
s_cmp_lg_u32 s73, 0
s_cbranch_scc1 .Lrt0
.Lrt0_r:
Mvav 104,128,132
s_waitcnt lgkmcnt(9)
ds_read_b128 v[152:155], v0 offset:35840
v_and_or_b32 v103, v103, v2, s27
Mvav 104,136,140
s_waitcnt lgkmcnt(9)
s_add_u32 m0, s70, 16384
Mvav 104,144,148
s_waitcnt lgkmcnt(8)
s_add_u32 s72, s15, 9437184
ds_read_b128 v[160:163], v0 offset:36864
Mvav 104,152,156
buffer_load_dwordx4 v0, s[48:51], s72 offen sc1 lds
Mvav 108,132,132
ds_read_b128 v[168:171], v0 offset:37888
Mvav 108,140,140
Mvav 108,148,148
ds_read_b128 v[176:179], v0 offset:38912
Mvav 108,156,156
s_waitcnt lgkmcnt(10)
buffer_store_dwordx4 v[100:103], v0, s[48:51], s58 offen offset:1024 sc1
Mvav 104,160,164
s_waitcnt lgkmcnt(9)
ds_read_b128 v[184:187], v0 offset:39936
Mvav 104,168,172
s_waitcnt lgkmcnt(9)
buffer_load_dwordx4 v0, s[48:51], s72 offen offset:1024 sc1 lds
Mvav 104,176,180
s_waitcnt lgkmcnt(8)
ds_read_b128 v[132:135], v0 offset:40960
Mvav 104,184,188
Mvav 108,164,164
ds_read_b128 v[140:143], v0 offset:41984
Mvav 108,172,172
buffer_load_dwordx4 v0, s[48:51], s72 offen offset:2048 sc1 lds
Mvav 108,180,180
ds_read_b128 v[148:151], v0 offset:43008
Mvav 108,188,188
s_waitcnt lgkmcnt(10)
Mvav 104,192,128
s_waitcnt vmcnt(15) lgkmcnt(9)
ds_read_b128 v[156:159], v0 offset:44032
Mvav 104,200,136
s_waitcnt lgkmcnt(9)
buffer_store_dwordx2 v[62:63], v13, s[48:51], s34 offen offset:64 sc1
Mvav 104,208,144
s_waitcnt lgkmcnt(8)
ds_read_b128 v[164:167], v0 offset:45056
Mvav 104,216,152
buffer_load_dwordx4 v0, s[48:51], s72 offen offset:3072 sc1 lds
Mvav 108,196,128
ds_read_b128 v[172:175], v0 offset:46080
Mvav 108,204,136
s_add_u32 m0, s70, 98304
buffer_load_dwordx4 v[32:35], v0, s[48:51], s62 offen sc1
Mvav 108,212,144
s_waitcnt lgkmcnt(9)
s_add_u32 s72, s15, 9584640
ds_read_b128 v[180:183], v0 offset:47104
Mvav 108,220,152
s_lshl_b32 s62, s77, 13
buffer_load_dwordx4 v0, s[48:51], s72 offen sc1 lds
Mvav 104,224,160
s_waitcnt lgkmcnt(8)
s_add_u32 s62, s62, s23
ds_read_b128 v[188:191], v0 offset:48128
Mvav 104,232,168
s_barrier
Mvav 104,240,176
s_waitcnt lgkmcnt(8)
s_add_u32 s62, s62, s32
ds_read_b128 v[128:131], v12 offset:16384
Mvav 104,248,184
s_add_u32 s62, s62, 0
Mvav 108,228,160
ds_read_b128 v[136:139], v12 offset:17408
Mvav 108,236,168
buffer_load_dwordx4 v0, s[48:51], s72 offen offset:1024 sc1 lds
Mvav 108,244,176
ds_read_b128 v[144:147], v12 offset:18432
Mvav 108,252,184
s_waitcnt lgkmcnt(10)
)"
R"(
buffer_load_dwordx4 v[36:39], v0, s[48:51], s62 offen sc1
Mvav 104,0,132
s_waitcnt lgkmcnt(8)
s_lshl_b32 s62, s78, 13
ds_read_b128 v[152:155], v12 offset:19456
Mvav 104,8,140
s_add_u32 s62, s62, s23
buffer_load_dwordx4 v0, s[48:51], s72 offen offset:2048 sc1 lds
Mvav 104,16,148
s_waitcnt lgkmcnt(8)
s_add_u32 s62, s62, s32
ds_read_b128 v[160:163], v12 offset:20480
Mvav 104,24,156
s_add_u32 s62, s62, 0
Mvav 108,4,132
ds_read_b128 v[168:171], v12 offset:21504
Mvav 108,12,140
s_waitcnt vmcnt(16)
v_lshlrev_b32 v112, 16, v44
v_and_b32 v113, 0xffff0000, v44
Mvav 108,20,148
ds_read_b128 v[176:179], v12 offset:22528
v_lshlrev_b32 v114, 16, v45
Mvav 108,28,156
s_waitcnt lgkmcnt(10)
buffer_load_dwordx4 v0, s[48:51], s72 offen offset:3072 sc1 lds
Mvav 104,32,164
s_waitcnt lgkmcnt(9)
ds_read_b128 v[184:187], v12 offset:23552
v_and_b32 v115, 0xffff0000, v45
Mvav 104,40,172
s_waitcnt lgkmcnt(9)
buffer_load_dwordx4 v[40:43], v0, s[48:51], s62 offen sc1
Mvav 104,48,180
s_waitcnt lgkmcnt(8)
ds_read_b128 v[132:135], v12 offset:24576
v_lshlrev_b32 v116, 16, v46
Mvav 104,56,188
v_and_b32 v117, 0xffff0000, v46
v_lshlrev_b32 v118, 16, v47
Mvav 108,36,164
ds_read_b128 v[140:143], v12 offset:25600
v_and_b32 v119, 0xffff0000, v47
Mvav 108,44,172
Mvav 108,52,180
ds_read_b128 v[148:151], v12 offset:26624
Mvav 108,60,188
s_waitcnt vmcnt(15) lgkmcnt(10)
v_lshlrev_b32 v56, 16, v48
v_add_f32 v112, v112, v56
Zvav 96,64,128
s_waitcnt lgkmcnt(9)
ds_read_b128 v[156:159], v12 offset:27648
v_and_b32 v57, 0xffff0000, v48
Mvav 96,72,136
s_waitcnt lgkmcnt(9)
v_add_f32 v113, v113, v57
v_lshlrev_b32 v60, 16, v49
Mvav 96,80,144
s_waitcnt lgkmcnt(8)
ds_read_b128 v[164:167], v12 offset:28672
v_add_f32 v114, v114, v60
Mvav 96,88,152
v_and_b32 v61, 0xffff0000, v49
v_add_f32 v115, v115, v61
Zvav 100,68,128
ds_read_b128 v[172:175], v12 offset:29696
v_lshlrev_b32 v200, 16, v224
Mvav 100,76,136
v_and_b32 v201, 0xffff0000, v224
v_lshlrev_b32 v202, 16, v225
Mvav 100,84,144
ds_read_b128 v[180:183], v12 offset:30720
v_and_b32 v203, 0xffff0000, v225
Mvav 100,92,152
s_waitcnt lgkmcnt(7)
v_lshlrev_b32 v208, 16, v226
v_and_b32 v209, 0xffff0000, v226
Mvav 96,96,160
s_lshl_b32 s58, s77, 17
ds_read_b128 v[188:191], v12 offset:31744
v_and_or_b32 v104, v104, v2, s24
Mvav 96,104,168
s_add_u32 s58, s58, s22
v_and_or_b32 v105, v105, v2, s25
v_and_or_b32 v106, v106, v2, s26
Mvav 96,112,176
s_add_u32 s58, s58, 98304
ds_read_b128 v[128:131], v12 offset:49152
v_and_or_b32 v107, v107, v2, s27
Mvav 96,120,184
buffer_store_dwordx4 v[104:107], v0, s[48:51], s58 offen sc1
Mvav 100,100,160
ds_read_b128 v[136:139], v12 offset:50176
v_and_or_b32 v108, v108, v2, s24
Mvav 100,108,168
v_and_or_b32 v109, v109, v2, s25
v_and_or_b32 v110, v110, v2, s26
Mvav 100,116,176
ds_read_b128 v[144:147], v12 offset:51200
v_and_or_b32 v111, v111, v2, s27
Mvav 100,124,184
s_waitcnt vmcnt(15) lgkmcnt(10)
v_lshlrev_b32 v56, 16, v52
v_add_f32 v112, v112, v56
Mvav 96,128,132
s_waitcnt lgkmcnt(7)
ds_read_b128 v[152:155], v12 offset:52224
v_and_b32 v57, 0xffff0000, v52
Mvav 96,136,140
s_add_u32 m0, s70, 131072
v_add_f32 v113, v113, v57
v_lshlrev_b32 v60, 16, v53
Mvav 96,144,148
s_add_u32 s72, s15, 14155776
ds_read_b128 v[160:163], v12 offset:53248
v_add_f32 v114, v114, v60
Mvav 96,152,156
buffer_load_dwordx4 v0, s[48:51], s72 offen sc1 lds
Mvav 100,132,132
ds_read_b128 v[168:171], v12 offset:54272
v_and_b32 v61, 0xffff0000, v53
Mvav 100,140,140
buffer_store_dwordx4 v[108:111], v0, s[48:51], s58 offen offset:1024 sc1
Mvav 100,148,148
ds_read_b128 v[176:179], v12 offset:55296
v_add_f32 v115, v115, v61
Mvav 100,156,156
s_waitcnt lgkmcnt(10)
v_lshlrev_b32 v56, 16, v50
v_add_f32 v116, v116, v56
Mvav 96,160,164
s_waitcnt lgkmcnt(9)
ds_read_b128 v[184:187], v12 offset:56320
v_lshlrev_b32 v56, 16, v54
Mvav 96,168,172
s_waitcnt lgkmcnt(9)
buffer_load_dwordx4 v0, s[48:51], s72 offen offset:1024 sc1 lds
Mvav 96,176,180
s_waitcnt lgkmcnt(8)
ds_read_b128 v[132:135], v12 offset:57344
v_add_f32 v116, v116, v56
Mvav 96,184,188
v_and_b32 v57, 0xffff0000, v50
v_add_f32 v117, v117, v57
Mvav 100,164,164
ds_read_b128 v[140:143], v12 offset:58368
v_and_b32 v57, 0xffff0000, v54
Mvav 100,172,172
buffer_load_dwordx4 v0, s[48:51], s72 offen offset:2048 sc1 lds
Mvav 100,180,180
ds_read_b128 v[148:151], v12 offset:59392
v_add_f32 v117, v117, v57
Mvav 100,188,188
s_waitcnt lgkmcnt(10)
buffer_load_dwordx2 v[30:31], v8, s[48:51], s33 offen sc1
Mvav 96,192,128
s_waitcnt lgkmcnt(9)
ds_read_b128 v[156:159], v12 offset:60416
v_lshlrev_b32 v60, 16, v51
Mvav 96,200,136
s_waitcnt lgkmcnt(9)
v_add_f32 v118, v118, v60
v_lshlrev_b32 v60, 16, v55
Mvav 96,208,144
s_waitcnt lgkmcnt(8)
ds_read_b128 v[164:167], v12 offset:61440
v_add_f32 v118, v118, v60
Mvav 96,216,152
buffer_load_dwordx4 v0, s[48:51], s72 offen offset:3072 sc1 lds
Mvav 100,196,128
ds_read_b128 v[172:175], v12 offset:62464
v_and_b32 v61, 0xffff0000, v51
Mvav 100,204,136
s_add_u32 m0, s70, 49152
v_add_f32 v119, v119, v61
v_and_b32 v61, 0xffff0000, v55
Mvav 100,212,144
s_add_u32 s72, s15, 14303232
ds_read_b128 v[180:183], v12 offset:63488
v_add_f32 v119, v119, v61
Mvav 100,220,152
s_waitcnt lgkmcnt(10)
buffer_load_dwordx4 v0, s[48:51], s72 offen sc1 lds
Mvav 96,224,160
s_waitcnt lgkmcnt(8)
ds_read_b128 v[188:191], v12 offset:64512
v_lshlrev_b32 v210, 16, v227
Mvav 96,232,168
s_barrier
Mvav 96,240,176
s_waitcnt lgkmcnt(8)
ds_read_b128 v[128:131], v14 offset:16384
v_and_b32 v211, 0xffff0000, v227
Mvav 96,248,184
s_waitcnt vmcnt(14)
v_lshlrev_b32 v120, 16, v32
v_and_b32 v121, 0xffff0000, v32
Mvav 100,228,160
ds_read_b128 v[136:139], v14 offset:17408
v_lshlrev_b32 v122, 16, v33
Mvav 100,236,168
buffer_load_dwordx4 v0, s[48:51], s72 offen offset:1024 sc1 lds
Mvav 100,244,176
ds_read_b128 v[144:147], v14 offset:18432
v_and_b32 v123, 0xffff0000, v33
Mvav 100,252,184
s_waitcnt lgkmcnt(10)
v_lshlrev_b32 v124, 16, v34
v_and_b32 v125, 0xffff0000, v34
Mvav 96,0,132
s_waitcnt lgkmcnt(9)
ds_read_b128 v[152:155], v14 offset:19456
v_lshlrev_b32 v126, 16, v35
Mvav 96,8,140
s_waitcnt lgkmcnt(9)
buffer_load_dwordx4 v0, s[48:51], s72 offen offset:2048 sc1 lds
Mvav 96,16,148
s_waitcnt lgkmcnt(8)
ds_read_b128 v[160:163], v14 offset:20480
v_and_b32 v127, 0xffff0000, v35
Mvav 96,24,156
s_cmp_lg_u32 s73, 0
s_cbranch_scc1 .Lrt1
.Lrt1_r:
Mvav 100,4,132
s_add_u32 s59, s63, 0
ds_read_b128 v[168:171], v14 offset:21504
v_lshlrev_b32 v58, 16, v233
Mvav 100,12,140
s_waitcnt vmcnt(13)
v_lshlrev_b32 v56, 16, v36
v_add_f32 v120, v120, v56
Mvav 100,20,148
ds_read_b128 v[176:179], v14 offset:22528
v_and_b32 v57, 0xffff0000, v36
Mvav 100,28,156
s_waitcnt lgkmcnt(10)
buffer_load_dwordx4 v0, s[48:51], s72 offen offset:3072 sc1 lds
Mvav 96,32,164
s_waitcnt lgkmcnt(9)
ds_read_b128 v[184:187], v14 offset:23552
v_add_f32 v121, v121, v57
Mvav 96,40,172
s_waitcnt lgkmcnt(9)
v_lshlrev_b32 v60, 16, v37
v_add_f32 v122, v122, v60
Mvav 96,48,180
s_waitcnt lgkmcnt(8)
ds_read_b128 v[132:135], v14 offset:24576
v_and_b32 v61, 0xffff0000, v37
Mvav 96,56,188
v_add_f32 v123, v123, v61
v_and_b32 v59, 0xffff0000, v233
Mvav 100,36,164
ds_read_b128 v[140:143], v14 offset:25600
v_lshlrev_b32 v24, 16, v234
Mvav 100,44,172
v_and_b32 v25, 0xffff0000, v234
v_lshlrev_b32 v26, 16, v235
Mvav 100,52,180
ds_read_b128 v[148:151], v14 offset:26624
v_and_b32 v27, 0xffff0000, v235
Mvav 100,60,188
s_waitcnt vmcnt(11) lgkmcnt(10)
v_lshlrev_b32 v56, 16, v40
v_add_f32 v120, v120, v56
Mvav 112,64,128
s_waitcnt lgkmcnt(9)
ds_read_b128 v[156:159], v14 offset:27648
v_and_b32 v57, 0xffff0000, v40
Mvav 112,72,136
s_waitcnt lgkmcnt(9)
v_add_f32 v121, v121, v57
v_lshlrev_b32 v60, 16, v41
Mvav 112,80,144
s_waitcnt lgkmcnt(8)
ds_read_b128 v[164:167], v14 offset:28672
v_add_f32 v122, v122, v60
Mvav 112,88,152
v_and_b32 v61, 0xffff0000, v41
v_add_f32 v123, v123, v61
Mvav 116,68,128
ds_read_b128 v[172:175], v14 offset:29696
v_lshlrev_b32 v56, 16, v38
Mvav 116,76,136
v_add_f32 v124, v124, v56
v_lshlrev_b32 v56, 16, v42
Mvav 116,84,144
ds_read_b128 v[180:183], v14 offset:30720
v_add_f32 v124, v124, v56
Mvav 116,92,152
s_waitcnt lgkmcnt(7)
v_and_b32 v57, 0xffff0000, v38
v_add_f32 v125, v125, v57
Mvav 112,96,160
s_lshl_b32 s58, s78, 17
ds_read_b128 v[188:191], v14 offset:31744
v_and_or_b32 v96, v96, v2, s24
Mvav 112,104,168
s_add_u32 s58, s58, s22
v_and_or_b32 v97, v97, v2, s25
v_and_or_b32 v98, v98, v2, s26
Mvav 112,112,176
s_add_u32 s58, s58, 98304
ds_read_b128 v[128:131], v0 offset:0
v_and_or_b32 v99, v99, v2, s27
Mvav 112,120,184
buffer_store_dwordx4 v[96:99], v0, s[48:51], s58 offen sc1
Mvav 116,100,160
ds_read_b128 v[136:139], v0 offset:1024
v_and_or_b32 v100, v100, v2, s24
Mvav 116,108,168
v_and_or_b32 v101, v101, v2, s25
v_and_or_b32 v102, v102, v2, s26
Mvav 116,116,176
ds_read_b128 v[144:147], v0 offset:2048
v_and_or_b32 v103, v103, v2, s27
Mvav 116,124,184
s_waitcnt lgkmcnt(10)
v_and_b32 v57, 0xffff0000, v42
v_add_f32 v125, v125, v57
Mvav 112,128,132
s_waitcnt lgkmcnt(9)
ds_read_b128 v[152:155], v0 offset:3072
v_lshlrev_b32 v60, 16, v39
Mvav 112,136,140
s_waitcnt lgkmcnt(9)
v_add_f32 v126, v126, v60
v_lshlrev_b32 v60, 16, v43
Mvav 112,144,148
s_waitcnt lgkmcnt(8)
ds_read_b128 v[160:163], v0 offset:4096
v_add_f32 v126, v126, v60
Mvav 112,152,156
v_and_b32 v61, 0xffff0000, v39
v_add_f32 v127, v127, v61
Mvav 116,132,132
ds_read_b128 v[168:171], v0 offset:5120
v_and_b32 v61, 0xffff0000, v43
Mvav 116,140,140
buffer_store_dwordx4 v[100:103], v0, s[48:51], s58 offen offset:1024 sc1
Mvav 116,148,148
ds_read_b128 v[176:179], v0 offset:6144
v_add_f32 v127, v127, v61
Mvav 116,156,156
s_waitcnt lgkmcnt(10)
v_add_f32 v88, v88, v120
v_add_f32 v89, v89, v121
Mvav 112,160,164
s_waitcnt lgkmcnt(9)
ds_read_b128 v[184:187], v0 offset:7168
v_add_f32 v90, v90, v122
Mvav 112,168,172
s_waitcnt lgkmcnt(9)
v_add_f32 v91, v91, v123
v_add_f32 v92, v92, v124
Mvav 112,176,180
s_waitcnt lgkmcnt(8)
ds_read_b128 v[132:135], v0 offset:8192
v_add_f32 v93, v93, v125
Mvav 112,184,188
v_add_f32 v94, v94, v126
v_add_f32 v95, v95, v127
Mvav 116,164,164
ds_read_b128 v[140:143], v0 offset:9216
v_cvt_pk_bf16_f32 v88, v88, v89
Mvav 116,172,172
ds_bpermute_b32 v216, v5, v88
v_cvt_pk_bf16_f32 v89, v90, v91
Mvav 116,180,180
ds_read_b128 v[148:151], v0 offset:10240
v_cvt_pk_bf16_f32 v90, v92, v93
Mvav 116,188,188
s_waitcnt lgkmcnt(11)
ds_bpermute_b32 v217, v5, v89
v_cvt_pk_bf16_f32 v91, v94, v95
Mvav 112,192,128
s_waitcnt lgkmcnt(11)
ds_read_b128 v[156:159], v0 offset:11264
v_lshlrev_b32 v56, 16, v232
Mvav 112,200,136
s_waitcnt lgkmcnt(11)
ds_bpermute_b32 v218, v5, v90
v_and_b32 v57, 0xffff0000, v232
Mvav 112,208,144
s_waitcnt lgkmcnt(11)
ds_read_b128 v[164:167], v0 offset:12288
Mvav 112,216,152
ds_bpermute_b32 v219, v5, v91
Mvav 116,196,128
ds_read_b128 v[172:175], v0 offset:13312
Mvav 116,204,136
s_waitcnt vmcnt(7)
v_xor_b32 v30, s74, v30
v_bitop3_b32 v30, v31, s75, v30 bitop3:0xbe
Mvav 116,212,144
s_waitcnt lgkmcnt(5)
ds_read_b128 v[180:183], v0 offset:14336
v_cmp_ne_u32 vcc, 0, v30
Mvav 116,220,152
s_and_b64 vcc, vcc, 0xf00
s_cbranch_vccnz .Lpf2
.Lpf2_r:
Mvav 112,224,160
s_waitcnt lgkmcnt(4)
s_lshl_b32 s62, s76, 13
ds_read_b128 v[188:191], v0 offset:15360
v_lshlrev_b32 v88, 16, v216
Mvav 112,232,168
s_barrier
Mvav 112,240,176
s_add_u32 s62, s62, s23
ds_read_b128 v[128:131], v0 offset:16384
v_and_b32 v89, 0xffff0000, v216
Mvav 112,248,184
s_add_u32 s62, s62, s32
v_lshlrev_b32 v90, 16, v217
v_and_b32 v91, 0xffff0000, v217
Mvav 116,228,160
s_add_u32 s62, s62, 65536
ds_read_b128 v[136:139], v0 offset:17408
v_lshlrev_b32 v92, 16, v218
Mvav 116,236,168
s_waitcnt lgkmcnt(5)
buffer_load_dwordx4 v[32:35], v0, s[48:51], s62 offen sc1
Mvav 116,244,176
s_lshl_b32 s62, s77, 13
ds_read_b128 v[144:147], v0 offset:18432
v_and_b32 v93, 0xffff0000, v218
Mvav 116,252,184
s_add_u32 s62, s62, s23
v_lshlrev_b32 v94, 16, v219
v_and_b32 v95, 0xffff0000, v219
Mvav 112,0,132
s_add_u32 s62, s62, s32
ds_read_b128 v[152:155], v0 offset:19456
v_fmac_f32 v200, v88, v64
Mvav 112,8,140
s_add_u32 s62, s62, 65536
v_fmac_f32 v201, v89, v65
v_fmac_f32 v202, v90, v66
Mvav 112,16,148
ds_read_b128 v[160:163], v0 offset:20480
)"
R"(
v_fmac_f32 v203, v91, v67
Mvav 112,24,156
v_fmac_f32 v208, v92, v68
v_fmac_f32 v209, v93, v69
Mvav 116,4,132
ds_read_b128 v[168:171], v0 offset:21504
v_fmac_f32 v210, v94, v70
Mvav 116,12,140
v_fmac_f32 v211, v95, v71
v_cvt_pk_bf16_f32 v216, v200, v201
Mvav 116,20,148
ds_read_b128 v[176:179], v0 offset:22528
v_cvt_pk_bf16_f32 v217, v202, v203
Mvav 116,28,156
s_waitcnt lgkmcnt(7)
buffer_load_dwordx4 v[36:39], v0, s[48:51], s62 offen sc1
Mvav 112,32,164
s_lshl_b32 s62, s78, 13
ds_read_b128 v[184:187], v0 offset:23552
v_cvt_pk_bf16_f32 v218, v208, v209
Mvav 112,40,172
s_add_u32 s62, s62, s23
v_cvt_pk_bf16_f32 v219, v210, v211
v_lshlrev_b32 v200, 16, v240
Mvav 112,48,180
s_add_u32 s62, s62, s32
ds_read_b128 v[132:135], v0 offset:24576
v_and_b32 v201, 0xffff0000, v240
Mvav 112,56,188
s_add_u32 s62, s62, 65536
v_lshlrev_b32 v202, 16, v241
v_and_b32 v203, 0xffff0000, v241
Mvav 116,36,164
ds_read_b128 v[140:143], v0 offset:25600
v_lshlrev_b32 v208, 16, v242
Mvav 116,44,172
v_and_b32 v209, 0xffff0000, v242
v_lshlrev_b32 v210, 16, v243
Mvav 116,52,180
ds_read_b128 v[148:151], v0 offset:26624
v_and_b32 v211, 0xffff0000, v243
Mvav 116,60,188
s_waitcnt lgkmcnt(10)
Zvav 120,64,128
s_waitcnt lgkmcnt(8)
s_lshl_b32 s4, s76, 13
ds_read_b128 v[156:159], v0 offset:27648
Mvav 120,72,136
s_add_u32 s4, s4, s23
buffer_load_dwordx4 v[40:43], v0, s[48:51], s62 offen sc1
Mvav 120,80,144
s_waitcnt lgkmcnt(8)
s_add_u32 s4, s4, 0x18000
ds_read_b128 v[164:167], v0 offset:28672
Mvav 120,88,152
s_lshl_b32 s5, s77, 13
Zvav 124,68,128
s_add_u32 s5, s5, s23
ds_read_b128 v[172:175], v0 offset:29696
Mvav 124,76,136
s_add_u32 s5, s5, 0x18000
Mvav 124,84,144
s_lshl_b32 s6, s78, 13
ds_read_b128 v[180:183], v0 offset:30720
Mvav 124,92,152
s_waitcnt lgkmcnt(9)
s_add_u32 s6, s6, s23
Mvav 120,96,160
s_add_u32 s6, s6, 0x18000
ds_read_b128 v[188:191], v0 offset:31744
v_cvt_pk_bf16_f32 v112, v112, v113
Mvav 120,104,168
s_waitcnt lgkmcnt(9)
ds_bpermute_b32 v192, v5, v112
v_cvt_pk_bf16_f32 v113, v114, v115
Mvav 120,112,176
s_waitcnt lgkmcnt(9)
ds_read_b128 v[128:131], v12 offset:32768
v_cvt_pk_bf16_f32 v114, v116, v117
Mvav 120,120,184
buffer_store_dwordx4 v[216:219], v4, s[52:55], s59 offen
Mvav 124,100,160
ds_read_b128 v[136:139], v12 offset:33792
v_cvt_pk_bf16_f32 v115, v118, v119
Mvav 124,108,168
s_add_u32 s59, s63, 1048576
ds_bpermute_b32 v193, v5, v113
Mvav 124,116,176
ds_read_b128 v[144:147], v12 offset:34816
Mvav 124,124,184
s_waitcnt lgkmcnt(12)
ds_bpermute_b32 v194, v5, v114
Mvav 120,128,132
s_waitcnt lgkmcnt(12)
ds_read_b128 v[152:155], v12 offset:35840
Mvav 120,136,140
s_waitcnt lgkmcnt(6)
ds_bpermute_b32 v195, v5, v115
v_lshlrev_b32 v112, 16, v192
Mvav 120,144,148
ds_read_b128 v[160:163], v12 offset:36864
v_and_b32 v113, 0xffff0000, v192
Mvav 120,152,156
v_fmac_f32 v56, v112, v72
v_fmac_f32 v57, v113, v73
Mvav 124,132,132
ds_read_b128 v[168:171], v12 offset:37888
v_cvt_pk_bf16_f32 v192, v56, v57
Mvav 124,140,140
s_waitcnt lgkmcnt(6)
v_lshlrev_b32 v114, 16, v193
v_and_b32 v115, 0xffff0000, v193
Mvav 124,148,148
ds_read_b128 v[176:179], v12 offset:38912
v_fmac_f32 v58, v114, v74
Mvav 124,156,156
s_waitcnt lgkmcnt(5)
v_lshlrev_b32 v116, 16, v194
v_and_b32 v117, 0xffff0000, v194
Mvav 120,160,164
ds_read_b128 v[184:187], v12 offset:39936
v_fmac_f32 v59, v115, v75
Mvav 120,168,172
s_waitcnt lgkmcnt(4)
v_lshlrev_b32 v118, 16, v195
v_and_b32 v119, 0xffff0000, v195
Mvav 120,176,180
ds_read_b128 v[132:135], v12 offset:40960
v_fmac_f32 v24, v116, v76
Mvav 120,184,188
v_fmac_f32 v25, v117, v77
v_fmac_f32 v26, v118, v78
Mvav 124,164,164
ds_read_b128 v[140:143], v12 offset:41984
v_fmac_f32 v27, v119, v79
Mvav 124,172,172
v_cvt_pk_bf16_f32 v193, v58, v59
v_cvt_pk_bf16_f32 v194, v24, v25
Mvav 124,180,180
ds_read_b128 v[148:151], v12 offset:43008
v_cvt_pk_bf16_f32 v195, v26, v27
Mvav 124,188,188
buffer_store_dwordx4 v[192:195], v4, s[52:55], s59 offen
Mvav 120,192,128
ds_read_b128 v[156:159], v12 offset:44032
v_lshlrev_b32 v56, 16, v248
Mvav 120,200,136
s_add_u32 s59, s63, 2097152
v_and_b32 v57, 0xffff0000, v248
v_lshlrev_b32 v58, 16, v249
Mvav 120,208,144
ds_read_b128 v[164:167], v12 offset:45056
v_and_b32 v59, 0xffff0000, v249
Mvav 120,216,152
v_lshlrev_b32 v24, 16, v250
v_and_b32 v25, 0xffff0000, v250
Mvav 124,196,128
ds_read_b128 v[172:175], v12 offset:46080
v_lshlrev_b32 v26, 16, v251
Mvav 124,204,136
v_and_b32 v27, 0xffff0000, v251
Mvav 124,212,144
ds_read_b128 v[180:183], v12 offset:47104
Mvav 124,220,152
s_waitcnt lgkmcnt(10)
Mvav 120,224,160
s_waitcnt lgkmcnt(9)
ds_read_b128 v[188:191], v12 offset:48128
Mvav 120,232,168
s_waitcnt vmcnt(7) lgkmcnt(9)
s_barrier
Mvav 120,240,176
s_waitcnt lgkmcnt(8)
ds_read_b128 v[128:131], v14 offset:0
Mvav 120,248,184
Mvav 124,228,160
ds_read_b128 v[136:139], v14 offset:1024
Mvav 124,236,168
Mvav 124,244,176
ds_read_b128 v[144:147], v14 offset:2048
Mvav 124,252,184
s_waitcnt lgkmcnt(10)
Mvav 120,0,132
s_waitcnt lgkmcnt(9)
ds_read_b128 v[152:155], v14 offset:3072
Mvav 120,8,140
s_waitcnt lgkmcnt(9)
Mvav 120,16,148
s_waitcnt lgkmcnt(8)
ds_read_b128 v[160:163], v14 offset:4096
Mvav 120,24,156
s_waitcnt vmcnt(4)
v_lshlrev_b32 v44, 16, v32
v_and_b32 v45, 0xffff0000, v32
Mvav 124,4,132
ds_read_b128 v[168:171], v14 offset:5120
v_lshlrev_b32 v46, 16, v33
Mvav 124,12,140
v_and_b32 v47, 0xffff0000, v33
v_lshlrev_b32 v48, 16, v34
Mvav 124,20,148
ds_read_b128 v[176:179], v14 offset:6144
v_and_b32 v49, 0xffff0000, v34
Mvav 124,28,156
s_waitcnt lgkmcnt(10)
v_lshlrev_b32 v50, 16, v35
v_and_b32 v51, 0xffff0000, v35
Mvav 120,32,164
s_waitcnt lgkmcnt(9)
ds_read_b128 v[184:187], v14 offset:7168
Mvav 120,40,172
s_waitcnt lgkmcnt(9)
Mvav 120,48,180
s_waitcnt lgkmcnt(8)
ds_read_b128 v[132:135], v14 offset:8192
Mvav 120,56,188
Mvav 124,36,164
ds_read_b128 v[140:143], v14 offset:9216
Mvav 124,44,172
s_waitcnt vmcnt(3)
v_lshlrev_b32 v52, 16, v36
v_add_f32 v44, v44, v52
Mvav 124,52,180
ds_read_b128 v[148:151], v14 offset:10240
v_and_b32 v53, 0xffff0000, v36
Mvav 124,60,188
s_waitcnt lgkmcnt(10)
v_add_f32 v45, v45, v53
v_lshlrev_b32 v54, 16, v37
Zvav 96,64,128
s_waitcnt lgkmcnt(9)
ds_read_b128 v[156:159], v14 offset:11264
v_add_f32 v46, v46, v54
Mvav 96,72,136
s_waitcnt lgkmcnt(9)
v_and_b32 v55, 0xffff0000, v37
v_add_f32 v47, v47, v55
Mvav 96,80,144
s_waitcnt lgkmcnt(8)
ds_read_b128 v[164:167], v14 offset:12288
Mvav 96,88,152
Zvav 100,68,128
ds_read_b128 v[172:175], v14 offset:13312
Mvav 100,76,136
buffer_load_dwordx4 v[196:199], v0, s[48:51], s4 offen sc1
Mvav 100,84,144
ds_read_b128 v[180:183], v14 offset:14336
Mvav 100,92,152
s_waitcnt lgkmcnt(10)
buffer_load_dwordx4 v[204:207], v0, s[48:51], s4 offen offset:1024 sc1
Mvav 96,96,160
s_waitcnt vmcnt(4) lgkmcnt(9)
ds_read_b128 v[188:191], v14 offset:15360
v_lshlrev_b32 v52, 16, v40
Mvav 96,104,168
s_waitcnt lgkmcnt(9)
buffer_load_dwordx4 v[212:215], v0, s[48:51], s5 offen sc1
Mvav 96,112,176
s_waitcnt lgkmcnt(8)
ds_read_b128 v[128:131], v0 offset:49152
v_add_f32 v44, v44, v52
Mvav 96,120,184
buffer_load_dwordx4 v[220:223], v0, s[48:51], s5 offen offset:1024 sc1
Mvav 100,100,160
ds_read_b128 v[136:139], v0 offset:50176
v_and_b32 v53, 0xffff0000, v40
Mvav 100,108,168
buffer_load_dwordx4 v[228:231], v0, s[48:51], s6 offen sc1
Mvav 100,116,176
ds_read_b128 v[144:147], v0 offset:51200
v_add_f32 v45, v45, v53
Mvav 100,124,184
s_waitcnt lgkmcnt(10)
buffer_load_dwordx4 v[236:239], v0, s[48:51], s6 offen offset:1024 sc1
Mvav 96,128,132
s_waitcnt lgkmcnt(9)
ds_read_b128 v[152:155], v0 offset:52224
v_lshlrev_b32 v54, 16, v41
Mvav 96,136,140
s_waitcnt lgkmcnt(9)
v_add_f32 v46, v46, v54
v_and_b32 v55, 0xffff0000, v41
Mvav 96,144,148
s_waitcnt lgkmcnt(8)
ds_read_b128 v[160:163], v0 offset:53248
v_add_f32 v47, v47, v55
Mvav 96,152,156
v_lshlrev_b32 v52, 16, v38
v_add_f32 v48, v48, v52
Mvav 100,132,132
ds_read_b128 v[168:171], v0 offset:54272
v_lshlrev_b32 v52, 16, v42
Mvav 100,140,140
v_add_f32 v48, v48, v52
v_and_b32 v53, 0xffff0000, v38
Mvav 100,148,148
ds_read_b128 v[176:179], v0 offset:55296
v_add_f32 v49, v49, v53
Mvav 100,156,156
s_waitcnt lgkmcnt(10)
v_and_b32 v53, 0xffff0000, v42
v_add_f32 v49, v49, v53
Mvav 96,160,164
s_waitcnt lgkmcnt(9)
ds_read_b128 v[184:187], v0 offset:56320
v_lshlrev_b32 v54, 16, v39
Mvav 96,168,172
s_waitcnt lgkmcnt(9)
v_add_f32 v50, v50, v54
v_lshlrev_b32 v54, 16, v43
Mvav 96,176,180
s_waitcnt lgkmcnt(8)
ds_read_b128 v[132:135], v0 offset:57344
v_add_f32 v50, v50, v54
Mvav 96,184,188
v_and_b32 v55, 0xffff0000, v39
v_add_f32 v51, v51, v55
Mvav 100,164,164
ds_read_b128 v[140:143], v0 offset:58368
v_and_b32 v55, 0xffff0000, v43
Mvav 100,172,172
v_add_f32 v51, v51, v55
v_add_f32 v120, v120, v44
Mvav 100,180,180
ds_read_b128 v[148:151], v0 offset:59392
v_add_f32 v121, v121, v45
Mvav 100,188,188
s_waitcnt lgkmcnt(10)
v_add_f32 v122, v122, v46
v_add_f32 v123, v123, v47
Mvav 96,192,128
s_waitcnt lgkmcnt(9)
ds_read_b128 v[156:159], v0 offset:60416
v_add_f32 v124, v124, v48
Mvav 96,200,136
s_waitcnt lgkmcnt(9)
v_add_f32 v125, v125, v49
v_add_f32 v126, v126, v50
Mvav 96,208,144
s_waitcnt lgkmcnt(8)
ds_read_b128 v[164:167], v0 offset:61440
v_add_f32 v127, v127, v51
Mvav 96,216,152
v_cvt_pk_bf16_f32 v120, v120, v121
v_cvt_pk_bf16_f32 v121, v122, v123
Mvav 100,196,128
ds_read_b128 v[172:175], v0 offset:62464
v_cvt_pk_bf16_f32 v122, v124, v125
Mvav 100,204,136
ds_bpermute_b32 v216, v5, v120
v_cvt_pk_bf16_f32 v123, v126, v127
Mvav 100,212,144
ds_read_b128 v[180:183], v0 offset:63488
Mvav 100,220,152
s_waitcnt lgkmcnt(11)
ds_bpermute_b32 v217, v5, v121
Mvav 96,224,160
s_waitcnt lgkmcnt(11)
ds_read_b128 v[188:191], v0 offset:64512
Mvav 96,232,168
s_waitcnt lgkmcnt(11)
ds_bpermute_b32 v218, v5, v122
Mvav 96,240,176
s_waitcnt lgkmcnt(11)
ds_bpermute_b32 v219, v5, v123
Mvav 96,248,184
Mvav 100,228,160
Mvav 100,236,168
s_waitcnt lgkmcnt(5)
v_lshlrev_b32 v120, 16, v216
v_and_b32 v121, 0xffff0000, v216
Mvav 100,244,176
v_fmac_f32 v200, v120, v80
v_fmac_f32 v201, v121, v81
Mvav 100,252,184
s_waitcnt lgkmcnt(3)
v_lshlrev_b32 v122, 16, v217
v_and_b32 v123, 0xffff0000, v217
Mvav 96,0,132
v_fmac_f32 v202, v122, v82
v_fmac_f32 v203, v123, v83
Mvav 96,8,140
s_waitcnt lgkmcnt(1)
v_lshlrev_b32 v124, 16, v218
v_and_b32 v125, 0xffff0000, v218
Mvav 96,16,148
s_waitcnt lgkmcnt(0)
v_lshlrev_b32 v126, 16, v219
v_and_b32 v127, 0xffff0000, v219
Mvav 96,24,156
v_fmac_f32 v208, v124, v84
v_fmac_f32 v209, v125, v85
Mvav 100,4,132
v_fmac_f32 v210, v126, v86
v_fmac_f32 v211, v127, v87
Mvav 100,12,140
v_cvt_pk_bf16_f32 v216, v200, v201
v_cvt_pk_bf16_f32 v217, v202, v203
Mvav 100,20,148
v_cvt_pk_bf16_f32 v218, v208, v209
v_cvt_pk_bf16_f32 v219, v210, v211
Mvav 100,28,156
buffer_store_dwordx4 v[216:219], v4, s[52:55], s59 offen
Mvav 96,32,164
Mvav 96,40,172
s_add_u32 s59, s63, 3145728
Mvav 96,48,180
Mvav 96,56,188
Mvav 100,36,164
Mvav 100,44,172
Mvav 100,52,180
Mvav 100,60,188
s_waitcnt vmcnt(6)
v_xor_b32 v196, s24, v196
v_xor_b32 v197, s25, v197
v_xor_b32 v198, s26, v198
v_xor_b32 v199, s27, v199
s_waitcnt vmcnt(5)
v_xor_b32 v204, s24, v204
v_xor_b32 v205, s25, v205
v_xor_b32 v206, s26, v206
v_xor_b32 v207, s27, v207
s_waitcnt vmcnt(4)
v_xor_b32 v212, s24, v212
v_xor_b32 v213, s25, v213
v_xor_b32 v214, s26, v214
v_xor_b32 v215, s27, v215
s_waitcnt vmcnt(3)
v_xor_b32 v220, s24, v220
v_xor_b32 v221, s25, v221
v_xor_b32 v222, s26, v222
v_xor_b32 v223, s27, v223
s_waitcnt vmcnt(2)
v_xor_b32 v228, s24, v228
v_xor_b32 v229, s25, v229
v_xor_b32 v230, s26, v230
v_xor_b32 v231, s27, v231
s_waitcnt vmcnt(1)
v_xor_b32 v236, s24, v236
v_xor_b32 v237, s25, v237
v_xor_b32 v238, s26, v238
v_xor_b32 v239, s27, v239
v_or3_b32 v9, v196, v197, v198
v_or3_b32 v9, v9, v199, v204
v_or3_b32 v9, v9, v205, v206
v_or3_b32 v9, v9, v207, v212
v_or3_b32 v9, v9, v213, v214
v_or3_b32 v9, v9, v215, v220
v_or3_b32 v9, v9, v221, v222
v_or3_b32 v9, v9, v223, v228
v_or3_b32 v9, v9, v229, v230
v_or3_b32 v9, v9, v231, v236
v_or3_b32 v9, v9, v237, v238
v_or_b32 v9, v9, v239
v_and_b32 v9, 0xff, v9
v_cmp_ne_u32 vcc, 0, v9
s_nop 0
s_cbranch_vccnz .Lpc3
.Lpc3_r:
v_add_f32 v196, v196, v212
v_add_f32 v197, v197, v213
v_add_f32 v198, v198, v214
)"
R"(
v_add_f32 v199, v199, v215
v_add_f32 v204, v204, v220
v_add_f32 v205, v205, v221
v_add_f32 v206, v206, v222
v_add_f32 v207, v207, v223
v_add_f32 v196, v196, v228
v_add_f32 v197, v197, v229
v_add_f32 v198, v198, v230
v_add_f32 v199, v199, v231
v_add_f32 v204, v204, v236
v_add_f32 v205, v205, v237
v_add_f32 v206, v206, v238
v_add_f32 v207, v207, v239
v_add_f32 v96, v96, v196
v_add_f32 v97, v97, v197
v_add_f32 v98, v98, v198
v_add_f32 v99, v99, v199
v_add_f32 v100, v100, v204
v_add_f32 v101, v101, v205
v_add_f32 v102, v102, v206
v_add_f32 v103, v103, v207
v_cvt_pk_bf16_f32 v96, v96, v97
v_cvt_pk_bf16_f32 v97, v98, v99
v_cvt_pk_bf16_f32 v98, v100, v101
v_cvt_pk_bf16_f32 v99, v102, v103
ds_bpermute_b32 v192, v5, v96
ds_bpermute_b32 v193, v5, v97
ds_bpermute_b32 v194, v5, v98
ds_bpermute_b32 v195, v5, v99
s_waitcnt lgkmcnt(3)
v_lshlrev_b32 v96, 16, v192
v_and_b32 v97, 0xffff0000, v192
s_waitcnt lgkmcnt(2)
v_lshlrev_b32 v98, 16, v193
v_and_b32 v99, 0xffff0000, v193
s_waitcnt lgkmcnt(1)
v_lshlrev_b32 v100, 16, v194
v_and_b32 v101, 0xffff0000, v194
s_waitcnt lgkmcnt(0)
v_lshlrev_b32 v102, 16, v195
v_and_b32 v103, 0xffff0000, v195
v_fmac_f32 v56, v96, v16
v_fmac_f32 v57, v97, v17
v_fmac_f32 v58, v98, v18
v_fmac_f32 v59, v99, v19
v_fmac_f32 v24, v100, v20
v_fmac_f32 v25, v101, v21
v_fmac_f32 v26, v102, v22
v_fmac_f32 v27, v103, v23
v_cvt_pk_bf16_f32 v192, v56, v57
v_cvt_pk_bf16_f32 v193, v58, v59
v_cvt_pk_bf16_f32 v194, v24, v25
v_cvt_pk_bf16_f32 v195, v26, v27
buffer_store_dwordx4 v[192:195], v4, s[52:55], s59 offen
s_endpgm
.Lbepi:
s_mov_b32 s97, s96
v_mov_b32 v132, s74
v_mov_b32 v133, s75
v_rcp_f32 v64, v114
v_rcp_f32 v65, v115
s_add_u32 s84, s94, 4096
v_accvgpr_read_b32 v66, a64
v_accvgpr_read_b32 v67, a65
v_mul_f32 v66, v66, v64
v_mul_f32 v67, v67, v64
v_cvt_pk_bf16_f32 v68, v66, v67
v_accvgpr_read_b32 v66, a66
v_accvgpr_read_b32 v67, a67
v_mul_f32 v66, v66, v64
v_mul_f32 v67, v67, v64
v_cvt_pk_bf16_f32 v69, v66, v67
v_accvgpr_read_b32 v66, a72
v_accvgpr_read_b32 v67, a73
v_mul_f32 v66, v66, v64
v_mul_f32 v67, v67, v64
v_cvt_pk_bf16_f32 v70, v66, v67
v_accvgpr_read_b32 v66, a74
v_accvgpr_read_b32 v67, a75
v_mul_f32 v66, v66, v64
v_mul_f32 v67, v67, v64
v_cvt_pk_bf16_f32 v71, v66, v67
buffer_store_dwordx4 v[68:71], v0, s[48:51], s94 offen sc1
v_accvgpr_read_b32 v66, a68
v_accvgpr_read_b32 v67, a69
v_mul_f32 v66, v66, v65
v_mul_f32 v67, v67, v65
v_cvt_pk_bf16_f32 v72, v66, v67
v_accvgpr_read_b32 v66, a70
v_accvgpr_read_b32 v67, a71
v_mul_f32 v66, v66, v65
v_mul_f32 v67, v67, v65
v_cvt_pk_bf16_f32 v73, v66, v67
v_accvgpr_read_b32 v66, a76
v_accvgpr_read_b32 v67, a77
v_mul_f32 v66, v66, v65
v_mul_f32 v67, v67, v65
v_cvt_pk_bf16_f32 v74, v66, v67
v_accvgpr_read_b32 v66, a78
v_accvgpr_read_b32 v67, a79
v_mul_f32 v66, v66, v65
v_mul_f32 v67, v67, v65
v_cvt_pk_bf16_f32 v75, v66, v67
buffer_store_dwordx4 v[72:75], v0, s[48:51], s84 offen sc1
v_accvgpr_read_b32 v66, a80
v_accvgpr_read_b32 v67, a81
v_mul_f32 v66, v66, v64
v_mul_f32 v67, v67, v64
v_cvt_pk_bf16_f32 v68, v66, v67
v_accvgpr_read_b32 v66, a82
v_accvgpr_read_b32 v67, a83
v_mul_f32 v66, v66, v64
v_mul_f32 v67, v67, v64
v_cvt_pk_bf16_f32 v69, v66, v67
v_accvgpr_read_b32 v66, a88
v_accvgpr_read_b32 v67, a89
v_mul_f32 v66, v66, v64
v_mul_f32 v67, v67, v64
v_cvt_pk_bf16_f32 v70, v66, v67
v_accvgpr_read_b32 v66, a90
v_accvgpr_read_b32 v67, a91
v_mul_f32 v66, v66, v64
v_mul_f32 v67, v67, v64
v_cvt_pk_bf16_f32 v71, v66, v67
buffer_store_dwordx4 v[68:71], v0, s[48:51], s94 offen offset:1024 sc1
v_accvgpr_read_b32 v66, a84
v_accvgpr_read_b32 v67, a85
v_mul_f32 v66, v66, v65
v_mul_f32 v67, v67, v65
v_cvt_pk_bf16_f32 v72, v66, v67
v_accvgpr_read_b32 v66, a86
v_accvgpr_read_b32 v67, a87
v_mul_f32 v66, v66, v65
v_mul_f32 v67, v67, v65
v_cvt_pk_bf16_f32 v73, v66, v67
v_accvgpr_read_b32 v66, a92
v_accvgpr_read_b32 v67, a93
v_mul_f32 v66, v66, v65
v_mul_f32 v67, v67, v65
v_cvt_pk_bf16_f32 v74, v66, v67
v_accvgpr_read_b32 v66, a94
v_accvgpr_read_b32 v67, a95
v_mul_f32 v66, v66, v65
v_mul_f32 v67, v67, v65
v_cvt_pk_bf16_f32 v75, v66, v67
buffer_store_dwordx4 v[72:75], v0, s[48:51], s84 offen offset:1024 sc1
v_accvgpr_read_b32 v66, a96
v_accvgpr_read_b32 v67, a97
v_mul_f32 v66, v66, v64
v_mul_f32 v67, v67, v64
v_cvt_pk_bf16_f32 v68, v66, v67
v_accvgpr_read_b32 v66, a98
v_accvgpr_read_b32 v67, a99
v_mul_f32 v66, v66, v64
v_mul_f32 v67, v67, v64
v_cvt_pk_bf16_f32 v69, v66, v67
v_accvgpr_read_b32 v66, a104
v_accvgpr_read_b32 v67, a105
v_mul_f32 v66, v66, v64
v_mul_f32 v67, v67, v64
v_cvt_pk_bf16_f32 v70, v66, v67
v_accvgpr_read_b32 v66, a106
v_accvgpr_read_b32 v67, a107
v_mul_f32 v66, v66, v64
v_mul_f32 v67, v67, v64
v_cvt_pk_bf16_f32 v71, v66, v67
buffer_store_dwordx4 v[68:71], v0, s[48:51], s94 offen offset:2048 sc1
v_accvgpr_read_b32 v66, a100
v_accvgpr_read_b32 v67, a101
v_mul_f32 v66, v66, v65
v_mul_f32 v67, v67, v65
v_cvt_pk_bf16_f32 v72, v66, v67
v_accvgpr_read_b32 v66, a102
v_accvgpr_read_b32 v67, a103
v_mul_f32 v66, v66, v65
v_mul_f32 v67, v67, v65
v_cvt_pk_bf16_f32 v73, v66, v67
v_accvgpr_read_b32 v66, a108
v_accvgpr_read_b32 v67, a109
v_mul_f32 v66, v66, v65
v_mul_f32 v67, v67, v65
v_cvt_pk_bf16_f32 v74, v66, v67
v_accvgpr_read_b32 v66, a110
v_accvgpr_read_b32 v67, a111
v_mul_f32 v66, v66, v65
v_mul_f32 v67, v67, v65
v_cvt_pk_bf16_f32 v75, v66, v67
buffer_store_dwordx4 v[72:75], v0, s[48:51], s84 offen offset:2048 sc1
v_accvgpr_read_b32 v66, a112
v_accvgpr_read_b32 v67, a113
v_mul_f32 v66, v66, v64
v_mul_f32 v67, v67, v64
v_cvt_pk_bf16_f32 v68, v66, v67
v_accvgpr_read_b32 v66, a114
v_accvgpr_read_b32 v67, a115
v_mul_f32 v66, v66, v64
v_mul_f32 v67, v67, v64
v_cvt_pk_bf16_f32 v69, v66, v67
v_accvgpr_read_b32 v66, a120
v_accvgpr_read_b32 v67, a121
v_mul_f32 v66, v66, v64
v_mul_f32 v67, v67, v64
v_cvt_pk_bf16_f32 v70, v66, v67
v_accvgpr_read_b32 v66, a122
v_accvgpr_read_b32 v67, a123
v_mul_f32 v66, v66, v64
v_mul_f32 v67, v67, v64
v_cvt_pk_bf16_f32 v71, v66, v67
buffer_store_dwordx4 v[68:71], v0, s[48:51], s94 offen offset:3072 sc1
v_accvgpr_read_b32 v66, a116
v_accvgpr_read_b32 v67, a117
v_mul_f32 v66, v66, v65
v_mul_f32 v67, v67, v65
v_cvt_pk_bf16_f32 v72, v66, v67
v_accvgpr_read_b32 v66, a118
v_accvgpr_read_b32 v67, a119
v_mul_f32 v66, v66, v65
v_mul_f32 v67, v67, v65
v_cvt_pk_bf16_f32 v73, v66, v67
v_accvgpr_read_b32 v66, a124
v_accvgpr_read_b32 v67, a125
v_mul_f32 v66, v66, v65
v_mul_f32 v67, v67, v65
v_cvt_pk_bf16_f32 v74, v66, v67
v_accvgpr_read_b32 v66, a126
v_accvgpr_read_b32 v67, a127
v_mul_f32 v66, v66, v65
v_mul_f32 v67, v67, v65
v_cvt_pk_bf16_f32 v75, v66, v67
buffer_store_dwordx4 v[72:75], v0, s[48:51], s84 offen offset:3072 sc1
s_setpc_b64 s[16:17]
.Lsb:
s_waitcnt vmcnt(15)
ds_read_b64_tr_b16 v[32:33], v24 offset:0
v_and_b32 v27, 16, v0
ds_read_b64_tr_b16 v[34:35], v24 offset:256
v_lshrrev_b32 v27, v27, s74
ds_read_b64_tr_b16 v[36:37], v24 offset:4096
v_lshrrev_b32 v120, 8, v27
ds_read_b64_tr_b16 v[38:39], v24 offset:4352
v_lshrrev_b32 v121, 1, v0
ds_read_b64_tr_b16 v[40:41], v24 offset:32
v_mov_b32 v124, 0xff
ds_read_b64_tr_b16 v[42:43], v24 offset:288
s_lshr_b32 s59, s60, 3
ds_read_b64_tr_b16 v[44:45], v24 offset:4128
s_lshl2_add_u32 s59, s91, s59
ds_read_b64_tr_b16 v[46:47], v24 offset:4384
s_lshl2_add_u32 s59, s59, s57
ds_read_b64_tr_b16 v[48:49], v24 offset:512
s_lshl_b32 s59, s59, 10
ds_read_b64_tr_b16 v[50:51], v24 offset:768
s_add_u32 s59, s59, 1048576
ds_read_b64_tr_b16 v[52:53], v24 offset:4608
s_cmp_eq_u32 s61, 0
ds_read_b64_tr_b16 v[54:55], v24 offset:4864
s_cselect_b32 s58, 0x80000000, s59
ds_read_b64_tr_b16 v[56:57], v24 offset:544
s_mov_b32 vcc_lo, 0xffff0000
ds_read_b64_tr_b16 v[58:59], v24 offset:800
s_mov_b32 vcc_hi, 0xffff0000
ds_read_b64_tr_b16 v[60:61], v24 offset:4640
s_waitcnt lgkmcnt(14)
ds_read_b64_tr_b16 v[62:63], v24 offset:4896
s_waitcnt lgkmcnt(14)
Zvvv 196,16,32
s_mov_b64 s[36:37], s[40:41]
s_lshr_b32 s59, s60, 3
s_waitcnt lgkmcnt(12)
Mvvv 196,20,36
s_lshl2_add_u32 s59, s59, s57
s_mul_i32 s59, s59, 458752
s_waitcnt lgkmcnt(10)
Zvvv 204,16,40
s_lshl_b32 s68, s91, 7
s_add_u32 s68, s68, s59
s_waitcnt lgkmcnt(8)
Mvvv 204,20,44
s_add_u32 s71, s69, 131072
s_add_u32 s73, s99, 8
s_waitcnt lgkmcnt(6)
Zvvv 212,16,48
s_mov_b32 s62, 196608
s_mov_b32 s63, 57344
s_waitcnt lgkmcnt(4)
Mvvv 212,20,52
s_mov_b32 s64, 1024
s_cmp_eq_u32 s61, 0
s_waitcnt lgkmcnt(2)
Zvvv 220,16,56
s_waitcnt lgkmcnt(0)
Mvvv 220,20,60
s_cselect_b32 s73, 0, s73
v_cndmask_b32 v196, v196, v204, vcc
v_cndmask_b32 v197, v197, v205, vcc
s_nop 4
v_cndmask_b32 v212, v212, v220, vcc
v_cndmask_b32 v213, v213, v221, vcc
s_mov_b64 vcc, 0xffffffff
v_cndmask_b32 v122, v212, v196, vcc
v_cndmask_b32 v123, v213, v197, vcc
v_bfi_b32 v122, v124, v27, v122
v_bfi_b32 v123, v124, v120, v123
buffer_store_dwordx2 v[122:123], v121, s[48:51], s58 offen sc1
s_swappc_b64 s[6:7], s[16:17]
s_waitcnt vmcnt(15)
ds_read_b64_tr_b16 v[32:33], v24 offset:0
v_and_b32 v27, 16, v0
ds_read_b64_tr_b16 v[34:35], v24 offset:256
v_lshrrev_b32 v27, v27, s74
ds_read_b64_tr_b16 v[36:37], v24 offset:4096
v_lshrrev_b32 v120, 8, v27
ds_read_b64_tr_b16 v[38:39], v24 offset:4352
v_lshrrev_b32 v121, 1, v0
ds_read_b64_tr_b16 v[40:41], v24 offset:32
v_mov_b32 v124, 0xff
ds_read_b64_tr_b16 v[42:43], v24 offset:288
s_lshr_b32 s59, s60, 3
ds_read_b64_tr_b16 v[44:45], v24 offset:4128
s_lshl2_add_u32 s59, s91, s59
ds_read_b64_tr_b16 v[46:47], v24 offset:4384
s_lshl2_add_u32 s59, s59, s57
ds_read_b64_tr_b16 v[48:49], v24 offset:512
s_lshl_b32 s59, s59, 10
ds_read_b64_tr_b16 v[50:51], v24 offset:768
s_add_u32 s59, s59, 1049088
ds_read_b64_tr_b16 v[52:53], v24 offset:4608
s_cmp_eq_u32 s61, 0
ds_read_b64_tr_b16 v[54:55], v24 offset:4864
s_cselect_b32 s58, 0x80000000, s59
ds_read_b64_tr_b16 v[56:57], v24 offset:544
s_mov_b32 vcc_lo, 0xffff0000
ds_read_b64_tr_b16 v[58:59], v24 offset:800
s_mov_b32 vcc_hi, 0xffff0000
ds_read_b64_tr_b16 v[60:61], v24 offset:4640
s_waitcnt lgkmcnt(14)
ds_read_b64_tr_b16 v[62:63], v24 offset:4896
s_waitcnt lgkmcnt(14)
Zvvv 196,16,32
v_bfe_u32 v25, v0, 6, 3
v_lshrrev_b32 v126, 9, v0
s_waitcnt lgkmcnt(12)
Mvvv 196,20,36
v_lshlrev_b32 v126, 6, v126
v_lshl_add_u32 v25, v25, 11, v126
s_waitcnt lgkmcnt(10)
Zvvv 204,16,40
v_and_b32 v126, 48, v0
v_add_u32 v25, v25, v126
s_waitcnt lgkmcnt(8)
Mvvv 204,20,44
s_mov_b64 s[36:37], s[28:29]
s_mov_b32 s38, s30
s_waitcnt lgkmcnt(6)
Zvvv 212,16,48
s_lshl3_add_u32 s68, s70, s67
s_add_u32 s71, s69, 131072
s_waitcnt lgkmcnt(4)
Mvvv 212,20,52
s_add_u32 s73, s99, 4
s_mov_b32 s62, 1024
s_waitcnt lgkmcnt(2)
Zvvv 220,16,56
s_mov_b32 s63, 16384
s_mov_b32 s64, 2048
s_waitcnt lgkmcnt(0)
Mvvv 220,20,60
s_lshr_b32 s83, s86, 1
s_lshl2_add_u32 s83, s83, s79
s_lshl_b32 s83, s83, 10
s_add_u32 s83, s83, 32768
buffer_load_dwordx4 v[16:19], v0, s[48:51], s83 offen sc1
v_cndmask_b32 v196, v196, v204, vcc
v_cndmask_b32 v197, v197, v205, vcc
s_nop 0
v_cndmask_b32 v212, v212, v220, vcc
v_cndmask_b32 v213, v213, v221, vcc
s_mov_b64 vcc, 0xffffffff
v_cndmask_b32 v122, v212, v196, vcc
v_cndmask_b32 v123, v213, v197, vcc
v_bfi_b32 v122, v124, v27, v122
v_bfi_b32 v123, v124, v120, v123
buffer_store_dwordx2 v[122:123], v121, s[48:51], s58 offen sc1
s_swappc_b64 s[6:7], s[16:17]
s_waitcnt vmcnt(15)
s_mov_b32 s18, 0xf000f
s_mov_b32 s19, 0xf000f
v_xor_b32 v16, s74, v16
v_bitop3_b32 v16, v17, s75, v16 bitop3:0xbe
v_bitop3_b32 v16, v18, s74, v16 bitop3:0xbe
v_bitop3_b32 v16, v19, s75, v16 bitop3:0xbe
v_cmp_ne_u32 vcc, 0, v16
s_nop 0
s_and_b64 vcc, vcc, s[18:19]
s_cbranch_vccnz .Lcf1
.Lcf1_r:
s_lshl2_add_u32 s58, s60, s57
s_lshl_b32 s58, s58, 10
s_mov_b32 s59, .Lhot_end-.Lstart
s_min_u32 s58, s58, s59
s_getpc_b64 s[18:19]
.Lcp2:
s_sub_u32 s18, s18, .Lcp2-.Lstart
s_subb_u32 s19, s19, 0
s_add_u32 s18, s18, s58
s_addc_u32 s19, s19, 0
s_and_b32 s18, s18, -16
global_load_dwordx4 v[28:31], v0, s[18:19]
s_swappc_b64 s[6:7], s[16:17]
s_waitcnt vmcnt(21)
s_cmp_eq_u32 s61, 6
s_cselect_b32 s58, s96, 0x80000000
buffer_store_dwordx2 v[132:133], v13, s[48:51], s58 offen sc1
v_mov_b32 v3, v25
s_mov_b64 s[12:13], s[28:29]
s_mov_b64 s[14:15], s[30:31]
s_mov_b32 s24, -896
s_mov_b32 s25, 14336
s_mov_b32 s26, 13440
)"
R"(
s_swappc_b64 s[6:7], s[16:17]
.Lm0:
s_nop 7
v_bfe_u32 v26, v0, 4, 4
v_lshrrev_b32 v27, 8, v0
v_lshlrev_b32 v27, 2, v27
v_sub_u32 v26, v26, v27
v_mov_b32 v27, 0xff800000
v_cmp_gt_i32 vcc, 0, v26
v_mov_b32 v56, v27
s_nop 0
v_cndmask_b32 v40, v40, v27, vcc
v_cndmask_b32 v60, v60, v27, vcc
v_cmp_gt_i32 vcc, 1, v26
v_mov_b32 v57, v27
s_nop 0
v_cndmask_b32 v41, v41, v27, vcc
v_cndmask_b32 v61, v61, v27, vcc
v_cmp_gt_i32 vcc, 2, v26
v_mov_b32 v58, v27
s_nop 0
v_cndmask_b32 v42, v42, v27, vcc
v_cndmask_b32 v62, v62, v27, vcc
v_cmp_gt_i32 vcc, 3, v26
v_mov_b32 v59, v27
s_nop 0
v_cndmask_b32 v43, v43, v27, vcc
v_cndmask_b32 v63, v63, v27, vcc
s_branch .Lm0_r
.Lm1:
v_bfe_u32 v120, v0, 4, 4
v_lshrrev_b32 v121, 8, v0
v_lshlrev_b32 v121, 2, v121
v_sub_u32 v121, v120, v121
v_mov_b32 v122, 0xff800000
v_cndmask_b32 v121, -16, v121, s[4:5]
v_cmp_gt_i32 vcc, 0, v121
v_cndmask_b32 v180, v122, v140, s[4:5]
s_nop 0
v_cndmask_b32 v172, v132, v122, vcc
v_cndmask_b32 v188, v140, v122, vcc
v_cmp_gt_i32 vcc, 1, v121
v_cndmask_b32 v181, v122, v140, s[4:5]
s_nop 0
v_cndmask_b32 v173, v132, v122, vcc
v_cndmask_b32 v189, v140, v122, vcc
v_cmp_gt_i32 vcc, 2, v121
v_cndmask_b32 v182, v122, v140, s[4:5]
s_nop 0
v_cndmask_b32 v174, v132, v122, vcc
v_cndmask_b32 v190, v140, v122, vcc
v_cmp_gt_i32 vcc, 3, v121
v_cndmask_b32 v183, v122, v140, s[4:5]
s_nop 0
v_cndmask_b32 v175, v132, v122, vcc
v_cndmask_b32 v191, v140, v122, vcc
s_nop 0
s_branch .Ldiag1
.Lhab1:
s_getpc_b64 s[16:17]
.Lhab1_g:
s_sub_u32 s16, s16, .Lhab1_g-.Lhab1_r
s_subb_u32 s17, s17, 0
s_and_b32 s58, s99, 3
s_cmp_eq_u32 s58, 1
s_cbranch_scc1 .Lbepi
s_setpc_b64 s[6:7]
.Lhd0:
s_getpc_b64 s[16:17]
.Lhd0_g:
s_sub_u32 s16, s16, .Lhd0_g-.Lhd0_r
s_subb_u32 s17, s17, 0
s_and_b32 s58, s99, 3
s_cmp_eq_u32 s58, 1
s_cbranch_scc1 .Lbepi
s_setpc_b64 s[6:7]
.Lhd1:
s_getpc_b64 s[16:17]
.Lhd1_g:
s_sub_u32 s16, s16, .Lhd1_g-.Lhd1_r
s_subb_u32 s17, s17, 0
s_and_b32 s58, s99, 3
s_cmp_eq_u32 s58, 1
s_cbranch_scc1 .Lbepi
s_setpc_b64 s[6:7]
.Lhy:
s_getpc_b64 s[16:17]
.Lhy_g:
s_sub_u32 s16, s16, .Lhy_g-.Lhy_r
s_subb_u32 s17, s17, 0
s_and_b32 s58, s99, 3
s_cmp_eq_u32 s58, 1
s_cbranch_scc1 .Lbepi
s_setpc_b64 s[6:7]
.Lhaa1:
s_getpc_b64 s[16:17]
.Lhaa1_g:
s_sub_u32 s16, s16, .Lhaa1_g-.Lhaa1_r
s_subb_u32 s17, s17, 0
s_and_b32 s58, s99, 3
s_cmp_eq_u32 s58, 1
s_cbranch_scc1 .Lbepi
s_setpc_b64 s[6:7]
.Lhaa0:
s_getpc_b64 s[16:17]
.Lhaa0_g:
s_sub_u32 s16, s16, .Lhaa0_g-.Lhaa0_r
s_subb_u32 s17, s17, 0
s_and_b32 s58, s99, 3
s_cmp_eq_u32 s58, 1
s_cbranch_scc1 .Lbepi
s_setpc_b64 s[6:7]
.Lrt0:
s_bitcmp1_b32 s73, 0
s_cselect_b32 s71, s89, s90
s_cselect_b32 s59, 0, 32
s_add_u32 s59, s59, s68
s_mul_i32 s35, s59, 7168
s_add_u32 s35, s35, 2097152
s_add_u32 s20, s35, 4096
buffer_load_dwordx4 v[196:199], v0, s[48:51], s35 offen sc1
buffer_load_dwordx4 v[204:207], v0, s[48:51], s35 offen offset:1024 sc1
buffer_load_dwordx4 v[212:215], v0, s[48:51], s35 offen offset:2048 sc1
buffer_load_dwordx4 v[220:223], v0, s[48:51], s35 offen offset:3072 sc1
buffer_load_dwordx4 v[228:231], v0, s[48:51], s20 offen sc1
buffer_load_dwordx4 v[236:239], v0, s[48:51], s20 offen offset:1024 sc1
buffer_load_dwordx4 v[244:247], v0, s[48:51], s20 offen offset:2048 sc1
s_not_b32 s21, s71
s_and_b32 s21, s21, 0x7ff
s_mul_i32 s59, s21, 2176
s_lshl_b32 s88, s68, 6
s_add_u32 s59, s59, s88
s_add_u32 s59, s59, 4194304
buffer_load_ushort v252, v7, s[48:51], s59 offen sc1
s_branch .Lrt0_r
.Lrt2:
s_bitcmp1_b32 s73, 0
s_cselect_b32 s71, s89, s90
s_cselect_b32 s59, 0, 32
s_add_u32 s59, s59, s68
s_mul_i32 s35, s59, 7168
s_add_u32 s35, s35, 2097152
s_add_u32 s20, s35, 4096
buffer_load_dwordx4 v[196:199], v0, s[48:51], s35 offen sc1
buffer_load_dwordx4 v[204:207], v0, s[48:51], s35 offen offset:1024 sc1
buffer_load_dwordx4 v[212:215], v0, s[48:51], s35 offen offset:2048 sc1
buffer_load_dwordx4 v[220:223], v0, s[48:51], s35 offen offset:3072 sc1
buffer_load_dwordx4 v[228:231], v0, s[48:51], s20 offen sc1
buffer_load_dwordx4 v[236:239], v0, s[48:51], s20 offen offset:1024 sc1
buffer_load_dwordx4 v[244:247], v0, s[48:51], s20 offen offset:2048 sc1
s_not_b32 s21, s71
s_and_b32 s21, s21, 0x7ff
s_mul_i32 s59, s21, 2176
s_lshl_b32 s88, s68, 6
s_add_u32 s59, s59, s88
s_add_u32 s59, s59, 4194304
buffer_load_ushort v252, v7, s[48:51], s59 offen sc1
s_waitcnt vmcnt(0)
.Lrt1:
s_waitcnt vmcnt(16)
s_and_b32 s59, s71, 0xffff0000
s_bitcmp1_b32 s71, 31
s_cselect_b32 s88, 0, 0x7fff0000
s_xor_b32 s59, s59, s88
v_mov_b32 v253, s59
v_mul_f32 v253, 0x3fb8aa3b, v253
v_exp_f32 v253, v253
v_lshlrev_b32 v252, 16, v252
v_add_f32 v253, 1.0, v253
v_rcp_f32 v253, v253
v_xor_b32 v196, s24, v196
v_xor_b32 v197, s25, v197
v_xor_b32 v198, s26, v198
v_xor_b32 v199, s27, v199
v_xor_b32 v204, s24, v204
v_xor_b32 v205, s25, v205
v_xor_b32 v206, s26, v206
v_xor_b32 v207, s27, v207
v_xor_b32 v212, s24, v212
v_xor_b32 v213, s25, v213
v_xor_b32 v214, s26, v214
v_xor_b32 v215, s27, v215
v_xor_b32 v220, s24, v220
v_xor_b32 v221, s25, v221
v_xor_b32 v222, s26, v222
v_xor_b32 v223, s27, v223
v_xor_b32 v228, s24, v228
v_xor_b32 v229, s25, v229
v_xor_b32 v230, s26, v230
v_xor_b32 v231, s27, v231
v_xor_b32 v236, s24, v236
v_xor_b32 v237, s25, v237
v_xor_b32 v238, s26, v238
v_xor_b32 v239, s27, v239
v_xor_b32 v244, s24, v244
v_xor_b32 v245, s25, v245
v_xor_b32 v246, s26, v246
v_xor_b32 v247, s27, v247
v_or3_b32 v254, v196, v197, v198
v_or3_b32 v254, v254, v199, v204
v_or3_b32 v254, v254, v205, v206
v_or3_b32 v254, v254, v207, v212
v_or3_b32 v254, v254, v213, v214
v_or3_b32 v254, v254, v215, v220
v_or3_b32 v254, v254, v221, v222
v_or3_b32 v254, v254, v223, v228
v_or3_b32 v254, v254, v229, v230
v_or3_b32 v254, v254, v231, v236
v_or3_b32 v254, v254, v237, v238
v_or3_b32 v254, v254, v239, v244
v_or3_b32 v254, v254, v245, v246
v_or_b32 v254, v254, v247
v_and_b32 v254, 0xff, v254
v_cmp_ne_u32 vcc, 0, v254
s_nop 0
s_cbranch_vccnz .Lyp
.Lyp_r:
v_cvt_pk_bf16_f32 v253, 0, v253
v_add_f32 v196, v196, v204
v_add_f32 v197, v197, v205
v_add_f32 v198, v198, v206
v_add_f32 v199, v199, v207
v_add_f32 v212, v212, v220
v_add_f32 v213, v213, v221
v_add_f32 v214, v214, v222
v_add_f32 v215, v215, v223
v_add_f32 v228, v228, v236
v_add_f32 v229, v229, v237
v_add_f32 v230, v230, v238
v_add_f32 v231, v231, v239
v_add_f32 v196, v196, v212
v_add_f32 v197, v197, v213
v_add_f32 v198, v198, v214
v_add_f32 v199, v199, v215
v_add_f32 v228, v228, v244
v_add_f32 v229, v229, v245
v_add_f32 v230, v230, v246
v_add_f32 v231, v231, v247
v_add_f32 v196, v196, v228
v_add_f32 v197, v197, v229
v_add_f32 v198, v198, v230
v_add_f32 v199, v199, v231
v_permlane16_swap_b32 v196, v197
s_nop 0
v_permlane16_swap_b32 v198, v199
v_add_f32 v196, v196, v197
v_add_f32 v198, v198, v199
v_lshrrev_b32 v204, 1, v6
s_lshl_b32 s59, s21, 2
v_permlane32_swap_b32 v196, v198
v_add_f32 v196, v196, v198
s_lshl_b64 vcc, 15, s59
s_lshr_b32 s59, s21, 9
v_add_f32_dpp v196, v196, v196 row_ror:8 row_mask:0xf bank_mask:0xf bound_ctrl:1
v_cvt_pk_bf16_f32 v196, 0, v196
v_mul_f32 v196, v196, v253
v_cvt_pk_bf16_f32 v196, 0, v196
v_add_f32 v196, v196, v252
v_cvt_pk_bf16_f32 v196, 0, v196
v_mul_f32 v197, 0xbfb8aa3b, v196
v_exp_f32 v197, v197
s_nop 0
v_add_f32 v197, 1.0, v197
v_rcp_f32 v197, v197
s_nop 0
v_mul_f32 v196, v196, v197
ds_bpermute_b32 v212, v204, v196 offset:0
ds_bpermute_b32 v213, v204, v196 offset:64
ds_bpermute_b32 v214, v204, v196 offset:128
ds_bpermute_b32 v215, v204, v196 offset:192
ds_bpermute_b32 v220, v204, v196 offset:4
ds_bpermute_b32 v221, v204, v196 offset:68
ds_bpermute_b32 v222, v204, v196 offset:132
ds_bpermute_b32 v223, v204, v196 offset:196
s_waitcnt lgkmcnt(0)
s_cmp_lg_u32 s59, 0
s_cbranch_scc1 .Lrt1_0
v_cndmask_b32 v64, v64, v212, vcc
v_cndmask_b32 v65, v65, v213, vcc
v_cndmask_b32 v66, v66, v214, vcc
v_cndmask_b32 v67, v67, v215, vcc
v_cndmask_b32 v68, v68, v220, vcc
v_cndmask_b32 v69, v69, v221, vcc
v_cndmask_b32 v70, v70, v222, vcc
v_cndmask_b32 v71, v71, v223, vcc
s_branch .Lrt1_e
.Lrt1_0:
s_cmp_lg_u32 s59, 1
s_cbranch_scc1 .Lrt1_1
v_cndmask_b32 v72, v72, v212, vcc
v_cndmask_b32 v73, v73, v213, vcc
v_cndmask_b32 v74, v74, v214, vcc
v_cndmask_b32 v75, v75, v215, vcc
v_cndmask_b32 v76, v76, v220, vcc
v_cndmask_b32 v77, v77, v221, vcc
v_cndmask_b32 v78, v78, v222, vcc
v_cndmask_b32 v79, v79, v223, vcc
s_branch .Lrt1_e
.Lrt1_1:
s_cmp_lg_u32 s59, 2
s_cbranch_scc1 .Lrt1_2
v_cndmask_b32 v80, v80, v212, vcc
v_cndmask_b32 v81, v81, v213, vcc
v_cndmask_b32 v82, v82, v214, vcc
v_cndmask_b32 v83, v83, v215, vcc
v_cndmask_b32 v84, v84, v220, vcc
v_cndmask_b32 v85, v85, v221, vcc
v_cndmask_b32 v86, v86, v222, vcc
v_cndmask_b32 v87, v87, v223, vcc
s_branch .Lrt1_e
.Lrt1_2:
v_cndmask_b32 v16, v16, v212, vcc
v_cndmask_b32 v17, v17, v213, vcc
v_cndmask_b32 v18, v18, v214, vcc
v_cndmask_b32 v19, v19, v215, vcc
v_cndmask_b32 v20, v20, v220, vcc
v_cndmask_b32 v21, v21, v221, vcc
v_cndmask_b32 v22, v22, v222, vcc
v_cndmask_b32 v23, v23, v223, vcc
.Lrt1_e:
s_cmp_eq_u32 s73, 3
s_mov_b32 s73, 2
s_cbranch_scc1 .Lrt2
s_branch .Lrt1_r
.Lhot_end:
.Lscan:
s_mov_b32 s88, 1048576
.Lscan_l:
s_sleep 8
buffer_load_dwordx4 v[32:35], v0, s[48:51], s79 offen sc1
buffer_load_dwordx4 v[36:39], v0, s[48:51], s79 offen offset:1024 sc1
buffer_load_dwordx4 v[40:43], v0, s[48:51], s79 offen offset:2048 sc1
buffer_load_dwordx4 v[44:47], v0, s[48:51], s79 offen offset:3072 sc1
s_waitcnt vmcnt(0)
v_xor_b32 v50, s74, v33
v_bitop3_b32 v50, v35, s75, v50 bitop3:0xbe
v_bitop3_b32 v50, v37, s74, v50 bitop3:0xbe
v_bitop3_b32 v50, v39, s75, v50 bitop3:0xbe
v_bitop3_b32 v50, v41, s74, v50 bitop3:0xbe
v_bitop3_b32 v50, v43, s75, v50 bitop3:0xbe
v_bitop3_b32 v50, v45, s74, v50 bitop3:0xbe
v_bitop3_b32 v50, v47, s75, v50 bitop3:0xbe
v_cmp_ne_u32 vcc, 0, v50
s_nop 0
s_cbranch_vccz .Lscan_r
s_sub_u32 s88, s88, 1
s_cmp_lg_u32 s88, 0
s_cbranch_scc1 .Lscan_l
s_branch .Lscan_r
.Lcf1:
s_mov_b32 s88, 1048576
.Lcf1_l:
s_sleep 4
buffer_load_dwordx4 v[16:19], v0, s[48:51], s83 offen sc1
s_waitcnt vmcnt(0)
s_mov_b32 s18, 0xf000f
s_mov_b32 s19, 0xf000f
v_xor_b32 v16, s74, v16
v_bitop3_b32 v16, v17, s75, v16 bitop3:0xbe
v_bitop3_b32 v16, v18, s74, v16 bitop3:0xbe
v_bitop3_b32 v16, v19, s75, v16 bitop3:0xbe
v_cmp_ne_u32 vcc, 0, v16
s_nop 0
s_and_b64 vcc, vcc, s[18:19]
s_cbranch_vccz .Lcf1_r
s_sub_u32 s88, s88, 1
s_cmp_lg_u32 s88, 0
s_cbranch_scc1 .Lcf1_l
s_branch .Lcf1_r
.Lkvf:
s_mov_b32 s88, 1048576
.Lkvf_l:
s_sleep 4
buffer_load_dwordx2 v[20:21], v1, s[48:51], s84 offen sc1
s_waitcnt vmcnt(0)
v_xor_b32 v20, s74, v20
v_bitop3_b32 v20, v21, s75, v20 bitop3:0xbe
v_cmp_ne_u32 vcc, 0, v20
s_nop 0
s_cbranch_vccz .Lkvf_r
s_sub_u32 s88, s88, 1
s_cmp_lg_u32 s88, 0
s_cbranch_scc1 .Lkvf_l
s_branch .Lkvf_r
.Lscale4:
v_accvgpr_read_b32 v28, a64
v_accvgpr_read_b32 v29, a65
v_accvgpr_read_b32 v30, a66
v_accvgpr_read_b32 v31, a67
v_mul_f32 v28, v28, v126
v_mul_f32 v29, v29, v126
v_mul_f32 v30, v30, v126
v_mul_f32 v31, v31, v126
v_accvgpr_write_b32 a64, v28
v_accvgpr_write_b32 a65, v29
v_accvgpr_write_b32 a66, v30
v_accvgpr_write_b32 a67, v31
v_accvgpr_read_b32 v28, a68
v_accvgpr_read_b32 v29, a69
v_accvgpr_read_b32 v30, a70
v_accvgpr_read_b32 v31, a71
v_mul_f32 v28, v28, v127
v_mul_f32 v29, v29, v127
v_mul_f32 v30, v30, v127
v_mul_f32 v31, v31, v127
v_accvgpr_write_b32 a68, v28
v_accvgpr_write_b32 a69, v29
v_accvgpr_write_b32 a70, v30
v_accvgpr_write_b32 a71, v31
v_accvgpr_read_b32 v28, a72
v_accvgpr_read_b32 v29, a73
v_accvgpr_read_b32 v30, a74
v_accvgpr_read_b32 v31, a75
v_mul_f32 v28, v28, v126
v_mul_f32 v29, v29, v126
v_mul_f32 v30, v30, v126
v_mul_f32 v31, v31, v126
v_accvgpr_write_b32 a72, v28
v_accvgpr_write_b32 a73, v29
v_accvgpr_write_b32 a74, v30
v_accvgpr_write_b32 a75, v31
v_accvgpr_read_b32 v28, a76
v_accvgpr_read_b32 v29, a77
v_accvgpr_read_b32 v30, a78
v_accvgpr_read_b32 v31, a79
v_mul_f32 v28, v28, v127
v_mul_f32 v29, v29, v127
v_mul_f32 v30, v30, v127
v_mul_f32 v31, v31, v127
v_accvgpr_write_b32 a76, v28
v_accvgpr_write_b32 a77, v29
v_accvgpr_write_b32 a78, v30
v_accvgpr_write_b32 a79, v31
v_accvgpr_read_b32 v28, a80
v_accvgpr_read_b32 v29, a81
v_accvgpr_read_b32 v30, a82
v_accvgpr_read_b32 v31, a83
)"
R"(
v_mul_f32 v28, v28, v126
v_mul_f32 v29, v29, v126
v_mul_f32 v30, v30, v126
v_mul_f32 v31, v31, v126
v_accvgpr_write_b32 a80, v28
v_accvgpr_write_b32 a81, v29
v_accvgpr_write_b32 a82, v30
v_accvgpr_write_b32 a83, v31
v_accvgpr_read_b32 v28, a84
v_accvgpr_read_b32 v29, a85
v_accvgpr_read_b32 v30, a86
v_accvgpr_read_b32 v31, a87
v_mul_f32 v28, v28, v127
v_mul_f32 v29, v29, v127
v_mul_f32 v30, v30, v127
v_mul_f32 v31, v31, v127
v_accvgpr_write_b32 a84, v28
v_accvgpr_write_b32 a85, v29
v_accvgpr_write_b32 a86, v30
v_accvgpr_write_b32 a87, v31
v_accvgpr_read_b32 v28, a88
v_accvgpr_read_b32 v29, a89
v_accvgpr_read_b32 v30, a90
v_accvgpr_read_b32 v31, a91
v_mul_f32 v28, v28, v126
v_mul_f32 v29, v29, v126
v_mul_f32 v30, v30, v126
v_mul_f32 v31, v31, v126
v_accvgpr_write_b32 a88, v28
v_accvgpr_write_b32 a89, v29
v_accvgpr_write_b32 a90, v30
v_accvgpr_write_b32 a91, v31
v_accvgpr_read_b32 v28, a92
v_accvgpr_read_b32 v29, a93
v_accvgpr_read_b32 v30, a94
v_accvgpr_read_b32 v31, a95
v_mul_f32 v28, v28, v127
v_mul_f32 v29, v29, v127
v_mul_f32 v30, v30, v127
v_mul_f32 v31, v31, v127
v_accvgpr_write_b32 a92, v28
v_accvgpr_write_b32 a93, v29
v_accvgpr_write_b32 a94, v30
v_accvgpr_write_b32 a95, v31
v_accvgpr_read_b32 v28, a96
v_accvgpr_read_b32 v29, a97
v_accvgpr_read_b32 v30, a98
v_accvgpr_read_b32 v31, a99
v_mul_f32 v28, v28, v126
v_mul_f32 v29, v29, v126
v_mul_f32 v30, v30, v126
v_mul_f32 v31, v31, v126
v_accvgpr_write_b32 a96, v28
v_accvgpr_write_b32 a97, v29
v_accvgpr_write_b32 a98, v30
v_accvgpr_write_b32 a99, v31
v_accvgpr_read_b32 v28, a100
v_accvgpr_read_b32 v29, a101
v_accvgpr_read_b32 v30, a102
v_accvgpr_read_b32 v31, a103
v_mul_f32 v28, v28, v127
v_mul_f32 v29, v29, v127
v_mul_f32 v30, v30, v127
v_mul_f32 v31, v31, v127
v_accvgpr_write_b32 a100, v28
v_accvgpr_write_b32 a101, v29
v_accvgpr_write_b32 a102, v30
v_accvgpr_write_b32 a103, v31
v_accvgpr_read_b32 v28, a104
v_accvgpr_read_b32 v29, a105
v_accvgpr_read_b32 v30, a106
v_accvgpr_read_b32 v31, a107
v_mul_f32 v28, v28, v126
v_mul_f32 v29, v29, v126
v_mul_f32 v30, v30, v126
v_mul_f32 v31, v31, v126
v_accvgpr_write_b32 a104, v28
v_accvgpr_write_b32 a105, v29
v_accvgpr_write_b32 a106, v30
v_accvgpr_write_b32 a107, v31
v_accvgpr_read_b32 v28, a108
v_accvgpr_read_b32 v29, a109
v_accvgpr_read_b32 v30, a110
v_accvgpr_read_b32 v31, a111
v_mul_f32 v28, v28, v127
v_mul_f32 v29, v29, v127
v_mul_f32 v30, v30, v127
v_mul_f32 v31, v31, v127
v_accvgpr_write_b32 a108, v28
v_accvgpr_write_b32 a109, v29
v_accvgpr_write_b32 a110, v30
v_accvgpr_write_b32 a111, v31
v_accvgpr_read_b32 v28, a112
v_accvgpr_read_b32 v29, a113
v_accvgpr_read_b32 v30, a114
v_accvgpr_read_b32 v31, a115
v_mul_f32 v28, v28, v126
v_mul_f32 v29, v29, v126
v_mul_f32 v30, v30, v126
v_mul_f32 v31, v31, v126
v_accvgpr_write_b32 a112, v28
v_accvgpr_write_b32 a113, v29
v_accvgpr_write_b32 a114, v30
v_accvgpr_write_b32 a115, v31
v_accvgpr_read_b32 v28, a116
v_accvgpr_read_b32 v29, a117
v_accvgpr_read_b32 v30, a118
v_accvgpr_read_b32 v31, a119
v_mul_f32 v28, v28, v127
v_mul_f32 v29, v29, v127
v_mul_f32 v30, v30, v127
v_mul_f32 v31, v31, v127
v_accvgpr_write_b32 a116, v28
v_accvgpr_write_b32 a117, v29
v_accvgpr_write_b32 a118, v30
v_accvgpr_write_b32 a119, v31
v_accvgpr_read_b32 v28, a120
v_accvgpr_read_b32 v29, a121
v_accvgpr_read_b32 v30, a122
v_accvgpr_read_b32 v31, a123
v_mul_f32 v28, v28, v126
v_mul_f32 v29, v29, v126
v_mul_f32 v30, v30, v126
v_mul_f32 v31, v31, v126
v_accvgpr_write_b32 a120, v28
v_accvgpr_write_b32 a121, v29
v_accvgpr_write_b32 a122, v30
v_accvgpr_write_b32 a123, v31
v_accvgpr_read_b32 v28, a124
v_accvgpr_read_b32 v29, a125
v_accvgpr_read_b32 v30, a126
v_accvgpr_read_b32 v31, a127
v_mul_f32 v28, v28, v127
v_mul_f32 v29, v29, v127
v_mul_f32 v30, v30, v127
v_mul_f32 v31, v31, v127
v_accvgpr_write_b32 a124, v28
v_accvgpr_write_b32 a125, v29
v_accvgpr_write_b32 a126, v30
v_accvgpr_write_b32 a127, v31
.Lscale2:
v_accvgpr_read_b32 v28, a0
v_accvgpr_read_b32 v29, a1
v_accvgpr_read_b32 v30, a2
v_accvgpr_read_b32 v31, a3
v_mul_f32 v28, v28, v124
v_mul_f32 v29, v29, v124
v_mul_f32 v30, v30, v124
v_mul_f32 v31, v31, v124
v_accvgpr_write_b32 a0, v28
v_accvgpr_write_b32 a1, v29
v_accvgpr_write_b32 a2, v30
v_accvgpr_write_b32 a3, v31
v_accvgpr_read_b32 v28, a4
v_accvgpr_read_b32 v29, a5
v_accvgpr_read_b32 v30, a6
v_accvgpr_read_b32 v31, a7
v_mul_f32 v28, v28, v125
v_mul_f32 v29, v29, v125
v_mul_f32 v30, v30, v125
v_mul_f32 v31, v31, v125
v_accvgpr_write_b32 a4, v28
v_accvgpr_write_b32 a5, v29
v_accvgpr_write_b32 a6, v30
v_accvgpr_write_b32 a7, v31
v_accvgpr_read_b32 v28, a8
v_accvgpr_read_b32 v29, a9
v_accvgpr_read_b32 v30, a10
v_accvgpr_read_b32 v31, a11
v_mul_f32 v28, v28, v124
v_mul_f32 v29, v29, v124
v_mul_f32 v30, v30, v124
v_mul_f32 v31, v31, v124
v_accvgpr_write_b32 a8, v28
v_accvgpr_write_b32 a9, v29
v_accvgpr_write_b32 a10, v30
v_accvgpr_write_b32 a11, v31
v_accvgpr_read_b32 v28, a12
v_accvgpr_read_b32 v29, a13
v_accvgpr_read_b32 v30, a14
v_accvgpr_read_b32 v31, a15
v_mul_f32 v28, v28, v125
v_mul_f32 v29, v29, v125
v_mul_f32 v30, v30, v125
v_mul_f32 v31, v31, v125
v_accvgpr_write_b32 a12, v28
v_accvgpr_write_b32 a13, v29
v_accvgpr_write_b32 a14, v30
v_accvgpr_write_b32 a15, v31
v_accvgpr_read_b32 v28, a16
v_accvgpr_read_b32 v29, a17
v_accvgpr_read_b32 v30, a18
v_accvgpr_read_b32 v31, a19
v_mul_f32 v28, v28, v124
v_mul_f32 v29, v29, v124
v_mul_f32 v30, v30, v124
v_mul_f32 v31, v31, v124
v_accvgpr_write_b32 a16, v28
v_accvgpr_write_b32 a17, v29
v_accvgpr_write_b32 a18, v30
v_accvgpr_write_b32 a19, v31
v_accvgpr_read_b32 v28, a20
v_accvgpr_read_b32 v29, a21
v_accvgpr_read_b32 v30, a22
v_accvgpr_read_b32 v31, a23
v_mul_f32 v28, v28, v125
v_mul_f32 v29, v29, v125
v_mul_f32 v30, v30, v125
v_mul_f32 v31, v31, v125
v_accvgpr_write_b32 a20, v28
v_accvgpr_write_b32 a21, v29
v_accvgpr_write_b32 a22, v30
v_accvgpr_write_b32 a23, v31
v_accvgpr_read_b32 v28, a24
v_accvgpr_read_b32 v29, a25
v_accvgpr_read_b32 v30, a26
v_accvgpr_read_b32 v31, a27
v_mul_f32 v28, v28, v124
v_mul_f32 v29, v29, v124
v_mul_f32 v30, v30, v124
v_mul_f32 v31, v31, v124
v_accvgpr_write_b32 a24, v28
v_accvgpr_write_b32 a25, v29
v_accvgpr_write_b32 a26, v30
v_accvgpr_write_b32 a27, v31
v_accvgpr_read_b32 v28, a28
v_accvgpr_read_b32 v29, a29
v_accvgpr_read_b32 v30, a30
v_accvgpr_read_b32 v31, a31
v_mul_f32 v28, v28, v125
v_mul_f32 v29, v29, v125
v_mul_f32 v30, v30, v125
v_mul_f32 v31, v31, v125
v_accvgpr_write_b32 a28, v28
v_accvgpr_write_b32 a29, v29
v_accvgpr_write_b32 a30, v30
v_accvgpr_write_b32 a31, v31
v_accvgpr_read_b32 v28, a32
v_accvgpr_read_b32 v29, a33
v_accvgpr_read_b32 v30, a34
v_accvgpr_read_b32 v31, a35
v_mul_f32 v28, v28, v124
v_mul_f32 v29, v29, v124
v_mul_f32 v30, v30, v124
v_mul_f32 v31, v31, v124
v_accvgpr_write_b32 a32, v28
v_accvgpr_write_b32 a33, v29
v_accvgpr_write_b32 a34, v30
v_accvgpr_write_b32 a35, v31
v_accvgpr_read_b32 v28, a36
v_accvgpr_read_b32 v29, a37
v_accvgpr_read_b32 v30, a38
v_accvgpr_read_b32 v31, a39
v_mul_f32 v28, v28, v125
v_mul_f32 v29, v29, v125
v_mul_f32 v30, v30, v125
v_mul_f32 v31, v31, v125
v_accvgpr_write_b32 a36, v28
v_accvgpr_write_b32 a37, v29
v_accvgpr_write_b32 a38, v30
v_accvgpr_write_b32 a39, v31
v_accvgpr_read_b32 v28, a40
v_accvgpr_read_b32 v29, a41
v_accvgpr_read_b32 v30, a42
v_accvgpr_read_b32 v31, a43
v_mul_f32 v28, v28, v124
v_mul_f32 v29, v29, v124
v_mul_f32 v30, v30, v124
v_mul_f32 v31, v31, v124
v_accvgpr_write_b32 a40, v28
v_accvgpr_write_b32 a41, v29
v_accvgpr_write_b32 a42, v30
v_accvgpr_write_b32 a43, v31
v_accvgpr_read_b32 v28, a44
v_accvgpr_read_b32 v29, a45
v_accvgpr_read_b32 v30, a46
v_accvgpr_read_b32 v31, a47
v_mul_f32 v28, v28, v125
v_mul_f32 v29, v29, v125
v_mul_f32 v30, v30, v125
v_mul_f32 v31, v31, v125
v_accvgpr_write_b32 a44, v28
v_accvgpr_write_b32 a45, v29
v_accvgpr_write_b32 a46, v30
v_accvgpr_write_b32 a47, v31
v_accvgpr_read_b32 v28, a48
v_accvgpr_read_b32 v29, a49
v_accvgpr_read_b32 v30, a50
v_accvgpr_read_b32 v31, a51
v_mul_f32 v28, v28, v124
v_mul_f32 v29, v29, v124
v_mul_f32 v30, v30, v124
v_mul_f32 v31, v31, v124
v_accvgpr_write_b32 a48, v28
v_accvgpr_write_b32 a49, v29
v_accvgpr_write_b32 a50, v30
v_accvgpr_write_b32 a51, v31
v_accvgpr_read_b32 v28, a52
v_accvgpr_read_b32 v29, a53
v_accvgpr_read_b32 v30, a54
v_accvgpr_read_b32 v31, a55
v_mul_f32 v28, v28, v125
v_mul_f32 v29, v29, v125
v_mul_f32 v30, v30, v125
v_mul_f32 v31, v31, v125
v_accvgpr_write_b32 a52, v28
v_accvgpr_write_b32 a53, v29
v_accvgpr_write_b32 a54, v30
v_accvgpr_write_b32 a55, v31
v_accvgpr_read_b32 v28, a56
v_accvgpr_read_b32 v29, a57
v_accvgpr_read_b32 v30, a58
v_accvgpr_read_b32 v31, a59
v_mul_f32 v28, v28, v124
v_mul_f32 v29, v29, v124
v_mul_f32 v30, v30, v124
v_mul_f32 v31, v31, v124
v_accvgpr_write_b32 a56, v28
v_accvgpr_write_b32 a57, v29
v_accvgpr_write_b32 a58, v30
v_accvgpr_write_b32 a59, v31
v_accvgpr_read_b32 v28, a60
v_accvgpr_read_b32 v29, a61
v_accvgpr_read_b32 v30, a62
v_accvgpr_read_b32 v31, a63
v_mul_f32 v28, v28, v125
v_mul_f32 v29, v29, v125
v_mul_f32 v30, v30, v125
v_mul_f32 v31, v31, v125
v_accvgpr_write_b32 a60, v28
v_accvgpr_write_b32 a61, v29
v_accvgpr_write_b32 a62, v30
v_accvgpr_write_b32 a63, v31
s_setpc_b64 s[16:17]
.Lab0:
s_waitcnt vmcnt(0)
s_nop 7
v_max3_f32 v120, v64, v65, v66
v_max3_f32 v120, v120, v67, v80
v_max3_f32 v120, v120, v81, v82
v_max_f32 v120, v120, v83
v_mov_b32 v26, v120
s_nop 1
v_permlane16_swap_b32 v120, v26
v_max_f32 v120, v120, v26
v_mov_b32 v26, v120
s_nop 1
v_permlane32_swap_b32 v120, v26
v_max_f32 v120, v120, v26
v_max_f32 v120, 0, v120
v_sub_f32 v26, 0, v120
v_exp_f32 v124, v26
s_nop 0
v_mul_f32 v116, v116, v124
v_max3_f32 v121, v68, v69, v70
v_max3_f32 v121, v121, v71, v84
v_max3_f32 v121, v121, v85, v86
v_max_f32 v121, v121, v87
v_mov_b32 v26, v121
s_nop 1
v_permlane16_swap_b32 v121, v26
v_max_f32 v121, v121, v26
v_mov_b32 v26, v121
s_nop 1
v_permlane32_swap_b32 v121, v26
v_max_f32 v121, v121, v26
v_max_f32 v121, 0, v121
v_sub_f32 v26, 0, v121
v_exp_f32 v125, v26
s_nop 0
v_mul_f32 v117, v117, v125
v_max3_f32 v122, v72, v73, v74
v_max3_f32 v122, v122, v75, v88
v_max3_f32 v122, v122, v89, v90
v_max_f32 v122, v122, v91
v_mov_b32 v26, v122
s_nop 1
v_permlane16_swap_b32 v122, v26
v_max_f32 v122, v122, v26
v_mov_b32 v26, v122
s_nop 1
v_permlane32_swap_b32 v122, v26
v_max_f32 v122, v122, v26
v_max_f32 v122, 0, v122
v_sub_f32 v26, 0, v122
v_exp_f32 v126, v26
s_nop 0
v_mul_f32 v118, v118, v126
v_max3_f32 v123, v76, v77, v78
v_max3_f32 v123, v123, v79, v92
v_max3_f32 v123, v123, v93, v94
v_max_f32 v123, v123, v95
v_mov_b32 v26, v123
s_nop 1
v_permlane16_swap_b32 v123, v26
v_max_f32 v123, v123, v26
v_mov_b32 v26, v123
s_nop 1
v_permlane32_swap_b32 v123, v26
v_max_f32 v123, v123, v26
v_max_f32 v123, 0, v123
v_sub_f32 v26, 0, v123
v_exp_f32 v127, v26
s_nop 0
v_mul_f32 v119, v119, v127
v_sub_f32 v28, v64, v120
v_sub_f32 v29, v65, v120
v_exp_f32 v28, v28
v_exp_f32 v29, v29
s_nop 0
v_cvt_pk_bf16_f32 v228, v28, v29
v_sub_f32 v28, v66, v120
v_sub_f32 v29, v67, v120
v_exp_f32 v28, v28
v_exp_f32 v29, v29
s_nop 0
v_cvt_pk_bf16_f32 v229, v28, v29
v_sub_f32 v32, v32, v120
v_sub_f32 v33, v33, v120
v_sub_f32 v34, v34, v120
v_sub_f32 v35, v35, v120
v_sub_f32 v28, v68, v121
v_sub_f32 v29, v69, v121
v_exp_f32 v28, v28
v_exp_f32 v29, v29
s_nop 0
v_cvt_pk_bf16_f32 v236, v28, v29
v_sub_f32 v28, v70, v121
v_sub_f32 v29, v71, v121
v_exp_f32 v28, v28
v_exp_f32 v29, v29
s_nop 0
v_cvt_pk_bf16_f32 v237, v28, v29
v_sub_f32 v36, v36, v121
v_sub_f32 v37, v37, v121
v_sub_f32 v38, v38, v121
v_sub_f32 v39, v39, v121
v_sub_f32 v28, v72, v122
v_sub_f32 v29, v73, v122
v_exp_f32 v28, v28
v_exp_f32 v29, v29
s_nop 0
v_cvt_pk_bf16_f32 v244, v28, v29
v_sub_f32 v28, v74, v122
v_sub_f32 v29, v75, v122
v_exp_f32 v28, v28
v_exp_f32 v29, v29
s_nop 0
v_cvt_pk_bf16_f32 v245, v28, v29
v_sub_f32 v40, v40, v122
v_sub_f32 v41, v41, v122
v_sub_f32 v42, v42, v122
v_sub_f32 v43, v43, v122
v_sub_f32 v28, v76, v123
v_sub_f32 v29, v77, v123
v_exp_f32 v28, v28
v_exp_f32 v29, v29
s_nop 0
v_cvt_pk_bf16_f32 v252, v28, v29
v_sub_f32 v28, v78, v123
v_sub_f32 v29, v79, v123
)"
R"(
v_exp_f32 v28, v28
v_exp_f32 v29, v29
s_nop 0
v_cvt_pk_bf16_f32 v253, v28, v29
v_sub_f32 v44, v44, v123
v_sub_f32 v45, v45, v123
v_sub_f32 v46, v46, v123
v_sub_f32 v47, v47, v123
v_sub_f32 v28, v80, v120
v_sub_f32 v29, v81, v120
v_exp_f32 v28, v28
v_exp_f32 v29, v29
s_nop 0
v_cvt_pk_bf16_f32 v230, v28, v29
v_sub_f32 v28, v82, v120
v_sub_f32 v29, v83, v120
v_exp_f32 v28, v28
v_exp_f32 v29, v29
s_nop 0
v_cvt_pk_bf16_f32 v231, v28, v29
v_sub_f32 v48, v48, v120
v_sub_f32 v49, v49, v120
v_sub_f32 v50, v50, v120
v_sub_f32 v51, v51, v120
v_sub_f32 v28, v84, v121
v_sub_f32 v29, v85, v121
v_exp_f32 v28, v28
v_exp_f32 v29, v29
s_nop 0
v_cvt_pk_bf16_f32 v238, v28, v29
v_sub_f32 v28, v86, v121
v_sub_f32 v29, v87, v121
v_exp_f32 v28, v28
v_exp_f32 v29, v29
s_nop 0
v_cvt_pk_bf16_f32 v239, v28, v29
v_sub_f32 v52, v52, v121
v_sub_f32 v53, v53, v121
v_sub_f32 v54, v54, v121
v_sub_f32 v55, v55, v121
v_sub_f32 v28, v88, v122
v_sub_f32 v29, v89, v122
v_exp_f32 v28, v28
v_exp_f32 v29, v29
s_nop 0
v_cvt_pk_bf16_f32 v246, v28, v29
v_sub_f32 v28, v90, v122
v_sub_f32 v29, v91, v122
v_exp_f32 v28, v28
v_exp_f32 v29, v29
s_nop 0
v_cvt_pk_bf16_f32 v247, v28, v29
v_sub_f32 v56, v56, v122
v_sub_f32 v57, v57, v122
v_sub_f32 v58, v58, v122
v_sub_f32 v59, v59, v122
v_sub_f32 v28, v92, v123
v_sub_f32 v29, v93, v123
v_exp_f32 v28, v28
v_exp_f32 v29, v29
s_nop 0
v_cvt_pk_bf16_f32 v254, v28, v29
v_sub_f32 v28, v94, v123
v_sub_f32 v29, v95, v123
v_exp_f32 v28, v28
v_exp_f32 v29, v29
s_nop 0
v_cvt_pk_bf16_f32 v255, v28, v29
v_sub_f32 v60, v60, v123
v_sub_f32 v61, v61, v123
v_sub_f32 v62, v62, v123
v_sub_f32 v63, v63, v123
v_sub_f32 v4, v4, v120
v_sub_f32 v5, v5, v120
v_sub_f32 v6, v6, v120
v_sub_f32 v7, v7, v120
v_sub_f32 v8, v8, v121
v_sub_f32 v9, v9, v121
v_sub_f32 v10, v10, v121
v_sub_f32 v11, v11, v121
v_sub_f32 v132, v132, v122
v_sub_f32 v133, v133, v122
v_sub_f32 v134, v134, v122
v_sub_f32 v135, v135, v122
v_sub_f32 v140, v140, v123
v_sub_f32 v141, v141, v123
v_sub_f32 v142, v142, v123
v_sub_f32 v143, v143, v123
s_getpc_b64 s[16:17]
.Lab0_g:
s_add_u32 s16, s16, .Lab0_b-.Lab0_g
s_addc_u32 s17, s17, 0
s_branch .Lscale4
.Lab0_b:
Cvvv 112,96,228,116
Mvvv 112,100,236
Mvvv 112,104,244
Mvvv 112,108,252
v_exp_f32 v120, v32
v_exp_f32 v121, v33
v_exp_f32 v122, v34
v_exp_f32 v123, v35
v_exp_f32 v124, v36
v_cvt_pk_bf16_f32 v196, v120, v121
v_cvt_pk_bf16_f32 v197, v122, v123
v_exp_f32 v125, v37
v_exp_f32 v126, v38
v_cvt_pk_bf16_f32 v204, v124, v125
v_exp_f32 v127, v39
v_exp_f32 v120, v40
v_cvt_pk_bf16_f32 v205, v126, v127
v_exp_f32 v121, v41
v_exp_f32 v122, v42
v_cvt_pk_bf16_f32 v212, v120, v121
v_exp_f32 v123, v43
v_exp_f32 v124, v44
s_nop 7
s_branch .Lab0_r
.Lab1:
s_waitcnt vmcnt(0)
s_nop 7
v_max3_f32 v120, v32, v33, v34
v_max3_f32 v120, v120, v35, v48
v_max3_f32 v120, v120, v49, v50
v_max_f32 v120, v120, v51
v_mov_b32 v26, v120
s_nop 1
v_permlane16_swap_b32 v120, v26
v_max_f32 v120, v120, v26
v_mov_b32 v26, v120
s_nop 1
v_permlane32_swap_b32 v120, v26
v_max_f32 v120, v120, v26
v_max_f32 v120, 0, v120
v_sub_f32 v26, 0, v120
v_exp_f32 v124, v26
s_nop 0
v_mul_f32 v112, v112, v124
v_max3_f32 v121, v36, v37, v38
v_max3_f32 v121, v121, v39, v52
v_max3_f32 v121, v121, v53, v54
v_max_f32 v121, v121, v55
v_mov_b32 v26, v121
s_nop 1
v_permlane16_swap_b32 v121, v26
v_max_f32 v121, v121, v26
v_mov_b32 v26, v121
s_nop 1
v_permlane32_swap_b32 v121, v26
v_max_f32 v121, v121, v26
v_max_f32 v121, 0, v121
v_sub_f32 v26, 0, v121
v_exp_f32 v125, v26
s_nop 0
v_mul_f32 v113, v113, v125
v_max3_f32 v122, v40, v41, v42
v_max3_f32 v122, v122, v43, v56
v_max3_f32 v122, v122, v57, v58
v_max_f32 v122, v122, v59
v_mov_b32 v26, v122
s_nop 1
v_permlane16_swap_b32 v122, v26
v_max_f32 v122, v122, v26
v_mov_b32 v26, v122
s_nop 1
v_permlane32_swap_b32 v122, v26
v_max_f32 v122, v122, v26
v_max_f32 v122, 0, v122
v_sub_f32 v26, 0, v122
v_exp_f32 v126, v26
s_nop 0
v_mul_f32 v114, v114, v126
v_max3_f32 v123, v44, v45, v46
v_max3_f32 v123, v123, v47, v60
v_max3_f32 v123, v123, v61, v62
v_max_f32 v123, v123, v63
v_mov_b32 v26, v123
s_nop 1
v_permlane16_swap_b32 v123, v26
v_max_f32 v123, v123, v26
v_mov_b32 v26, v123
s_nop 1
v_permlane32_swap_b32 v123, v26
v_max_f32 v123, v123, v26
v_max_f32 v123, 0, v123
v_sub_f32 v26, 0, v123
v_exp_f32 v127, v26
s_nop 0
v_mul_f32 v115, v115, v127
v_sub_f32 v28, v32, v120
v_sub_f32 v29, v33, v120
v_exp_f32 v28, v28
v_exp_f32 v29, v29
s_nop 0
v_cvt_pk_bf16_f32 v196, v28, v29
v_sub_f32 v28, v34, v120
v_sub_f32 v29, v35, v120
v_exp_f32 v28, v28
v_exp_f32 v29, v29
s_nop 0
v_cvt_pk_bf16_f32 v197, v28, v29
v_sub_f32 v64, v64, v120
v_sub_f32 v65, v65, v120
v_sub_f32 v66, v66, v120
v_sub_f32 v67, v67, v120
v_sub_f32 v28, v36, v121
v_sub_f32 v29, v37, v121
v_exp_f32 v28, v28
v_exp_f32 v29, v29
s_nop 0
v_cvt_pk_bf16_f32 v204, v28, v29
v_sub_f32 v28, v38, v121
v_sub_f32 v29, v39, v121
v_exp_f32 v28, v28
v_exp_f32 v29, v29
s_nop 0
v_cvt_pk_bf16_f32 v205, v28, v29
v_sub_f32 v68, v68, v121
v_sub_f32 v69, v69, v121
v_sub_f32 v70, v70, v121
v_sub_f32 v71, v71, v121
v_sub_f32 v28, v40, v122
v_sub_f32 v29, v41, v122
v_exp_f32 v28, v28
v_exp_f32 v29, v29
s_nop 0
v_cvt_pk_bf16_f32 v212, v28, v29
v_sub_f32 v28, v42, v122
v_sub_f32 v29, v43, v122
v_exp_f32 v28, v28
v_exp_f32 v29, v29
s_nop 0
v_cvt_pk_bf16_f32 v213, v28, v29
v_sub_f32 v72, v72, v122
v_sub_f32 v73, v73, v122
v_sub_f32 v74, v74, v122
v_sub_f32 v75, v75, v122
v_sub_f32 v28, v44, v123
v_sub_f32 v29, v45, v123
v_exp_f32 v28, v28
v_exp_f32 v29, v29
s_nop 0
v_cvt_pk_bf16_f32 v220, v28, v29
v_sub_f32 v28, v46, v123
v_sub_f32 v29, v47, v123
v_exp_f32 v28, v28
v_exp_f32 v29, v29
s_nop 0
v_cvt_pk_bf16_f32 v221, v28, v29
v_sub_f32 v76, v76, v123
v_sub_f32 v77, v77, v123
v_sub_f32 v78, v78, v123
v_sub_f32 v79, v79, v123
v_sub_f32 v28, v48, v120
v_sub_f32 v29, v49, v120
v_exp_f32 v28, v28
v_exp_f32 v29, v29
s_nop 0
v_cvt_pk_bf16_f32 v198, v28, v29
v_sub_f32 v28, v50, v120
v_sub_f32 v29, v51, v120
v_exp_f32 v28, v28
v_exp_f32 v29, v29
s_nop 0
v_cvt_pk_bf16_f32 v199, v28, v29
v_sub_f32 v80, v80, v120
v_sub_f32 v81, v81, v120
v_sub_f32 v82, v82, v120
v_sub_f32 v83, v83, v120
v_sub_f32 v28, v52, v121
v_sub_f32 v29, v53, v121
v_exp_f32 v28, v28
v_exp_f32 v29, v29
s_nop 0
v_cvt_pk_bf16_f32 v206, v28, v29
v_sub_f32 v28, v54, v121
v_sub_f32 v29, v55, v121
v_exp_f32 v28, v28
v_exp_f32 v29, v29
s_nop 0
v_cvt_pk_bf16_f32 v207, v28, v29
v_sub_f32 v84, v84, v121
v_sub_f32 v85, v85, v121
v_sub_f32 v86, v86, v121
v_sub_f32 v87, v87, v121
v_sub_f32 v28, v56, v122
v_sub_f32 v29, v57, v122
v_exp_f32 v28, v28
v_exp_f32 v29, v29
s_nop 0
v_cvt_pk_bf16_f32 v214, v28, v29
v_sub_f32 v28, v58, v122
v_sub_f32 v29, v59, v122
v_exp_f32 v28, v28
v_exp_f32 v29, v29
s_nop 0
v_cvt_pk_bf16_f32 v215, v28, v29
v_sub_f32 v88, v88, v122
v_sub_f32 v89, v89, v122
v_sub_f32 v90, v90, v122
v_sub_f32 v91, v91, v122
v_sub_f32 v28, v60, v123
v_sub_f32 v29, v61, v123
v_exp_f32 v28, v28
v_exp_f32 v29, v29
s_nop 0
v_cvt_pk_bf16_f32 v222, v28, v29
v_sub_f32 v28, v62, v123
v_sub_f32 v29, v63, v123
v_exp_f32 v28, v28
v_exp_f32 v29, v29
s_nop 0
v_cvt_pk_bf16_f32 v223, v28, v29
v_sub_f32 v92, v92, v123
v_sub_f32 v93, v93, v123
v_sub_f32 v94, v94, v123
v_sub_f32 v95, v95, v123
v_sub_f32 v4, v4, v120
v_sub_f32 v5, v5, v120
v_sub_f32 v6, v6, v120
v_sub_f32 v7, v7, v120
v_sub_f32 v8, v8, v121
v_sub_f32 v9, v9, v121
v_sub_f32 v10, v10, v121
v_sub_f32 v11, v11, v121
v_sub_f32 v132, v132, v122
v_sub_f32 v133, v133, v122
v_sub_f32 v134, v134, v122
v_sub_f32 v135, v135, v122
v_sub_f32 v140, v140, v123
v_sub_f32 v141, v141, v123
v_sub_f32 v142, v142, v123
v_sub_f32 v143, v143, v123
s_getpc_b64 s[16:17]
.Lab1_g:
s_add_u32 s16, s16, .Lab1_b-.Lab1_g
s_addc_u32 s17, s17, 0
s_branch .Lscale4
.Lab1_b:
Cvvv 116,96,196,112
Mvvv 116,100,204
Mvvv 116,104,212
Mvvv 116,108,220
v_exp_f32 v120, v64
v_exp_f32 v121, v65
s_nop 0
v_cvt_pk_bf16_f32 v228, v120, v121
v_exp_f32 v122, v66
v_exp_f32 v123, v67
v_exp_f32 v124, v68
v_exp_f32 v125, v69
v_exp_f32 v126, v70
v_cvt_pk_bf16_f32 v229, v122, v123
v_exp_f32 v127, v71
v_exp_f32 v120, v72
v_exp_f32 v121, v73
v_cvt_pk_bf16_f32 v236, v124, v125
v_exp_f32 v122, v74
v_cvt_pk_bf16_f32 v237, v126, v127
v_exp_f32 v123, v75
v_exp_f32 v124, v76
v_cvt_pk_bf16_f32 v244, v120, v121
v_exp_f32 v125, v77
s_nop 7
s_branch .Lab1_r
.Ld0:
s_waitcnt vmcnt(0)
s_nop 7
v_max3_f32 v120, v64, v65, v66
v_max3_f32 v120, v120, v67, v80
v_max3_f32 v120, v120, v81, v82
v_max_f32 v120, v120, v83
v_mov_b32 v26, v120
s_nop 1
v_permlane16_swap_b32 v120, v26
v_max_f32 v120, v120, v26
v_mov_b32 v26, v120
s_nop 1
v_permlane32_swap_b32 v120, v26
v_max_f32 v120, v120, v26
v_max_f32 v120, 0, v120
v_sub_f32 v26, 0, v120
v_exp_f32 v124, v26
s_nop 0
v_mul_f32 v116, v116, v124
v_max3_f32 v121, v68, v69, v70
v_max3_f32 v121, v121, v71, v84
v_max3_f32 v121, v121, v85, v86
v_max_f32 v121, v121, v87
v_mov_b32 v26, v121
s_nop 1
v_permlane16_swap_b32 v121, v26
v_max_f32 v121, v121, v26
v_mov_b32 v26, v121
s_nop 1
v_permlane32_swap_b32 v121, v26
v_max_f32 v121, v121, v26
v_max_f32 v121, 0, v121
v_sub_f32 v26, 0, v121
v_exp_f32 v125, v26
s_nop 0
v_mul_f32 v117, v117, v125
v_max3_f32 v122, v72, v73, v74
v_max3_f32 v122, v122, v75, v88
v_max3_f32 v122, v122, v89, v90
v_max_f32 v122, v122, v91
v_mov_b32 v26, v122
s_nop 1
v_permlane16_swap_b32 v122, v26
v_max_f32 v122, v122, v26
v_mov_b32 v26, v122
s_nop 1
v_permlane32_swap_b32 v122, v26
v_max_f32 v122, v122, v26
v_max_f32 v122, 0, v122
v_sub_f32 v26, 0, v122
v_exp_f32 v126, v26
s_nop 0
v_mul_f32 v118, v118, v126
v_max3_f32 v123, v76, v77, v78
v_max3_f32 v123, v123, v79, v92
v_max3_f32 v123, v123, v93, v94
v_max_f32 v123, v123, v95
v_mov_b32 v26, v123
s_nop 1
v_permlane16_swap_b32 v123, v26
v_max_f32 v123, v123, v26
v_mov_b32 v26, v123
s_nop 1
v_permlane32_swap_b32 v123, v26
v_max_f32 v123, v123, v26
v_max_f32 v123, 0, v123
v_sub_f32 v26, 0, v123
v_exp_f32 v127, v26
s_nop 0
v_mul_f32 v119, v119, v127
v_sub_f32 v28, v64, v120
v_sub_f32 v29, v65, v120
v_exp_f32 v28, v28
v_exp_f32 v29, v29
s_nop 0
v_cvt_pk_bf16_f32 v228, v28, v29
v_sub_f32 v28, v66, v120
v_sub_f32 v29, v67, v120
v_exp_f32 v28, v28
v_exp_f32 v29, v29
s_nop 0
v_cvt_pk_bf16_f32 v229, v28, v29
v_sub_f32 v32, v32, v120
v_sub_f32 v33, v33, v120
v_sub_f32 v34, v34, v120
v_sub_f32 v35, v35, v120
v_sub_f32 v28, v68, v121
v_sub_f32 v29, v69, v121
v_exp_f32 v28, v28
v_exp_f32 v29, v29
s_nop 0
v_cvt_pk_bf16_f32 v236, v28, v29
v_sub_f32 v28, v70, v121
v_sub_f32 v29, v71, v121
v_exp_f32 v28, v28
v_exp_f32 v29, v29
s_nop 0
v_cvt_pk_bf16_f32 v237, v28, v29
v_sub_f32 v36, v36, v121
v_sub_f32 v37, v37, v121
v_sub_f32 v38, v38, v121
v_sub_f32 v39, v39, v121
v_sub_f32 v28, v72, v122
v_sub_f32 v29, v73, v122
v_exp_f32 v28, v28
v_exp_f32 v29, v29
s_nop 0
v_cvt_pk_bf16_f32 v244, v28, v29
v_sub_f32 v28, v74, v122
v_sub_f32 v29, v75, v122
v_exp_f32 v28, v28
v_exp_f32 v29, v29
s_nop 0
v_cvt_pk_bf16_f32 v245, v28, v29
v_sub_f32 v40, v40, v122
v_sub_f32 v41, v41, v122
v_sub_f32 v42, v42, v122
v_sub_f32 v43, v43, v122
v_sub_f32 v28, v76, v123
v_sub_f32 v29, v77, v123
v_exp_f32 v28, v28
v_exp_f32 v29, v29
s_nop 0
v_cvt_pk_bf16_f32 v252, v28, v29
v_sub_f32 v28, v78, v123
v_sub_f32 v29, v79, v123
v_exp_f32 v28, v28
v_exp_f32 v29, v29
s_nop 0
v_cvt_pk_bf16_f32 v253, v28, v29
v_sub_f32 v44, v44, v123
v_sub_f32 v45, v45, v123
v_sub_f32 v46, v46, v123
v_sub_f32 v47, v47, v123
v_sub_f32 v28, v80, v120
v_sub_f32 v29, v81, v120
v_exp_f32 v28, v28
v_exp_f32 v29, v29
s_nop 0
v_cvt_pk_bf16_f32 v230, v28, v29
v_sub_f32 v28, v82, v120
v_sub_f32 v29, v83, v120
v_exp_f32 v28, v28
v_exp_f32 v29, v29
s_nop 0
v_cvt_pk_bf16_f32 v231, v28, v29
v_sub_f32 v48, v48, v120
v_sub_f32 v49, v49, v120
v_sub_f32 v50, v50, v120
v_sub_f32 v51, v51, v120
v_sub_f32 v28, v84, v121
v_sub_f32 v29, v85, v121
v_exp_f32 v28, v28
v_exp_f32 v29, v29
s_nop 0
v_cvt_pk_bf16_f32 v238, v28, v29
v_sub_f32 v28, v86, v121
v_sub_f32 v29, v87, v121
v_exp_f32 v28, v28
v_exp_f32 v29, v29
s_nop 0
v_cvt_pk_bf16_f32 v239, v28, v29
v_sub_f32 v52, v52, v121
v_sub_f32 v53, v53, v121
v_sub_f32 v54, v54, v121
v_sub_f32 v55, v55, v121
v_sub_f32 v28, v88, v122
)"
R"(
v_sub_f32 v29, v89, v122
v_exp_f32 v28, v28
v_exp_f32 v29, v29
s_nop 0
v_cvt_pk_bf16_f32 v246, v28, v29
v_sub_f32 v28, v90, v122
v_sub_f32 v29, v91, v122
v_exp_f32 v28, v28
v_exp_f32 v29, v29
s_nop 0
v_cvt_pk_bf16_f32 v247, v28, v29
v_sub_f32 v56, v56, v122
v_sub_f32 v57, v57, v122
v_sub_f32 v58, v58, v122
v_sub_f32 v59, v59, v122
v_sub_f32 v28, v92, v123
v_sub_f32 v29, v93, v123
v_exp_f32 v28, v28
v_exp_f32 v29, v29
s_nop 0
v_cvt_pk_bf16_f32 v254, v28, v29
v_sub_f32 v28, v94, v123
v_sub_f32 v29, v95, v123
v_exp_f32 v28, v28
v_exp_f32 v29, v29
s_nop 0
v_cvt_pk_bf16_f32 v255, v28, v29
v_sub_f32 v60, v60, v123
v_sub_f32 v61, v61, v123
v_sub_f32 v62, v62, v123
v_sub_f32 v63, v63, v123
v_sub_f32 v4, v4, v120
v_sub_f32 v5, v5, v120
v_sub_f32 v6, v6, v120
v_sub_f32 v7, v7, v120
v_sub_f32 v8, v8, v121
v_sub_f32 v9, v9, v121
v_sub_f32 v10, v10, v121
v_sub_f32 v11, v11, v121
v_sub_f32 v132, v132, v122
v_sub_f32 v133, v133, v122
v_sub_f32 v134, v134, v122
v_sub_f32 v135, v135, v122
v_sub_f32 v140, v140, v123
v_sub_f32 v141, v141, v123
v_sub_f32 v142, v142, v123
v_sub_f32 v143, v143, v123
s_getpc_b64 s[16:17]
.Ld0_g:
s_add_u32 s16, s16, .Ld0_b-.Ld0_g
s_addc_u32 s17, s17, 0
s_branch .Lscale4
.Ld0_b:
Cvvv 112,96,228,116
Mvvv 112,100,236
Mvvv 112,104,244
Mvvv 112,108,252
v_exp_f32 v120, v32
v_exp_f32 v121, v33
s_nop 0
v_cvt_pk_bf16_f32 v196, v120, v121
v_exp_f32 v122, v34
v_exp_f32 v123, v35
v_exp_f32 v124, v36
v_exp_f32 v125, v37
v_exp_f32 v126, v38
v_exp_f32 v127, v39
v_exp_f32 v120, v44
v_cvt_pk_bf16_f32 v197, v122, v123
v_cvt_pk_bf16_f32 v204, v124, v125
v_exp_f32 v121, v45
v_exp_f32 v122, v46
v_exp_f32 v123, v47
v_cvt_pk_bf16_f32 v205, v126, v127
v_cvt_pk_bf16_f32 v220, v120, v121
v_exp_f32 v124, v48
s_nop 7
s_branch .Ld0_r
.Ld1:
s_waitcnt vmcnt(0)
s_nop 7
v_max3_f32 v120, v32, v33, v34
v_max3_f32 v120, v120, v35, v48
v_max3_f32 v120, v120, v49, v50
v_max_f32 v120, v120, v51
v_mov_b32 v26, v120
s_nop 1
v_permlane16_swap_b32 v120, v26
v_max_f32 v120, v120, v26
v_mov_b32 v26, v120
s_nop 1
v_permlane32_swap_b32 v120, v26
v_max_f32 v120, v120, v26
v_max_f32 v120, 0, v120
v_sub_f32 v26, 0, v120
v_exp_f32 v124, v26
s_nop 0
v_mul_f32 v112, v112, v124
v_max3_f32 v121, v36, v37, v38
v_max3_f32 v121, v121, v39, v52
v_max3_f32 v121, v121, v53, v54
v_max_f32 v121, v121, v55
v_mov_b32 v26, v121
s_nop 1
v_permlane16_swap_b32 v121, v26
v_max_f32 v121, v121, v26
v_mov_b32 v26, v121
s_nop 1
v_permlane32_swap_b32 v121, v26
v_max_f32 v121, v121, v26
v_max_f32 v121, 0, v121
v_sub_f32 v26, 0, v121
v_exp_f32 v125, v26
s_nop 0
v_mul_f32 v113, v113, v125
v_max3_f32 v122, v40, v41, v42
v_max3_f32 v122, v122, v43, v56
v_max3_f32 v122, v122, v57, v58
v_max_f32 v122, v122, v59
v_mov_b32 v26, v122
s_nop 1
v_permlane16_swap_b32 v122, v26
v_max_f32 v122, v122, v26
v_mov_b32 v26, v122
s_nop 1
v_permlane32_swap_b32 v122, v26
v_max_f32 v122, v122, v26
v_max_f32 v122, 0, v122
v_sub_f32 v26, 0, v122
v_exp_f32 v126, v26
s_nop 0
v_mul_f32 v114, v114, v126
v_max3_f32 v123, v44, v45, v46
v_max3_f32 v123, v123, v47, v60
v_max3_f32 v123, v123, v61, v62
v_max_f32 v123, v123, v63
v_mov_b32 v26, v123
s_nop 1
v_permlane16_swap_b32 v123, v26
v_max_f32 v123, v123, v26
v_mov_b32 v26, v123
s_nop 1
v_permlane32_swap_b32 v123, v26
v_max_f32 v123, v123, v26
v_max_f32 v123, 0, v123
v_sub_f32 v26, 0, v123
v_exp_f32 v127, v26
s_nop 0
v_mul_f32 v115, v115, v127
v_sub_f32 v28, v32, v120
v_sub_f32 v29, v33, v120
v_exp_f32 v28, v28
v_exp_f32 v29, v29
s_nop 0
v_cvt_pk_bf16_f32 v196, v28, v29
v_sub_f32 v28, v34, v120
v_sub_f32 v29, v35, v120
v_exp_f32 v28, v28
v_exp_f32 v29, v29
s_nop 0
v_cvt_pk_bf16_f32 v197, v28, v29
v_sub_f32 v64, v64, v120
v_sub_f32 v65, v65, v120
v_sub_f32 v66, v66, v120
v_sub_f32 v67, v67, v120
v_sub_f32 v28, v36, v121
v_sub_f32 v29, v37, v121
v_exp_f32 v28, v28
v_exp_f32 v29, v29
s_nop 0
v_cvt_pk_bf16_f32 v204, v28, v29
v_sub_f32 v28, v38, v121
v_sub_f32 v29, v39, v121
v_exp_f32 v28, v28
v_exp_f32 v29, v29
s_nop 0
v_cvt_pk_bf16_f32 v205, v28, v29
v_sub_f32 v68, v68, v121
v_sub_f32 v69, v69, v121
v_sub_f32 v70, v70, v121
v_sub_f32 v71, v71, v121
v_sub_f32 v28, v40, v122
v_sub_f32 v29, v41, v122
v_exp_f32 v28, v28
v_exp_f32 v29, v29
s_nop 0
v_cvt_pk_bf16_f32 v212, v28, v29
v_sub_f32 v28, v42, v122
v_sub_f32 v29, v43, v122
v_exp_f32 v28, v28
v_exp_f32 v29, v29
s_nop 0
v_cvt_pk_bf16_f32 v213, v28, v29
v_sub_f32 v72, v72, v122
v_sub_f32 v73, v73, v122
v_sub_f32 v74, v74, v122
v_sub_f32 v75, v75, v122
v_sub_f32 v28, v44, v123
v_sub_f32 v29, v45, v123
v_exp_f32 v28, v28
v_exp_f32 v29, v29
s_nop 0
v_cvt_pk_bf16_f32 v220, v28, v29
v_sub_f32 v28, v46, v123
v_sub_f32 v29, v47, v123
v_exp_f32 v28, v28
v_exp_f32 v29, v29
s_nop 0
v_cvt_pk_bf16_f32 v221, v28, v29
v_sub_f32 v76, v76, v123
v_sub_f32 v77, v77, v123
v_sub_f32 v78, v78, v123
v_sub_f32 v79, v79, v123
v_sub_f32 v28, v48, v120
v_sub_f32 v29, v49, v120
v_exp_f32 v28, v28
v_exp_f32 v29, v29
s_nop 0
v_cvt_pk_bf16_f32 v198, v28, v29
v_sub_f32 v28, v50, v120
v_sub_f32 v29, v51, v120
v_exp_f32 v28, v28
v_exp_f32 v29, v29
s_nop 0
v_cvt_pk_bf16_f32 v199, v28, v29
v_sub_f32 v80, v80, v120
v_sub_f32 v81, v81, v120
v_sub_f32 v82, v82, v120
v_sub_f32 v83, v83, v120
v_sub_f32 v28, v52, v121
v_sub_f32 v29, v53, v121
v_exp_f32 v28, v28
v_exp_f32 v29, v29
s_nop 0
v_cvt_pk_bf16_f32 v206, v28, v29
v_sub_f32 v28, v54, v121
v_sub_f32 v29, v55, v121
v_exp_f32 v28, v28
v_exp_f32 v29, v29
s_nop 0
v_cvt_pk_bf16_f32 v207, v28, v29
v_sub_f32 v84, v84, v121
v_sub_f32 v85, v85, v121
v_sub_f32 v86, v86, v121
v_sub_f32 v87, v87, v121
v_sub_f32 v28, v56, v122
v_sub_f32 v29, v57, v122
v_exp_f32 v28, v28
v_exp_f32 v29, v29
s_nop 0
v_cvt_pk_bf16_f32 v214, v28, v29
v_sub_f32 v28, v58, v122
v_sub_f32 v29, v59, v122
v_exp_f32 v28, v28
v_exp_f32 v29, v29
s_nop 0
v_cvt_pk_bf16_f32 v215, v28, v29
v_sub_f32 v88, v88, v122
v_sub_f32 v89, v89, v122
v_sub_f32 v90, v90, v122
v_sub_f32 v91, v91, v122
v_sub_f32 v28, v60, v123
v_sub_f32 v29, v61, v123
v_exp_f32 v28, v28
v_exp_f32 v29, v29
s_nop 0
v_cvt_pk_bf16_f32 v222, v28, v29
v_sub_f32 v28, v62, v123
v_sub_f32 v29, v63, v123
v_exp_f32 v28, v28
v_exp_f32 v29, v29
s_nop 0
v_cvt_pk_bf16_f32 v223, v28, v29
v_sub_f32 v92, v92, v123
v_sub_f32 v93, v93, v123
v_sub_f32 v94, v94, v123
v_sub_f32 v95, v95, v123
v_sub_f32 v4, v4, v120
v_sub_f32 v5, v5, v120
v_sub_f32 v6, v6, v120
v_sub_f32 v7, v7, v120
v_sub_f32 v8, v8, v121
v_sub_f32 v9, v9, v121
v_sub_f32 v10, v10, v121
v_sub_f32 v11, v11, v121
v_sub_f32 v132, v132, v122
v_sub_f32 v133, v133, v122
v_sub_f32 v134, v134, v122
v_sub_f32 v135, v135, v122
v_sub_f32 v140, v140, v123
v_sub_f32 v141, v141, v123
v_sub_f32 v142, v142, v123
v_sub_f32 v143, v143, v123
s_getpc_b64 s[16:17]
.Ld1_g:
s_add_u32 s16, s16, .Ld1_b-.Ld1_g
s_addc_u32 s17, s17, 0
s_branch .Lscale4
.Ld1_b:
Cvvv 116,96,196,112
Mvvv 116,100,204
Mvvv 116,104,212
Mvvv 116,108,220
v_exp_f32 v120, v64
v_exp_f32 v121, v65
v_exp_f32 v122, v66
v_exp_f32 v123, v67
s_nop 0
v_cvt_pk_bf16_f32 v229, v122, v123
v_exp_f32 v124, v68
v_exp_f32 v125, v69
v_exp_f32 v126, v70
v_exp_f32 v127, v71
v_cvt_pk_bf16_f32 v228, v120, v121
v_cvt_pk_bf16_f32 v237, v126, v127
v_exp_f32 v120, v72
v_exp_f32 v121, v73
v_exp_f32 v122, v74
v_cvt_pk_bf16_f32 v236, v124, v125
v_exp_f32 v123, v75
s_nop 7
s_branch .Ld1_r
.Ly:
s_waitcnt vmcnt(0)
s_nop 7
v_max3_f32 v120, v64, v65, v66
v_max3_f32 v120, v120, v67, v80
v_max3_f32 v120, v120, v81, v82
v_max_f32 v120, v120, v83
v_mov_b32 v26, v120
s_nop 1
v_permlane16_swap_b32 v120, v26
v_max_f32 v120, v120, v26
v_mov_b32 v26, v120
s_nop 1
v_permlane32_swap_b32 v120, v26
v_max_f32 v120, v120, v26
v_max_f32 v120, 0, v120
v_sub_f32 v26, 0, v120
v_exp_f32 v124, v26
s_nop 0
v_mul_f32 v116, v116, v124
v_max3_f32 v121, v68, v69, v70
v_max3_f32 v121, v121, v71, v84
v_max3_f32 v121, v121, v85, v86
v_max_f32 v121, v121, v87
v_mov_b32 v26, v121
s_nop 1
v_permlane16_swap_b32 v121, v26
v_max_f32 v121, v121, v26
v_mov_b32 v26, v121
s_nop 1
v_permlane32_swap_b32 v121, v26
v_max_f32 v121, v121, v26
v_max_f32 v121, 0, v121
v_sub_f32 v26, 0, v121
v_exp_f32 v125, v26
s_nop 0
v_mul_f32 v117, v117, v125
v_max3_f32 v122, v72, v73, v74
v_max3_f32 v122, v122, v75, v88
v_max3_f32 v122, v122, v89, v90
v_max_f32 v122, v122, v91
v_mov_b32 v26, v122
s_nop 1
v_permlane16_swap_b32 v122, v26
v_max_f32 v122, v122, v26
v_mov_b32 v26, v122
s_nop 1
v_permlane32_swap_b32 v122, v26
v_max_f32 v122, v122, v26
v_max_f32 v122, 0, v122
v_sub_f32 v26, 0, v122
v_exp_f32 v126, v26
s_nop 0
v_mul_f32 v118, v118, v126
v_max3_f32 v123, v76, v77, v78
v_max3_f32 v123, v123, v79, v92
v_max3_f32 v123, v123, v93, v94
v_max_f32 v123, v123, v95
v_mov_b32 v26, v123
s_nop 1
v_permlane16_swap_b32 v123, v26
v_max_f32 v123, v123, v26
v_mov_b32 v26, v123
s_nop 1
v_permlane32_swap_b32 v123, v26
v_max_f32 v123, v123, v26
v_max_f32 v123, 0, v123
v_sub_f32 v26, 0, v123
v_exp_f32 v127, v26
s_nop 0
v_mul_f32 v119, v119, v127
v_sub_f32 v28, v64, v120
v_sub_f32 v29, v65, v120
v_exp_f32 v28, v28
v_exp_f32 v29, v29
s_nop 0
v_cvt_pk_bf16_f32 v228, v28, v29
v_sub_f32 v28, v66, v120
v_sub_f32 v29, v67, v120
v_exp_f32 v28, v28
v_exp_f32 v29, v29
s_nop 0
v_cvt_pk_bf16_f32 v229, v28, v29
v_sub_f32 v32, v32, v120
v_sub_f32 v33, v33, v120
v_sub_f32 v34, v34, v120
v_sub_f32 v35, v35, v120
v_sub_f32 v28, v68, v121
v_sub_f32 v29, v69, v121
v_exp_f32 v28, v28
v_exp_f32 v29, v29
s_nop 0
v_cvt_pk_bf16_f32 v236, v28, v29
v_sub_f32 v28, v70, v121
v_sub_f32 v29, v71, v121
v_exp_f32 v28, v28
v_exp_f32 v29, v29
s_nop 0
v_cvt_pk_bf16_f32 v237, v28, v29
v_sub_f32 v36, v36, v121
v_sub_f32 v37, v37, v121
v_sub_f32 v38, v38, v121
v_sub_f32 v39, v39, v121
v_sub_f32 v28, v72, v122
v_sub_f32 v29, v73, v122
v_exp_f32 v28, v28
v_exp_f32 v29, v29
s_nop 0
v_cvt_pk_bf16_f32 v244, v28, v29
v_sub_f32 v28, v74, v122
v_sub_f32 v29, v75, v122
v_exp_f32 v28, v28
v_exp_f32 v29, v29
s_nop 0
v_cvt_pk_bf16_f32 v245, v28, v29
v_sub_f32 v40, v40, v120
v_sub_f32 v41, v41, v120
v_sub_f32 v42, v42, v120
v_sub_f32 v43, v43, v120
v_sub_f32 v28, v76, v123
v_sub_f32 v29, v77, v123
v_exp_f32 v28, v28
v_exp_f32 v29, v29
s_nop 0
v_cvt_pk_bf16_f32 v252, v28, v29
v_sub_f32 v28, v78, v123
v_sub_f32 v29, v79, v123
v_exp_f32 v28, v28
v_exp_f32 v29, v29
s_nop 0
v_cvt_pk_bf16_f32 v253, v28, v29
v_sub_f32 v44, v44, v121
v_sub_f32 v45, v45, v121
v_sub_f32 v46, v46, v121
v_sub_f32 v47, v47, v121
v_sub_f32 v28, v80, v120
v_sub_f32 v29, v81, v120
v_exp_f32 v28, v28
v_exp_f32 v29, v29
s_nop 0
v_cvt_pk_bf16_f32 v230, v28, v29
v_sub_f32 v28, v82, v120
v_sub_f32 v29, v83, v120
v_exp_f32 v28, v28
v_exp_f32 v29, v29
s_nop 0
v_cvt_pk_bf16_f32 v231, v28, v29
v_sub_f32 v48, v48, v120
v_sub_f32 v49, v49, v120
v_sub_f32 v50, v50, v120
v_sub_f32 v51, v51, v120
v_sub_f32 v28, v84, v121
v_sub_f32 v29, v85, v121
v_exp_f32 v28, v28
v_exp_f32 v29, v29
s_nop 0
v_cvt_pk_bf16_f32 v238, v28, v29
v_sub_f32 v28, v86, v121
v_sub_f32 v29, v87, v121
v_exp_f32 v28, v28
v_exp_f32 v29, v29
s_nop 0
v_cvt_pk_bf16_f32 v239, v28, v29
v_sub_f32 v52, v52, v121
v_sub_f32 v53, v53, v121
v_sub_f32 v54, v54, v121
v_sub_f32 v55, v55, v121
v_sub_f32 v28, v88, v122
v_sub_f32 v29, v89, v122
v_exp_f32 v28, v28
v_exp_f32 v29, v29
s_nop 0
v_cvt_pk_bf16_f32 v246, v28, v29
v_sub_f32 v28, v90, v122
v_sub_f32 v29, v91, v122
v_exp_f32 v28, v28
v_exp_f32 v29, v29
s_nop 0
v_cvt_pk_bf16_f32 v247, v28, v29
v_sub_f32 v56, v56, v120
v_sub_f32 v57, v57, v120
v_sub_f32 v58, v58, v120
v_sub_f32 v59, v59, v120
v_sub_f32 v28, v92, v123
v_sub_f32 v29, v93, v123
v_exp_f32 v28, v28
v_exp_f32 v29, v29
s_nop 0
v_cvt_pk_bf16_f32 v254, v28, v29
v_sub_f32 v28, v94, v123
v_sub_f32 v29, v95, v123
v_exp_f32 v28, v28
v_exp_f32 v29, v29
s_nop 0
v_cvt_pk_bf16_f32 v255, v28, v29
v_sub_f32 v60, v60, v121
v_sub_f32 v61, v61, v121
v_sub_f32 v62, v62, v121
v_sub_f32 v63, v63, v121
v_sub_f32 v4, v4, v120
v_sub_f32 v5, v5, v120
v_sub_f32 v6, v6, v120
v_sub_f32 v7, v7, v120
v_sub_f32 v8, v8, v121
v_sub_f32 v9, v9, v121
v_sub_f32 v10, v10, v121
v_sub_f32 v11, v11, v121
v_sub_f32 v132, v132, v122
v_sub_f32 v133, v133, v122
v_sub_f32 v134, v134, v122
v_sub_f32 v135, v135, v122
v_sub_f32 v140, v140, v123
)"
R"(
v_sub_f32 v141, v141, v123
v_sub_f32 v142, v142, v123
v_sub_f32 v143, v143, v123
s_getpc_b64 s[16:17]
.Ly_g:
s_add_u32 s16, s16, .Ly_b-.Ly_g
s_addc_u32 s17, s17, 0
s_branch .Lscale4
.Ly_b:
Cvvv 112,96,228,116
Mvvv 112,100,236
Mvvv 112,104,244
Mvvv 112,108,252
v_exp_f32 v120, v32
v_exp_f32 v121, v33
v_exp_f32 v122, v34
v_exp_f32 v123, v35
v_exp_f32 v124, v36
v_exp_f32 v125, v37
v_cvt_pk_bf16_f32 v196, v120, v121
v_cvt_pk_bf16_f32 v197, v122, v123
v_exp_f32 v126, v38
v_exp_f32 v127, v39
v_exp_f32 v120, v40
v_cvt_pk_bf16_f32 v204, v124, v125
v_exp_f32 v121, v41
v_exp_f32 v122, v42
v_cvt_pk_bf16_f32 v198, v120, v121
s_nop 7
s_branch .Ly_r
.Laa1:
s_waitcnt vmcnt(0)
s_nop 7
v_max3_f32 v120, v32, v33, v34
v_max3_f32 v120, v120, v35, v40
v_max3_f32 v120, v120, v41, v42
v_max3_f32 v120, v120, v43, v48
v_max3_f32 v120, v120, v49, v50
v_max3_f32 v120, v120, v51, v56
v_max3_f32 v120, v120, v57, v58
v_max_f32 v120, v120, v59
v_mov_b32 v26, v120
s_nop 1
v_permlane16_swap_b32 v120, v26
v_max_f32 v120, v120, v26
v_mov_b32 v26, v120
s_nop 1
v_permlane32_swap_b32 v120, v26
v_max_f32 v120, v120, v26
v_max_f32 v120, 0, v120
v_sub_f32 v26, 0, v120
v_exp_f32 v124, v26
s_nop 0
v_mul_f32 v112, v112, v124
v_max3_f32 v121, v36, v37, v38
v_max3_f32 v121, v121, v39, v44
v_max3_f32 v121, v121, v45, v46
v_max3_f32 v121, v121, v47, v52
v_max3_f32 v121, v121, v53, v54
v_max3_f32 v121, v121, v55, v60
v_max3_f32 v121, v121, v61, v62
v_max_f32 v121, v121, v63
v_mov_b32 v26, v121
s_nop 1
v_permlane16_swap_b32 v121, v26
v_max_f32 v121, v121, v26
v_mov_b32 v26, v121
s_nop 1
v_permlane32_swap_b32 v121, v26
v_max_f32 v121, v121, v26
v_max_f32 v121, 0, v121
v_sub_f32 v26, 0, v121
v_exp_f32 v125, v26
s_nop 0
v_mul_f32 v113, v113, v125
v_sub_f32 v28, v32, v120
v_sub_f32 v29, v33, v120
v_exp_f32 v28, v28
v_exp_f32 v29, v29
s_nop 0
v_cvt_pk_bf16_f32 v196, v28, v29
v_sub_f32 v28, v34, v120
v_sub_f32 v29, v35, v120
v_exp_f32 v28, v28
v_exp_f32 v29, v29
s_nop 0
v_cvt_pk_bf16_f32 v197, v28, v29
v_sub_f32 v64, v64, v120
v_sub_f32 v65, v65, v120
v_sub_f32 v66, v66, v120
v_sub_f32 v67, v67, v120
v_sub_f32 v28, v36, v121
v_sub_f32 v29, v37, v121
v_exp_f32 v28, v28
v_exp_f32 v29, v29
s_nop 0
v_cvt_pk_bf16_f32 v204, v28, v29
v_sub_f32 v28, v38, v121
v_sub_f32 v29, v39, v121
v_exp_f32 v28, v28
v_exp_f32 v29, v29
s_nop 0
v_cvt_pk_bf16_f32 v205, v28, v29
v_sub_f32 v68, v68, v121
v_sub_f32 v69, v69, v121
v_sub_f32 v70, v70, v121
v_sub_f32 v71, v71, v121
v_sub_f32 v28, v40, v120
v_sub_f32 v29, v41, v120
v_exp_f32 v28, v28
v_exp_f32 v29, v29
s_nop 0
v_cvt_pk_bf16_f32 v198, v28, v29
v_sub_f32 v28, v42, v120
v_sub_f32 v29, v43, v120
v_exp_f32 v28, v28
v_exp_f32 v29, v29
s_nop 0
v_cvt_pk_bf16_f32 v199, v28, v29
v_sub_f32 v72, v72, v120
v_sub_f32 v73, v73, v120
v_sub_f32 v74, v74, v120
v_sub_f32 v75, v75, v120
v_sub_f32 v28, v44, v121
v_sub_f32 v29, v45, v121
v_exp_f32 v28, v28
v_exp_f32 v29, v29
s_nop 0
v_cvt_pk_bf16_f32 v206, v28, v29
v_sub_f32 v28, v46, v121
v_sub_f32 v29, v47, v121
v_exp_f32 v28, v28
v_exp_f32 v29, v29
s_nop 0
v_cvt_pk_bf16_f32 v207, v28, v29
v_sub_f32 v76, v76, v121
v_sub_f32 v77, v77, v121
v_sub_f32 v78, v78, v121
v_sub_f32 v79, v79, v121
v_sub_f32 v28, v48, v120
v_sub_f32 v29, v49, v120
v_exp_f32 v28, v28
v_exp_f32 v29, v29
s_nop 0
v_cvt_pk_bf16_f32 v212, v28, v29
v_sub_f32 v28, v50, v120
v_sub_f32 v29, v51, v120
v_exp_f32 v28, v28
v_exp_f32 v29, v29
s_nop 0
v_cvt_pk_bf16_f32 v213, v28, v29
v_sub_f32 v80, v80, v120
v_sub_f32 v81, v81, v120
v_sub_f32 v82, v82, v120
v_sub_f32 v83, v83, v120
v_sub_f32 v28, v52, v121
v_sub_f32 v29, v53, v121
v_exp_f32 v28, v28
v_exp_f32 v29, v29
s_nop 0
v_cvt_pk_bf16_f32 v220, v28, v29
v_sub_f32 v28, v54, v121
v_sub_f32 v29, v55, v121
v_exp_f32 v28, v28
v_exp_f32 v29, v29
s_nop 0
v_cvt_pk_bf16_f32 v221, v28, v29
v_sub_f32 v84, v84, v121
v_sub_f32 v85, v85, v121
v_sub_f32 v86, v86, v121
v_sub_f32 v87, v87, v121
v_sub_f32 v28, v56, v120
v_sub_f32 v29, v57, v120
v_exp_f32 v28, v28
v_exp_f32 v29, v29
s_nop 0
v_cvt_pk_bf16_f32 v214, v28, v29
v_sub_f32 v28, v58, v120
v_sub_f32 v29, v59, v120
v_exp_f32 v28, v28
v_exp_f32 v29, v29
s_nop 0
v_cvt_pk_bf16_f32 v215, v28, v29
v_sub_f32 v88, v88, v120
v_sub_f32 v89, v89, v120
v_sub_f32 v90, v90, v120
v_sub_f32 v91, v91, v120
v_sub_f32 v28, v60, v121
v_sub_f32 v29, v61, v121
v_exp_f32 v28, v28
v_exp_f32 v29, v29
s_nop 0
v_cvt_pk_bf16_f32 v222, v28, v29
v_sub_f32 v28, v62, v121
v_sub_f32 v29, v63, v121
v_exp_f32 v28, v28
v_exp_f32 v29, v29
s_nop 0
v_cvt_pk_bf16_f32 v223, v28, v29
v_sub_f32 v92, v92, v121
v_sub_f32 v93, v93, v121
v_sub_f32 v94, v94, v121
v_sub_f32 v95, v95, v121
v_sub_f32 v4, v4, v120
v_sub_f32 v5, v5, v120
v_sub_f32 v6, v6, v120
v_sub_f32 v7, v7, v120
v_sub_f32 v8, v8, v121
v_sub_f32 v9, v9, v121
v_sub_f32 v10, v10, v121
v_sub_f32 v11, v11, v121
s_getpc_b64 s[16:17]
.Laa1_g:
s_add_u32 s16, s16, .Laa1_b-.Laa1_g
s_addc_u32 s17, s17, 0
s_branch .Lscale2
.Laa1_b:
Cvvv 116,96,196,112
Mvvv 116,100,204
Mvvv 116,96,212
Mvvv 116,100,220
v_exp_f32 v120, v64
v_exp_f32 v121, v65
s_nop 0
v_cvt_pk_bf16_f32 v228, v120, v121
v_exp_f32 v122, v66
v_exp_f32 v123, v67
v_exp_f32 v124, v68
v_exp_f32 v125, v69
v_cvt_pk_bf16_f32 v229, v122, v123
v_exp_f32 v126, v70
v_exp_f32 v127, v71
v_cvt_pk_bf16_f32 v236, v124, v125
v_exp_f32 v120, v72
v_exp_f32 v121, v73
v_cvt_pk_bf16_f32 v237, v126, v127
v_exp_f32 v122, v74
v_exp_f32 v123, v75
v_cvt_pk_bf16_f32 v230, v120, v121
v_exp_f32 v124, v76
v_exp_f32 v125, v77
s_nop 7
s_branch .Laa1_r
.Laa0:
s_waitcnt vmcnt(0)
s_nop 7
v_max3_f32 v120, v64, v65, v66
v_max3_f32 v120, v120, v67, v72
v_max3_f32 v120, v120, v73, v74
v_max3_f32 v120, v120, v75, v80
v_max3_f32 v120, v120, v81, v82
v_max3_f32 v120, v120, v83, v88
v_max3_f32 v120, v120, v89, v90
v_max_f32 v120, v120, v91
v_mov_b32 v26, v120
s_nop 1
v_permlane16_swap_b32 v120, v26
v_max_f32 v120, v120, v26
v_mov_b32 v26, v120
s_nop 1
v_permlane32_swap_b32 v120, v26
v_max_f32 v120, v120, v26
v_max_f32 v120, 0, v120
v_sub_f32 v26, 0, v120
v_exp_f32 v124, v26
s_nop 0
v_mul_f32 v116, v116, v124
v_max3_f32 v121, v68, v69, v70
v_max3_f32 v121, v121, v71, v76
v_max3_f32 v121, v121, v77, v78
v_max3_f32 v121, v121, v79, v84
v_max3_f32 v121, v121, v85, v86
v_max3_f32 v121, v121, v87, v92
v_max3_f32 v121, v121, v93, v94
v_max_f32 v121, v121, v95
v_mov_b32 v26, v121
s_nop 1
v_permlane16_swap_b32 v121, v26
v_max_f32 v121, v121, v26
v_mov_b32 v26, v121
s_nop 1
v_permlane32_swap_b32 v121, v26
v_max_f32 v121, v121, v26
v_max_f32 v121, 0, v121
v_sub_f32 v26, 0, v121
v_exp_f32 v125, v26
s_nop 0
v_mul_f32 v117, v117, v125
v_sub_f32 v28, v64, v120
v_sub_f32 v29, v65, v120
v_exp_f32 v28, v28
v_exp_f32 v29, v29
s_nop 0
v_cvt_pk_bf16_f32 v228, v28, v29
v_sub_f32 v28, v66, v120
v_sub_f32 v29, v67, v120
v_exp_f32 v28, v28
v_exp_f32 v29, v29
s_nop 0
v_cvt_pk_bf16_f32 v229, v28, v29
v_sub_f32 v32, v32, v120
v_sub_f32 v33, v33, v120
v_sub_f32 v34, v34, v120
v_sub_f32 v35, v35, v120
v_sub_f32 v28, v68, v121
v_sub_f32 v29, v69, v121
v_exp_f32 v28, v28
v_exp_f32 v29, v29
s_nop 0
v_cvt_pk_bf16_f32 v236, v28, v29
v_sub_f32 v28, v70, v121
v_sub_f32 v29, v71, v121
v_exp_f32 v28, v28
v_exp_f32 v29, v29
s_nop 0
v_cvt_pk_bf16_f32 v237, v28, v29
v_sub_f32 v36, v36, v121
v_sub_f32 v37, v37, v121
v_sub_f32 v38, v38, v121
v_sub_f32 v39, v39, v121
v_sub_f32 v28, v72, v120
v_sub_f32 v29, v73, v120
v_exp_f32 v28, v28
v_exp_f32 v29, v29
s_nop 0
v_cvt_pk_bf16_f32 v230, v28, v29
v_sub_f32 v28, v74, v120
v_sub_f32 v29, v75, v120
v_exp_f32 v28, v28
v_exp_f32 v29, v29
s_nop 0
v_cvt_pk_bf16_f32 v231, v28, v29
v_sub_f32 v40, v40, v120
v_sub_f32 v41, v41, v120
v_sub_f32 v42, v42, v120
v_sub_f32 v43, v43, v120
v_sub_f32 v28, v76, v121
v_sub_f32 v29, v77, v121
v_exp_f32 v28, v28
v_exp_f32 v29, v29
s_nop 0
v_cvt_pk_bf16_f32 v238, v28, v29
v_sub_f32 v28, v78, v121
v_sub_f32 v29, v79, v121
v_exp_f32 v28, v28
v_exp_f32 v29, v29
s_nop 0
v_cvt_pk_bf16_f32 v239, v28, v29
v_sub_f32 v44, v44, v121
v_sub_f32 v45, v45, v121
v_sub_f32 v46, v46, v121
v_sub_f32 v47, v47, v121
v_sub_f32 v28, v80, v120
v_sub_f32 v29, v81, v120
v_exp_f32 v28, v28
v_exp_f32 v29, v29
s_nop 0
v_cvt_pk_bf16_f32 v244, v28, v29
v_sub_f32 v28, v82, v120
v_sub_f32 v29, v83, v120
v_exp_f32 v28, v28
v_exp_f32 v29, v29
s_nop 0
v_cvt_pk_bf16_f32 v245, v28, v29
v_sub_f32 v48, v48, v120
v_sub_f32 v49, v49, v120
v_sub_f32 v50, v50, v120
v_sub_f32 v51, v51, v120
v_sub_f32 v28, v84, v121
v_sub_f32 v29, v85, v121
v_exp_f32 v28, v28
v_exp_f32 v29, v29
s_nop 0
v_cvt_pk_bf16_f32 v252, v28, v29
v_sub_f32 v28, v86, v121
v_sub_f32 v29, v87, v121
v_exp_f32 v28, v28
v_exp_f32 v29, v29
s_nop 0
v_cvt_pk_bf16_f32 v253, v28, v29
v_sub_f32 v52, v52, v121
v_sub_f32 v53, v53, v121
v_sub_f32 v54, v54, v121
v_sub_f32 v55, v55, v121
v_sub_f32 v28, v88, v120
v_sub_f32 v29, v89, v120
v_exp_f32 v28, v28
v_exp_f32 v29, v29
s_nop 0
v_cvt_pk_bf16_f32 v246, v28, v29
v_sub_f32 v28, v90, v120
v_sub_f32 v29, v91, v120
v_exp_f32 v28, v28
v_exp_f32 v29, v29
s_nop 0
v_cvt_pk_bf16_f32 v247, v28, v29
v_sub_f32 v56, v56, v120
v_sub_f32 v57, v57, v120
v_sub_f32 v58, v58, v120
v_sub_f32 v59, v59, v120
v_sub_f32 v28, v92, v121
v_sub_f32 v29, v93, v121
v_exp_f32 v28, v28
v_exp_f32 v29, v29
s_nop 0
v_cvt_pk_bf16_f32 v254, v28, v29
v_sub_f32 v28, v94, v121
v_sub_f32 v29, v95, v121
v_exp_f32 v28, v28
v_exp_f32 v29, v29
s_nop 0
v_cvt_pk_bf16_f32 v255, v28, v29
v_sub_f32 v60, v60, v121
v_sub_f32 v61, v61, v121
v_sub_f32 v62, v62, v121
v_sub_f32 v63, v63, v121
v_sub_f32 v4, v4, v120
v_sub_f32 v5, v5, v120
v_sub_f32 v6, v6, v120
v_sub_f32 v7, v7, v120
v_sub_f32 v8, v8, v121
v_sub_f32 v9, v9, v121
v_sub_f32 v10, v10, v121
v_sub_f32 v11, v11, v121
s_getpc_b64 s[16:17]
.Laa0_g:
s_add_u32 s16, s16, .Laa0_b-.Laa0_g
s_addc_u32 s17, s17, 0
s_branch .Lscale2
.Laa0_b:
Cvvv 112,96,228,116
Mvvv 112,100,236
Mvvv 112,96,244
Mvvv 112,100,252
v_exp_f32 v120, v32
v_exp_f32 v121, v33
s_nop 0
v_cvt_pk_bf16_f32 v196, v120, v121
v_exp_f32 v122, v34
v_exp_f32 v123, v35
v_exp_f32 v124, v36
v_exp_f32 v125, v37
v_cvt_pk_bf16_f32 v197, v122, v123
v_exp_f32 v126, v38
v_exp_f32 v127, v39
v_cvt_pk_bf16_f32 v204, v124, v125
v_exp_f32 v120, v40
v_exp_f32 v121, v41
v_cvt_pk_bf16_f32 v205, v126, v127
v_exp_f32 v122, v42
v_exp_f32 v123, v43
v_cvt_pk_bf16_f32 v198, v120, v121
v_exp_f32 v124, v44
v_exp_f32 v125, v45
s_nop 7
s_branch .Laa0_r
.Ldr:
s_waitcnt vmcnt(0)
s_nop 7
v_max3_f32 v120, v32, v33, v34
v_max3_f32 v120, v120, v35, v40
v_max3_f32 v120, v120, v41, v42
v_max3_f32 v120, v120, v43, v48
v_max3_f32 v120, v120, v49, v50
v_max3_f32 v120, v120, v51, v56
v_max3_f32 v120, v120, v57, v58
v_max_f32 v120, v120, v59
v_mov_b32 v26, v120
s_nop 1
v_permlane16_swap_b32 v120, v26
v_max_f32 v120, v120, v26
v_mov_b32 v26, v120
s_nop 1
v_permlane32_swap_b32 v120, v26
v_max_f32 v120, v120, v26
v_max_f32 v120, 0, v120
v_sub_f32 v26, 0, v120
v_exp_f32 v124, v26
s_nop 0
v_mul_f32 v112, v112, v124
v_max3_f32 v121, v36, v37, v38
v_max3_f32 v121, v121, v39, v44
v_max3_f32 v121, v121, v45, v46
v_max3_f32 v121, v121, v47, v52
v_max3_f32 v121, v121, v53, v54
v_max3_f32 v121, v121, v55, v60
v_max3_f32 v121, v121, v61, v62
v_max_f32 v121, v121, v63
v_mov_b32 v26, v121
s_nop 1
v_permlane16_swap_b32 v121, v26
v_max_f32 v121, v121, v26
v_mov_b32 v26, v121
s_nop 1
v_permlane32_swap_b32 v121, v26
v_max_f32 v121, v121, v26
v_max_f32 v121, 0, v121
v_sub_f32 v26, 0, v121
v_exp_f32 v125, v26
s_nop 0
v_mul_f32 v113, v113, v125
v_sub_f32 v28, v32, v120
v_sub_f32 v29, v33, v120
v_exp_f32 v28, v28
v_exp_f32 v29, v29
s_nop 0
v_cvt_pk_bf16_f32 v196, v28, v29
v_sub_f32 v28, v34, v120
v_sub_f32 v29, v35, v120
v_exp_f32 v28, v28
v_exp_f32 v29, v29
s_nop 0
v_cvt_pk_bf16_f32 v197, v28, v29
v_sub_f32 v28, v36, v121
v_sub_f32 v29, v37, v121
v_exp_f32 v28, v28
v_exp_f32 v29, v29
s_nop 0
v_cvt_pk_bf16_f32 v204, v28, v29
v_sub_f32 v28, v38, v121
v_sub_f32 v29, v39, v121
v_exp_f32 v28, v28
v_exp_f32 v29, v29
s_nop 0
v_cvt_pk_bf16_f32 v205, v28, v29
v_sub_f32 v28, v40, v120
v_sub_f32 v29, v41, v120
v_exp_f32 v28, v28
v_exp_f32 v29, v29
s_nop 0
v_cvt_pk_bf16_f32 v198, v28, v29
v_sub_f32 v28, v42, v120
)"
R"(
v_sub_f32 v29, v43, v120
v_exp_f32 v28, v28
v_exp_f32 v29, v29
s_nop 0
v_cvt_pk_bf16_f32 v199, v28, v29
v_sub_f32 v28, v44, v121
v_sub_f32 v29, v45, v121
v_exp_f32 v28, v28
v_exp_f32 v29, v29
s_nop 0
v_cvt_pk_bf16_f32 v206, v28, v29
v_sub_f32 v28, v46, v121
v_sub_f32 v29, v47, v121
v_exp_f32 v28, v28
v_exp_f32 v29, v29
s_nop 0
v_cvt_pk_bf16_f32 v207, v28, v29
v_sub_f32 v28, v48, v120
v_sub_f32 v29, v49, v120
v_exp_f32 v28, v28
v_exp_f32 v29, v29
s_nop 0
v_cvt_pk_bf16_f32 v212, v28, v29
v_sub_f32 v28, v50, v120
v_sub_f32 v29, v51, v120
v_exp_f32 v28, v28
v_exp_f32 v29, v29
s_nop 0
v_cvt_pk_bf16_f32 v213, v28, v29
v_sub_f32 v28, v52, v121
v_sub_f32 v29, v53, v121
v_exp_f32 v28, v28
v_exp_f32 v29, v29
s_nop 0
v_cvt_pk_bf16_f32 v220, v28, v29
v_sub_f32 v28, v54, v121
v_sub_f32 v29, v55, v121
v_exp_f32 v28, v28
v_exp_f32 v29, v29
s_nop 0
v_cvt_pk_bf16_f32 v221, v28, v29
v_sub_f32 v28, v56, v120
v_sub_f32 v29, v57, v120
v_exp_f32 v28, v28
v_exp_f32 v29, v29
s_nop 0
v_cvt_pk_bf16_f32 v214, v28, v29
v_sub_f32 v28, v58, v120
v_sub_f32 v29, v59, v120
v_exp_f32 v28, v28
v_exp_f32 v29, v29
s_nop 0
v_cvt_pk_bf16_f32 v215, v28, v29
v_sub_f32 v28, v60, v121
v_sub_f32 v29, v61, v121
v_exp_f32 v28, v28
v_exp_f32 v29, v29
s_nop 0
v_cvt_pk_bf16_f32 v222, v28, v29
v_sub_f32 v28, v62, v121
v_sub_f32 v29, v63, v121
v_exp_f32 v28, v28
v_exp_f32 v29, v29
s_nop 0
v_cvt_pk_bf16_f32 v223, v28, v29
s_getpc_b64 s[16:17]
.Ldr_g:
s_add_u32 s16, s16, .Ldr_b-.Ldr_g
s_addc_u32 s17, s17, 0
s_branch .Lscale2
.Ldr_b:
Cvvv 116,96,196,112
Mvvv 116,100,204
Mvvv 116,96,212
Mvvv 116,100,220
s_nop 7
s_branch .Ldr_r
.Lcf2:
s_mov_b32 s88, 1048576
.Lcf2_l:
s_sleep 4
buffer_load_dwordx4 v[16:19], v0, s[48:51], s83 offen sc1
s_waitcnt vmcnt(0)
s_mov_b32 s18, 0x700070
s_mov_b32 s19, 0x700070
v_xor_b32 v16, s74, v16
v_bitop3_b32 v16, v17, s75, v16 bitop3:0xbe
v_bitop3_b32 v16, v18, s74, v16 bitop3:0xbe
v_bitop3_b32 v16, v19, s75, v16 bitop3:0xbe
v_cmp_ne_u32 vcc, 0, v16
s_nop 0
s_and_b64 vcc, vcc, s[18:19]
s_cbranch_vccz .Lcf2_r
s_sub_u32 s88, s88, 1
s_cmp_lg_u32 s88, 0
s_cbranch_scc1 .Lcf2_l
s_branch .Lcf2_r
.Lcf3:
s_mov_b32 s88, 1048576
.Lcf3_l:
s_sleep 4
buffer_load_dwordx4 v[16:19], v0, s[48:51], s83 offen sc1
s_waitcnt vmcnt(0)
v_xor_b32 v16, s74, v16
v_bitop3_b32 v16, v17, s75, v16 bitop3:0xbe
v_bitop3_b32 v16, v18, s74, v16 bitop3:0xbe
v_bitop3_b32 v16, v19, s75, v16 bitop3:0xbe
v_cmp_ne_u32 vcc, 0, v16
s_nop 0
s_cbranch_vccz .Lcf3_r
s_sub_u32 s88, s88, 1
s_cmp_lg_u32 s88, 0
s_cbranch_scc1 .Lcf3_l
s_branch .Lcf3_r
.Lgu:
s_mov_b32 s88, 1048576
.Lgu_l:
s_sleep 4
buffer_load_dwordx4 v[40:43], v11, s[48:51], s21 offen sc1
buffer_load_dwordx4 v[44:47], v11, s[48:51], s21 offen offset:1024 sc1
buffer_load_dwordx4 v[48:51], v11, s[48:51], s21 offen offset:2048 sc1
buffer_load_dwordx4 v[52:55], v11, s[48:51], s21 offen offset:3072 sc1
s_waitcnt vmcnt(0)
v_xor_b32 v40, s24, v40
v_xor_b32 v41, s25, v41
v_xor_b32 v42, s26, v42
v_xor_b32 v43, s27, v43
v_xor_b32 v44, s24, v44
v_xor_b32 v45, s25, v45
v_xor_b32 v46, s26, v46
v_xor_b32 v47, s27, v47
v_xor_b32 v48, s24, v48
v_xor_b32 v49, s25, v49
v_xor_b32 v50, s26, v50
v_xor_b32 v51, s27, v51
v_xor_b32 v52, s24, v52
v_xor_b32 v53, s25, v53
v_xor_b32 v54, s26, v54
v_xor_b32 v55, s27, v55
v_or3_b32 v56, v40, v41, v42
v_or3_b32 v56, v56, v43, v44
v_or3_b32 v56, v56, v45, v46
v_or3_b32 v56, v56, v47, v48
v_or3_b32 v56, v56, v49, v50
v_or3_b32 v56, v56, v51, v52
v_or3_b32 v56, v56, v53, v54
v_or_b32 v56, v56, v55
v_and_b32 v56, 0xff, v56
v_cmp_ne_u32 vcc, 0, v56
s_nop 0
s_and_b64 vcc, vcc, s[36:37]
s_cbranch_vccz .Lgu_r
s_sub_u32 s88, s88, 1
s_cmp_lg_u32 s88, 0
s_cbranch_scc1 .Lgu_l
s_branch .Lgu_r
.Lpf1:
s_mov_b32 s88, 1048576
.Lpf1_l:
s_sleep 1
buffer_load_dwordx2 v[58:59], v8, s[48:51], s33 offen sc1
s_waitcnt vmcnt(0)
v_xor_b32 v58, s74, v58
v_bitop3_b32 v58, v59, s75, v58 bitop3:0xbe
v_cmp_ne_u32 vcc, 0, v58
s_nop 0
s_and_b64 vcc, vcc, 0xff
s_cbranch_vccz .Lpf1_r
s_sub_u32 s88, s88, 1
s_cmp_lg_u32 s88, 0
s_cbranch_scc1 .Lpf1_l
s_branch .Lpf1_r
.Lyp:
s_mov_b32 s88, 1048576
.Lyp_l:
s_sleep 4
buffer_load_dwordx4 v[196:199], v0, s[48:51], s35 offen sc1
buffer_load_dwordx4 v[204:207], v0, s[48:51], s35 offen offset:1024 sc1
buffer_load_dwordx4 v[212:215], v0, s[48:51], s35 offen offset:2048 sc1
buffer_load_dwordx4 v[220:223], v0, s[48:51], s35 offen offset:3072 sc1
buffer_load_dwordx4 v[228:231], v0, s[48:51], s20 offen sc1
buffer_load_dwordx4 v[236:239], v0, s[48:51], s20 offen offset:1024 sc1
buffer_load_dwordx4 v[244:247], v0, s[48:51], s20 offen offset:2048 sc1
s_waitcnt vmcnt(0)
v_xor_b32 v196, s24, v196
v_xor_b32 v197, s25, v197
v_xor_b32 v198, s26, v198
v_xor_b32 v199, s27, v199
v_xor_b32 v204, s24, v204
v_xor_b32 v205, s25, v205
v_xor_b32 v206, s26, v206
v_xor_b32 v207, s27, v207
v_xor_b32 v212, s24, v212
v_xor_b32 v213, s25, v213
v_xor_b32 v214, s26, v214
v_xor_b32 v215, s27, v215
v_xor_b32 v220, s24, v220
v_xor_b32 v221, s25, v221
v_xor_b32 v222, s26, v222
v_xor_b32 v223, s27, v223
v_xor_b32 v228, s24, v228
v_xor_b32 v229, s25, v229
v_xor_b32 v230, s26, v230
v_xor_b32 v231, s27, v231
v_xor_b32 v236, s24, v236
v_xor_b32 v237, s25, v237
v_xor_b32 v238, s26, v238
v_xor_b32 v239, s27, v239
v_xor_b32 v244, s24, v244
v_xor_b32 v245, s25, v245
v_xor_b32 v246, s26, v246
v_xor_b32 v247, s27, v247
v_or3_b32 v254, v196, v197, v198
v_or3_b32 v254, v254, v199, v204
v_or3_b32 v254, v254, v205, v206
v_or3_b32 v254, v254, v207, v212
v_or3_b32 v254, v254, v213, v214
v_or3_b32 v254, v254, v215, v220
v_or3_b32 v254, v254, v221, v222
v_or3_b32 v254, v254, v223, v228
v_or3_b32 v254, v254, v229, v230
v_or3_b32 v254, v254, v231, v236
v_or3_b32 v254, v254, v237, v238
v_or3_b32 v254, v254, v239, v244
v_or3_b32 v254, v254, v245, v246
v_or_b32 v254, v254, v247
v_and_b32 v254, 0xff, v254
v_cmp_ne_u32 vcc, 0, v254
s_nop 0
s_cbranch_vccz .Lyp_r
s_sub_u32 s88, s88, 1
s_cmp_lg_u32 s88, 0
s_cbranch_scc1 .Lyp_l
s_branch .Lyp_r
.Lpf2:
s_mov_b32 s88, 1048576
.Lpf2_l:
s_sleep 1
buffer_load_dwordx2 v[30:31], v8, s[48:51], s33 offen sc1
s_waitcnt vmcnt(0)
v_xor_b32 v30, s74, v30
v_bitop3_b32 v30, v31, s75, v30 bitop3:0xbe
v_cmp_ne_u32 vcc, 0, v30
s_nop 0
s_and_b64 vcc, vcc, 0xf00
s_cbranch_vccz .Lpf2_r
s_sub_u32 s88, s88, 1
s_cmp_lg_u32 s88, 0
s_cbranch_scc1 .Lpf2_l
s_branch .Lpf2_r
.Lpc3:
s_mov_b32 s88, 1048576
.Lpc3_l:
s_sleep 1
buffer_load_dwordx4 v[196:199], v0, s[48:51], s4 offen sc1
buffer_load_dwordx4 v[204:207], v0, s[48:51], s4 offen offset:1024 sc1
buffer_load_dwordx4 v[212:215], v0, s[48:51], s5 offen sc1
buffer_load_dwordx4 v[220:223], v0, s[48:51], s5 offen offset:1024 sc1
buffer_load_dwordx4 v[228:231], v0, s[48:51], s6 offen sc1
buffer_load_dwordx4 v[236:239], v0, s[48:51], s6 offen offset:1024 sc1
s_waitcnt vmcnt(0)
v_xor_b32 v196, s24, v196
v_xor_b32 v197, s25, v197
v_xor_b32 v198, s26, v198
v_xor_b32 v199, s27, v199
v_xor_b32 v204, s24, v204
v_xor_b32 v205, s25, v205
v_xor_b32 v206, s26, v206
v_xor_b32 v207, s27, v207
v_xor_b32 v212, s24, v212
v_xor_b32 v213, s25, v213
v_xor_b32 v214, s26, v214
v_xor_b32 v215, s27, v215
v_xor_b32 v220, s24, v220
v_xor_b32 v221, s25, v221
v_xor_b32 v222, s26, v222
v_xor_b32 v223, s27, v223
v_xor_b32 v228, s24, v228
v_xor_b32 v229, s25, v229
v_xor_b32 v230, s26, v230
v_xor_b32 v231, s27, v231
v_xor_b32 v236, s24, v236
v_xor_b32 v237, s25, v237
v_xor_b32 v238, s26, v238
v_xor_b32 v239, s27, v239
v_or3_b32 v9, v196, v197, v198
v_or3_b32 v9, v9, v199, v204
v_or3_b32 v9, v9, v205, v206
v_or3_b32 v9, v9, v207, v212
v_or3_b32 v9, v9, v213, v214
v_or3_b32 v9, v9, v215, v220
v_or3_b32 v9, v9, v221, v222
v_or3_b32 v9, v9, v223, v228
v_or3_b32 v9, v9, v229, v230
v_or3_b32 v9, v9, v231, v236
v_or3_b32 v9, v9, v237, v238
v_or_b32 v9, v9, v239
v_and_b32 v9, 0xff, v9
v_cmp_ne_u32 vcc, 0, v9
s_nop 0
s_cbranch_vccz .Lpc3_r
s_sub_u32 s88, s88, 1
s_cmp_lg_u32 s88, 0
s_cbranch_scc1 .Lpc3_l
s_branch .Lpc3_r
s_endpgm
s_nop 0
s_nop 0
s_nop 0
s_nop 0
s_nop 0
s_nop 0
s_nop 0
s_nop 0
s_nop 0
s_nop 0
s_nop 0
s_nop 0
s_nop 0
s_nop 0
s_nop 0
s_nop 0
s_nop 0
s_nop 0
s_nop 0
s_nop 0
s_nop 0
s_nop 0
s_nop 0
s_nop 0
s_nop 0
s_nop 0
s_nop 0
s_nop 0
s_nop 0
s_nop 0
s_nop 0
s_nop 0
s_nop 0
s_nop 0
s_nop 0
s_nop 0
s_nop 0
s_nop 0
s_nop 0
s_nop 0
s_nop 0
s_nop 0
s_nop 0
s_nop 0
s_nop 0
s_nop 0
s_nop 0
s_nop 0
s_nop 0
s_nop 0
s_nop 0
s_nop 0
s_nop 0
s_nop 0
s_nop 0
s_nop 0
s_nop 0
s_nop 0
s_nop 0
s_nop 0
s_nop 0
s_nop 0
s_nop 0
s_nop 0
s_nop 0
s_nop 0
s_nop 0
s_nop 0
s_nop 0
s_nop 0
s_nop 0
s_nop 0
s_nop 0
s_nop 0
s_nop 0
s_nop 0
s_nop 0
s_nop 0
s_nop 0
s_nop 0
s_nop 0
s_nop 0
s_nop 0
s_nop 0
s_nop 0
s_nop 0
s_nop 0
s_nop 0
s_nop 0
s_nop 0
s_nop 0
s_nop 0
s_nop 0
s_nop 0
s_nop 0
s_nop 0
s_nop 0
s_nop 0
s_nop 0
s_nop 0
s_nop 0
s_nop 0
s_nop 0
s_nop 0
s_nop 0
s_nop 0
s_nop 0
s_nop 0
s_nop 0
s_nop 0
s_nop 0
s_nop 0
s_nop 0
s_nop 0
s_nop 0
s_nop 0
s_nop 0
s_nop 0
s_nop 0
s_nop 0
s_nop 0
s_nop 0
s_nop 0
s_nop 0
s_nop 0
s_nop 0
s_nop 0
s_nop 0
s_nop 0
s_nop 0
s_nop 0
s_nop 0
s_nop 0
s_nop 0
s_nop 0
s_nop 0
s_nop 0
s_nop 0
s_nop 0
s_nop 0
s_nop 0
s_nop 0
s_nop 0
s_nop 0
s_nop 0
s_nop 0
s_nop 0
s_nop 0
s_nop 0
s_nop 0
s_nop 0
s_nop 0
s_nop 0
s_nop 0
s_nop 0
s_nop 0
s_nop 0
s_nop 0
s_nop 0
s_nop 0
s_nop 0
s_nop 0
s_nop 0
s_nop 0
s_nop 0
s_nop 0
s_nop 0
s_nop 0
s_nop 0
s_nop 0
s_nop 0
s_nop 0
s_nop 0
s_nop 0
s_nop 0
s_nop 0
s_nop 0
s_nop 0
s_nop 0
s_nop 0
s_nop 0
s_nop 0
s_nop 0
s_nop 0
s_nop 0
s_nop 0
s_nop 0
s_nop 0
s_nop 0
s_nop 0
s_nop 0
s_nop 0
s_nop 0
s_nop 0
s_nop 0
s_nop 0
s_nop 0
s_nop 0
s_nop 0
s_nop 0
s_nop 0
s_nop 0
s_nop 0
s_nop 0
s_nop 0
s_nop 0
s_nop 0
s_nop 0
s_nop 0
s_nop 0
s_nop 0
s_nop 0
s_nop 0
s_nop 0
s_nop 0
s_nop 0
s_nop 0
s_nop 0
s_nop 0
s_nop 0
s_nop 0
s_nop 0
s_nop 0
s_nop 0
s_nop 0
s_nop 0
s_nop 0
s_nop 0
s_nop 0
s_nop 0
s_nop 0
s_nop 0
s_nop 0
s_nop 0
s_nop 0
s_nop 0
s_nop 0
s_nop 0
s_nop 0
s_nop 0
s_nop 0
s_nop 0
s_nop 0
s_nop 0
s_nop 0
s_nop 0
s_nop 0
s_nop 0
s_nop 0
s_nop 0
s_nop 0
s_nop 0
s_nop 0
s_nop 0
s_nop 0
s_nop 0
s_nop 0
s_nop 0
s_nop 0
s_nop 0
)"

        :: [ka] "s"(__builtin_amdgcn_kernarg_segment_ptr()), [b] "s"(blockIdx.x), [w] "s"(wave)
        : "memory", "scc", "vcc", "v0", "v1", "v2", "v3", "v4", "v5", "v6", "v7", "v8", "v9", "v10", "v11", "v12", "v13", "v14", "v15", "v16", "v17", "v18", "v19", "v20", "v21", "v22", "v23", "v24", "v25", "v26", "v27", "v28", "v29", "v30", "v31", "v32", "v33", "v34", "v35", "v36", "v37", "v38", "v39", "v40", "v41", "v42", "v43", "v44", "v45", "v46", "v47", "v48", "v49", "v50", "v51", "v52", "v53", "v54", "v55", "v56", "v57", "v58", "v59", "v60", "v61", "v62", "v63", "v64", "v65", "v66", "v67", "v68", "v69", "v70", "v71", "v72", "v73", "v74", "v75", "v76", "v77", "v78", "v79", "v80", "v81", "v82", "v83", "v84", "v85", "v86", "v87", "v88", "v89", "v90", "v91", "v92", "v93", "v94", "v95", "v96", "v97", "v98", "v99", "v100", "v101", "v102", "v103", "v104", "v105", "v106", "v107", "v108", "v109", "v110", "v111", "v112", "v113", "v114", "v115", "v116", "v117", "v118", "v119", "v120", "v121", "v122", "v123", "v124", "v125", "v126", "v127", "v128", "v129", "v130", "v131", "v132", "v133", "v134", "v135", "v136", "v137", "v138", "v139", "v140", "v141", "v142", "v143", "v144", "v145", "v146", "v147", "v148", "v149", "v150", "v151", "v152", "v153", "v154", "v155", "v156", "v157", "v158", "v159", "v160", "v161", "v162", "v163", "v164", "v165", "v166", "v167", "v168", "v169", "v170", "v171", "v172", "v173", "v174", "v175", "v176", "v177", "v178", "v179", "v180", "v181", "v182", "v183", "v184", "v185", "v186", "v187", "v188", "v189", "v190", "v191", "v192", "v193", "v194", "v195", "v196", "v197", "v198", "v199", "v200", "v201", "v202", "v203", "v204", "v205", "v206", "v207", "v208", "v209", "v210", "v211", "v212", "v213", "v214", "v215", "v216", "v217", "v218", "v219", "v220", "v221", "v222", "v223", "v224", "v225", "v226", "v227", "v228", "v229", "v230", "v231", "v232", "v233", "v234", "v235", "v236", "v237", "v238", "v239", "v240", "v241", "v242", "v243", "v244", "v245", "v246", "v247", "v248", "v249", "v250", "v251", "v252", "v253", "v254", "v255", "a0", "a1", "a2", "a3", "a4", "a5", "a6", "a7", "a8", "a9", "a10", "a11", "a12", "a13", "a14", "a15", "a16", "a17", "a18", "a19", "a20", "a21", "a22", "a23", "a24", "a25", "a26", "a27", "a28", "a29", "a30", "a31", "a32", "a33", "a34", "a35", "a36", "a37", "a38", "a39", "a40", "a41", "a42", "a43", "a44", "a45", "a46", "a47", "a48", "a49", "a50", "a51", "a52", "a53", "a54", "a55", "a56", "a57", "a58", "a59", "a60", "a61", "a62", "a63", "a64", "a65", "a66", "a67", "a68", "a69", "a70", "a71", "a72", "a73", "a74", "a75", "a76", "a77", "a78", "a79", "a80", "a81", "a82", "a83", "a84", "a85", "a86", "a87", "a88", "a89", "a90", "a91", "a92", "a93", "a94", "a95", "a96", "a97", "a98", "a99", "a100", "a101", "a102", "a103", "a104", "a105", "a106", "a107", "a108", "a109", "a110", "a111", "a112", "a113", "a114", "a115", "a116", "a117", "a118", "a119", "a120", "a121", "a122", "a123", "a124", "a125", "a126", "a127", "a128", "a129", "a130", "a131", "a132", "a133", "a134", "a135", "a136", "a137", "a138", "a139", "a140", "a141", "a142", "a143", "a144", "a145", "a146", "a147", "a148", "a149", "a150", "a151", "a152", "a153", "a154", "a155", "a156", "a157", "a158", "a159", "a160", "a161", "a162", "a163", "a164", "a165", "a166", "a167", "a168", "a169", "a170", "a171", "a172", "a173", "a174", "a175", "a176", "a177", "a178", "a179", "a180", "a181", "a182", "a183", "a184", "a185", "a186", "a187", "a188", "a189", "a190", "a191", "a192", "a193", "a194", "a195", "a196", "a197", "a198", "a199", "a200", "a201", "a202", "a203", "a204", "a205", "a206", "a207", "a208", "a209", "a210", "a211", "a212", "a213", "a214", "a215", "a216", "a217", "a218", "a219", "a220", "a221", "a222", "a223", "a224", "a225", "a226", "a227", "a228", "a229", "a230", "a231", "a232", "a233", "a234", "a235", "a236", "a237", "a238", "a239", "a240", "a241", "a242", "a243", "a244", "a245", "a246", "a247", "a248", "a249", "a250", "a251", "a252", "a253", "a254", "a255", "s4", "s5", "s6", "s7", "s8", "s9", "s10", "s11", "s12", "s13", "s14", "s15", "s16", "s17", "s18", "s19", "s20", "s21", "s22", "s23", "s24", "s25", "s26", "s27", "s28", "s29", "s30", "s31", "s32", "s33", "s34", "s35", "s36", "s37", "s38", "s39", "s40", "s41", "s42", "s43", "s44", "s45", "s46", "s47", "s48", "s49", "s50", "s51", "s52", "s53", "s54", "s55", "s56", "s57", "s58", "s59", "s60", "s61", "s62", "s63", "s64", "s65", "s66", "s67", "s68", "s69", "s70", "s71", "s72", "s73", "s74", "s75", "s76", "s77", "s78", "s79", "s80", "s81", "s82", "s83", "s84", "s85", "s86", "s87", "s88", "s89", "s90", "s91", "s92", "s93", "s94", "s95", "s96", "s97", "s98", "s99");
}
extern "C" void kernel_launch(void* const* d_in, const int* in_sizes, int n_in, void* d_out, int out_size, void* d_ws, size_t ws_size, hipStream_t stream) {
    (void)n_in; (void)ws_size;
    (void)hipFuncSetAttribute((const void*)k_all, hipFuncAttributeMaxDynamicSharedMemorySize, 163840);
#define IN(i) d_in[i], 2u * (unsigned)in_sizes[i], 0x20000u
    k_all<<<256, 256, 163840, stream>>>(IN(0), IN(1), IN(2), IN(3), IN(4), IN(5), IN(7), IN(8), IN(9), IN(10), d_ws, 109051904u, 0x20000u, d_out, 2u * (unsigned)out_size, 0x20000u);
}
```

```cpp
#pragma clang diagnostic ignored "-Winline-asm"
#include <hip/hip_runtime.h>
#include <stdint.h>
#define SRD(n) const void* p##n, unsigned n##_bytes, unsigned n##_fmt
__global__ __launch_bounds__(256, 1) void k_all(SRD(x), SRD(ckv), SRD(wq), SRD(wuk), SRD(wuv), SRD(wo), SRD(wr), SRD(wg), SRD(wu), SRD(wd), SRD(ws), SRD(out)) {
    const int wave = __builtin_amdgcn_readfirstlane(threadIdx.x >> 6);
    asm volatile(
R"(
.macro Mvvv d,a,b
v_mfma_f32_16x16x32_bf16 v[\d:\d+3], v[\a:\a+3], v[\b:\b+3], v[\d:\d+3]
.endm
.macro Zvvv d,a,b
v_mfma_f32_16x16x32_bf16 v[\d:\d+3], v[\a:\a+3], v[\b:\b+3], 0
.endm
.macro Cvvv d,a,b,c
v_mfma_f32_16x16x32_bf16 v[\d:\d+3], v[\a:\a+3], v[\b:\b+3], v[\c:\c+3]
.endm
.macro Mvav d,a,b
v_mfma_f32_16x16x32_bf16 v[\d:\d+3], a[\a:\a+3], v[\b:\b+3], v[\d:\d+3]
.endm
.macro Zvav d,a,b
v_mfma_f32_16x16x32_bf16 v[\d:\d+3], a[\a:\a+3], v[\b:\b+3], 0
.endm
.macro Cvav d,a,b,c
v_mfma_f32_16x16x32_bf16 v[\d:\d+3], a[\a:\a+3], v[\b:\b+3], v[\c:\c+3]
.endm
.macro Mvaa d,a,b
v_mfma_f32_16x16x32_bf16 v[\d:\d+3], a[\a:\a+3], a[\b:\b+3], v[\d:\d+3]
.endm
.macro Zvaa d,a,b
v_mfma_f32_16x16x32_bf16 v[\d:\d+3], a[\a:\a+3], a[\b:\b+3], 0
.endm
.macro Mavv d,a,b
v_mfma_f32_16x16x32_bf16 a[\d:\d+3], v[\a:\a+3], v[\b:\b+3], a[\d:\d+3]
.endm
.macro Zavv d,a,b
v_mfma_f32_16x16x32_bf16 a[\d:\d+3], v[\a:\a+3], v[\b:\b+3], 0
.endm
.macro Maav d,a,b
v_mfma_f32_16x16x32_bf16 a[\d:\d+3], a[\a:\a+3], v[\b:\b+3], a[\d:\d+3]
.endm
s_load_dwordx16 s[8:23], %[ka], 0x0
s_load_dwordx16 s[24:39], %[ka], 0x40
s_load_dwordx16 s[40:55], %[ka], 0x80
s_getpc_b64 s[60:61]
.Lstart:
s_mov_b32 s56, %[b]
s_mov_b32 s57, %[w]
v_mbcnt_lo_u32_b32 v16, -1, 0
v_mbcnt_hi_u32_b32 v16, -1, v16
v_lshlrev_b32 v0, 4, v16
s_lshr_b32 s62, s56, 3
s_lshl2_add_u32 s58, s62, s57
s_lshl_b32 s58, s58, 10
s_min_u32 s58, s58, .Lhot_end-.Lstart
s_add_u32 s60, s60, s58
s_addc_u32 s61, s61, 0
global_load_dwordx4 v[28:31], v0, s[60:61]
s_and_b32 s63, s62, 7
s_and_b32 s65, s56, 7
s_lshl_b32 s65, s65, 2
s_lshr_b32 s65, 0x73625140, s65
s_and_b32 s65, s65, 7
s_lshr_b32 s58, s62, 3
s_lshl2_add_u32 s64, s65, s58
v_lshrrev_b32 v17, 3, v16
v_and_b32 v18, 7, v16
v_and_b32 v19, 15, v16
v_lshrrev_b32 v20, 4, v16
s_and_b32 s59, s57, 1
s_lshl_b32 s58, s59, 6
v_xor_b32 v21, v18, v20
v_lshlrev_b32 v21, 4, v21
v_lshl_add_u32 v1, v17, 10, v21
v_xor_b32 v1, s58, v1
s_lshl_b32 s58, s59, 5
v_bfe_u32 v22, v16, 2, 3
v_lshrrev_b32 v23, 5, v16
v_lshlrev_b32 v23, 6, v23
v_lshl_add_u32 v22, v22, 13, v23
v_and_b32 v23, 3, v16
v_lshl_add_u32 v3, v23, 4, v22
v_xor_b32 v3, s58, v3
s_lshr_b32 s58, s57, 1
s_lshl_b32 s66, s58, 14
s_lshl_b32 s58, s59, 10
s_add_u32 s66, s66, s58
s_lshl_b32 s67, s63, 18
s_lshl3_add_u32 s67, s66, s67
s_lshl_b32 s68, s57, 16
s_lshl_b32 s58, s64, 8
s_add_u32 s68, s68, s58
s_lshl_b32 s69, s57, 12
s_add_u32 m0, s69, 98304
s_add_u32 s70, s68, 0
s_waitcnt lgkmcnt(0)
buffer_load_dwordx4 v3, s[20:23], s70 offen lds
s_add_u32 m0, s69, 99200
s_add_u32 s70, s68, 0
buffer_load_dwordx4 v3, s[20:23], s70 offen offset:128 lds
s_add_u32 m0, s69, 100352
s_add_u32 s70, s68, 0
buffer_load_dwordx4 v3, s[24:27], s70 offen lds
s_add_u32 m0, s69, 101248
s_add_u32 s70, s68, 0
buffer_load_dwordx4 v3, s[24:27], s70 offen offset:128 lds
s_add_u32 m0, s66, 0
s_add_u32 s70, s67, 0
buffer_load_dwordx4 v1, s[12:15], s70 offen lds
s_add_u32 m0, s66, 2048
s_add_u32 s70, s67, 16384
buffer_load_dwordx4 v1, s[12:15], s70 offen lds
s_add_u32 m0, s66, 4096
s_add_u32 s70, s67, 32768
buffer_load_dwordx4 v1, s[12:15], s70 offen lds
v_lshrrev_b32 v21, 1, v19
v_xor_b32 v21, v21, v20
v_lshlrev_b32 v21, 4, v21
v_lshl_add_u32 v24, v19, 7, v21
s_and_b32 s58, s66, 0x4000
v_add_u32 v5, s58, v24
v_lshrrev_b32 v21, 2, v19
v_lshlrev_b32 v21, 6, v21
v_and_b32 v22, 1, v20
v_lshl_add_u32 v21, v22, 5, v21
v_and_b32 v22, 3, v19
v_lshl_add_u32 v26, v22, 3, v21
v_lshl_add_u32 v21, v20, 12, v26
s_and_b32 s58, s66, 0x400
s_add_u32 s58, s58, 0x18000
v_add_u32 v9, s58, v21
v_xor_b32 v10, 32, v9
s_add_u32 m0, s66, 6144
s_add_u32 s70, s67, 49152
buffer_load_dwordx4 v1, s[12:15], s70 offen lds
s_add_u32 m0, s66, 8192
s_add_u32 s70, s67, 65536
buffer_load_dwordx4 v1, s[12:15], s70 offen lds
s_add_u32 m0, s66, 10240
s_add_u32 s70, s67, 81920
buffer_load_dwordx4 v1, s[12:15], s70 offen lds
s_add_u32 m0, s66, 12288
s_add_u32 s70, s67, 98304
buffer_load_dwordx4 v1, s[12:15], s70 offen lds
s_add_u32 m0, s66, 14336
s_add_u32 s70, s67, 114688
buffer_load_dwordx4 v1, s[12:15], s70 offen lds
s_add_u32 m0, s69, 114688
s_add_u32 s70, s68, 262144
buffer_load_dwordx4 v3, s[20:23], s70 offen lds
s_add_u32 m0, s69, 115584
s_add_u32 s70, s68, 262144
buffer_load_dwordx4 v3, s[20:23], s70 offen offset:128 lds
s_add_u32 m0, s69, 116736
s_add_u32 s70, s68, 262144
buffer_load_dwordx4 v3, s[24:27], s70 offen lds
s_add_u32 m0, s69, 117632
s_add_u32 s70, s68, 262144
buffer_load_dwordx4 v3, s[24:27], s70 offen offset:128 lds
s_waitcnt vmcnt(9)
s_barrier
ds_read_b128 v[96:99], v5 offset:0
ds_read_b64_tr_b16 v[128:129], v9 offset:0
ds_read_b64_tr_b16 v[130:131], v9 offset:256
ds_read_b64_tr_b16 v[136:137], v10 offset:0
ds_read_b64_tr_b16 v[138:139], v10 offset:256
ds_read_b64_tr_b16 v[144:145], v9 offset:512
ds_read_b64_tr_b16 v[146:147], v9 offset:768
ds_read_b64_tr_b16 v[152:153], v10 offset:512
ds_read_b64_tr_b16 v[154:155], v10 offset:768
ds_read_b64_tr_b16 v[160:161], v9 offset:2048
ds_read_b64_tr_b16 v[162:163], v9 offset:2304
s_waitcnt lgkmcnt(8)
Zavv 0,128,96
s_waitcnt lgkmcnt(6)
ds_read_b64_tr_b16 v[168:169], v10 offset:2048
ds_read_b64_tr_b16 v[170:171], v10 offset:2304
Zavv 4,136,96
s_waitcnt lgkmcnt(4)
ds_read_b64_tr_b16 v[176:177], v9 offset:2560
ds_read_b64_tr_b16 v[178:179], v9 offset:2816
Zavv 8,144,96
s_add_u32 m0, s69, 131072
s_add_u32 s70, s68, 524288
ds_read_b64_tr_b16 v[184:185], v10 offset:2560
ds_read_b64_tr_b16 v[186:187], v10 offset:2816
buffer_load_dwordx4 v3, s[20:23], s70 offen lds
Zavv 12,152,96
s_waitcnt lgkmcnt(6)
ds_read_b128 v[104:107], v5 offset:2048
Zavv 16,96,160
s_waitcnt lgkmcnt(5)
s_add_u32 m0, s69, 131968
s_add_u32 s70, s68, 524288
ds_read_b128 v[112:115], v5 offset:4096
buffer_load_dwordx4 v3, s[20:23], s70 offen offset:128 lds
Zavv 20,96,168
s_waitcnt lgkmcnt(4)
s_add_u32 m0, s69, 133120
s_add_u32 s70, s68, 524288
v_xor_b32 v6, 64, v5
Zavv 24,96,176
s_waitcnt lgkmcnt(2)
s_lshl2_add_u32 s71, s62, s57
buffer_load_dwordx4 v3, s[24:27], s70 offen lds
Zavv 28,96,184
s_waitcnt lgkmcnt(1)
s_lshl3_add_u32 s71, s71, s65
s_add_u32 m0, s69, 134016
s_add_u32 s70, s68, 524288
Zavv 32,128,104
s_lshl_b32 s71, s71, 10
buffer_load_dwordx4 v3, s[24:27], s70 offen offset:128 lds
Zavv 36,136,104
s_lshl2_add_u32 s58, s56, s57
s_add_u32 m0, s69, 147456
s_add_u32 s70, s68, 786432
Zavv 40,144,104
s_lshl_b32 s72, s58, 12
buffer_load_dwordx4 v3, s[20:23], s70 offen lds
Zavv 44,152,104
v_lshl_add_u32 v84, v20, 3, v18
Zavv 48,104,160
s_waitcnt vmcnt(9)
s_add_u32 m0, s69, 148352
s_add_u32 s70, s68, 786432
buffer_load_dwordx4 v3, s[20:23], s70 offen offset:128 lds
v_lshlrev_b32 v84, 4, v84
Zavv 52,104,168
s_barrier
v_and_b32 v22, 1, v17
Zavv 56,104,176
ds_read_b128 v[120:123], v5 offset:6144
v_lshl_add_u32 v79, v22, 11, v84
Zavv 60,104,184
s_waitcnt lgkmcnt(1)
Zavv 64,128,112
s_add_u32 m0, s69, 149504
s_add_u32 s70, s68, 786432
ds_read_b128 v[96:99], v5 offset:8192
Zavv 68,136,112
buffer_load_dwordx4 v3, s[24:27], s70 offen lds
Zavv 72,144,112
v_mul_u32_u24 v77, 2176, v22
v_add_u32 v77, v77, v84
Zavv 76,152,112
s_mul_i32 s73, s58, 4352
buffer_load_dwordx4 v[32:35], v79, s[8:11], s72 offen
Zavv 80,112,160
s_add_u32 m0, s69, 150400
s_add_u32 s70, s68, 786432
s_add_u32 s73, s73, 4194304
Zavv 84,112,168
s_waitcnt lgkmcnt(1)
s_lshl4_add_u32 s74, s58, 4096
buffer_load_dwordx4 v3, s[24:27], s70 offen offset:128 lds
Zavv 88,112,176
s_add_u32 m0, s66, 32768
s_add_u32 s70, s67, 128
s_lshl_b32 s58, s58, 1
Zavv 92,112,184
s_sub_u32 s58, 0x7ff, s58
buffer_load_dwordx4 v1, s[12:15], s70 offen lds
Zavv 96,128,120
ds_read_b128 v[104:107], v5 offset:10240
v_sub_u32 v78, s58, v22
Zavv 100,136,120
s_add_u32 s75, s69, 4096
buffer_load_dwordx4 v[36:39], v79, s[8:11], s72 offen offset:512
Zavv 104,144,120
s_add_u32 m0, s66, 34816
s_add_u32 s70, s67, 16512
s_lshl_b32 s76, s57, 13
Zavv 108,152,120
s_lshl_b32 s77, s57, 11
buffer_load_dwordx4 v1, s[12:15], s70 offen lds
Zavv 112,120,160
s_add_u32 m0, s66, 36864
s_add_u32 s70, s67, 32896
v_cmp_eq_u32 vcc, 0, v16
Zavv 116,120,168
s_waitcnt vmcnt(12)
buffer_load_dwordx4 v1, s[12:15], s70 offen lds
Zavv 120,120,176
s_barrier
Zavv 124,120,184
s_waitcnt lgkmcnt(1)
ds_read_b64_tr_b16 v[132:133], v9 offset:16384
v_mov_b32 v23, 0x80000000
Zavv 128,128,96
ds_read_b128 v[112:115], v5 offset:12288
buffer_load_dwordx4 v[40:43], v79, s[8:11], s72 offen offset:1024
Zavv 132,136,96
ds_read_b64_tr_b16 v[134:135], v9 offset:16640
v_cndmask_b32 v13, v23, 0, vcc
Zavv 136,144,96
s_add_u32 m0, s66, 38912
s_add_u32 s70, s67, 49280
ds_read_b64_tr_b16 v[140:141], v10 offset:16384
buffer_load_dwordx4 v1, s[12:15], s70 offen lds
Zavv 140,152,96
ds_read_b64_tr_b16 v[142:143], v10 offset:16640
Zavv 144,96,160
s_add_u32 m0, s66, 40960
s_add_u32 s70, s67, 65664
ds_read_b64_tr_b16 v[148:149], v9 offset:16896
buffer_load_dwordx4 v1, s[12:15], s70 offen lds
Zavv 148,96,168
ds_read_b64_tr_b16 v[150:151], v9 offset:17152
Zavv 152,96,176
s_add_u32 m0, s66, 43008
s_add_u32 s70, s67, 82048
ds_read_b64_tr_b16 v[156:157], v10 offset:16896
buffer_load_dwordx4 v1, s[12:15], s70 offen lds
Zavv 156,96,184
s_waitcnt lgkmcnt(8)
ds_read_b64_tr_b16 v[158:159], v10 offset:17152
Zavv 160,128,104
ds_read_b128 v[120:123], v5 offset:14336
buffer_load_dwordx4 v[44:47], v79, s[8:11], s72 offen offset:1536
Zavv 164,136,104
ds_read_b64_tr_b16 v[164:165], v9 offset:18432
Zavv 168,144,104
s_add_u32 s70, s71, 1048576
ds_read_b128 v[100:103], v6 offset:0
buffer_load_dwordx4 v[80:83], v0, s[20:23], s70 offen
Zavv 172,152,104
ds_read_b64_tr_b16 v[166:167], v9 offset:18688
Zavv 176,104,160
s_add_u32 m0, s66, 45056
s_add_u32 s70, s67, 98432
ds_read_b128 v[108:111], v6 offset:2048
buffer_load_dwordx4 v1, s[12:15], s70 offen lds
Zavv 180,104,168
s_waitcnt lgkmcnt(11)
ds_read_b64_tr_b16 v[172:173], v10 offset:18432
Zavv 184,104,176
s_add_u32 m0, s66, 47104
s_add_u32 s70, s67, 114816
ds_read_b128 v[116:119], v6 offset:4096
buffer_load_dwordx4 v1, s[12:15], s70 offen lds
Zavv 188,104,184
ds_read_b64_tr_b16 v[174:175], v10 offset:18688
Zavv 192,128,112
ds_read_b128 v[124:127], v6 offset:6144
Zavv 196,136,112
s_waitcnt lgkmcnt(12)
ds_read_b64_tr_b16 v[180:181], v9 offset:18944
Zavv 200,144,112
ds_read_b64_tr_b16 v[182:183], v9 offset:19200
Zavv 204,152,112
ds_read_b64_tr_b16 v[188:189], v10 offset:18944
Zavv 208,112,160
s_waitcnt lgkmcnt(12)
ds_read_b64_tr_b16 v[190:191], v10 offset:19200
Zavv 212,112,168
buffer_load_dwordx2 v[86:87], v13, s[48:51], 0 offen sc1
Zavv 216,112,176
s_waitcnt lgkmcnt(12)
v_add_u32 v7, 0x10000, v5
v_add_u32 v8, 0x10000, v6
Zavv 220,112,184
s_and_b32 s78, s69, 0x2000
s_add_u32 s78, s78, s69
s_add_u32 s59, s78, 0x10000
Zavv 224,128,120
v_add_u32 v24, s59, v24
v_xor_b32 v25, 64, v24
Zavv 228,136,120
v_lshl_add_u32 v26, v20, 11, v26
v_add_u32 v26, 0x18000, v26
Zavv 232,144,120
s_waitcnt vmcnt(13) lgkmcnt(0)
v_xor_b32 v27, 32, v26
Zavv 236,152,120
s_barrier
s_lshr_b32 s58, s57, 1
Zavv 240,120,160
s_and_b32 s59, s57, 1
s_lshl_b32 s79, s64, 4
ds_read_b64_tr_b16 v[192:193], v9 offset:32768
Zavv 244,120,168
s_add_u32 m0, s69, 98304
s_add_u32 s70, s68, 1048576
ds_read_b64_tr_b16 v[194:195], v9 offset:33024
buffer_load_dwordx4 v3, s[20:23], s70 offen lds
Zavv 248,120,176
s_lshl1_add_u32 s79, s63, s79
s_add_u32 s79, s79, s58
ds_read_b64_tr_b16 v[196:197], v9 offset:49152
Zavv 252,120,184
s_mul_i32 s79, s79, 69632
s_lshl_b32 s80, s59, 13
ds_read_b64_tr_b16 v[198:199], v9 offset:49408
Mavv 0,132,100
s_add_u32 s80, s80, s79
s_add_u32 s80, s80, 16793600
ds_read_b64_tr_b16 v[200:201], v10 offset:32768
Mavv 4,140,100
s_lshl_b32 s59, s59, 11
s_add_u32 s79, s79, s59
ds_read_b64_tr_b16 v[202:203], v10 offset:33024
Mavv 8,148,100
s_add_u32 m0, s69, 99200
s_add_u32 s70, s68, 1048576
ds_read_b64_tr_b16 v[204:205], v10 offset:49152
buffer_load_dwordx4 v3, s[20:23], s70 offen offset:128 lds
Mavv 12,156,100
s_add_u32 s79, s79, 16777216
s_lshl2_add_u32 s58, s63, s57
ds_read_b64_tr_b16 v[206:207], v10 offset:49408
Mavv 16,100,164
s_mul_i32 s81, s58, 69632
s_sub_u32 s82, 8581120, s81
ds_read_b64_tr_b16 v[208:209], v9 offset:33280
Mavv 20,100,172
s_add_u32 s81, s81, 4194304
)"
R"(
ds_read_b64_tr_b16 v[210:211], v9 offset:33536
v_xor_b32 v21, v18, v20
Mavv 24,100,180
ds_read_b64_tr_b16 v[212:213], v9 offset:49664
v_lshlrev_b32 v21, 4, v21
Mavv 28,100,188
s_add_u32 m0, s69, 100352
s_add_u32 s70, s68, 1048576
ds_read_b64_tr_b16 v[214:215], v9 offset:49920
buffer_load_dwordx4 v3, s[24:27], s70 offen lds
Mavv 32,132,108
ds_read_b128 v[100:103], v6 offset:8192
v_mul_u32_u24 v22, 2176, v17
Mavv 36,140,108
ds_read_b64_tr_b16 v[216:217], v10 offset:33280
v_add_u32 v11, v22, v21
Mavv 40,148,108
ds_read_b64_tr_b16 v[218:219], v10 offset:33536
v_xor_b32 v21, 64, v21
Mavv 44,156,108
s_waitcnt lgkmcnt(12)
ds_read_b64_tr_b16 v[220:221], v10 offset:49664
v_add_u32 v22, 17408, v22
Mavv 48,108,164
s_add_u32 m0, s69, 101248
s_add_u32 s70, s68, 1048576
ds_read_b64_tr_b16 v[222:223], v10 offset:49920
buffer_load_dwordx4 v3, s[24:27], s70 offen offset:128 lds
Mavv 52,108,172
s_waitcnt lgkmcnt(11)
ds_read_b64_tr_b16 v[224:225], v9 offset:34816
v_add_u32 v12, v22, v21
Mavv 56,108,180
s_lshl_b32 s83, s64, 5
s_add_u32 s58, s58, s83
ds_read_b64_tr_b16 v[226:227], v9 offset:35072
Mavv 60,108,188
s_lshl3_add_u32 s84, s58, 24576
s_lshl3_add_u32 s83, s83, 24576
ds_read_b64_tr_b16 v[228:229], v9 offset:51200
Mavv 64,132,116
s_waitcnt lgkmcnt(11)
ds_read_b128 v[108:111], v6 offset:10240
Mavv 68,140,116
s_add_u32 m0, s69, 114688
s_add_u32 s70, s68, 1310720
ds_read_b64_tr_b16 v[230:231], v9 offset:51456
buffer_load_dwordx4 v3, s[20:23], s70 offen lds
Mavv 72,148,116
ds_read_b64_tr_b16 v[232:233], v10 offset:34816
Mavv 76,156,116
s_waitcnt lgkmcnt(11)
ds_read_b64_tr_b16 v[234:235], v10 offset:35072
Mavv 80,116,164
s_add_u32 s70, s71, 1048576
ds_read_b64_tr_b16 v[236:237], v10 offset:51200
buffer_load_dwordx4 v[80:83], v0, s[24:27], s70 offen
Mavv 84,116,172
ds_read_b64_tr_b16 v[238:239], v10 offset:51456
Mavv 88,116,180
s_add_u32 m0, s69, 115584
s_add_u32 s70, s68, 1310720
ds_read_b64_tr_b16 v[240:241], v9 offset:35328
buffer_load_dwordx4 v3, s[20:23], s70 offen offset:128 lds
Mavv 92,116,188
s_waitcnt lgkmcnt(12)
ds_read_b64_tr_b16 v[242:243], v9 offset:35584
Mavv 96,132,124
ds_read_b128 v[116:119], v6 offset:12288
Mavv 100,140,124
ds_read_b64_tr_b16 v[244:245], v9 offset:51712
Mavv 104,148,124
s_waitcnt lgkmcnt(12)
ds_read_b64_tr_b16 v[246:247], v9 offset:51968
Mavv 108,156,124
s_add_u32 m0, s69, 116736
s_add_u32 s70, s68, 1310720
ds_read_b64_tr_b16 v[248:249], v10 offset:35328
buffer_load_dwordx4 v3, s[24:27], s70 offen lds
Mavv 112,124,164
ds_read_b64_tr_b16 v[250:251], v10 offset:35584
Mavv 116,124,172
s_waitcnt lgkmcnt(12)
ds_read_b64_tr_b16 v[252:253], v10 offset:51712
Mavv 120,124,180
ds_read_b64_tr_b16 v[254:255], v10 offset:51968
Mavv 124,124,188
Mavv 128,132,100
s_add_u32 m0, s69, 117632
s_add_u32 s70, s68, 1310720
ds_read_b128 v[124:127], v6 offset:14336
Mavv 132,140,100
buffer_load_dwordx4 v3, s[24:27], s70 offen offset:128 lds
Mavv 136,148,100
Mavv 140,156,100
Mavv 144,100,164
s_add_u32 m0, s66, 65536
s_add_u32 s70, s67, 256
Mavv 148,100,172
buffer_load_dwordx4 v1, s[12:15], s70 offen lds
Mavv 152,100,180
s_waitcnt vmcnt(11) lgkmcnt(0)
Mavv 156,100,188
s_barrier
Mavv 160,132,108
Mavv 164,140,108
s_add_u32 m0, s66, 67584
s_add_u32 s70, s67, 16640
ds_read_b128 v[96:99], v5 offset:32768
Mavv 168,148,108
buffer_load_dwordx4 v1, s[12:15], s70 offen lds
Mavv 172,156,108
ds_read_b128 v[100:103], v6 offset:32768
Mavv 176,108,164
Mavv 180,108,172
Mavv 184,108,180
s_add_u32 m0, s66, 69632
s_add_u32 s70, s67, 33024
Mavv 188,108,188
buffer_load_dwordx4 v1, s[12:15], s70 offen lds
Mavv 192,132,116
Mavv 196,140,116
Mavv 200,148,116
Mavv 204,156,116
s_add_u32 m0, s66, 71680
s_add_u32 s70, s67, 49408
Mavv 208,116,164
buffer_load_dwordx4 v1, s[12:15], s70 offen lds
Mavv 212,116,172
ds_read_b128 v[104:107], v5 offset:34816
Mavv 216,116,180
Mavv 220,116,188
ds_read_b128 v[108:111], v6 offset:34816
Mavv 224,132,124
s_add_u32 m0, s66, 73728
s_add_u32 s70, s67, 65792
v_mov_b32 v64, 0x3f803f80
Mavv 228,140,124
buffer_load_dwordx4 v1, s[12:15], s70 offen lds
Mavv 232,148,124
v_mov_b32 v65, 0x3f803f80
v_mov_b32 v66, 0x3f803f80
Mavv 236,156,124
v_mov_b32 v67, 0x3f803f80
Mavv 240,124,164
Mavv 244,124,172
s_add_u32 m0, s66, 75776
s_add_u32 s70, s67, 82176
Zvvv 68,64,32
Mavv 248,124,180
buffer_load_dwordx4 v1, s[12:15], s70 offen lds
Mvvv 68,64,36
Mavv 252,124,188
s_waitcnt lgkmcnt(3)
Mvvv 68,64,40
Mavv 0,192,96
s_waitcnt lgkmcnt(2)
s_add_u32 s70, s71, 2097152
ds_read_b128 v[112:115], v5 offset:36864
Mvvv 68,64,44
Mavv 0,196,100
buffer_load_dwordx4 v[80:83], v0, s[20:23], s70 offen
Mavv 4,200,96
s_add_u32 m0, s66, 77824
s_add_u32 s70, s67, 98560
ds_read_b128 v[116:119], v6 offset:36864
Mavv 4,204,100
buffer_load_dwordx4 v1, s[12:15], s70 offen lds
Mavv 8,208,96
Mavv 8,212,100
Mavv 12,216,96
Mavv 12,220,100
s_add_u32 m0, s66, 79872
s_add_u32 s70, s67, 114944
Mavv 16,96,224
buffer_load_dwordx4 v1, s[12:15], s70 offen lds
Mavv 16,100,228
v_mov_b32 v72, v68
Mavv 20,96,232
Mavv 20,100,236
v_add_f32_dpp v72, v72, v72 quad_perm:[1,0,3,2] row_mask:0xf bank_mask:0xf bound_ctrl:1
Mavv 24,96,240
s_add_u32 m0, s69, 131072
s_add_u32 s70, s68, 1572864
Mavv 24,100,244
buffer_load_dwordx4 v3, s[20:23], s70 offen lds
Mavv 28,96,248
v_add_f32_dpp v72, v72, v72 quad_perm:[2,3,0,1] row_mask:0xf bank_mask:0xf bound_ctrl:1
Mavv 28,100,252
s_waitcnt lgkmcnt(3)
Mavv 32,192,104
s_waitcnt lgkmcnt(2)
ds_read_b128 v[120:123], v5 offset:38912
v_add_f32_dpp v72, v72, v72 row_half_mirror row_mask:0xf bank_mask:0xf bound_ctrl:1
Mavv 32,196,108
v_mul_f32 v72, 0x3a800000, v72
v_cvt_pk_bf16_f32 v73, 0, v72
Mavv 36,200,104
s_add_u32 m0, s69, 131968
s_add_u32 s70, s68, 1572864
ds_read_b128 v[124:127], v6 offset:38912
Mavv 36,204,108
buffer_load_dwordx4 v3, s[20:23], s70 offen offset:128 lds
Mavv 40,208,104
v_sub_f32 v72, v72, v73
v_mul_f32 v72, v72, v72
Mavv 40,212,108
v_mul_f32 v72, 0xc2000000, v72
v_lshlrev_b32 v75, 16, v32
Mavv 44,216,104
s_add_u32 m0, s69, 133120
s_add_u32 s70, s68, 1572864
v_and_b32 v76, 0xffff0000, v32
Mavv 44,220,108
buffer_load_dwordx4 v3, s[24:27], s70 offen lds
Mavv 48,104,224
v_sub_f32 v75, v75, v73
v_sub_f32 v76, v76, v73
Mavv 48,108,228
v_fmac_f32 v72, v75, v75
v_cvt_pk_bf16_f32 v32, v75, v76
Mavv 52,104,232
v_fmac_f32 v72, v76, v76
v_lshlrev_b32 v75, 16, v33
Mavv 52,108,236
s_add_u32 m0, s69, 134016
s_add_u32 s70, s68, 1572864
v_and_b32 v76, 0xffff0000, v33
Mavv 56,104,240
buffer_load_dwordx4 v3, s[24:27], s70 offen offset:128 lds
Mavv 56,108,244
v_sub_f32 v75, v75, v73
v_sub_f32 v76, v76, v73
Mavv 60,104,248
v_fmac_f32 v72, v75, v75
v_cvt_pk_bf16_f32 v33, v75, v76
Mavv 60,108,252
s_waitcnt lgkmcnt(2)
v_fmac_f32 v72, v76, v76
v_lshlrev_b32 v75, 16, v34
Mavv 64,192,112
s_add_u32 m0, s69, 147456
s_add_u32 s70, s68, 1835008
ds_read_b128 v[96:99], v5 offset:40960
Mavv 64,196,116
buffer_load_dwordx4 v3, s[20:23], s70 offen lds
Mavv 68,200,112
ds_read_b128 v[100:103], v6 offset:40960
v_and_b32 v76, 0xffff0000, v34
Mavv 68,204,116
v_sub_f32 v75, v75, v73
v_sub_f32 v76, v76, v73
Mavv 72,208,112
v_fmac_f32 v72, v75, v75
v_cvt_pk_bf16_f32 v34, v75, v76
Mavv 72,212,116
s_add_u32 m0, s69, 148352
s_add_u32 s70, s68, 1835008
v_fmac_f32 v72, v76, v76
Mavv 76,216,112
buffer_load_dwordx4 v3, s[20:23], s70 offen offset:128 lds
Mavv 76,220,116
v_lshlrev_b32 v75, 16, v35
v_and_b32 v76, 0xffff0000, v35
Mavv 80,112,224
v_sub_f32 v75, v75, v73
v_sub_f32 v76, v76, v73
Mavv 80,116,228
v_fmac_f32 v72, v75, v75
v_cvt_pk_bf16_f32 v35, v75, v76
Mavv 84,112,232
s_add_u32 m0, s69, 149504
s_add_u32 s70, s68, 1835008
v_fmac_f32 v72, v76, v76
Mavv 84,116,236
buffer_load_dwordx4 v3, s[24:27], s70 offen lds
Mavv 88,112,240
s_add_u32 s70, s71, 2097152
v_lshlrev_b32 v75, 16, v36
v_and_b32 v76, 0xffff0000, v36
Mavv 88,116,244
buffer_load_dwordx4 v[80:83], v0, s[24:27], s70 offen
Mavv 92,112,248
v_sub_f32 v75, v75, v73
v_sub_f32 v76, v76, v73
Mavv 92,116,252
s_waitcnt lgkmcnt(2)
v_fmac_f32 v72, v75, v75
v_cvt_pk_bf16_f32 v36, v75, v76
Mavv 96,192,120
s_add_u32 m0, s69, 150400
s_add_u32 s70, s68, 1835008
ds_read_b128 v[104:107], v5 offset:43008
Mavv 96,196,124
buffer_load_dwordx4 v3, s[24:27], s70 offen offset:128 lds
Mavv 100,200,120
ds_read_b128 v[108:111], v6 offset:43008
v_fmac_f32 v72, v76, v76
Mavv 100,204,124
s_waitcnt vmcnt(18)
v_lshlrev_b32 v75, 16, v37
v_and_b32 v76, 0xffff0000, v37
Mavv 104,208,120
s_barrier
Mavv 104,212,124
ds_read_b64_tr_b16 v[128:129], v9 offset:0
v_sub_f32 v75, v75, v73
Mavv 108,216,120
ds_read_b64_tr_b16 v[130:131], v9 offset:256
v_sub_f32 v76, v76, v73
Mavv 108,220,124
ds_read_b64_tr_b16 v[132:133], v9 offset:16384
v_fmac_f32 v72, v75, v75
Mavv 112,120,224
ds_read_b64_tr_b16 v[134:135], v9 offset:16640
v_cvt_pk_bf16_f32 v37, v75, v76
Mavv 112,124,228
ds_read_b64_tr_b16 v[136:137], v10 offset:0
v_fmac_f32 v72, v76, v76
Mavv 116,120,232
ds_read_b64_tr_b16 v[138:139], v10 offset:256
v_lshlrev_b32 v75, 16, v38
Mavv 116,124,236
ds_read_b64_tr_b16 v[140:141], v10 offset:16384
v_and_b32 v76, 0xffff0000, v38
Mavv 120,120,240
ds_read_b64_tr_b16 v[142:143], v10 offset:16640
v_sub_f32 v75, v75, v73
Mavv 120,124,244
ds_read_b64_tr_b16 v[144:145], v9 offset:512
v_sub_f32 v76, v76, v73
Mavv 124,120,248
ds_read_b64_tr_b16 v[146:147], v9 offset:768
v_fmac_f32 v72, v75, v75
Mavv 124,124,252
s_waitcnt lgkmcnt(13)
ds_read_b64_tr_b16 v[148:149], v9 offset:16896
v_cvt_pk_bf16_f32 v38, v75, v76
Mavv 128,192,96
s_waitcnt lgkmcnt(13)
ds_read_b128 v[112:115], v5 offset:45056
v_fmac_f32 v72, v76, v76
Mavv 128,196,100
ds_read_b64_tr_b16 v[150:151], v9 offset:17152
v_lshlrev_b32 v75, 16, v39
Mavv 132,200,96
s_waitcnt lgkmcnt(12)
ds_read_b128 v[116:119], v6 offset:45056
v_and_b32 v76, 0xffff0000, v39
Mavv 132,204,100
ds_read_b64_tr_b16 v[152:153], v10 offset:512
v_sub_f32 v75, v75, v73
Mavv 136,208,96
ds_read_b64_tr_b16 v[154:155], v10 offset:768
v_sub_f32 v76, v76, v73
Mavv 136,212,100
s_waitcnt lgkmcnt(12)
ds_read_b64_tr_b16 v[156:157], v10 offset:16896
v_fmac_f32 v72, v75, v75
Mavv 140,216,96
ds_read_b64_tr_b16 v[158:159], v10 offset:17152
v_cvt_pk_bf16_f32 v39, v75, v76
Mavv 140,220,100
ds_read_b64_tr_b16 v[160:161], v9 offset:2048
v_fmac_f32 v72, v76, v76
Mavv 144,96,224
s_waitcnt lgkmcnt(12)
ds_read_b64_tr_b16 v[162:163], v9 offset:2304
v_lshlrev_b32 v75, 16, v40
Mavv 144,100,228
ds_read_b64_tr_b16 v[164:165], v9 offset:18432
v_and_b32 v76, 0xffff0000, v40
Mavv 148,96,232
ds_read_b64_tr_b16 v[166:167], v9 offset:18688
v_sub_f32 v75, v75, v73
Mavv 148,100,236
s_waitcnt lgkmcnt(12)
ds_read_b64_tr_b16 v[168:169], v10 offset:2048
v_sub_f32 v76, v76, v73
Mavv 152,96,240
ds_read_b64_tr_b16 v[170:171], v10 offset:2304
v_fmac_f32 v72, v75, v75
Mavv 152,100,244
ds_read_b64_tr_b16 v[172:173], v10 offset:18432
v_cvt_pk_bf16_f32 v40, v75, v76
Mavv 156,96,248
s_waitcnt lgkmcnt(12)
ds_read_b64_tr_b16 v[174:175], v10 offset:18688
v_fmac_f32 v72, v76, v76
Mavv 156,100,252
ds_read_b64_tr_b16 v[176:177], v9 offset:2560
v_lshlrev_b32 v75, 16, v41
Mavv 160,192,104
ds_read_b128 v[120:123], v5 offset:47104
v_and_b32 v76, 0xffff0000, v41
Mavv 160,196,108
s_waitcnt lgkmcnt(12)
ds_read_b64_tr_b16 v[178:179], v9 offset:2816
v_sub_f32 v75, v75, v73
Mavv 164,200,104
ds_read_b128 v[124:127], v6 offset:47104
v_sub_f32 v76, v76, v73
Mavv 164,204,108
ds_read_b64_tr_b16 v[180:181], v9 offset:18944
v_fmac_f32 v72, v75, v75
Mavv 168,208,104
s_waitcnt lgkmcnt(12)
ds_read_b64_tr_b16 v[182:183], v9 offset:19200
v_cvt_pk_bf16_f32 v41, v75, v76
Mavv 168,212,108
ds_read_b64_tr_b16 v[184:185], v10 offset:2560
v_fmac_f32 v72, v76, v76
Mavv 172,216,104
ds_read_b64_tr_b16 v[186:187], v10 offset:2816
v_lshlrev_b32 v75, 16, v42
Mavv 172,220,108
s_waitcnt lgkmcnt(12)
ds_read_b64_tr_b16 v[188:189], v10 offset:18944
v_and_b32 v76, 0xffff0000, v42
Mavv 176,104,224
ds_read_b64_tr_b16 v[190:191], v10 offset:19200
v_sub_f32 v75, v75, v73
Mavv 176,108,228
buffer_load_dwordx4 v[48:51], v84, s[32:35], 0 offen
Mavv 180,104,232
v_sub_f32 v76, v76, v73
v_fmac_f32 v72, v75, v75
Mavv 180,108,236
buffer_load_dwordx4 v[52:55], v84, s[32:35], 0 offen offset:512
Mavv 184,104,240
s_add_u32 s70, s71, 3145728
)"
R"(
v_cvt_pk_bf16_f32 v42, v75, v76
v_fmac_f32 v72, v76, v76
Mavv 184,108,244
buffer_load_dwordx4 v[80:83], v0, s[20:23], s70 offen
Mavv 188,104,248
v_lshlrev_b32 v75, 16, v43
v_and_b32 v76, 0xffff0000, v43
Mavv 188,108,252
buffer_load_dwordx4 v[56:59], v84, s[32:35], 0 offen offset:1024
Mavv 192,192,112
s_add_u32 m0, s66, 0
s_add_u32 s70, s67, 384
v_sub_f32 v75, v75, v73
Mavv 192,196,116
buffer_load_dwordx4 v1, s[12:15], s70 offen lds
Mavv 196,200,112
v_sub_f32 v76, v76, v73
v_fmac_f32 v72, v75, v75
Mavv 196,204,116
s_waitcnt vmcnt(14) lgkmcnt(0)
v_cvt_pk_bf16_f32 v43, v75, v76
v_fmac_f32 v72, v76, v76
Mavv 200,208,112
s_barrier
Mavv 200,212,116
v_lshlrev_b32 v75, 16, v44
v_and_b32 v76, 0xffff0000, v44
Mavv 204,216,112
s_add_u32 m0, s66, 2048
s_add_u32 s70, s67, 16768
ds_read_b128 v[96:99], v7 offset:0
Mavv 204,220,116
buffer_load_dwordx4 v1, s[12:15], s70 offen lds
Mavv 208,112,224
ds_read_b128 v[100:103], v8 offset:0
v_sub_f32 v75, v75, v73
Mavv 208,116,228
v_sub_f32 v76, v76, v73
v_fmac_f32 v72, v75, v75
Mavv 212,112,232
s_add_u32 m0, s66, 4096
s_add_u32 s70, s67, 33152
v_cvt_pk_bf16_f32 v44, v75, v76
Mavv 212,116,236
buffer_load_dwordx4 v1, s[12:15], s70 offen lds
Mavv 216,112,240
v_fmac_f32 v72, v76, v76
v_lshlrev_b32 v75, 16, v45
Mavv 216,116,244
v_and_b32 v76, 0xffff0000, v45
v_sub_f32 v75, v75, v73
Mavv 220,112,248
v_sub_f32 v76, v76, v73
v_fmac_f32 v72, v75, v75
Mavv 220,116,252
v_cvt_pk_bf16_f32 v45, v75, v76
v_fmac_f32 v72, v76, v76
Mavv 224,192,120
buffer_load_dwordx4 v[60:63], v84, s[32:35], 0 offen offset:1536
Mavv 224,196,124
v_lshlrev_b32 v75, 16, v46
v_and_b32 v76, 0xffff0000, v46
Mavv 228,200,120
s_add_u32 m0, s66, 6144
s_add_u32 s70, s67, 49536
ds_read_b128 v[104:107], v7 offset:2048
Mavv 228,204,124
buffer_load_dwordx4 v1, s[12:15], s70 offen lds
Mavv 232,208,120
ds_read_b128 v[108:111], v8 offset:2048
v_sub_f32 v75, v75, v73
Mavv 232,212,124
v_sub_f32 v76, v76, v73
v_fmac_f32 v72, v75, v75
Mavv 236,216,120
s_add_u32 m0, s66, 8192
s_add_u32 s70, s67, 65920
v_cvt_pk_bf16_f32 v46, v75, v76
Mavv 236,220,124
buffer_load_dwordx4 v1, s[12:15], s70 offen lds
Mavv 240,120,224
v_fmac_f32 v72, v76, v76
v_lshlrev_b32 v75, 16, v47
Mavv 240,124,228
v_and_b32 v76, 0xffff0000, v47
v_sub_f32 v75, v75, v73
Mavv 244,120,232
v_sub_f32 v76, v76, v73
v_fmac_f32 v72, v75, v75
Mavv 244,124,236
s_add_u32 m0, s66, 10240
s_add_u32 s70, s67, 82304
v_cvt_pk_bf16_f32 v47, v75, v76
Mavv 248,120,240
buffer_load_dwordx4 v1, s[12:15], s70 offen lds
Mavv 248,124,244
v_fmac_f32 v72, v76, v76
Mavv 252,120,248
Mavv 252,124,252
s_waitcnt lgkmcnt(2)
v_add_f32_dpp v72, v72, v72 quad_perm:[1,0,3,2] row_mask:0xf bank_mask:0xf bound_ctrl:1
Mavv 0,128,96
s_add_u32 m0, s66, 12288
s_add_u32 s70, s67, 98688
ds_read_b128 v[112:115], v7 offset:4096
Mavv 0,132,100
buffer_load_dwordx4 v1, s[12:15], s70 offen lds
Mavv 4,136,96
ds_read_b128 v[116:119], v8 offset:4096
v_add_f32_dpp v72, v72, v72 quad_perm:[2,3,0,1] row_mask:0xf bank_mask:0xf bound_ctrl:1
Mavv 4,140,100
s_add_u32 s70, s71, 3145728
Mavv 8,144,96
buffer_load_dwordx4 v[80:83], v0, s[24:27], s70 offen
Mavv 8,148,100
s_add_u32 m0, s66, 14336
s_add_u32 s70, s67, 115072
v_add_f32_dpp v72, v72, v72 row_half_mirror row_mask:0xf bank_mask:0xf bound_ctrl:1
Mavv 12,152,96
buffer_load_dwordx4 v1, s[12:15], s70 offen lds
Mavv 12,156,100
v_mov_b32 v75, v72
Mavv 16,96,160
Mavv 16,100,164
v_permlane16_swap_b32 v72, v75
v_add_f32 v72, v72, v75
Mavv 20,96,168
s_add_u32 m0, s69, 98304
s_add_u32 s70, s68, 2097152
v_mov_b32 v75, v72
Mavv 20,100,172
buffer_load_dwordx4 v3, s[20:23], s70 offen lds
Mavv 24,96,176
v_permlane32_swap_b32 v72, v75
v_add_f32 v72, v72, v75
Mavv 24,100,180
v_mul_f32 v72, 0x3a800000, v72
v_cvt_pk_bf16_f32 v72, 0, v72
Mavv 28,96,184
v_add_f32 v72, 0x3727c5ac, v72
v_cvt_pk_bf16_f32 v72, 0, v72
Mavv 28,100,188
s_waitcnt lgkmcnt(2)
v_sqrt_f32 v74, v72
Mavv 32,128,104
s_add_u32 m0, s69, 99200
s_add_u32 s70, s68, 2097152
ds_read_b128 v[120:123], v7 offset:6144
Mavv 32,132,108
buffer_load_dwordx4 v3, s[20:23], s70 offen offset:128 lds
Mavv 36,136,104
ds_read_b128 v[124:127], v8 offset:6144
v_cvt_pk_bf16_f32 v74, 0, v74
Mavv 36,140,108
v_rcp_f32 v74, v74
Mavv 40,144,104
s_add_u32 m0, s69, 100352
s_add_u32 s70, s68, 2097152
v_lshlrev_b32 v75, 16, v32
Mavv 40,148,108
buffer_load_dwordx4 v3, s[24:27], s70 offen lds
Mavv 44,152,104
v_and_b32 v76, 0xffff0000, v32
v_mul_f32 v75, v75, v74
Mavv 44,156,108
v_mul_f32 v76, v76, v74
v_cvt_pk_bf16_f32 v32, v75, v76
Mavv 48,104,160
v_lshlrev_b32 v75, 16, v33
v_and_b32 v76, 0xffff0000, v33
Mavv 48,108,164
s_add_u32 m0, s69, 101248
s_add_u32 s70, s68, 2097152
v_mul_f32 v75, v75, v74
Mavv 52,104,168
buffer_load_dwordx4 v3, s[24:27], s70 offen offset:128 lds
Mavv 52,108,172
v_mul_f32 v76, v76, v74
v_cvt_pk_bf16_f32 v33, v75, v76
Mavv 56,104,176
v_lshlrev_b32 v75, 16, v34
v_and_b32 v76, 0xffff0000, v34
Mavv 56,108,180
v_mul_f32 v75, v75, v74
v_mul_f32 v76, v76, v74
Mavv 60,104,184
s_add_u32 m0, s69, 114688
s_add_u32 s70, s68, 2359296
v_cvt_pk_bf16_f32 v34, v75, v76
Mavv 60,108,188
s_waitcnt lgkmcnt(3)
buffer_load_dwordx4 v3, s[20:23], s70 offen lds
Mavv 64,128,112
s_waitcnt lgkmcnt(2)
ds_read_b128 v[96:99], v7 offset:8192
v_lshlrev_b32 v75, 16, v35
Mavv 64,132,116
v_and_b32 v76, 0xffff0000, v35
v_mul_f32 v75, v75, v74
Mavv 68,136,112
ds_read_b128 v[100:103], v8 offset:8192
v_mul_f32 v76, v76, v74
Mavv 68,140,116
s_add_u32 m0, s69, 115584
s_add_u32 s70, s68, 2359296
v_cvt_pk_bf16_f32 v35, v75, v76
Mavv 72,144,112
buffer_load_dwordx4 v3, s[20:23], s70 offen offset:128 lds
Mavv 72,148,116
buffer_store_dwordx4 v[32:35], v77, s[48:51], s73 offen sc1
Mavv 76,152,112
v_lshlrev_b32 v75, 16, v36
v_and_b32 v76, 0xffff0000, v36
Mavv 76,156,116
v_mul_f32 v75, v75, v74
v_mul_f32 v76, v76, v74
Mavv 80,112,160
s_add_u32 m0, s69, 116736
s_add_u32 s70, s68, 2359296
v_cvt_pk_bf16_f32 v36, v75, v76
Mavv 80,116,164
buffer_load_dwordx4 v3, s[24:27], s70 offen lds
Mavv 84,112,168
v_lshlrev_b32 v75, 16, v37
v_and_b32 v76, 0xffff0000, v37
Mavv 84,116,172
v_mul_f32 v75, v75, v74
v_mul_f32 v76, v76, v74
Mavv 88,112,176
v_cvt_pk_bf16_f32 v37, v75, v76
v_lshlrev_b32 v75, 16, v38
Mavv 88,116,180
s_add_u32 m0, s69, 117632
s_add_u32 s70, s68, 2359296
v_and_b32 v76, 0xffff0000, v38
Mavv 92,112,184
buffer_load_dwordx4 v3, s[24:27], s70 offen offset:128 lds
Mavv 92,116,188
s_waitcnt lgkmcnt(2)
v_mul_f32 v75, v75, v74
v_mul_f32 v76, v76, v74
Mavv 96,128,120
s_add_u32 s70, s71, 0
ds_read_b128 v[104:107], v7 offset:10240
v_cvt_pk_bf16_f32 v38, v75, v76
Mavv 96,132,124
buffer_load_dwordx4 v[80:83], v0, s[16:19], s70 offen
Mavv 100,136,120
s_add_u32 m0, s66, 32768
s_add_u32 s70, s67, 512
ds_read_b128 v[108:111], v8 offset:10240
Mavv 100,140,124
s_waitcnt vmcnt(24)
buffer_load_dwordx4 v1, s[12:15], s70 offen lds
Mavv 104,144,120
s_barrier
Mavv 104,148,124
ds_read_b64_tr_b16 v[192:193], v9 offset:32768
v_lshlrev_b32 v75, 16, v39
Mavv 108,152,120
ds_read_b64_tr_b16 v[194:195], v9 offset:33024
v_and_b32 v76, 0xffff0000, v39
Mavv 108,156,124
ds_read_b64_tr_b16 v[196:197], v9 offset:49152
v_mul_f32 v75, v75, v74
Mavv 112,120,160
ds_read_b64_tr_b16 v[198:199], v9 offset:49408
v_mul_f32 v76, v76, v74
Mavv 112,124,164
ds_read_b64_tr_b16 v[200:201], v10 offset:32768
v_cvt_pk_bf16_f32 v39, v75, v76
Mavv 116,120,168
ds_read_b64_tr_b16 v[202:203], v10 offset:33024
Mavv 116,124,172
ds_read_b64_tr_b16 v[204:205], v10 offset:49152
Mavv 120,120,176
ds_read_b64_tr_b16 v[206:207], v10 offset:49408
Mavv 120,124,180
ds_read_b64_tr_b16 v[208:209], v9 offset:33280
Mavv 124,120,184
ds_read_b64_tr_b16 v[210:211], v9 offset:33536
Mavv 124,124,188
s_waitcnt lgkmcnt(13)
ds_read_b64_tr_b16 v[212:213], v9 offset:49664
Mavv 128,128,96
s_waitcnt lgkmcnt(13)
ds_read_b128 v[112:115], v7 offset:12288
Mavv 128,132,100
ds_read_b64_tr_b16 v[214:215], v9 offset:49920
Mavv 132,136,96
s_waitcnt lgkmcnt(12)
ds_read_b128 v[116:119], v8 offset:12288
Mavv 132,140,100
ds_read_b64_tr_b16 v[216:217], v10 offset:33280
Mavv 136,144,96
ds_read_b64_tr_b16 v[218:219], v10 offset:33536
Mavv 136,148,100
s_waitcnt lgkmcnt(12)
ds_read_b64_tr_b16 v[220:221], v10 offset:49664
Mavv 140,152,96
ds_read_b64_tr_b16 v[222:223], v10 offset:49920
Mavv 140,156,100
ds_read_b64_tr_b16 v[224:225], v9 offset:34816
Mavv 144,96,160
s_waitcnt lgkmcnt(12)
ds_read_b64_tr_b16 v[226:227], v9 offset:35072
Mavv 144,100,164
ds_read_b64_tr_b16 v[228:229], v9 offset:51200
Mavv 148,96,168
ds_read_b64_tr_b16 v[230:231], v9 offset:51456
v_readfirstlane_b32 s86, v86
Mavv 148,100,172
s_waitcnt lgkmcnt(12)
ds_read_b64_tr_b16 v[232:233], v10 offset:34816
v_readfirstlane_b32 s87, v87
Mavv 152,96,176
s_add_u32 s86, s86, 0x7f4a7c15
s_addc_u32 s87, s87, 0x9e3779b9
ds_read_b64_tr_b16 v[234:235], v10 offset:35072
Mavv 152,100,180
ds_read_b64_tr_b16 v[236:237], v10 offset:51200
Mavv 156,96,184
s_waitcnt lgkmcnt(12)
ds_read_b64_tr_b16 v[238:239], v10 offset:51456
Mavv 156,100,188
ds_read_b64_tr_b16 v[240:241], v9 offset:35328
Mavv 160,128,104
ds_read_b128 v[120:123], v7 offset:14336
Mavv 160,132,108
s_waitcnt lgkmcnt(12)
ds_read_b64_tr_b16 v[242:243], v9 offset:35584
Mavv 164,136,104
ds_read_b128 v[124:127], v8 offset:14336
Mavv 164,140,108
ds_read_b64_tr_b16 v[244:245], v9 offset:51712
Mavv 168,144,104
s_waitcnt lgkmcnt(12)
ds_read_b64_tr_b16 v[246:247], v9 offset:51968
Mavv 168,148,108
ds_read_b64_tr_b16 v[248:249], v10 offset:35328
Mavv 172,152,104
ds_read_b64_tr_b16 v[250:251], v10 offset:35584
Mavv 172,156,108
s_waitcnt lgkmcnt(12)
ds_read_b64_tr_b16 v[252:253], v10 offset:51712
Mavv 176,104,160
s_add_u32 m0, s66, 34816
s_add_u32 s70, s67, 16896
ds_read_b64_tr_b16 v[254:255], v10 offset:51968
Mavv 176,108,164
buffer_load_dwordx4 v1, s[12:15], s70 offen lds
Mavv 180,104,168
s_add_u32 m0, s66, 36864
s_add_u32 s70, s67, 33280
Mavv 180,108,172
buffer_load_dwordx4 v1, s[12:15], s70 offen lds
Mavv 184,104,176
s_add_u32 s70, s71, 1048576
buffer_store_dwordx4 v[36:39], v77, s[48:51], s73 offen offset:512 sc1
Mavv 184,108,180
buffer_load_dwordx4 v[80:83], v0, s[16:19], s70 offen
Mavv 188,104,184
s_add_u32 m0, s66, 38912
s_add_u32 s70, s67, 49664
v_lshlrev_b32 v75, 16, v40
Mavv 188,108,188
buffer_load_dwordx4 v1, s[12:15], s70 offen lds
Mavv 192,128,112
v_and_b32 v76, 0xffff0000, v40
v_mul_f32 v75, v75, v74
Mavv 192,132,116
v_mul_f32 v76, v76, v74
v_cvt_pk_bf16_f32 v40, v75, v76
Mavv 196,136,112
v_lshlrev_b32 v75, 16, v41
v_and_b32 v76, 0xffff0000, v41
Mavv 196,140,116
s_waitcnt vmcnt(16) lgkmcnt(0)
v_mul_f32 v75, v75, v74
v_mul_f32 v76, v76, v74
Mavv 200,144,112
s_barrier
Mavv 200,148,116
v_cvt_pk_bf16_f32 v41, v75, v76
v_lshlrev_b32 v75, 16, v42
Mavv 204,152,112
s_add_u32 m0, s66, 40960
s_add_u32 s70, s67, 66048
ds_read_b128 v[96:99], v5 offset:0
Mavv 204,156,116
buffer_load_dwordx4 v1, s[12:15], s70 offen lds
Mavv 208,112,160
s_add_u32 m0, s66, 43008
s_add_u32 s70, s67, 82432
ds_read_b128 v[100:103], v6 offset:0
Mavv 208,116,164
buffer_load_dwordx4 v1, s[12:15], s70 offen lds
Mavv 212,112,168
v_and_b32 v76, 0xffff0000, v42
v_mul_f32 v75, v75, v74
Mavv 212,116,172
v_mul_f32 v76, v76, v74
v_cvt_pk_bf16_f32 v42, v75, v76
Mavv 216,112,176
v_lshlrev_b32 v75, 16, v43
v_and_b32 v76, 0xffff0000, v43
Mavv 216,116,180
s_add_u32 m0, s66, 45056
s_add_u32 s70, s67, 98816
v_mul_f32 v75, v75, v74
Mavv 220,112,184
buffer_load_dwordx4 v1, s[12:15], s70 offen lds
Mavv 220,116,188
v_mul_f32 v76, v76, v74
v_cvt_pk_bf16_f32 v43, v75, v76
Mavv 224,128,120
buffer_store_dwordx4 v[40:43], v77, s[48:51], s73 offen offset:1024 sc1
Mavv 224,132,124
v_lshlrev_b32 v75, 16, v44
v_and_b32 v76, 0xffff0000, v44
Mavv 228,136,120
s_add_u32 m0, s66, 47104
s_add_u32 s70, s67, 115200
ds_read_b128 v[104:107], v5 offset:2048
Mavv 228,140,124
buffer_load_dwordx4 v1, s[12:15], s70 offen lds
Mavv 232,144,120
ds_read_b128 v[108:111], v6 offset:2048
v_mul_f32 v75, v75, v74
Mavv 232,148,124
v_mul_f32 v76, v76, v74
v_cvt_pk_bf16_f32 v44, v75, v76
Mavv 236,152,120
v_lshlrev_b32 v75, 16, v45
v_and_b32 v76, 0xffff0000, v45
Mavv 236,156,124
s_add_u32 m0, s69, 131072
s_add_u32 s70, s68, 2621440
v_mul_f32 v75, v75, v74
)"
R"(
Mavv 240,120,160
buffer_load_dwordx4 v3, s[20:23], s70 offen lds
Mavv 240,124,164
v_mul_f32 v76, v76, v74
v_cvt_pk_bf16_f32 v45, v75, v76
Mavv 244,120,168
v_lshlrev_b32 v75, 16, v46
v_and_b32 v76, 0xffff0000, v46
Mavv 244,124,172
v_mul_f32 v75, v75, v74
v_mul_f32 v76, v76, v74
Mavv 248,120,176
s_add_u32 m0, s69, 131968
s_add_u32 s70, s68, 2621440
v_cvt_pk_bf16_f32 v46, v75, v76
Mavv 248,124,180
buffer_load_dwordx4 v3, s[20:23], s70 offen offset:128 lds
Mavv 252,120,184
v_lshlrev_b32 v75, 16, v47
v_and_b32 v76, 0xffff0000, v47
Mavv 252,124,188
s_waitcnt lgkmcnt(3)
v_mul_f32 v75, v75, v74
v_mul_f32 v76, v76, v74
Mavv 0,192,96
s_waitcnt lgkmcnt(2)
ds_read_b128 v[112:115], v5 offset:4096
v_cvt_pk_bf16_f32 v47, v75, v76
Mavv 0,196,100
buffer_store_dwordx4 v[44:47], v77, s[48:51], s73 offen offset:1536 sc1
Zvvv 68,48,32
Mavv 4,200,96
s_add_u32 m0, s69, 133120
s_add_u32 s70, s68, 2621440
ds_read_b128 v[116:119], v6 offset:4096
Mvvv 68,52,36
Mavv 4,204,100
buffer_load_dwordx4 v3, s[24:27], s70 offen lds
Mvvv 68,56,40
Mavv 8,208,96
Mvvv 68,60,44
Mavv 8,212,100
s_mov_b32 s88, 0x100001
s_mov_b32 s89, 0x10000100
Mavv 12,216,96
s_add_u32 m0, s69, 134016
s_add_u32 s70, s68, 2621440
Mavv 12,220,100
buffer_load_dwordx4 v3, s[24:27], s70 offen offset:128 lds
Mavv 16,96,224
s_add_u32 s70, s71, 2097152
Mavv 16,100,228
buffer_load_dwordx4 v[80:83], v0, s[16:19], s70 offen
Mavv 20,96,232
Mavv 20,100,236
s_add_u32 m0, s69, 147456
s_add_u32 s70, s68, 2883584
Mavv 24,96,240
buffer_load_dwordx4 v3, s[20:23], s70 offen lds
Mavv 24,100,244
v_cndmask_b32 v72, 0, v68, s[88:89]
Mavv 28,96,248
s_lshl_b64 s[88:89], s[88:89], 1
v_cndmask_b32 v72, v72, v69, s[88:89]
Mavv 28,100,252
s_waitcnt lgkmcnt(2)
s_lshl_b64 s[88:89], s[88:89], 1
v_cndmask_b32 v72, v72, v70, s[88:89]
Mavv 32,192,104
s_add_u32 m0, s69, 148352
s_add_u32 s70, s68, 2883584
ds_read_b128 v[120:123], v5 offset:6144
Mavv 32,196,108
s_lshl_b64 s[88:89], s[88:89], 1
buffer_load_dwordx4 v3, s[20:23], s70 offen offset:128 lds
Mavv 36,200,104
ds_read_b128 v[124:127], v6 offset:6144
v_cndmask_b32 v72, v72, v71, s[88:89]
Mavv 36,204,108
Mavv 40,208,104
v_add_f32_dpp v72, v72, v72 quad_perm:[1,0,3,2] row_mask:0xf bank_mask:0xf bound_ctrl:1
Mavv 40,212,108
s_add_u32 m0, s69, 149504
s_add_u32 s70, s68, 2883584
Mavv 44,216,104
buffer_load_dwordx4 v3, s[24:27], s70 offen lds
Mavv 44,220,108
v_add_f32_dpp v72, v72, v72 quad_perm:[2,3,0,1] row_mask:0xf bank_mask:0xf bound_ctrl:1
Mavv 48,104,224
Mavv 48,108,228
v_add_f32_dpp v72, v72, v72 row_half_mirror row_mask:0xf bank_mask:0xf bound_ctrl:1
v_mov_b32 v75, v72
Mavv 52,104,232
s_add_u32 m0, s69, 150400
s_add_u32 s70, s68, 2883584
Mavv 52,108,236
buffer_load_dwordx4 v3, s[24:27], s70 offen offset:128 lds
Mavv 56,104,240
v_permlane16_swap_b32 v72, v75
v_add_f32 v72, v72, v75
Mavv 56,108,244
v_mov_b32 v75, v72
Mavv 60,104,248
Mavv 60,108,252
s_waitcnt lgkmcnt(2)
v_permlane32_swap_b32 v72, v75
v_add_f32 v72, v72, v75
Mavv 64,192,112
s_add_u32 m0, s66, 65536
s_add_u32 s70, s67, 640
ds_read_b128 v[96:99], v5 offset:8192
Mavv 64,196,116
buffer_load_dwordx4 v1, s[12:15], s70 offen lds
Mavv 68,200,112
ds_read_b128 v[100:103], v6 offset:8192
v_cvt_pk_bf16_f32 v72, 0, v72
Mavv 68,204,116
v_add_f32 v72, 0, v72
v_ashrrev_i32 v75, 31, v72
Mavv 72,208,112
s_add_u32 m0, s66, 67584
s_add_u32 s70, s67, 17024
v_or_b32 v75, 0x80000000, v75
Mavv 72,212,116
buffer_load_dwordx4 v1, s[12:15], s70 offen lds
Mavv 76,216,112
v_xor_b32 v75, v75, v72
v_and_b32 v75, 0xffff0000, v75
Mavv 76,220,116
v_cmp_o_f32 vcc, v72, v72
Mavv 80,112,224
Mavv 80,116,228
s_add_u32 m0, s66, 69632
s_add_u32 s70, s67, 33408
v_cndmask_b32 v75, 0, v75, vcc
Mavv 84,112,232
buffer_load_dwordx4 v1, s[12:15], s70 offen lds
Mavv 84,116,236
v_or_b32 v68, v75, v78
Mavv 88,112,240
Mavv 88,116,244
v_mov_b32_dpp v70, v68 row_ror:8 row_mask:0xf bank_mask:0xf bound_ctrl:1
v_mov_b32 v69, s86
Mavv 92,112,248
s_add_u32 m0, s66, 71680
s_add_u32 s70, s67, 49792
v_mov_b32 v71, s87
Mavv 92,116,252
s_waitcnt lgkmcnt(3)
buffer_load_dwordx4 v1, s[12:15], s70 offen lds
Mavv 96,192,120
s_waitcnt lgkmcnt(2)
ds_read_b128 v[104:107], v5 offset:10240
Mavv 96,196,124
Mavv 100,200,120
ds_read_b128 v[108:111], v6 offset:10240
Mavv 100,204,124
s_waitcnt vmcnt(26)
Mavv 104,208,120
s_barrier
Mavv 104,212,124
ds_read_b64_tr_b16 v[128:129], v9 offset:0
Mavv 108,216,120
ds_read_b64_tr_b16 v[130:131], v9 offset:256
Mavv 108,220,124
ds_read_b64_tr_b16 v[132:133], v9 offset:16384
Mavv 112,120,224
ds_read_b64_tr_b16 v[134:135], v9 offset:16640
Mavv 112,124,228
ds_read_b64_tr_b16 v[136:137], v10 offset:0
Mavv 116,120,232
ds_read_b64_tr_b16 v[138:139], v10 offset:256
Mavv 116,124,236
ds_read_b64_tr_b16 v[140:141], v10 offset:16384
Mavv 120,120,240
ds_read_b64_tr_b16 v[142:143], v10 offset:16640
Mavv 120,124,244
ds_read_b64_tr_b16 v[144:145], v9 offset:512
Mavv 124,120,248
ds_read_b64_tr_b16 v[146:147], v9 offset:768
Mavv 124,124,252
s_waitcnt lgkmcnt(13)
ds_read_b64_tr_b16 v[148:149], v9 offset:16896
Mavv 128,192,96
s_waitcnt lgkmcnt(13)
ds_read_b128 v[112:115], v5 offset:12288
Mavv 128,196,100
ds_read_b64_tr_b16 v[150:151], v9 offset:17152
Mavv 132,200,96
s_waitcnt lgkmcnt(12)
ds_read_b128 v[116:119], v6 offset:12288
Mavv 132,204,100
ds_read_b64_tr_b16 v[152:153], v10 offset:512
Mavv 136,208,96
ds_read_b64_tr_b16 v[154:155], v10 offset:768
Mavv 136,212,100
s_waitcnt lgkmcnt(12)
ds_read_b64_tr_b16 v[156:157], v10 offset:16896
Mavv 140,216,96
ds_read_b64_tr_b16 v[158:159], v10 offset:17152
Mavv 140,220,100
ds_read_b64_tr_b16 v[160:161], v9 offset:2048
Mavv 144,96,224
s_waitcnt lgkmcnt(12)
ds_read_b64_tr_b16 v[162:163], v9 offset:2304
Mavv 144,100,228
ds_read_b64_tr_b16 v[164:165], v9 offset:18432
Mavv 148,96,232
ds_read_b64_tr_b16 v[166:167], v9 offset:18688
Mavv 148,100,236
s_waitcnt lgkmcnt(12)
ds_read_b64_tr_b16 v[168:169], v10 offset:2048
Mavv 152,96,240
ds_read_b64_tr_b16 v[170:171], v10 offset:2304
Mavv 152,100,244
ds_read_b64_tr_b16 v[172:173], v10 offset:18432
Mavv 156,96,248
s_waitcnt lgkmcnt(12)
ds_read_b64_tr_b16 v[174:175], v10 offset:18688
Mavv 156,100,252
ds_read_b64_tr_b16 v[176:177], v9 offset:2560
Mavv 160,192,104
ds_read_b128 v[120:123], v5 offset:14336
Mavv 160,196,108
s_waitcnt lgkmcnt(12)
ds_read_b64_tr_b16 v[178:179], v9 offset:2816
Mavv 164,200,104
ds_read_b128 v[124:127], v6 offset:14336
Mavv 164,204,108
ds_read_b64_tr_b16 v[180:181], v9 offset:18944
Mavv 168,208,104
s_waitcnt lgkmcnt(12)
ds_read_b64_tr_b16 v[182:183], v9 offset:19200
Mavv 168,212,108
ds_read_b64_tr_b16 v[184:185], v10 offset:2560
Mavv 172,216,104
ds_read_b64_tr_b16 v[186:187], v10 offset:2816
Mavv 172,220,108
s_waitcnt lgkmcnt(12)
ds_read_b64_tr_b16 v[188:189], v10 offset:18944
Mavv 176,104,224
s_add_u32 s70, s71, 3145728
ds_read_b64_tr_b16 v[190:191], v10 offset:19200
Mavv 176,108,228
buffer_load_dwordx4 v[80:83], v0, s[16:19], s70 offen
Mavv 180,104,232
s_add_u32 m0, s66, 73728
s_add_u32 s70, s67, 66176
Mavv 180,108,236
buffer_load_dwordx4 v1, s[12:15], s70 offen lds
Mavv 184,104,240
Mavv 184,108,244
Mavv 188,104,248
Mavv 188,108,252
s_add_u32 s70, s71, 4194304
Mavv 192,192,112
buffer_load_dwordx4 v[80:83], v0, s[16:19], s70 offen
Mavv 192,196,116
s_add_u32 m0, s66, 75776
s_add_u32 s70, s67, 82560
Mavv 196,200,112
buffer_load_dwordx4 v1, s[12:15], s70 offen lds
Mavv 196,204,116
s_waitcnt vmcnt(18) lgkmcnt(0)
Mavv 200,208,112
s_barrier
Mavv 200,212,116
Mavv 204,216,112
s_add_u32 m0, s66, 77824
s_add_u32 s70, s67, 98944
ds_read_b128 v[96:99], v5 offset:32768
Mavv 204,220,116
buffer_load_dwordx4 v1, s[12:15], s70 offen lds
Mavv 208,112,224
ds_read_b128 v[100:103], v6 offset:32768
Mavv 208,116,228
Mavv 212,112,232
Mavv 212,116,236
s_add_u32 m0, s66, 79872
s_add_u32 s70, s67, 115328
Mavv 216,112,240
buffer_load_dwordx4 v1, s[12:15], s70 offen lds
Mavv 216,116,244
Mavv 220,112,248
Mavv 220,116,252
Mavv 224,192,120
s_add_u32 m0, s69, 98304
s_add_u32 s70, s68, 3145728
Mavv 224,196,124
buffer_load_dwordx4 v3, s[20:23], s70 offen lds
Mavv 228,200,120
ds_read_b128 v[104:107], v5 offset:34816
Mavv 228,204,124
Mavv 232,208,120
ds_read_b128 v[108:111], v6 offset:34816
Mavv 232,212,124
s_add_u32 m0, s69, 99200
s_add_u32 s70, s68, 3145728
Mavv 236,216,120
buffer_load_dwordx4 v3, s[20:23], s70 offen offset:128 lds
Mavv 236,220,124
Mavv 240,120,224
Mavv 240,124,228
Mavv 244,120,232
s_add_u32 m0, s69, 100352
s_add_u32 s70, s68, 3145728
Mavv 244,124,236
buffer_load_dwordx4 v3, s[24:27], s70 offen lds
Mavv 248,120,240
Mavv 248,124,244
Mavv 252,120,248
Mavv 252,124,252
s_waitcnt lgkmcnt(2)
Mavv 0,128,96
s_add_u32 m0, s69, 101248
s_add_u32 s70, s68, 3145728
ds_read_b128 v[112:115], v5 offset:36864
Mavv 0,132,100
buffer_load_dwordx4 v3, s[24:27], s70 offen offset:128 lds
Mavv 4,136,96
ds_read_b128 v[116:119], v6 offset:36864
Mavv 4,140,100
Mavv 8,144,96
s_add_u32 m0, s69, 114688
s_add_u32 s70, s68, 3407872
Mavv 8,148,100
buffer_load_dwordx4 v3, s[20:23], s70 offen lds
Mavv 12,152,96
Mavv 12,156,100
Mavv 16,96,160
Mavv 16,100,164
s_add_u32 m0, s69, 115584
s_add_u32 s70, s68, 3407872
Mavv 20,96,168
buffer_load_dwordx4 v3, s[20:23], s70 offen offset:128 lds
Mavv 20,100,172
s_add_u32 s70, s71, 5242880
Mavv 24,96,176
buffer_load_dwordx4 v[80:83], v0, s[16:19], s70 offen
Mavv 24,100,180
Mavv 28,96,184
s_add_u32 m0, s69, 116736
s_add_u32 s70, s68, 3407872
Mavv 28,100,188
s_waitcnt lgkmcnt(3)
buffer_load_dwordx4 v3, s[24:27], s70 offen lds
Mavv 32,128,104
s_waitcnt lgkmcnt(2)
ds_read_b128 v[120:123], v5 offset:38912
Mavv 32,132,108
Mavv 36,136,104
ds_read_b128 v[124:127], v6 offset:38912
Mavv 36,140,108
s_add_u32 m0, s69, 117632
s_add_u32 s70, s68, 3407872
Mavv 40,144,104
buffer_load_dwordx4 v3, s[24:27], s70 offen offset:128 lds
Mavv 40,148,108
Mavv 44,152,104
Mavv 44,156,108
Mavv 48,104,160
s_add_u32 m0, s66, 0
s_add_u32 s70, s67, 768
Mavv 48,108,164
buffer_load_dwordx4 v1, s[12:15], s70 offen lds
Mavv 52,104,168
Mavv 52,108,172
Mavv 56,104,176
s_waitcnt vmcnt(27)
buffer_store_dwordx4 v[68:71], v13, s[48:51], s74 offen sc1
Mavv 56,108,180
s_add_u32 m0, s66, 2048
s_add_u32 s70, s67, 17152
Mavv 60,104,184
buffer_load_dwordx4 v1, s[12:15], s70 offen lds
Mavv 60,108,188
s_waitcnt lgkmcnt(3)
Mavv 64,128,112
s_waitcnt lgkmcnt(2)
ds_read_b128 v[96:99], v5 offset:40960
Mavv 64,132,116
Mavv 68,136,112
s_add_u32 m0, s66, 4096
s_add_u32 s70, s67, 33536
ds_read_b128 v[100:103], v6 offset:40960
Mavv 68,140,116
buffer_load_dwordx4 v1, s[12:15], s70 offen lds
Mavv 72,144,112
Mavv 72,148,116
Mavv 76,152,112
Mavv 76,156,116
s_add_u32 m0, s66, 6144
s_add_u32 s70, s67, 49920
Mavv 80,112,160
buffer_load_dwordx4 v1, s[12:15], s70 offen lds
Mavv 80,116,164
Mavv 84,112,168
Mavv 84,116,172
Mavv 88,112,176
s_add_u32 m0, s66, 8192
s_add_u32 s70, s67, 66304
Mavv 88,116,180
buffer_load_dwordx4 v1, s[12:15], s70 offen lds
Mavv 92,112,184
Mavv 92,116,188
s_waitcnt lgkmcnt(3)
Mavv 96,128,120
s_waitcnt lgkmcnt(2)
ds_read_b128 v[104:107], v5 offset:43008
Mavv 96,132,124
Mavv 100,136,120
s_add_u32 m0, s66, 10240
s_add_u32 s70, s67, 82688
ds_read_b128 v[108:111], v6 offset:43008
Mavv 100,140,124
s_waitcnt vmcnt(25)
buffer_load_dwordx4 v1, s[12:15], s70 offen lds
Mavv 104,144,120
s_barrier
Mavv 104,148,124
ds_read_b64_tr_b16 v[192:193], v9 offset:32768
Mavv 108,152,120
ds_read_b64_tr_b16 v[194:195], v9 offset:33024
Mavv 108,156,124
ds_read_b64_tr_b16 v[196:197], v9 offset:49152
Mavv 112,120,160
ds_read_b64_tr_b16 v[198:199], v9 offset:49408
Mavv 112,124,164
ds_read_b64_tr_b16 v[200:201], v10 offset:32768
Mavv 116,120,168
ds_read_b64_tr_b16 v[202:203], v10 offset:33024
Mavv 116,124,172
ds_read_b64_tr_b16 v[204:205], v10 offset:49152
Mavv 120,120,176
ds_read_b64_tr_b16 v[206:207], v10 offset:49408
Mavv 120,124,180
ds_read_b64_tr_b16 v[208:209], v9 offset:33280
Mavv 124,120,184
ds_read_b64_tr_b16 v[210:211], v9 offset:33536
Mavv 124,124,188
s_waitcnt lgkmcnt(13)
ds_read_b64_tr_b16 v[212:213], v9 offset:49664
Mavv 128,128,96
s_waitcnt lgkmcnt(13)
ds_read_b128 v[112:115], v5 offset:45056
Mavv 128,132,100
ds_read_b64_tr_b16 v[214:215], v9 offset:49920
Mavv 132,136,96
s_waitcnt lgkmcnt(12)
)"
R"(
ds_read_b128 v[116:119], v6 offset:45056
Mavv 132,140,100
ds_read_b64_tr_b16 v[216:217], v10 offset:33280
Mavv 136,144,96
ds_read_b64_tr_b16 v[218:219], v10 offset:33536
Mavv 136,148,100
s_waitcnt lgkmcnt(12)
ds_read_b64_tr_b16 v[220:221], v10 offset:49664
Mavv 140,152,96
ds_read_b64_tr_b16 v[222:223], v10 offset:49920
Mavv 140,156,100
ds_read_b64_tr_b16 v[224:225], v9 offset:34816
Mavv 144,96,160
s_waitcnt lgkmcnt(12)
ds_read_b64_tr_b16 v[226:227], v9 offset:35072
Mavv 144,100,164
ds_read_b64_tr_b16 v[228:229], v9 offset:51200
Mavv 148,96,168
ds_read_b64_tr_b16 v[230:231], v9 offset:51456
Mavv 148,100,172
s_waitcnt lgkmcnt(12)
ds_read_b64_tr_b16 v[232:233], v10 offset:34816
Mavv 152,96,176
ds_read_b64_tr_b16 v[234:235], v10 offset:35072
Mavv 152,100,180
ds_read_b64_tr_b16 v[236:237], v10 offset:51200
Mavv 156,96,184
s_waitcnt lgkmcnt(12)
ds_read_b64_tr_b16 v[238:239], v10 offset:51456
Mavv 156,100,188
ds_read_b64_tr_b16 v[240:241], v9 offset:35328
Mavv 160,128,104
ds_read_b128 v[120:123], v5 offset:47104
Mavv 160,132,108
s_waitcnt lgkmcnt(12)
ds_read_b64_tr_b16 v[242:243], v9 offset:35584
Mavv 164,136,104
ds_read_b128 v[124:127], v6 offset:47104
Mavv 164,140,108
ds_read_b64_tr_b16 v[244:245], v9 offset:51712
Mavv 168,144,104
s_waitcnt lgkmcnt(12)
ds_read_b64_tr_b16 v[246:247], v9 offset:51968
Mavv 168,148,108
ds_read_b64_tr_b16 v[248:249], v10 offset:35328
Mavv 172,152,104
ds_read_b64_tr_b16 v[250:251], v10 offset:35584
Mavv 172,156,108
s_waitcnt lgkmcnt(12)
ds_read_b64_tr_b16 v[252:253], v10 offset:51712
Mavv 176,104,160
s_add_u32 m0, s66, 12288
s_add_u32 s70, s67, 99072
ds_read_b64_tr_b16 v[254:255], v10 offset:51968
Mavv 176,108,164
buffer_load_dwordx4 v1, s[12:15], s70 offen lds
Mavv 180,104,168
s_add_u32 s70, s71, 6291456
Mavv 180,108,172
buffer_load_dwordx4 v[80:83], v0, s[16:19], s70 offen
Mavv 184,104,176
s_add_u32 m0, s66, 14336
s_add_u32 s70, s67, 115456
Mavv 184,108,180
buffer_load_dwordx4 v1, s[12:15], s70 offen lds
Mavv 188,104,184
Mavv 188,108,188
Mavv 192,128,112
Mavv 192,132,116
Mavv 196,136,112
Mavv 196,140,116
s_waitcnt vmcnt(19) lgkmcnt(0)
Mavv 200,144,112
s_barrier
s_add_u32 s70, s71, 7340032
Mavv 200,148,116
buffer_load_dwordx4 v[80:83], v0, s[16:19], s70 offen
Mavv 204,152,112
s_add_u32 m0, s69, 131072
s_add_u32 s70, s68, 3670016
ds_read_b128 v[96:99], v7 offset:0
Mavv 204,156,116
buffer_load_dwordx4 v3, s[20:23], s70 offen lds
Mavv 208,112,160
ds_read_b128 v[100:103], v8 offset:0
Mavv 208,116,164
Mavv 212,112,168
Mavv 212,116,172
s_add_u32 m0, s69, 131968
s_add_u32 s70, s68, 3670016
Mavv 216,112,176
buffer_load_dwordx4 v3, s[20:23], s70 offen offset:128 lds
Mavv 216,116,180
Mavv 220,112,184
Mavv 220,116,188
Mavv 224,128,120
s_add_u32 m0, s69, 133120
s_add_u32 s70, s68, 3670016
Mavv 224,132,124
buffer_load_dwordx4 v3, s[24:27], s70 offen lds
Mavv 228,136,120
ds_read_b128 v[104:107], v7 offset:2048
Mavv 228,140,124
Mavv 232,144,120
ds_read_b128 v[108:111], v8 offset:2048
Mavv 232,148,124
s_add_u32 m0, s69, 134016
s_add_u32 s70, s68, 3670016
Mavv 236,152,120
buffer_load_dwordx4 v3, s[24:27], s70 offen offset:128 lds
Mavv 236,156,124
Mavv 240,120,160
Mavv 240,124,164
Mavv 244,120,168
s_add_u32 m0, s69, 147456
s_add_u32 s70, s68, 3932160
Mavv 244,124,172
buffer_load_dwordx4 v3, s[20:23], s70 offen lds
Mavv 248,120,176
Mavv 248,124,180
Mavv 252,120,184
Mavv 252,124,188
s_waitcnt lgkmcnt(2)
Mavv 0,192,96
s_add_u32 m0, s69, 148352
s_add_u32 s70, s68, 3932160
ds_read_b128 v[112:115], v7 offset:4096
Mavv 0,196,100
buffer_load_dwordx4 v3, s[20:23], s70 offen offset:128 lds
Mavv 4,200,96
ds_read_b128 v[116:119], v8 offset:4096
Mavv 4,204,100
Mavv 8,208,96
s_add_u32 m0, s69, 149504
s_add_u32 s70, s68, 3932160
Mavv 8,212,100
buffer_load_dwordx4 v3, s[24:27], s70 offen lds
Mavv 12,216,96
Mavv 12,220,100
Mavv 16,96,224
Mavv 16,100,228
s_add_u32 m0, s69, 150400
s_add_u32 s70, s68, 3932160
Mavv 20,96,232
buffer_load_dwordx4 v3, s[24:27], s70 offen offset:128 lds
Mavv 20,100,236
Mavv 24,96,240
Mavv 24,100,244
Mavv 28,96,248
s_add_u32 m0, s66, 32768
s_add_u32 s70, s67, 896
Mavv 28,100,252
s_waitcnt lgkmcnt(3)
buffer_load_dwordx4 v1, s[12:15], s70 offen lds
Mavv 32,192,104
s_waitcnt lgkmcnt(2)
ds_read_b128 v[120:123], v7 offset:6144
Mavv 32,196,108
Mavv 36,200,104
ds_read_b128 v[124:127], v8 offset:6144
Mavv 36,204,108
s_add_u32 m0, s66, 34816
s_add_u32 s70, s67, 17280
Mavv 40,208,104
buffer_load_dwordx4 v1, s[12:15], s70 offen lds
Mavv 40,212,108
Mavv 44,216,104
Mavv 44,220,108
Mavv 48,104,224
s_add_u32 m0, s66, 36864
s_add_u32 s70, s67, 33664
Mavv 48,108,228
buffer_load_dwordx4 v1, s[12:15], s70 offen lds
Mavv 52,104,232
Mavv 52,108,236
Mavv 56,104,240
Mavv 56,108,244
s_add_u32 m0, s66, 38912
s_add_u32 s70, s67, 50048
Mavv 60,104,248
buffer_load_dwordx4 v1, s[12:15], s70 offen lds
Mavv 60,108,252
s_waitcnt lgkmcnt(3)
Mavv 64,192,112
s_waitcnt lgkmcnt(2)
ds_read_b128 v[96:99], v7 offset:8192
Mavv 64,196,116
Mavv 68,200,112
s_add_u32 m0, s66, 40960
s_add_u32 s70, s67, 66432
ds_read_b128 v[100:103], v8 offset:8192
Mavv 68,204,116
buffer_load_dwordx4 v1, s[12:15], s70 offen lds
Mavv 72,208,112
Mavv 72,212,116
Mavv 76,216,112
Mavv 76,220,116
s_add_u32 m0, s66, 43008
s_add_u32 s70, s67, 82816
Mavv 80,112,224
buffer_load_dwordx4 v1, s[12:15], s70 offen lds
Mavv 80,116,228
Mavv 84,112,232
Mavv 84,116,236
Mavv 88,112,240
s_add_u32 m0, s66, 45056
s_add_u32 s70, s67, 99200
Mavv 88,116,244
buffer_load_dwordx4 v1, s[12:15], s70 offen lds
Mavv 92,112,248
Mavv 92,116,252
s_waitcnt lgkmcnt(3)
Mavv 96,192,120
s_waitcnt lgkmcnt(2)
ds_read_b128 v[104:107], v7 offset:10240
Mavv 96,196,124
Mavv 100,200,120
s_add_u32 m0, s66, 47104
s_add_u32 s70, s67, 115584
ds_read_b128 v[108:111], v8 offset:10240
Mavv 100,204,124
s_waitcnt vmcnt(26)
buffer_load_dwordx4 v1, s[12:15], s70 offen lds
Mavv 104,208,120
s_barrier
Mavv 104,212,124
ds_read_b64_tr_b16 v[128:129], v9 offset:0
Mavv 108,216,120
ds_read_b64_tr_b16 v[130:131], v9 offset:256
Mavv 108,220,124
ds_read_b64_tr_b16 v[132:133], v9 offset:16384
Mavv 112,120,224
ds_read_b64_tr_b16 v[134:135], v9 offset:16640
Mavv 112,124,228
ds_read_b64_tr_b16 v[136:137], v10 offset:0
Mavv 116,120,232
ds_read_b64_tr_b16 v[138:139], v10 offset:256
Mavv 116,124,236
ds_read_b64_tr_b16 v[140:141], v10 offset:16384
Mavv 120,120,240
ds_read_b64_tr_b16 v[142:143], v10 offset:16640
Mavv 120,124,244
ds_read_b64_tr_b16 v[144:145], v9 offset:512
Mavv 124,120,248
ds_read_b64_tr_b16 v[146:147], v9 offset:768
Mavv 124,124,252
s_waitcnt lgkmcnt(13)
ds_read_b64_tr_b16 v[148:149], v9 offset:16896
Mavv 128,192,96
s_waitcnt lgkmcnt(13)
ds_read_b128 v[112:115], v7 offset:12288
Mavv 128,196,100
ds_read_b64_tr_b16 v[150:151], v9 offset:17152
Mavv 132,200,96
s_waitcnt lgkmcnt(12)
ds_read_b128 v[116:119], v8 offset:12288
Mavv 132,204,100
ds_read_b64_tr_b16 v[152:153], v10 offset:512
Mavv 136,208,96
ds_read_b64_tr_b16 v[154:155], v10 offset:768
Mavv 136,212,100
s_waitcnt lgkmcnt(12)
ds_read_b64_tr_b16 v[156:157], v10 offset:16896
Mavv 140,216,96
ds_read_b64_tr_b16 v[158:159], v10 offset:17152
Mavv 140,220,100
ds_read_b64_tr_b16 v[160:161], v9 offset:2048
Mavv 144,96,224
s_waitcnt lgkmcnt(12)
ds_read_b64_tr_b16 v[162:163], v9 offset:2304
Mavv 144,100,228
ds_read_b64_tr_b16 v[164:165], v9 offset:18432
Mavv 148,96,232
ds_read_b64_tr_b16 v[166:167], v9 offset:18688
Mavv 148,100,236
s_waitcnt lgkmcnt(12)
ds_read_b64_tr_b16 v[168:169], v10 offset:2048
Mavv 152,96,240
ds_read_b64_tr_b16 v[170:171], v10 offset:2304
Mavv 152,100,244
ds_read_b64_tr_b16 v[172:173], v10 offset:18432
Mavv 156,96,248
s_waitcnt lgkmcnt(12)
ds_read_b64_tr_b16 v[174:175], v10 offset:18688
Mavv 156,100,252
ds_read_b64_tr_b16 v[176:177], v9 offset:2560
Mavv 160,192,104
ds_read_b128 v[120:123], v7 offset:14336
Mavv 160,196,108
s_waitcnt lgkmcnt(12)
ds_read_b64_tr_b16 v[178:179], v9 offset:2816
Mavv 164,200,104
ds_read_b128 v[124:127], v8 offset:14336
Mavv 164,204,108
ds_read_b64_tr_b16 v[180:181], v9 offset:18944
Mavv 168,208,104
s_waitcnt lgkmcnt(12)
ds_read_b64_tr_b16 v[182:183], v9 offset:19200
Mavv 168,212,108
ds_read_b64_tr_b16 v[184:185], v10 offset:2560
Mavv 172,216,104
ds_read_b64_tr_b16 v[186:187], v10 offset:2816
Mavv 172,220,108
s_waitcnt lgkmcnt(12)
ds_read_b64_tr_b16 v[188:189], v10 offset:18944
Mavv 176,104,224
ds_read_b64_tr_b16 v[190:191], v10 offset:19200
Mavv 176,108,228
buffer_load_dwordx4 v[32:35], v0, s[48:51], s75 offen sc1
Mavv 180,104,232
Mavv 180,108,236
Mavv 184,104,240
Mavv 184,108,244
buffer_load_dwordx4 v[36:39], v0, s[48:51], s75 offen offset:1024 sc1
Mavv 188,104,248
Mavv 188,108,252
Mavv 192,192,112
Mavv 192,196,116
buffer_load_dwordx4 v[40:43], v0, s[48:51], s75 offen offset:2048 sc1
Mavv 196,200,112
Mavv 196,204,116
s_waitcnt vmcnt(11) lgkmcnt(0)
Mavv 200,208,112
s_barrier
Mavv 200,212,116
buffer_load_dwordx4 v[44:47], v0, s[48:51], s75 offen offset:3072 sc1
Mavv 204,216,112
s_add_u32 m0, s77, 98304
s_add_u32 s70, s68, 0
ds_read_b128 v[96:99], v5 offset:0
Mavv 204,220,116
buffer_load_dwordx4 v3, s[16:19], s70 offen lds
Mavv 208,112,224
ds_read_b128 v[100:103], v6 offset:0
Mavv 208,116,228
Mavv 212,112,232
s_add_u32 m0, s77, 99200
s_add_u32 s70, s68, 0
Mavv 212,116,236
buffer_load_dwordx4 v3, s[16:19], s70 offen offset:128 lds
Mavv 216,112,240
Mavv 216,116,244
Mavv 220,112,248
Mavv 220,116,252
s_add_u32 m0, s77, 106496
s_add_u32 s70, s68, 262144
Mavv 224,192,120
buffer_load_dwordx4 v3, s[16:19], s70 offen lds
Mavv 224,196,124
Mavv 228,200,120
ds_read_b128 v[104:107], v5 offset:2048
Mavv 228,204,124
Mavv 232,208,120
s_add_u32 m0, s77, 107392
s_add_u32 s70, s68, 262144
ds_read_b128 v[108:111], v6 offset:2048
Mavv 232,212,124
buffer_load_dwordx4 v3, s[16:19], s70 offen offset:128 lds
Mavv 236,216,120
Mavv 236,220,124
Mavv 240,120,224
Mavv 240,124,228
s_add_u32 m0, s77, 114688
s_add_u32 s70, s68, 524288
Mavv 244,120,232
buffer_load_dwordx4 v3, s[16:19], s70 offen lds
Mavv 244,124,236
Mavv 248,120,240
Mavv 248,124,244
Mavv 252,120,248
s_add_u32 m0, s77, 115584
s_add_u32 s70, s68, 524288
Mavv 252,124,252
s_waitcnt lgkmcnt(3)
buffer_load_dwordx4 v3, s[16:19], s70 offen offset:128 lds
Mavv 0,128,96
s_waitcnt lgkmcnt(2)
ds_read_b128 v[112:115], v5 offset:4096
Mavv 0,132,100
ds_read_b64_tr_b16 v[192:193], v9 offset:32768
Mavv 4,136,96
ds_read_b128 v[116:119], v6 offset:4096
Mavv 4,140,100
ds_read_b64_tr_b16 v[194:195], v9 offset:33024
Mavv 8,144,96
ds_read_b64_tr_b16 v[196:197], v9 offset:49152
Mavv 8,148,100
ds_read_b64_tr_b16 v[198:199], v9 offset:49408
Mavv 12,152,96
ds_read_b64_tr_b16 v[200:201], v10 offset:32768
Mavv 12,156,100
ds_read_b64_tr_b16 v[202:203], v10 offset:33024
Mavv 16,96,160
ds_read_b64_tr_b16 v[204:205], v10 offset:49152
Mavv 16,100,164
ds_read_b64_tr_b16 v[206:207], v10 offset:49408
Mavv 20,96,168
ds_read_b64_tr_b16 v[208:209], v9 offset:33280
Mavv 20,100,172
ds_read_b64_tr_b16 v[210:211], v9 offset:33536
Mavv 24,96,176
ds_read_b64_tr_b16 v[212:213], v9 offset:49664
Mavv 24,100,180
s_waitcnt lgkmcnt(12)
ds_read_b64_tr_b16 v[214:215], v9 offset:49920
Mavv 28,96,184
ds_read_b64_tr_b16 v[216:217], v10 offset:33280
Mavv 28,100,188
ds_read_b64_tr_b16 v[218:219], v10 offset:33536
Mavv 32,128,104
s_waitcnt lgkmcnt(12)
ds_read_b64_tr_b16 v[220:221], v10 offset:49664
Mavv 32,132,108
ds_read_b64_tr_b16 v[222:223], v10 offset:49920
Mavv 36,136,104
ds_read_b64_tr_b16 v[224:225], v9 offset:34816
Mavv 36,140,108
s_waitcnt lgkmcnt(12)
ds_read_b64_tr_b16 v[226:227], v9 offset:35072
Mavv 40,144,104
ds_read_b64_tr_b16 v[228:229], v9 offset:51200
Mavv 40,148,108
ds_read_b64_tr_b16 v[230:231], v9 offset:51456
Mavv 44,152,104
s_waitcnt vmcnt(9) lgkmcnt(12)
ds_read_b64_tr_b16 v[232:233], v10 offset:34816
v_xor_b32 v50, s86, v33
Mavv 44,156,108
ds_read_b64_tr_b16 v[234:235], v10 offset:35072
v_bitop3_b32 v50, v35, s87, v50 bitop3:0xbe
Mavv 48,104,160
s_waitcnt vmcnt(8)
ds_read_b64_tr_b16 v[236:237], v10 offset:51200
v_bitop3_b32 v50, v37, s86, v50 bitop3:0xbe
Mavv 48,108,164
s_barrier
Mavv 52,104,168
s_waitcnt lgkmcnt(12)
ds_read_b64_tr_b16 v[238:239], v10 offset:51456
v_bitop3_b32 v50, v39, s87, v50 bitop3:0xbe
)"
R"(
Mavv 52,108,172
s_waitcnt vmcnt(7)
ds_read_b128 v[120:123], v5 offset:32768
v_bitop3_b32 v50, v41, s86, v50 bitop3:0xbe
Mavv 56,104,176
ds_read_b64_tr_b16 v[240:241], v9 offset:35328
v_bitop3_b32 v50, v43, s87, v50 bitop3:0xbe
Mavv 56,108,180
s_waitcnt vmcnt(6) lgkmcnt(12)
ds_read_b128 v[124:127], v6 offset:32768
v_bitop3_b32 v50, v45, s86, v50 bitop3:0xbe
Mavv 60,104,184
ds_read_b64_tr_b16 v[242:243], v9 offset:35584
v_bitop3_b32 v50, v47, s87, v50 bitop3:0xbe
Mavv 60,108,188
ds_read_b64_tr_b16 v[244:245], v9 offset:51712
v_cmp_ne_u32 vcc, 0, v50
Mavv 64,128,112
s_waitcnt lgkmcnt(12)
ds_read_b64_tr_b16 v[246:247], v9 offset:51968
Mavv 64,132,116
ds_read_b128 v[96:99], v5 offset:34816
Mavv 68,136,112
ds_read_b64_tr_b16 v[248:249], v10 offset:35328
Mavv 68,140,116
s_waitcnt lgkmcnt(12)
ds_read_b128 v[100:103], v6 offset:34816
Mavv 72,144,112
ds_read_b64_tr_b16 v[250:251], v10 offset:35584
Mavv 72,148,116
s_waitcnt lgkmcnt(11)
ds_read_b64_tr_b16 v[252:253], v10 offset:51712
Mavv 76,152,112
s_add_u32 m0, s77, 122880
s_add_u32 s70, s68, 786432
ds_read_b64_tr_b16 v[254:255], v10 offset:51968
Mavv 76,156,116
buffer_load_dwordx4 v3, s[16:19], s70 offen lds
Mavv 80,112,160
s_add_u32 m0, s77, 123776
s_add_u32 s70, s68, 786432
Mavv 80,116,164
buffer_load_dwordx4 v3, s[16:19], s70 offen offset:128 lds
Mavv 84,112,168
s_cbranch_vccnz .Lscan
.Lscan_r:
Mavv 84,116,172
v_max_u32 v48, v32, v34
v_min_u32 v49, v32, v34
Mavv 88,112,176
v_med3_u32 v49, v48, v49, v36
v_max_u32 v48, v48, v36
Mavv 88,116,180
v_med3_u32 v49, v48, v49, v38
v_max_u32 v48, v48, v38
Mavv 92,112,184
v_med3_u32 v49, v48, v49, v40
v_max_u32 v48, v48, v40
Mavv 92,116,188
s_waitcnt lgkmcnt(11)
v_med3_u32 v49, v48, v49, v42
v_max_u32 v48, v48, v42
Mavv 0,192,120
s_waitcnt lgkmcnt(9)
v_med3_u32 v49, v48, v49, v44
v_max_u32 v48, v48, v44
Mavv 0,196,124
v_med3_u32 v49, v48, v49, v46
v_max_u32 v48, v48, v46
Mavv 4,200,120
v_mov_b32 v50, v48
Mavv 4,204,124
s_waitcnt lgkmcnt(0)
Mavv 8,208,120
s_barrier
Mavv 8,212,124
s_cmp_eq_u32 s74, 4096
s_cselect_b32 s58, 0, 0x80000000
v_max_u32_dpp v50, v50, v50 quad_perm:[1,0,3,2] row_mask:0xf bank_mask:0xf bound_ctrl:1
Mavv 12,216,120
s_add_u32 m0, s78, 65536
s_add_u32 s70, s82, 0
ds_read_b128 v[104:107], v5 offset:36864
Mavv 12,220,124
buffer_load_dwordx4 v11, s[48:51], s70 offen sc1 lds
Mavv 16,120,224
ds_read_b128 v[108:111], v6 offset:36864
v_max_u32_dpp v50, v50, v50 quad_perm:[2,3,0,1] row_mask:0xf bank_mask:0xf bound_ctrl:1
Mavv 16,124,228
v_mov_b32 v52, s86
v_mov_b32 v53, s87
Mavv 20,120,232
s_add_u32 m0, s78, 66560
s_add_u32 s70, s82, 0
v_max_u32_dpp v50, v50, v50 row_half_mirror row_mask:0xf bank_mask:0xf bound_ctrl:1
Mavv 20,124,236
buffer_load_dwordx4 v12, s[48:51], s70 offen sc1 lds
Mavv 24,120,240
v_max_u32_dpp v50, v50, v50 row_mirror row_mask:0xf bank_mask:0xf bound_ctrl:1
v_mov_b32 v51, v50
Mavv 24,124,244
buffer_store_dwordx2 v[52:53], v13, s[48:51], s58 offen sc1
Mavv 28,120,248
v_permlane16_swap_b32 v50, v51
v_max_u32 v50, v50, v51
Mavv 28,124,252
s_add_u32 m0, s78, 67584
s_add_u32 s70, s82, 34816
v_mov_b32 v51, v50
Mavv 32,192,96
buffer_load_dwordx4 v11, s[48:51], s70 offen sc1 lds
Mavv 32,196,100
v_permlane32_swap_b32 v50, v51
v_max_u32 v50, v50, v51
Mavv 36,200,96
ds_read_b128 v[112:115], v5 offset:6144
v_cmp_eq_u32 vcc, v50, v48
Mavv 36,204,100
Mavv 40,208,96
s_add_u32 m0, s78, 68608
s_add_u32 s70, s82, 34816
ds_read_b128 v[116:119], v6 offset:6144
Mavv 40,212,100
buffer_load_dwordx4 v12, s[48:51], s70 offen sc1 lds
Mavv 44,216,96
v_cndmask_b32 v49, v48, v49, vcc
Mavv 44,220,100
global_load_dwordx4 v[80:83], v0, s[60:61]
Mavv 48,96,224
v_max_u32_dpp v49, v49, v49 quad_perm:[1,0,3,2] row_mask:0xf bank_mask:0xf bound_ctrl:1
Mavv 48,100,228
s_add_u32 m0, s78, 73728
s_add_u32 s70, s81, 0
v_accvgpr_read_b32 v30, a0
Mavv 52,96,232
buffer_load_dwordx4 v11, s[48:51], s70 offen sc1 lds
Mavv 52,100,236
v_max_u32_dpp v49, v49, v49 quad_perm:[2,3,0,1] row_mask:0xf bank_mask:0xf bound_ctrl:1
v_accvgpr_read_b32 v31, a1
Mavv 56,96,240
v_mul_f32 v30, 0x3e0293ee, v30
v_mul_f32 v31, 0x3e0293ee, v31
Mavv 56,100,244
v_max_u32_dpp v49, v49, v49 row_half_mirror row_mask:0xf bank_mask:0xf bound_ctrl:1
v_cvt_pk_bf16_f32 v16, v30, v31
Mavv 60,96,248
s_add_u32 m0, s78, 74752
s_add_u32 s70, s81, 0
v_accvgpr_read_b32 v30, a2
Mavv 60,100,252
s_waitcnt lgkmcnt(3)
buffer_load_dwordx4 v12, s[48:51], s70 offen sc1 lds
Mavv 64,192,104
s_waitcnt lgkmcnt(2)
v_max_u32_dpp v49, v49, v49 row_mirror row_mask:0xf bank_mask:0xf bound_ctrl:1
v_mov_b32 v51, v49
Mavv 64,196,108
v_accvgpr_read_b32 v31, a3
v_mul_f32 v30, 0x3e0293ee, v30
Mavv 68,200,104
ds_read_b128 v[120:123], v5 offset:38912
v_permlane16_swap_b32 v49, v51
Mavv 68,204,108
v_max_u32 v49, v49, v51
v_mov_b32 v51, v49
Mavv 72,208,104
s_add_u32 m0, s78, 75776
s_add_u32 s70, s81, 34816
ds_read_b128 v[124:127], v6 offset:38912
Mavv 72,212,108
buffer_load_dwordx4 v11, s[48:51], s70 offen sc1 lds
Mavv 76,216,104
v_permlane32_swap_b32 v49, v51
v_max_u32 v49, v49, v51
Mavv 76,220,108
v_readfirstlane_b32 s90, v50
v_readfirstlane_b32 s91, v49
Mavv 80,104,224
s_add_u32 m0, s78, 76800
s_add_u32 s70, s81, 34816
v_mul_f32 v31, 0x3e0293ee, v31
Mavv 80,108,228
buffer_load_dwordx4 v12, s[48:51], s70 offen sc1 lds
Mavv 84,104,232
v_cvt_pk_bf16_f32 v17, v30, v31
v_accvgpr_read_b32 v30, a4
Mavv 84,108,236
v_accvgpr_read_b32 v31, a5
v_mul_f32 v30, 0x3e0293ee, v30
Mavv 88,104,240
v_mul_f32 v31, 0x3e0293ee, v31
v_cvt_pk_bf16_f32 v18, v30, v31
Mavv 88,108,244
s_add_u32 m0, s77, 131072
s_add_u32 s70, s68, 1048576
v_accvgpr_read_b32 v30, a6
Mavv 92,104,248
buffer_load_dwordx4 v3, s[16:19], s70 offen lds
Mavv 92,108,252
s_waitcnt lgkmcnt(3)
v_accvgpr_read_b32 v31, a7
v_mul_f32 v30, 0x3e0293ee, v30
Mavv 96,128,112
s_waitcnt lgkmcnt(0)
v_mul_f32 v31, 0x3e0293ee, v31
v_cvt_pk_bf16_f32 v19, v30, v31
Mavv 96,132,116
s_add_u32 s72, s79, 0
buffer_store_dwordx4 v[16:19], v0, s[48:51], s72 offen sc1
Mavv 96,192,120
s_add_u32 m0, s77, 131968
s_add_u32 s70, s68, 1048576
ds_read_b128 v[96:99], v5 offset:8192
Mavv 96,196,124
buffer_load_dwordx4 v3, s[16:19], s70 offen offset:128 lds
Mavv 100,136,112
ds_read_b128 v[100:103], v6 offset:8192
v_accvgpr_read_b32 v30, a8
Mavv 100,140,116
v_accvgpr_read_b32 v31, a9
v_mul_f32 v30, 0x3e0293ee, v30
Mavv 100,200,120
ds_read_b128 v[104:107], v5 offset:40960
v_mul_f32 v31, 0x3e0293ee, v31
Mavv 100,204,124
v_cvt_pk_bf16_f32 v20, v30, v31
v_accvgpr_read_b32 v30, a10
Mavv 104,144,112
s_add_u32 m0, s77, 139264
s_add_u32 s70, s68, 1310720
ds_read_b128 v[108:111], v6 offset:40960
Mavv 104,148,116
buffer_load_dwordx4 v3, s[16:19], s70 offen lds
Mavv 104,208,120
v_accvgpr_read_b32 v31, a11
v_mul_f32 v30, 0x3e0293ee, v30
Mavv 104,212,124
v_mul_f32 v31, 0x3e0293ee, v31
v_cvt_pk_bf16_f32 v21, v30, v31
Mavv 108,152,112
s_add_u32 m0, s77, 140160
s_add_u32 s70, s68, 1310720
v_accvgpr_read_b32 v30, a12
Mavv 108,156,116
buffer_load_dwordx4 v3, s[16:19], s70 offen offset:128 lds
Mavv 108,216,120
v_accvgpr_read_b32 v31, a13
v_mul_f32 v30, 0x3e0293ee, v30
Mavv 108,220,124
s_waitcnt lgkmcnt(0)
v_mul_f32 v31, 0x3e0293ee, v31
v_cvt_pk_bf16_f32 v22, v30, v31
Mavv 112,112,160
s_barrier
Mavv 112,116,164
s_add_u32 m0, s78, 0
s_add_u32 s70, s82, 128
v_accvgpr_read_b32 v30, a14
Mavv 112,120,224
buffer_load_dwordx4 v11, s[48:51], s70 offen sc1 lds
Mavv 112,124,228
v_accvgpr_read_b32 v31, a15
v_mul_f32 v30, 0x3e0293ee, v30
Mavv 116,112,168
v_mul_f32 v31, 0x3e0293ee, v31
v_cvt_pk_bf16_f32 v23, v30, v31
Mavv 116,116,172
s_add_u32 s72, s79, 1024
buffer_store_dwordx4 v[20:23], v0, s[48:51], s72 offen sc1
Mavv 116,120,232
s_add_u32 m0, s78, 1024
s_add_u32 s70, s82, 128
v_accvgpr_read_b32 v30, a32
Mavv 116,124,236
buffer_load_dwordx4 v12, s[48:51], s70 offen sc1 lds
Mavv 120,112,176
v_accvgpr_read_b32 v31, a33
v_mul_f32 v30, 0x3e0293ee, v30
Mavv 120,116,180
v_mul_f32 v31, 0x3e0293ee, v31
v_cvt_pk_bf16_f32 v16, v30, v31
Mavv 120,120,240
v_accvgpr_read_b32 v30, a34
v_accvgpr_read_b32 v31, a35
Mavv 120,124,244
s_add_u32 m0, s78, 2048
s_add_u32 s70, s82, 34944
v_mul_f32 v30, 0x3e0293ee, v30
Mavv 124,112,184
buffer_load_dwordx4 v11, s[48:51], s70 offen sc1 lds
Mavv 124,116,188
v_mul_f32 v31, 0x3e0293ee, v31
v_cvt_pk_bf16_f32 v17, v30, v31
Mavv 124,120,248
v_accvgpr_read_b32 v30, a36
v_accvgpr_read_b32 v31, a37
Mavv 124,124,252
v_mul_f32 v30, 0x3e0293ee, v30
v_mul_f32 v31, 0x3e0293ee, v31
Mavv 128,128,96
s_add_u32 m0, s78, 3072
s_add_u32 s70, s82, 34944
v_cvt_pk_bf16_f32 v18, v30, v31
Mavv 128,132,100
buffer_load_dwordx4 v12, s[48:51], s70 offen sc1 lds
Mavv 128,192,104
ds_read_b128 v[112:115], v5 offset:10240
v_accvgpr_read_b32 v30, a38
Mavv 128,196,108
v_accvgpr_read_b32 v31, a39
v_mul_f32 v30, 0x3e0293ee, v30
Mavv 132,136,96
ds_read_b128 v[116:119], v6 offset:10240
v_mul_f32 v31, 0x3e0293ee, v31
Mavv 132,140,100
v_cvt_pk_bf16_f32 v19, v30, v31
v_accvgpr_read_b32 v30, a40
Mavv 132,200,104
s_add_u32 m0, s78, 32768
s_add_u32 s70, s82, 256
ds_read_b128 v[120:123], v5 offset:43008
Mavv 132,204,108
buffer_load_dwordx4 v11, s[48:51], s70 offen sc1 lds
Mavv 136,144,96
ds_read_b128 v[124:127], v6 offset:43008
v_accvgpr_read_b32 v31, a41
Mavv 136,148,100
s_add_u32 s72, s79, 4096
buffer_store_dwordx4 v[16:19], v0, s[48:51], s72 offen sc1
Mavv 136,208,104
s_add_u32 m0, s78, 33792
s_add_u32 s70, s82, 256
v_mul_f32 v30, 0x3e0293ee, v30
Mavv 136,212,108
buffer_load_dwordx4 v12, s[48:51], s70 offen sc1 lds
Mavv 140,152,96
v_mul_f32 v31, 0x3e0293ee, v31
v_cvt_pk_bf16_f32 v20, v30, v31
Mavv 140,156,100
v_accvgpr_read_b32 v30, a42
v_accvgpr_read_b32 v31, a43
Mavv 140,216,104
v_mul_f32 v30, 0x3e0293ee, v30
v_mul_f32 v31, 0x3e0293ee, v31
Mavv 140,220,108
s_add_u32 m0, s78, 34816
s_add_u32 s70, s82, 35072
v_cvt_pk_bf16_f32 v21, v30, v31
Mavv 144,96,160
buffer_load_dwordx4 v11, s[48:51], s70 offen sc1 lds
Mavv 144,100,164
v_accvgpr_read_b32 v30, a44
v_accvgpr_read_b32 v31, a45
Mavv 144,104,224
v_mul_f32 v30, 0x3e0293ee, v30
v_mul_f32 v31, 0x3e0293ee, v31
Mavv 144,108,228
v_cvt_pk_bf16_f32 v22, v30, v31
v_accvgpr_read_b32 v30, a46
Mavv 148,96,168
s_add_u32 m0, s78, 35840
s_add_u32 s70, s82, 35072
v_accvgpr_read_b32 v31, a47
Mavv 148,100,172
buffer_load_dwordx4 v12, s[48:51], s70 offen sc1 lds
Mavv 148,104,232
v_mul_f32 v30, 0x3e0293ee, v30
v_mul_f32 v31, 0x3e0293ee, v31
Mavv 148,108,236
v_cvt_pk_bf16_f32 v23, v30, v31
v_accvgpr_read_b32 v30, a16
Mavv 152,96,176
s_add_u32 s72, s79, 5120
buffer_store_dwordx4 v[20:23], v0, s[48:51], s72 offen sc1
Mavv 152,100,180
s_add_u32 m0, s77, 147456
s_add_u32 s70, s68, 1572864
v_accvgpr_read_b32 v31, a17
Mavv 152,104,240
buffer_load_dwordx4 v3, s[16:19], s70 offen lds
Mavv 152,108,244
v_cvt_pk_bf16_f32 v16, v30, v31
v_accvgpr_read_b32 v30, a18
Mavv 156,96,184
v_accvgpr_read_b32 v31, a19
v_cvt_pk_bf16_f32 v17, v30, v31
Mavv 156,100,188
v_accvgpr_read_b32 v30, a48
v_accvgpr_read_b32 v31, a49
Mavv 156,104,248
s_add_u32 m0, s77, 148352
s_add_u32 s70, s68, 1572864
v_cvt_pk_bf16_f32 v18, v30, v31
Mavv 156,108,252
s_waitcnt lgkmcnt(3)
buffer_load_dwordx4 v3, s[16:19], s70 offen offset:128 lds
Mavv 160,128,112
s_waitcnt lgkmcnt(2)
v_accvgpr_read_b32 v30, a50
v_accvgpr_read_b32 v31, a51
Mavv 160,132,116
s_waitcnt lgkmcnt(0)
v_cvt_pk_bf16_f32 v19, v30, v31
v_accvgpr_read_b32 v30, a20
Mavv 160,192,120
s_add_u32 s72, s80, 0
ds_read_b128 v[96:99], v5 offset:12288
buffer_store_dwordx4 v[16:19], v0, s[48:51], s72 offen sc1
Mavv 160,196,124
v_accvgpr_read_b32 v31, a21
v_cvt_pk_bf16_f32 v20, v30, v31
Mavv 164,136,112
s_add_u32 m0, s77, 155648
s_add_u32 s70, s68, 1835008
ds_read_b128 v[100:103], v6 offset:12288
Mavv 164,140,116
buffer_load_dwordx4 v3, s[16:19], s70 offen lds
Mavv 164,200,120
ds_read_b128 v[104:107], v5 offset:45056
v_accvgpr_read_b32 v30, a22
Mavv 164,204,124
v_accvgpr_read_b32 v31, a23
v_cvt_pk_bf16_f32 v21, v30, v31
Mavv 168,144,112
s_add_u32 m0, s77, 156544
s_add_u32 s70, s68, 1835008
ds_read_b128 v[108:111], v6 offset:45056
Mavv 168,148,116
buffer_load_dwordx4 v3, s[16:19], s70 offen offset:128 lds
Mavv 168,208,120
s_waitcnt vmcnt(21)
v_accvgpr_read_b32 v30, a52
v_accvgpr_read_b32 v31, a53
)"
R"(
Mavv 168,212,124
ds_read_b128 v[32:35], v24 offset:0
v_cvt_pk_bf16_f32 v22, v30, v31
Mavv 172,152,112
v_accvgpr_read_b32 v30, a54
v_accvgpr_read_b32 v31, a55
Mavv 172,156,116
ds_read_b128 v[36:39], v25 offset:0
v_cvt_pk_bf16_f32 v23, v30, v31
Mavv 172,216,120
s_add_u32 s72, s80, 2048
buffer_store_dwordx4 v[20:23], v0, s[48:51], s72 offen sc1
Mavv 172,220,124
ds_read_b128 v[40:43], v24 offset:2048
v_accvgpr_read_b32 v30, a24
Mavv 176,112,160
v_accvgpr_read_b32 v31, a25
v_cvt_pk_bf16_f32 v16, v30, v31
Mavv 176,116,164
ds_read_b128 v[44:47], v25 offset:2048
v_accvgpr_read_b32 v30, a26
Mavv 176,120,224
v_accvgpr_read_b32 v31, a27
v_cvt_pk_bf16_f32 v17, v30, v31
Mavv 176,124,228
ds_read_b128 v[48:51], v24 offset:8192
v_accvgpr_read_b32 v30, a56
Mavv 180,112,168
v_accvgpr_read_b32 v31, a57
v_cvt_pk_bf16_f32 v18, v30, v31
Mavv 180,116,172
ds_read_b128 v[52:55], v25 offset:8192
v_accvgpr_read_b32 v30, a58
Mavv 180,120,232
v_accvgpr_read_b32 v31, a59
v_cvt_pk_bf16_f32 v19, v30, v31
Mavv 180,124,236
s_add_u32 s72, s80, 4096
ds_read_b128 v[56:59], v24 offset:10240
buffer_store_dwordx4 v[16:19], v0, s[48:51], s72 offen sc1
Mavv 184,112,176
v_accvgpr_read_b32 v30, a28
v_accvgpr_read_b32 v31, a29
Mavv 184,116,180
ds_read_b128 v[60:63], v25 offset:10240
v_cvt_pk_bf16_f32 v20, v30, v31
Mavv 184,120,240
v_accvgpr_read_b32 v30, a30
v_accvgpr_read_b32 v31, a31
Mavv 184,124,244
v_cvt_pk_bf16_f32 v21, v30, v31
v_accvgpr_read_b32 v30, a60
Mavv 188,112,184
v_accvgpr_read_b32 v31, a61
v_cvt_pk_bf16_f32 v22, v30, v31
Mavv 188,116,188
v_accvgpr_read_b32 v30, a62
v_accvgpr_read_b32 v31, a63
Mavv 188,120,248
s_waitcnt lgkmcnt(11)
v_cvt_pk_bf16_f32 v23, v30, v31
v_accvgpr_read_b32 v30, a64
Mavv 188,124,252
s_add_u32 s72, s80, 6144
buffer_store_dwordx4 v[20:23], v0, s[48:51], s72 offen sc1
Mavv 192,128,96
s_waitcnt lgkmcnt(10)
v_accvgpr_read_b32 v31, a65
v_mul_f32 v30, 0x3e0293ee, v30
Mavv 192,132,100
s_waitcnt lgkmcnt(9)
v_mul_f32 v31, 0x3e0293ee, v31
v_cvt_pk_bf16_f32 v16, v30, v31
Mavv 192,192,104
s_waitcnt lgkmcnt(8)
ds_read_b128 v[112:115], v5 offset:14336
v_accvgpr_read_b32 v30, a66
Mavv 192,196,108
v_accvgpr_read_b32 v31, a67
v_mul_f32 v30, 0x3e0293ee, v30
Mavv 196,136,96
ds_read_b128 v[116:119], v6 offset:14336
v_mul_f32 v31, 0x3e0293ee, v31
Mavv 196,140,100
v_cvt_pk_bf16_f32 v17, v30, v31
v_accvgpr_read_b32 v30, a68
Mavv 196,200,104
ds_read_b128 v[120:123], v5 offset:47104
v_accvgpr_read_b32 v31, a69
Mavv 196,204,108
v_mul_f32 v30, 0x3e0293ee, v30
v_mul_f32 v31, 0x3e0293ee, v31
Mavv 200,144,96
ds_read_b128 v[124:127], v6 offset:47104
v_cvt_pk_bf16_f32 v18, v30, v31
Mavv 200,148,100
v_accvgpr_read_b32 v30, a70
v_accvgpr_read_b32 v31, a71
Mavv 200,208,104
v_mul_f32 v30, 0x3e0293ee, v30
v_mul_f32 v31, 0x3e0293ee, v31
Mavv 200,212,108
v_cvt_pk_bf16_f32 v19, v30, v31
v_accvgpr_read_b32 v30, a72
Mavv 204,152,96
s_add_u32 s72, s79, 8192
buffer_store_dwordx4 v[16:19], v0, s[48:51], s72 offen sc1
Mavv 204,156,100
v_accvgpr_read_b32 v31, a73
v_mul_f32 v30, 0x3e0293ee, v30
Mavv 204,216,104
v_mul_f32 v31, 0x3e0293ee, v31
v_cvt_pk_bf16_f32 v20, v30, v31
Mavv 204,220,108
v_accvgpr_read_b32 v30, a74
v_accvgpr_read_b32 v31, a75
Mavv 208,96,160
v_mul_f32 v30, 0x3e0293ee, v30
v_mul_f32 v31, 0x3e0293ee, v31
Mavv 208,100,164
v_cvt_pk_bf16_f32 v21, v30, v31
v_accvgpr_read_b32 v30, a76
Mavv 208,104,224
v_accvgpr_read_b32 v31, a77
v_mul_f32 v30, 0x3e0293ee, v30
Mavv 208,108,228
v_mul_f32 v31, 0x3e0293ee, v31
v_cvt_pk_bf16_f32 v22, v30, v31
Mavv 212,96,168
v_accvgpr_read_b32 v30, a78
v_accvgpr_read_b32 v31, a79
Mavv 212,100,172
v_mul_f32 v30, 0x3e0293ee, v30
v_mul_f32 v31, 0x3e0293ee, v31
Mavv 212,104,232
v_cvt_pk_bf16_f32 v23, v30, v31
v_accvgpr_read_b32 v30, a96
Mavv 212,108,236
s_add_u32 s72, s79, 9216
buffer_store_dwordx4 v[20:23], v0, s[48:51], s72 offen sc1
Mavv 216,96,176
v_subrev_u32 v5, 0x10000, v24
v_subrev_u32 v6, 0x10000, v25
Mavv 216,100,180
s_waitcnt lgkmcnt(0)
v_mov_b32 v7, v24
v_mov_b32 v8, v25
Mavv 216,104,240
s_barrier
Mavv 216,108,244
s_add_u32 m0, s78, 8192
s_add_u32 s70, s81, 128
v_accvgpr_read_b32 v31, a97
Mavv 220,96,184
buffer_load_dwordx4 v11, s[48:51], s70 offen sc1 lds
Mavv 220,100,188
s_add_u32 m0, s78, 9216
s_add_u32 s70, s81, 128
v_mul_f32 v30, 0x3e0293ee, v30
Mavv 220,104,248
buffer_load_dwordx4 v12, s[48:51], s70 offen sc1 lds
Mavv 220,108,252
s_add_u32 m0, s78, 10240
s_add_u32 s70, s81, 34944
v_mul_f32 v31, 0x3e0293ee, v31
Mavv 224,128,112
buffer_load_dwordx4 v11, s[48:51], s70 offen sc1 lds
Mavv 224,132,116
v_cvt_pk_bf16_f32 v16, v30, v31
v_accvgpr_read_b32 v30, a98
Mavv 224,192,120
s_add_u32 m0, s78, 11264
s_add_u32 s70, s81, 34944
ds_read_b64_tr_b16 v[96:97], v26 offset:0
Mavv 224,196,124
buffer_load_dwordx4 v12, s[48:51], s70 offen sc1 lds
Mavv 228,136,112
v_accvgpr_read_b32 v31, a99
v_mul_f32 v30, 0x3e0293ee, v30
Mavv 228,140,116
s_add_u32 m0, s78, 40960
s_add_u32 s70, s81, 256
ds_read_b64_tr_b16 v[98:99], v26 offset:256
Mavv 228,200,120
buffer_load_dwordx4 v11, s[48:51], s70 offen sc1 lds
Mavv 228,204,124
v_mul_f32 v31, 0x3e0293ee, v31
v_cvt_pk_bf16_f32 v17, v30, v31
Mavv 232,144,112
ds_read_b64_tr_b16 v[100:101], v26 offset:8192
v_accvgpr_read_b32 v30, a100
Mavv 232,148,116
s_add_u32 m0, s78, 41984
s_add_u32 s70, s81, 256
v_accvgpr_read_b32 v31, a101
Mavv 232,208,120
buffer_load_dwordx4 v12, s[48:51], s70 offen sc1 lds
Mavv 232,212,124
ds_read_b64_tr_b16 v[102:103], v26 offset:8448
v_mul_f32 v30, 0x3e0293ee, v30
Mavv 236,152,112
v_mul_f32 v31, 0x3e0293ee, v31
v_cvt_pk_bf16_f32 v18, v30, v31
Mavv 236,156,116
v_accvgpr_read_b32 v30, a102
v_accvgpr_read_b32 v31, a103
Mavv 236,216,120
s_add_u32 m0, s78, 43008
s_add_u32 s70, s81, 35072
ds_read_b64_tr_b16 v[104:105], v27 offset:0
Mavv 236,220,124
buffer_load_dwordx4 v11, s[48:51], s70 offen sc1 lds
Mavv 240,112,160
v_mul_f32 v30, 0x3e0293ee, v30
v_mul_f32 v31, 0x3e0293ee, v31
Mavv 240,116,164
s_add_u32 m0, s78, 44032
s_add_u32 s70, s81, 35072
ds_read_b64_tr_b16 v[106:107], v27 offset:256
Mavv 240,120,224
buffer_load_dwordx4 v12, s[48:51], s70 offen sc1 lds
Mavv 240,124,228
v_cvt_pk_bf16_f32 v19, v30, v31
v_accvgpr_read_b32 v30, a104
Mavv 244,112,168
s_add_u32 s72, s79, 12288
ds_read_b64_tr_b16 v[108:109], v27 offset:8192
buffer_store_dwordx4 v[16:19], v0, s[48:51], s72 offen sc1
Mavv 244,116,172
s_add_u32 m0, s78, 65536
s_add_u32 s70, s82, 384
v_accvgpr_read_b32 v31, a105
Mavv 244,120,232
buffer_load_dwordx4 v11, s[48:51], s70 offen sc1 lds
Mavv 244,124,236
ds_read_b64_tr_b16 v[110:111], v27 offset:8448
v_mul_f32 v30, 0x3e0293ee, v30
Mavv 248,112,176
v_mul_f32 v31, 0x3e0293ee, v31
v_cvt_pk_bf16_f32 v20, v30, v31
Mavv 248,116,180
s_add_u32 m0, s78, 66560
s_add_u32 s70, s82, 384
v_accvgpr_read_b32 v30, a106
Mavv 248,120,240
buffer_load_dwordx4 v12, s[48:51], s70 offen sc1 lds
Mavv 248,124,244
v_accvgpr_read_b32 v31, a107
v_mul_f32 v30, 0x3e0293ee, v30
Mavv 252,112,184
v_mul_f32 v31, 0x3e0293ee, v31
v_cvt_pk_bf16_f32 v21, v30, v31
Mavv 252,116,188
s_add_u32 m0, s78, 67584
s_add_u32 s70, s82, 35200
v_accvgpr_read_b32 v30, a108
Mavv 252,120,248
buffer_load_dwordx4 v11, s[48:51], s70 offen sc1 lds
Mavv 252,124,252
s_waitcnt lgkmcnt(6)
v_accvgpr_read_b32 v31, a109
v_mul_f32 v30, 0x3e0293ee, v30
Zvvv 128,96,32
s_waitcnt lgkmcnt(4)
ds_read_b64_tr_b16 v[112:113], v26 offset:512
v_mul_f32 v31, 0x3e0293ee, v31
Mvvv 128,100,36
v_cvt_pk_bf16_f32 v22, v30, v31
v_accvgpr_read_b32 v30, a110
Zvvv 160,96,40
s_add_u32 m0, s78, 68608
s_add_u32 s70, s82, 35200
ds_read_b64_tr_b16 v[114:115], v26 offset:768
Mvvv 160,100,44
buffer_load_dwordx4 v12, s[48:51], s70 offen sc1 lds
Zvvv 192,96,48
ds_read_b64_tr_b16 v[116:117], v26 offset:8704
v_accvgpr_read_b32 v31, a111
Mvvv 192,100,52
v_mul_f32 v30, 0x3e0293ee, v30
v_mul_f32 v31, 0x3e0293ee, v31
Zvvv 224,96,56
s_add_u32 m0, s78, 73728
s_add_u32 s70, s81, 384
ds_read_b64_tr_b16 v[118:119], v26 offset:8960
Mvvv 224,100,60
s_waitcnt lgkmcnt(6)
buffer_load_dwordx4 v11, s[48:51], s70 offen sc1 lds
Zvvv 132,104,32
s_waitcnt lgkmcnt(4)
ds_read_b64_tr_b16 v[120:121], v27 offset:512
v_cvt_pk_bf16_f32 v23, v30, v31
Mvvv 132,108,36
s_add_u32 s72, s79, 13312
buffer_store_dwordx4 v[20:23], v0, s[48:51], s72 offen sc1
Zvvv 164,104,40
s_add_u32 m0, s78, 74752
s_add_u32 s70, s81, 384
ds_read_b64_tr_b16 v[122:123], v27 offset:768
Mvvv 164,108,44
buffer_load_dwordx4 v12, s[48:51], s70 offen sc1 lds
Zvvv 196,104,48
ds_read_b64_tr_b16 v[124:125], v27 offset:8704
v_accvgpr_read_b32 v30, a80
Mvvv 196,108,52
v_accvgpr_read_b32 v31, a81
v_cvt_pk_bf16_f32 v16, v30, v31
Zvvv 228,104,56
s_add_u32 m0, s78, 75776
s_add_u32 s70, s81, 35200
ds_read_b64_tr_b16 v[126:127], v27 offset:8960
Mvvv 228,108,60
s_waitcnt lgkmcnt(6)
buffer_load_dwordx4 v11, s[48:51], s70 offen sc1 lds
Zvvv 136,112,32
s_waitcnt lgkmcnt(4)
ds_read_b64_tr_b16 v[96:97], v26 offset:1024
v_accvgpr_read_b32 v30, a82
Mvvv 136,116,36
v_accvgpr_read_b32 v31, a83
v_cvt_pk_bf16_f32 v17, v30, v31
Zvvv 168,112,40
s_add_u32 m0, s78, 76800
s_add_u32 s70, s81, 35200
ds_read_b64_tr_b16 v[98:99], v26 offset:1280
Mvvv 168,116,44
s_waitcnt lgkmcnt(4)
buffer_load_dwordx4 v12, s[48:51], s70 offen sc1 lds
Zvvv 200,112,48
s_lshl_b32 s61, s63, 1
ds_read_b64_tr_b16 v[100:101], v26 offset:9216
v_accvgpr_read_b32 v30, a112
Mvvv 200,116,52
s_mul_i32 s60, s64, 1114112
v_accvgpr_read_b32 v31, a113
v_cvt_pk_bf16_f32 v18, v30, v31
Zvvv 232,112,56
s_add_u32 s60, s60, s69
ds_read_b64_tr_b16 v[102:103], v26 offset:9472
v_accvgpr_read_b32 v30, a114
Mvvv 232,116,60
s_add_u32 s60, s60, 16777216
v_accvgpr_read_b32 v31, a115
v_cvt_pk_bf16_f32 v19, v30, v31
Zvvv 140,120,32
s_waitcnt lgkmcnt(4)
ds_read_b64_tr_b16 v[104:105], v27 offset:1024
v_accvgpr_read_b32 v30, a84
Mvvv 140,124,36
v_accvgpr_read_b32 v31, a85
v_cvt_pk_bf16_f32 v20, v30, v31
Zvvv 172,120,40
ds_read_b64_tr_b16 v[106:107], v27 offset:1280
v_accvgpr_read_b32 v30, a86
Mvvv 172,124,44
v_accvgpr_read_b32 v31, a87
v_cvt_pk_bf16_f32 v21, v30, v31
Zvvv 204,120,48
ds_read_b64_tr_b16 v[108:109], v27 offset:9216
v_accvgpr_read_b32 v30, a116
Mvvv 204,124,52
s_add_u32 s72, s80, 1024
buffer_store_dwordx4 v[16:19], v0, s[48:51], s72 offen sc1
Zvvv 236,120,56
s_waitcnt vmcnt(15)
ds_read_b64_tr_b16 v[110:111], v27 offset:9472
v_accvgpr_read_b32 v31, a117
Mvvv 236,124,60
s_waitcnt lgkmcnt(6)
ds_read_b128 v[64:67], v5 offset:0
v_cvt_pk_bf16_f32 v22, v30, v31
Zvvv 144,96,32
s_waitcnt lgkmcnt(5)
ds_read_b64_tr_b16 v[112:113], v26 offset:1536
v_accvgpr_read_b32 v30, a118
Mvvv 144,100,36
ds_read_b128 v[68:71], v6 offset:0
v_accvgpr_read_b32 v31, a119
Zvvv 176,96,40
ds_read_b64_tr_b16 v[114:115], v26 offset:1792
v_cvt_pk_bf16_f32 v23, v30, v31
Mvvv 176,100,44
ds_read_b128 v[72:75], v5 offset:2048
v_accvgpr_read_b32 v30, a88
Zvvv 208,96,48
ds_read_b64_tr_b16 v[116:117], v26 offset:9728
v_accvgpr_read_b32 v31, a89
Mvvv 208,100,52
ds_read_b128 v[76:79], v6 offset:2048
v_cvt_pk_bf16_f32 v16, v30, v31
Zvvv 240,96,56
ds_read_b64_tr_b16 v[118:119], v26 offset:9984
v_accvgpr_read_b32 v30, a90
Mvvv 240,100,60
s_waitcnt lgkmcnt(10)
ds_read_b128 v[80:83], v5 offset:8192
v_accvgpr_read_b32 v31, a91
Zvvv 148,104,32
s_waitcnt lgkmcnt(9)
ds_read_b64_tr_b16 v[120:121], v27 offset:1536
v_cvt_pk_bf16_f32 v17, v30, v31
Mvvv 148,108,36
ds_read_b128 v[84:87], v6 offset:8192
v_accvgpr_read_b32 v30, a120
Zvvv 180,104,40
ds_read_b64_tr_b16 v[122:123], v27 offset:1792
v_accvgpr_read_b32 v31, a121
Mvvv 180,108,44
ds_read_b128 v[88:91], v5 offset:10240
v_cvt_pk_bf16_f32 v18, v30, v31
Zvvv 212,104,48
ds_read_b64_tr_b16 v[124:125], v27 offset:9728
v_accvgpr_read_b32 v30, a122
Mvvv 212,108,52
ds_read_b128 v[92:95], v6 offset:10240
v_accvgpr_read_b32 v31, a123
Zvvv 244,104,56
s_waitcnt lgkmcnt(12)
ds_read_b64_tr_b16 v[126:127], v27 offset:9984
v_cvt_pk_bf16_f32 v19, v30, v31
Mvvv 244,108,60
s_waitcnt lgkmcnt(12)
v_lshrrev_b32 v1, 4, v0
v_add_u32 v1, 4, v1
Zvvv 152,112,32
s_waitcnt lgkmcnt(8)
v_and_b32 v1, 63, v1
v_subrev_u32 v2, s61, v1
Mvvv 152,116,36
s_add_u32 s72, s80, 3072
buffer_store_dwordx4 v[20:23], v0, s[48:51], s72 offen sc1
Zvvv 184,112,40
v_add_u32 v4, -2, v1
v_sub_u32 v9, 33, v1
Mvvv 184,116,44
)"
R"(
v_accvgpr_read_b32 v30, a92
v_accvgpr_read_b32 v31, a93
Zvvv 216,112,48
v_cvt_pk_bf16_f32 v20, v30, v31
v_accvgpr_read_b32 v30, a94
Mvvv 216,116,52
s_waitcnt lgkmcnt(0)
v_accvgpr_read_b32 v31, a95
v_cvt_pk_bf16_f32 v21, v30, v31
Zvvv 248,112,56
s_barrier
Mvvv 248,116,60
ds_read_b64_tr_b16 v[96:97], v26 offset:16384
v_accvgpr_read_b32 v30, a124
Zvvv 156,120,32
ds_read_b64_tr_b16 v[98:99], v26 offset:16640
v_accvgpr_read_b32 v31, a125
Mvvv 156,124,36
ds_read_b64_tr_b16 v[100:101], v26 offset:24576
v_cvt_pk_bf16_f32 v22, v30, v31
Zvvv 188,120,40
s_add_u32 m0, s77, 98304
s_add_u32 s70, s68, 2097152
ds_read_b64_tr_b16 v[102:103], v26 offset:24832
Mvvv 188,124,44
buffer_load_dwordx4 v3, s[16:19], s70 offen lds
Zvvv 220,120,48
ds_read_b64_tr_b16 v[104:105], v27 offset:16384
v_accvgpr_read_b32 v30, a126
Mvvv 220,124,52
ds_read_b64_tr_b16 v[106:107], v27 offset:16640
v_accvgpr_read_b32 v31, a127
Zvvv 252,120,56
ds_read_b64_tr_b16 v[108:109], v27 offset:24576
v_cvt_pk_bf16_f32 v23, v30, v31
Mvvv 252,124,60
s_waitcnt lgkmcnt(5)
ds_read_b64_tr_b16 v[110:111], v27 offset:24832
v_cmp_eq_u32 vcc, 3, v2
Mvvv 128,96,64
s_waitcnt lgkmcnt(4)
s_add_u32 m0, s77, 99200
s_add_u32 s70, s68, 2097152
ds_read_b64_tr_b16 v[112:113], v26 offset:16896
Mvvv 128,100,68
buffer_load_dwordx4 v3, s[16:19], s70 offen offset:128 lds
Mvvv 160,96,72
s_add_u32 m0, s77, 106496
s_add_u32 s70, s68, 2359296
ds_read_b64_tr_b16 v[114:115], v26 offset:17152
Mvvv 160,100,76
buffer_load_dwordx4 v3, s[16:19], s70 offen lds
Mvvv 192,96,80
s_add_u32 m0, s77, 107392
s_add_u32 s70, s68, 2359296
ds_read_b64_tr_b16 v[116:117], v26 offset:25088
Mvvv 192,100,84
buffer_load_dwordx4 v3, s[16:19], s70 offen offset:128 lds
Mvvv 224,96,88
s_waitcnt vmcnt(16) lgkmcnt(5)
ds_read_b64_tr_b16 v[118:119], v26 offset:25344
v_cndmask_b32 v4, v4, v9, vcc
Mvvv 224,100,92
s_add_u32 s72, s80, 5120
ds_read_b128 v[32:35], v5 offset:32768
buffer_store_dwordx4 v[16:19], v0, s[48:51], s72 offen sc1
Mvvv 132,104,64
s_waitcnt lgkmcnt(5)
ds_read_b64_tr_b16 v[120:121], v27 offset:16896
v_add_u32 v9, -1, v1
Mvvv 132,108,68
ds_read_b128 v[36:39], v6 offset:32768
v_accvgpr_read_b32 v30, a128
Mvvv 164,104,72
ds_read_b64_tr_b16 v[122:123], v27 offset:17152
v_accvgpr_read_b32 v31, a129
Mvvv 164,108,76
ds_read_b128 v[40:43], v5 offset:34816
v_mul_f32 v30, 0x3e0293ee, v30
Mvvv 196,104,80
ds_read_b64_tr_b16 v[124:125], v27 offset:25088
v_mul_f32 v31, 0x3e0293ee, v31
Mvvv 196,108,84
ds_read_b128 v[44:47], v6 offset:34816
v_cvt_pk_bf16_f32 v16, v30, v31
Mvvv 228,104,88
ds_read_b64_tr_b16 v[126:127], v27 offset:25344
v_accvgpr_read_b32 v30, a130
Mvvv 228,108,92
s_waitcnt lgkmcnt(10)
ds_read_b128 v[48:51], v5 offset:40960
v_accvgpr_read_b32 v31, a131
Mvvv 136,112,64
s_waitcnt lgkmcnt(9)
ds_read_b64_tr_b16 v[96:97], v26 offset:17408
v_mul_f32 v30, 0x3e0293ee, v30
Mvvv 136,116,68
ds_read_b128 v[52:55], v6 offset:40960
v_mul_f32 v31, 0x3e0293ee, v31
Mvvv 168,112,72
ds_read_b64_tr_b16 v[98:99], v26 offset:17664
v_cvt_pk_bf16_f32 v17, v30, v31
Mvvv 168,116,76
ds_read_b128 v[56:59], v5 offset:43008
v_accvgpr_read_b32 v30, a132
Mvvv 200,112,80
ds_read_b64_tr_b16 v[100:101], v26 offset:25600
v_accvgpr_read_b32 v31, a133
Mvvv 200,116,84
s_waitcnt lgkmcnt(11)
ds_read_b128 v[60:63], v6 offset:43008
v_mul_f32 v30, 0x3e0293ee, v30
Mvvv 232,112,88
s_add_u32 m0, s78, 0
s_add_u32 s70, s82, 512
ds_read_b64_tr_b16 v[102:103], v26 offset:25856
Mvvv 232,116,92
s_waitcnt lgkmcnt(8)
buffer_load_dwordx4 v11, s[48:51], s70 offen sc1 lds
Mvvv 140,120,64
s_add_u32 m0, s78, 1024
s_add_u32 s70, s82, 512
ds_read_b64_tr_b16 v[104:105], v27 offset:17408
Mvvv 140,124,68
buffer_load_dwordx4 v12, s[48:51], s70 offen sc1 lds
Mvvv 172,120,72
s_add_u32 m0, s78, 2048
s_add_u32 s70, s82, 35328
ds_read_b64_tr_b16 v[106:107], v27 offset:17664
Mvvv 172,124,76
buffer_load_dwordx4 v11, s[48:51], s70 offen sc1 lds
Mvvv 204,120,80
s_add_u32 m0, s78, 3072
s_add_u32 s70, s82, 35328
ds_read_b64_tr_b16 v[108:109], v27 offset:25600
Mvvv 204,124,84
buffer_load_dwordx4 v12, s[48:51], s70 offen sc1 lds
Mvvv 236,120,88
s_waitcnt lgkmcnt(3)
ds_read_b64_tr_b16 v[110:111], v27 offset:25856
v_mul_f32 v31, 0x3e0293ee, v31
Mvvv 236,124,92
s_add_u32 s72, s80, 7168
buffer_store_dwordx4 v[20:23], v0, s[48:51], s72 offen sc1
Mvvv 144,96,64
s_add_u32 m0, s78, 8192
s_add_u32 s70, s81, 512
ds_read_b64_tr_b16 v[112:113], v26 offset:17920
Mvvv 144,100,68
buffer_load_dwordx4 v11, s[48:51], s70 offen sc1 lds
Mvvv 176,96,72
ds_read_b64_tr_b16 v[114:115], v26 offset:18176
v_cvt_pk_bf16_f32 v18, v30, v31
Mvvv 176,100,76
v_accvgpr_read_b32 v30, a134
v_accvgpr_read_b32 v31, a135
Mvvv 208,96,80
s_add_u32 m0, s78, 9216
s_add_u32 s70, s81, 512
ds_read_b64_tr_b16 v[116:117], v26 offset:26112
Mvvv 208,100,84
buffer_load_dwordx4 v12, s[48:51], s70 offen sc1 lds
Mvvv 240,96,88
ds_read_b64_tr_b16 v[118:119], v26 offset:26368
v_mul_f32 v30, 0x3e0293ee, v30
Mvvv 240,100,92
s_waitcnt lgkmcnt(4)
v_mul_f32 v31, 0x3e0293ee, v31
v_cvt_pk_bf16_f32 v19, v30, v31
Mvvv 148,104,64
s_add_u32 m0, s78, 10240
s_add_u32 s70, s81, 35328
ds_read_b64_tr_b16 v[120:121], v27 offset:17920
Mvvv 148,108,68
buffer_load_dwordx4 v11, s[48:51], s70 offen sc1 lds
Mvvv 180,104,72
ds_read_b64_tr_b16 v[122:123], v27 offset:18176
v_accvgpr_read_b32 v30, a136
Mvvv 180,108,76
v_accvgpr_read_b32 v31, a137
v_mul_f32 v30, 0x3e0293ee, v30
Mvvv 212,104,80
s_add_u32 m0, s78, 11264
s_add_u32 s70, s81, 35328
ds_read_b64_tr_b16 v[124:125], v27 offset:26112
Mvvv 212,108,84
buffer_load_dwordx4 v12, s[48:51], s70 offen sc1 lds
Mvvv 244,104,88
ds_read_b64_tr_b16 v[126:127], v27 offset:26368
v_mul_f32 v31, 0x3e0293ee, v31
Mvvv 244,108,92
s_waitcnt lgkmcnt(6)
v_cvt_pk_bf16_f32 v20, v30, v31
v_accvgpr_read_b32 v30, a138
Mvvv 152,112,64
s_waitcnt lgkmcnt(4)
v_accvgpr_read_b32 v31, a139
v_mul_f32 v30, 0x3e0293ee, v30
Mvvv 152,116,68
v_mul_f32 v31, 0x3e0293ee, v31
v_cvt_pk_bf16_f32 v21, v30, v31
Mvvv 184,112,72
v_accvgpr_read_b32 v30, a140
v_accvgpr_read_b32 v31, a141
Mvvv 184,116,76
s_add_u32 s72, s79, 34816
buffer_store_dwordx4 v[16:19], v0, s[48:51], s72 offen sc1
Mvvv 216,112,80
v_mul_f32 v30, 0x3e0293ee, v30
v_mul_f32 v31, 0x3e0293ee, v31
Mvvv 216,116,84
s_waitcnt lgkmcnt(0)
v_cvt_pk_bf16_f32 v22, v30, v31
v_accvgpr_read_b32 v30, a142
Mvvv 248,112,88
s_barrier
Mvvv 248,116,92
ds_read_b64_tr_b16 v[96:97], v26 offset:32768
v_accvgpr_read_b32 v31, a143
Mvvv 156,120,64
ds_read_b64_tr_b16 v[98:99], v26 offset:33024
v_mul_f32 v30, 0x3e0293ee, v30
Mvvv 156,124,68
ds_read_b64_tr_b16 v[100:101], v26 offset:40960
v_mul_f32 v31, 0x3e0293ee, v31
Mvvv 188,120,72
s_add_u32 m0, s77, 114688
s_add_u32 s70, s68, 2621440
ds_read_b64_tr_b16 v[102:103], v26 offset:41216
Mvvv 188,124,76
buffer_load_dwordx4 v3, s[16:19], s70 offen lds
Mvvv 220,120,80
ds_read_b64_tr_b16 v[104:105], v27 offset:32768
v_cvt_pk_bf16_f32 v23, v30, v31
Mvvv 220,124,84
ds_read_b64_tr_b16 v[106:107], v27 offset:33024
v_accvgpr_read_b32 v30, a160
Mvvv 252,120,88
ds_read_b64_tr_b16 v[108:109], v27 offset:40960
v_accvgpr_read_b32 v31, a161
Mvvv 252,124,92
s_waitcnt lgkmcnt(5)
ds_read_b64_tr_b16 v[110:111], v27 offset:41216
v_mul_f32 v30, 0x3e0293ee, v30
Mvvv 128,96,32
s_waitcnt lgkmcnt(4)
s_add_u32 m0, s77, 115584
s_add_u32 s70, s68, 2621440
ds_read_b64_tr_b16 v[112:113], v26 offset:33280
Mvvv 128,100,36
buffer_load_dwordx4 v3, s[16:19], s70 offen offset:128 lds
Mvvv 160,96,40
s_add_u32 m0, s77, 122880
s_add_u32 s70, s68, 2883584
ds_read_b64_tr_b16 v[114:115], v26 offset:33536
Mvvv 160,100,44
buffer_load_dwordx4 v3, s[16:19], s70 offen lds
Mvvv 192,96,48
s_add_u32 m0, s77, 123776
s_add_u32 s70, s68, 2883584
ds_read_b64_tr_b16 v[116:117], v26 offset:41472
Mvvv 192,100,52
buffer_load_dwordx4 v3, s[16:19], s70 offen offset:128 lds
Mvvv 224,96,56
s_waitcnt vmcnt(21) lgkmcnt(5)
ds_read_b64_tr_b16 v[118:119], v26 offset:41728
v_mul_f32 v31, 0x3e0293ee, v31
Mvvv 224,100,60
s_add_u32 s72, s79, 35840
ds_read_b128 v[64:67], v7 offset:0
buffer_store_dwordx4 v[20:23], v0, s[48:51], s72 offen sc1
Mvvv 132,104,32
s_waitcnt lgkmcnt(5)
ds_read_b64_tr_b16 v[120:121], v27 offset:33280
v_cvt_pk_bf16_f32 v16, v30, v31
Mvvv 132,108,36
ds_read_b128 v[68:71], v8 offset:0
v_accvgpr_read_b32 v30, a162
Mvvv 164,104,40
ds_read_b64_tr_b16 v[122:123], v27 offset:33536
v_accvgpr_read_b32 v31, a163
Mvvv 164,108,44
ds_read_b128 v[72:75], v7 offset:2048
v_mul_f32 v30, 0x3e0293ee, v30
Mvvv 196,104,48
ds_read_b64_tr_b16 v[124:125], v27 offset:41472
v_mul_f32 v31, 0x3e0293ee, v31
Mvvv 196,108,52
ds_read_b128 v[76:79], v8 offset:2048
v_cvt_pk_bf16_f32 v17, v30, v31
Mvvv 228,104,56
ds_read_b64_tr_b16 v[126:127], v27 offset:41728
v_accvgpr_read_b32 v30, a164
Mvvv 228,108,60
s_waitcnt lgkmcnt(10)
ds_read_b128 v[80:83], v7 offset:8192
v_accvgpr_read_b32 v31, a165
Mvvv 136,112,32
s_waitcnt lgkmcnt(9)
ds_read_b64_tr_b16 v[96:97], v26 offset:33792
v_mul_f32 v30, 0x3e0293ee, v30
Mvvv 136,116,36
ds_read_b128 v[84:87], v8 offset:8192
v_mul_f32 v31, 0x3e0293ee, v31
Mvvv 168,112,40
ds_read_b64_tr_b16 v[98:99], v26 offset:34048
v_cvt_pk_bf16_f32 v18, v30, v31
Mvvv 168,116,44
ds_read_b128 v[88:91], v7 offset:10240
v_accvgpr_read_b32 v30, a166
Mvvv 200,112,48
ds_read_b64_tr_b16 v[100:101], v26 offset:41984
v_accvgpr_read_b32 v31, a167
Mvvv 200,116,52
s_waitcnt lgkmcnt(11)
ds_read_b128 v[92:95], v8 offset:10240
v_mul_f32 v30, 0x3e0293ee, v30
Mvvv 232,112,56
s_add_u32 m0, s78, 32768
s_add_u32 s70, s82, 640
ds_read_b64_tr_b16 v[102:103], v26 offset:42240
Mvvv 232,116,60
s_waitcnt lgkmcnt(8)
buffer_load_dwordx4 v11, s[48:51], s70 offen sc1 lds
Mvvv 140,120,32
s_add_u32 m0, s78, 33792
s_add_u32 s70, s82, 640
ds_read_b64_tr_b16 v[104:105], v27 offset:33792
Mvvv 140,124,36
buffer_load_dwordx4 v12, s[48:51], s70 offen sc1 lds
Mvvv 172,120,40
s_add_u32 m0, s78, 34816
s_add_u32 s70, s82, 35456
ds_read_b64_tr_b16 v[106:107], v27 offset:34048
Mvvv 172,124,44
buffer_load_dwordx4 v11, s[48:51], s70 offen sc1 lds
Mvvv 204,120,48
s_add_u32 m0, s78, 35840
s_add_u32 s70, s82, 35456
ds_read_b64_tr_b16 v[108:109], v27 offset:41984
Mvvv 204,124,52
buffer_load_dwordx4 v12, s[48:51], s70 offen sc1 lds
Mvvv 236,120,56
ds_read_b64_tr_b16 v[110:111], v27 offset:42240
v_mul_f32 v31, 0x3e0293ee, v31
Mvvv 236,124,60
s_waitcnt lgkmcnt(4)
v_cvt_pk_bf16_f32 v19, v30, v31
v_accvgpr_read_b32 v30, a168
Mvvv 144,96,32
s_add_u32 m0, s78, 40960
s_add_u32 s70, s81, 640
ds_read_b64_tr_b16 v[112:113], v26 offset:34304
Mvvv 144,100,36
buffer_load_dwordx4 v11, s[48:51], s70 offen sc1 lds
Mvvv 176,96,40
ds_read_b64_tr_b16 v[114:115], v26 offset:34560
v_accvgpr_read_b32 v31, a169
Mvvv 176,100,44
s_add_u32 s72, s79, 38912
buffer_store_dwordx4 v[16:19], v0, s[48:51], s72 offen sc1
Mvvv 208,96,48
s_add_u32 m0, s78, 41984
s_add_u32 s70, s81, 640
ds_read_b64_tr_b16 v[116:117], v26 offset:42496
Mvvv 208,100,52
buffer_load_dwordx4 v12, s[48:51], s70 offen sc1 lds
Mvvv 240,96,56
ds_read_b64_tr_b16 v[118:119], v26 offset:42752
v_mul_f32 v30, 0x3e0293ee, v30
Mvvv 240,100,60
s_waitcnt lgkmcnt(4)
v_mul_f32 v31, 0x3e0293ee, v31
v_cvt_pk_bf16_f32 v20, v30, v31
Mvvv 148,104,32
s_add_u32 m0, s78, 43008
s_add_u32 s70, s81, 35456
ds_read_b64_tr_b16 v[120:121], v27 offset:34304
Mvvv 148,108,36
buffer_load_dwordx4 v11, s[48:51], s70 offen sc1 lds
Mvvv 180,104,40
ds_read_b64_tr_b16 v[122:123], v27 offset:34560
v_accvgpr_read_b32 v30, a170
Mvvv 180,108,44
v_accvgpr_read_b32 v31, a171
v_mul_f32 v30, 0x3e0293ee, v30
Mvvv 212,104,48
s_add_u32 m0, s78, 44032
s_add_u32 s70, s81, 35456
ds_read_b64_tr_b16 v[124:125], v27 offset:42496
Mvvv 212,108,52
buffer_load_dwordx4 v12, s[48:51], s70 offen sc1 lds
Mvvv 244,104,56
ds_read_b64_tr_b16 v[126:127], v27 offset:42752
v_mul_f32 v31, 0x3e0293ee, v31
Mvvv 244,108,60
s_waitcnt lgkmcnt(6)
v_cvt_pk_bf16_f32 v21, v30, v31
v_accvgpr_read_b32 v30, a172
Mvvv 152,112,32
s_waitcnt lgkmcnt(4)
v_accvgpr_read_b32 v31, a173
v_mul_f32 v30, 0x3e0293ee, v30
Mvvv 152,116,36
v_mul_f32 v31, 0x3e0293ee, v31
v_cvt_pk_bf16_f32 v22, v30, v31
Mvvv 184,112,40
v_accvgpr_read_b32 v30, a174
v_accvgpr_read_b32 v31, a175
Mvvv 184,116,44
v_mul_f32 v30, 0x3e0293ee, v30
)"
R"(
v_mul_f32 v31, 0x3e0293ee, v31
Mvvv 216,112,48
v_cvt_pk_bf16_f32 v23, v30, v31
v_accvgpr_read_b32 v30, a144
Mvvv 216,116,52
s_waitcnt lgkmcnt(0)
v_accvgpr_read_b32 v31, a145
v_cvt_pk_bf16_f32 v16, v30, v31
Mvvv 248,112,56
s_barrier
Mvvv 248,116,60
s_add_u32 s72, s79, 39936
ds_read_b64_tr_b16 v[96:97], v26 offset:49152
buffer_store_dwordx4 v[20:23], v0, s[48:51], s72 offen sc1
Mvvv 156,120,32
ds_read_b64_tr_b16 v[98:99], v26 offset:49408
v_accvgpr_read_b32 v30, a146
Mvvv 156,124,36
ds_read_b64_tr_b16 v[100:101], v26 offset:57344
v_accvgpr_read_b32 v31, a147
Mvvv 188,120,40
s_add_u32 m0, s77, 131072
s_add_u32 s70, s68, 3145728
ds_read_b64_tr_b16 v[102:103], v26 offset:57600
Mvvv 188,124,44
buffer_load_dwordx4 v3, s[16:19], s70 offen lds
Mvvv 220,120,48
ds_read_b64_tr_b16 v[104:105], v27 offset:49152
v_cvt_pk_bf16_f32 v17, v30, v31
Mvvv 220,124,52
ds_read_b64_tr_b16 v[106:107], v27 offset:49408
v_accvgpr_read_b32 v30, a176
Mvvv 252,120,56
ds_read_b64_tr_b16 v[108:109], v27 offset:57344
v_accvgpr_read_b32 v31, a177
Mvvv 252,124,60
s_waitcnt lgkmcnt(5)
ds_read_b64_tr_b16 v[110:111], v27 offset:57600
v_cvt_pk_bf16_f32 v18, v30, v31
Mvvv 128,96,64
s_waitcnt lgkmcnt(4)
s_add_u32 m0, s77, 131968
s_add_u32 s70, s68, 3145728
ds_read_b64_tr_b16 v[112:113], v26 offset:49664
Mvvv 128,100,68
buffer_load_dwordx4 v3, s[16:19], s70 offen offset:128 lds
Mvvv 160,96,72
s_add_u32 m0, s77, 139264
s_add_u32 s70, s68, 3407872
ds_read_b64_tr_b16 v[114:115], v26 offset:49920
Mvvv 160,100,76
buffer_load_dwordx4 v3, s[16:19], s70 offen lds
Mvvv 192,96,80
s_add_u32 m0, s77, 140160
s_add_u32 s70, s68, 3407872
ds_read_b64_tr_b16 v[116:117], v26 offset:57856
Mvvv 192,100,84
buffer_load_dwordx4 v3, s[16:19], s70 offen offset:128 lds
Mvvv 224,96,88
s_waitcnt vmcnt(20)
ds_read_b64_tr_b16 v[118:119], v26 offset:58112
v_accvgpr_read_b32 v30, a178
Mvvv 224,100,92
s_waitcnt lgkmcnt(6)
ds_read_b128 v[32:35], v5 offset:0
v_accvgpr_read_b32 v31, a179
Mvvv 132,104,64
s_waitcnt lgkmcnt(5)
ds_read_b64_tr_b16 v[120:121], v27 offset:49664
v_cvt_pk_bf16_f32 v19, v30, v31
Mvvv 132,108,68
ds_read_b128 v[36:39], v6 offset:0
v_accvgpr_read_b32 v30, a148
Mvvv 164,104,72
ds_read_b64_tr_b16 v[122:123], v27 offset:49920
v_accvgpr_read_b32 v31, a149
Mvvv 164,108,76
s_add_u32 s72, s80, 34816
ds_read_b128 v[40:43], v5 offset:2048
buffer_store_dwordx4 v[16:19], v0, s[48:51], s72 offen sc1
Mvvv 196,104,80
ds_read_b64_tr_b16 v[124:125], v27 offset:57856
v_cvt_pk_bf16_f32 v20, v30, v31
Mvvv 196,108,84
ds_read_b128 v[44:47], v6 offset:2048
v_accvgpr_read_b32 v30, a150
Mvvv 228,104,88
ds_read_b64_tr_b16 v[126:127], v27 offset:58112
v_accvgpr_read_b32 v31, a151
Mvvv 228,108,92
s_waitcnt lgkmcnt(10)
ds_read_b128 v[48:51], v5 offset:8192
v_cvt_pk_bf16_f32 v21, v30, v31
Mvvv 136,112,64
s_waitcnt lgkmcnt(9)
ds_read_b64_tr_b16 v[96:97], v26 offset:50176
v_accvgpr_read_b32 v30, a180
Mvvv 136,116,68
ds_read_b128 v[52:55], v6 offset:8192
v_accvgpr_read_b32 v31, a181
Mvvv 168,112,72
ds_read_b64_tr_b16 v[98:99], v26 offset:50432
v_cvt_pk_bf16_f32 v22, v30, v31
Mvvv 168,116,76
ds_read_b128 v[56:59], v5 offset:10240
v_accvgpr_read_b32 v30, a182
Mvvv 200,112,80
ds_read_b64_tr_b16 v[100:101], v26 offset:58368
v_accvgpr_read_b32 v31, a183
Mvvv 200,116,84
s_waitcnt lgkmcnt(11)
ds_read_b128 v[60:63], v6 offset:10240
v_cvt_pk_bf16_f32 v23, v30, v31
Mvvv 232,112,88
s_add_u32 m0, s78, 65536
s_add_u32 s70, s82, 768
ds_read_b64_tr_b16 v[102:103], v26 offset:58624
Mvvv 232,116,92
s_waitcnt lgkmcnt(8)
buffer_load_dwordx4 v11, s[48:51], s70 offen sc1 lds
Mvvv 140,120,64
s_add_u32 m0, s78, 66560
s_add_u32 s70, s82, 768
ds_read_b64_tr_b16 v[104:105], v27 offset:50176
Mvvv 140,124,68
buffer_load_dwordx4 v12, s[48:51], s70 offen sc1 lds
Mvvv 172,120,72
s_add_u32 m0, s78, 67584
s_add_u32 s70, s82, 35584
ds_read_b64_tr_b16 v[106:107], v27 offset:50432
Mvvv 172,124,76
buffer_load_dwordx4 v11, s[48:51], s70 offen sc1 lds
Mvvv 204,120,80
s_add_u32 m0, s78, 68608
s_add_u32 s70, s82, 35584
ds_read_b64_tr_b16 v[108:109], v27 offset:58368
Mvvv 204,124,84
buffer_load_dwordx4 v12, s[48:51], s70 offen sc1 lds
Mvvv 236,120,88
s_waitcnt lgkmcnt(3)
ds_read_b64_tr_b16 v[110:111], v27 offset:58624
v_accvgpr_read_b32 v30, a152
Mvvv 236,124,92
s_add_u32 s72, s80, 36864
buffer_store_dwordx4 v[20:23], v0, s[48:51], s72 offen sc1
Mvvv 144,96,64
s_add_u32 m0, s78, 73728
s_add_u32 s70, s81, 768
ds_read_b64_tr_b16 v[112:113], v26 offset:50688
Mvvv 144,100,68
buffer_load_dwordx4 v11, s[48:51], s70 offen sc1 lds
Mvvv 176,96,72
ds_read_b64_tr_b16 v[114:115], v26 offset:50944
v_accvgpr_read_b32 v31, a153
Mvvv 176,100,76
v_cvt_pk_bf16_f32 v16, v30, v31
v_accvgpr_read_b32 v30, a154
Mvvv 208,96,80
s_add_u32 m0, s78, 74752
s_add_u32 s70, s81, 768
ds_read_b64_tr_b16 v[116:117], v26 offset:58880
Mvvv 208,100,84
buffer_load_dwordx4 v12, s[48:51], s70 offen sc1 lds
Mvvv 240,96,88
ds_read_b64_tr_b16 v[118:119], v26 offset:59136
v_accvgpr_read_b32 v31, a155
Mvvv 240,100,92
s_waitcnt lgkmcnt(4)
v_cvt_pk_bf16_f32 v17, v30, v31
v_accvgpr_read_b32 v30, a184
Mvvv 148,104,64
s_add_u32 m0, s78, 75776
s_add_u32 s70, s81, 35584
ds_read_b64_tr_b16 v[120:121], v27 offset:50688
Mvvv 148,108,68
buffer_load_dwordx4 v11, s[48:51], s70 offen sc1 lds
Mvvv 180,104,72
ds_read_b64_tr_b16 v[122:123], v27 offset:50944
v_accvgpr_read_b32 v31, a185
Mvvv 180,108,76
v_cvt_pk_bf16_f32 v18, v30, v31
v_accvgpr_read_b32 v30, a186
Mvvv 212,104,80
s_add_u32 m0, s78, 76800
s_add_u32 s70, s81, 35584
ds_read_b64_tr_b16 v[124:125], v27 offset:58880
Mvvv 212,108,84
buffer_load_dwordx4 v12, s[48:51], s70 offen sc1 lds
Mvvv 244,104,88
ds_read_b64_tr_b16 v[126:127], v27 offset:59136
v_accvgpr_read_b32 v31, a187
Mvvv 244,108,92
s_waitcnt lgkmcnt(6)
v_cvt_pk_bf16_f32 v19, v30, v31
v_accvgpr_read_b32 v30, a156
Mvvv 152,112,64
s_waitcnt lgkmcnt(4)
v_accvgpr_read_b32 v31, a157
v_cvt_pk_bf16_f32 v20, v30, v31
Mvvv 152,116,68
v_accvgpr_read_b32 v30, a158
v_accvgpr_read_b32 v31, a159
Mvvv 184,112,72
v_cvt_pk_bf16_f32 v21, v30, v31
v_accvgpr_read_b32 v30, a188
Mvvv 184,116,76
s_add_u32 s72, s80, 38912
buffer_store_dwordx4 v[16:19], v0, s[48:51], s72 offen sc1
Mvvv 216,112,80
v_accvgpr_read_b32 v31, a189
v_cvt_pk_bf16_f32 v22, v30, v31
Mvvv 216,116,84
s_waitcnt lgkmcnt(0)
v_accvgpr_read_b32 v30, a190
v_accvgpr_read_b32 v31, a191
Mvvv 248,112,88
s_barrier
Mvvv 248,116,92
ds_read_b64_tr_b16 v[96:97], v26 offset:0
v_cvt_pk_bf16_f32 v23, v30, v31
Mvvv 156,120,64
ds_read_b64_tr_b16 v[98:99], v26 offset:256
v_accvgpr_read_b32 v30, a192
Mvvv 156,124,68
ds_read_b64_tr_b16 v[100:101], v26 offset:8192
v_accvgpr_read_b32 v31, a193
Mvvv 188,120,72
s_add_u32 m0, s77, 147456
s_add_u32 s70, s68, 3670016
ds_read_b64_tr_b16 v[102:103], v26 offset:8448
Mvvv 188,124,76
buffer_load_dwordx4 v3, s[16:19], s70 offen lds
Mvvv 220,120,80
ds_read_b64_tr_b16 v[104:105], v27 offset:0
v_mul_f32 v30, 0x3e0293ee, v30
Mvvv 220,124,84
ds_read_b64_tr_b16 v[106:107], v27 offset:256
v_mul_f32 v31, 0x3e0293ee, v31
Mvvv 252,120,88
ds_read_b64_tr_b16 v[108:109], v27 offset:8192
v_cvt_pk_bf16_f32 v16, v30, v31
Mvvv 252,124,92
s_waitcnt lgkmcnt(5)
ds_read_b64_tr_b16 v[110:111], v27 offset:8448
v_accvgpr_read_b32 v30, a194
Mvvv 128,96,32
s_waitcnt lgkmcnt(4)
s_add_u32 m0, s77, 148352
s_add_u32 s70, s68, 3670016
ds_read_b64_tr_b16 v[112:113], v26 offset:512
Mvvv 128,100,36
buffer_load_dwordx4 v3, s[16:19], s70 offen offset:128 lds
Mvvv 160,96,40
s_add_u32 m0, s77, 155648
s_add_u32 s70, s68, 3932160
ds_read_b64_tr_b16 v[114:115], v26 offset:768
Mvvv 160,100,44
buffer_load_dwordx4 v3, s[16:19], s70 offen lds
Mvvv 192,96,48
s_add_u32 m0, s77, 156544
s_add_u32 s70, s68, 3932160
ds_read_b64_tr_b16 v[116:117], v26 offset:8704
Mvvv 192,100,52
buffer_load_dwordx4 v3, s[16:19], s70 offen offset:128 lds
Mvvv 224,96,56
s_waitcnt vmcnt(20) lgkmcnt(5)
ds_read_b64_tr_b16 v[118:119], v26 offset:8960
v_accvgpr_read_b32 v31, a195
Mvvv 224,100,60
s_add_u32 s72, s80, 40960
ds_read_b128 v[64:67], v5 offset:32768
buffer_store_dwordx4 v[20:23], v0, s[48:51], s72 offen sc1
Mvvv 132,104,32
s_waitcnt lgkmcnt(5)
ds_read_b64_tr_b16 v[120:121], v27 offset:512
v_mul_f32 v30, 0x3e0293ee, v30
Mvvv 132,108,36
ds_read_b128 v[68:71], v6 offset:32768
v_mul_f32 v31, 0x3e0293ee, v31
Mvvv 164,104,40
ds_read_b64_tr_b16 v[122:123], v27 offset:768
v_cvt_pk_bf16_f32 v17, v30, v31
Mvvv 164,108,44
ds_read_b128 v[72:75], v5 offset:34816
v_accvgpr_read_b32 v30, a196
Mvvv 196,104,48
ds_read_b64_tr_b16 v[124:125], v27 offset:8704
v_accvgpr_read_b32 v31, a197
Mvvv 196,108,52
ds_read_b128 v[76:79], v6 offset:34816
v_mul_f32 v30, 0x3e0293ee, v30
Mvvv 228,104,56
ds_read_b64_tr_b16 v[126:127], v27 offset:8960
v_mul_f32 v31, 0x3e0293ee, v31
Mvvv 228,108,60
s_waitcnt lgkmcnt(10)
ds_read_b128 v[80:83], v5 offset:40960
v_cvt_pk_bf16_f32 v18, v30, v31
Mvvv 136,112,32
s_waitcnt lgkmcnt(9)
ds_read_b64_tr_b16 v[96:97], v26 offset:1024
v_accvgpr_read_b32 v30, a198
Mvvv 136,116,36
ds_read_b128 v[84:87], v6 offset:40960
v_accvgpr_read_b32 v31, a199
Mvvv 168,112,40
ds_read_b64_tr_b16 v[98:99], v26 offset:1280
v_mul_f32 v30, 0x3e0293ee, v30
Mvvv 168,116,44
ds_read_b128 v[88:91], v5 offset:43008
v_mul_f32 v31, 0x3e0293ee, v31
Mvvv 200,112,48
ds_read_b64_tr_b16 v[100:101], v26 offset:9216
v_cvt_pk_bf16_f32 v19, v30, v31
Mvvv 200,116,52
s_waitcnt lgkmcnt(11)
ds_read_b128 v[92:95], v6 offset:43008
v_accvgpr_read_b32 v30, a200
Mvvv 232,112,56
s_add_u32 m0, s78, 0
s_add_u32 s70, s82, 896
ds_read_b64_tr_b16 v[102:103], v26 offset:9472
Mvvv 232,116,60
s_waitcnt lgkmcnt(8)
buffer_load_dwordx4 v11, s[48:51], s70 offen sc1 lds
Mvvv 140,120,32
s_add_u32 m0, s78, 1024
s_add_u32 s70, s82, 896
ds_read_b64_tr_b16 v[104:105], v27 offset:1024
Mvvv 140,124,36
buffer_load_dwordx4 v12, s[48:51], s70 offen sc1 lds
Mvvv 172,120,40
s_add_u32 m0, s78, 2048
s_add_u32 s70, s82, 35712
ds_read_b64_tr_b16 v[106:107], v27 offset:1280
Mvvv 172,124,44
buffer_load_dwordx4 v11, s[48:51], s70 offen sc1 lds
Mvvv 204,120,48
s_add_u32 m0, s78, 3072
s_add_u32 s70, s82, 35712
ds_read_b64_tr_b16 v[108:109], v27 offset:9216
Mvvv 204,124,52
buffer_load_dwordx4 v12, s[48:51], s70 offen sc1 lds
Mvvv 236,120,56
s_waitcnt lgkmcnt(3)
ds_read_b64_tr_b16 v[110:111], v27 offset:9472
v_accvgpr_read_b32 v31, a201
Mvvv 236,124,60
s_add_u32 s72, s79, 43008
buffer_store_dwordx4 v[16:19], v0, s[48:51], s72 offen sc1
Mvvv 144,96,32
s_add_u32 m0, s78, 8192
s_add_u32 s70, s81, 896
ds_read_b64_tr_b16 v[112:113], v26 offset:1536
Mvvv 144,100,36
buffer_load_dwordx4 v11, s[48:51], s70 offen sc1 lds
Mvvv 176,96,40
ds_read_b64_tr_b16 v[114:115], v26 offset:1792
v_mul_f32 v30, 0x3e0293ee, v30
Mvvv 176,100,44
v_mul_f32 v31, 0x3e0293ee, v31
v_cvt_pk_bf16_f32 v20, v30, v31
Mvvv 208,96,48
s_add_u32 m0, s78, 9216
s_add_u32 s70, s81, 896
ds_read_b64_tr_b16 v[116:117], v26 offset:9728
Mvvv 208,100,52
buffer_load_dwordx4 v12, s[48:51], s70 offen sc1 lds
Mvvv 240,96,56
ds_read_b64_tr_b16 v[118:119], v26 offset:9984
v_accvgpr_read_b32 v30, a202
Mvvv 240,100,60
s_waitcnt lgkmcnt(4)
v_accvgpr_read_b32 v31, a203
v_mul_f32 v30, 0x3e0293ee, v30
Mvvv 148,104,32
s_add_u32 m0, s78, 10240
s_add_u32 s70, s81, 35712
ds_read_b64_tr_b16 v[120:121], v27 offset:1536
Mvvv 148,108,36
buffer_load_dwordx4 v11, s[48:51], s70 offen sc1 lds
Mvvv 180,104,40
ds_read_b64_tr_b16 v[122:123], v27 offset:1792
v_mul_f32 v31, 0x3e0293ee, v31
Mvvv 180,108,44
v_cvt_pk_bf16_f32 v21, v30, v31
v_accvgpr_read_b32 v30, a204
Mvvv 212,104,48
s_add_u32 m0, s78, 11264
s_add_u32 s70, s81, 35712
ds_read_b64_tr_b16 v[124:125], v27 offset:9728
Mvvv 212,108,52
buffer_load_dwordx4 v12, s[48:51], s70 offen sc1 lds
Mvvv 244,104,56
ds_read_b64_tr_b16 v[126:127], v27 offset:9984
v_accvgpr_read_b32 v31, a205
Mvvv 244,108,60
s_waitcnt lgkmcnt(6)
v_mul_f32 v30, 0x3e0293ee, v30
v_mul_f32 v31, 0x3e0293ee, v31
Mvvv 152,112,32
s_waitcnt lgkmcnt(4)
v_cvt_pk_bf16_f32 v22, v30, v31
v_accvgpr_read_b32 v30, a206
Mvvv 152,116,36
v_accvgpr_read_b32 v31, a207
v_mul_f32 v30, 0x3e0293ee, v30
Mvvv 184,112,40
v_mul_f32 v31, 0x3e0293ee, v31
v_cvt_pk_bf16_f32 v23, v30, v31
Mvvv 184,116,44
s_add_u32 s72, s79, 44032
)"
R"(
buffer_store_dwordx4 v[20:23], v0, s[48:51], s72 offen sc1
Mvvv 216,112,48
v_accvgpr_read_b32 v30, a224
v_accvgpr_read_b32 v31, a225
Mvvv 216,116,52
s_waitcnt lgkmcnt(0)
v_mul_f32 v30, 0x3e0293ee, v30
v_mul_f32 v31, 0x3e0293ee, v31
Mvvv 248,112,56
s_barrier
Mvvv 248,116,60
ds_read_b64_tr_b16 v[96:97], v26 offset:16384
v_cvt_pk_bf16_f32 v16, v30, v31
Mvvv 156,120,32
ds_read_b64_tr_b16 v[98:99], v26 offset:16640
v_accvgpr_read_b32 v30, a226
Mvvv 156,124,36
ds_read_b64_tr_b16 v[100:101], v26 offset:24576
v_accvgpr_read_b32 v31, a227
Mvvv 188,120,40
s_add_u32 m0, s77, 98304
s_add_u32 s70, s68, 4194304
ds_read_b64_tr_b16 v[102:103], v26 offset:24832
Mvvv 188,124,44
buffer_load_dwordx4 v3, s[16:19], s70 offen lds
Mvvv 220,120,48
ds_read_b64_tr_b16 v[104:105], v27 offset:16384
v_mul_f32 v30, 0x3e0293ee, v30
Mvvv 220,124,52
ds_read_b64_tr_b16 v[106:107], v27 offset:16640
v_mul_f32 v31, 0x3e0293ee, v31
Mvvv 252,120,56
ds_read_b64_tr_b16 v[108:109], v27 offset:24576
v_cvt_pk_bf16_f32 v17, v30, v31
Mvvv 252,124,60
s_waitcnt lgkmcnt(5)
ds_read_b64_tr_b16 v[110:111], v27 offset:24832
v_accvgpr_read_b32 v30, a228
Mvvv 128,96,64
s_waitcnt lgkmcnt(4)
s_add_u32 m0, s77, 99200
s_add_u32 s70, s68, 4194304
ds_read_b64_tr_b16 v[112:113], v26 offset:16896
Mvvv 128,100,68
buffer_load_dwordx4 v3, s[16:19], s70 offen offset:128 lds
Mvvv 160,96,72
s_add_u32 m0, s77, 106496
s_add_u32 s70, s68, 4456448
ds_read_b64_tr_b16 v[114:115], v26 offset:17152
Mvvv 160,100,76
buffer_load_dwordx4 v3, s[16:19], s70 offen lds
Mvvv 192,96,80
s_add_u32 m0, s77, 107392
s_add_u32 s70, s68, 4456448
ds_read_b64_tr_b16 v[116:117], v26 offset:25088
Mvvv 192,100,84
buffer_load_dwordx4 v3, s[16:19], s70 offen offset:128 lds
Mvvv 224,96,88
s_waitcnt vmcnt(20)
ds_read_b64_tr_b16 v[118:119], v26 offset:25344
v_accvgpr_read_b32 v31, a229
Mvvv 224,100,92
s_waitcnt lgkmcnt(6)
ds_read_b128 v[32:35], v7 offset:0
v_mul_f32 v30, 0x3e0293ee, v30
Mvvv 132,104,64
s_waitcnt lgkmcnt(5)
ds_read_b64_tr_b16 v[120:121], v27 offset:16896
v_mul_f32 v31, 0x3e0293ee, v31
Mvvv 132,108,68
ds_read_b128 v[36:39], v8 offset:0
v_cvt_pk_bf16_f32 v18, v30, v31
Mvvv 164,104,72
ds_read_b64_tr_b16 v[122:123], v27 offset:17152
v_accvgpr_read_b32 v30, a230
Mvvv 164,108,76
ds_read_b128 v[40:43], v7 offset:2048
v_accvgpr_read_b32 v31, a231
Mvvv 196,104,80
ds_read_b64_tr_b16 v[124:125], v27 offset:25088
v_mul_f32 v30, 0x3e0293ee, v30
Mvvv 196,108,84
ds_read_b128 v[44:47], v8 offset:2048
v_mul_f32 v31, 0x3e0293ee, v31
Mvvv 228,104,88
s_waitcnt lgkmcnt(9)
ds_read_b64_tr_b16 v[126:127], v27 offset:25344
v_cvt_pk_bf16_f32 v19, v30, v31
Mvvv 228,108,92
s_add_u32 s72, s79, 47104
ds_read_b128 v[48:51], v7 offset:8192
buffer_store_dwordx4 v[16:19], v0, s[48:51], s72 offen sc1
Mvvv 136,112,64
s_waitcnt lgkmcnt(9)
ds_read_b64_tr_b16 v[96:97], v26 offset:17408
v_accvgpr_read_b32 v30, a232
Mvvv 136,116,68
ds_read_b128 v[52:55], v8 offset:8192
v_accvgpr_read_b32 v31, a233
Mvvv 168,112,72
ds_read_b64_tr_b16 v[98:99], v26 offset:17664
v_mul_f32 v30, 0x3e0293ee, v30
Mvvv 168,116,76
ds_read_b128 v[56:59], v7 offset:10240
v_mul_f32 v31, 0x3e0293ee, v31
Mvvv 200,112,80
ds_read_b64_tr_b16 v[100:101], v26 offset:25600
v_cvt_pk_bf16_f32 v20, v30, v31
Mvvv 200,116,84
s_waitcnt lgkmcnt(11)
ds_read_b128 v[60:63], v8 offset:10240
v_accvgpr_read_b32 v30, a234
Mvvv 232,112,88
s_add_u32 m0, s78, 32768
s_add_u32 s70, s82, 1024
ds_read_b64_tr_b16 v[102:103], v26 offset:25856
Mvvv 232,116,92
s_waitcnt lgkmcnt(8)
buffer_load_dwordx4 v11, s[48:51], s70 offen sc1 lds
Mvvv 140,120,64
s_add_u32 m0, s78, 33792
s_add_u32 s70, s82, 1024
ds_read_b64_tr_b16 v[104:105], v27 offset:17408
Mvvv 140,124,68
buffer_load_dwordx4 v12, s[48:51], s70 offen sc1 lds
Mvvv 172,120,72
s_add_u32 m0, s78, 34816
s_add_u32 s70, s82, 35840
ds_read_b64_tr_b16 v[106:107], v27 offset:17664
Mvvv 172,124,76
buffer_load_dwordx4 v11, s[48:51], s70 offen sc1 lds
Mvvv 204,120,80
s_add_u32 m0, s78, 35840
s_add_u32 s70, s82, 35840
ds_read_b64_tr_b16 v[108:109], v27 offset:25600
Mvvv 204,124,84
buffer_load_dwordx4 v12, s[48:51], s70 offen sc1 lds
Mvvv 236,120,88
ds_read_b64_tr_b16 v[110:111], v27 offset:25856
v_accvgpr_read_b32 v31, a235
Mvvv 236,124,92
s_waitcnt lgkmcnt(4)
v_mul_f32 v30, 0x3e0293ee, v30
v_mul_f32 v31, 0x3e0293ee, v31
Mvvv 144,96,64
s_add_u32 m0, s78, 40960
s_add_u32 s70, s81, 1024
ds_read_b64_tr_b16 v[112:113], v26 offset:17920
Mvvv 144,100,68
buffer_load_dwordx4 v11, s[48:51], s70 offen sc1 lds
Mvvv 176,96,72
ds_read_b64_tr_b16 v[114:115], v26 offset:18176
v_cvt_pk_bf16_f32 v21, v30, v31
Mvvv 176,100,76
v_accvgpr_read_b32 v30, a236
v_accvgpr_read_b32 v31, a237
Mvvv 208,96,80
s_add_u32 m0, s78, 41984
s_add_u32 s70, s81, 1024
ds_read_b64_tr_b16 v[116:117], v26 offset:26112
Mvvv 208,100,84
buffer_load_dwordx4 v12, s[48:51], s70 offen sc1 lds
Mvvv 240,96,88
ds_read_b64_tr_b16 v[118:119], v26 offset:26368
v_mul_f32 v30, 0x3e0293ee, v30
Mvvv 240,100,92
s_waitcnt lgkmcnt(4)
v_mul_f32 v31, 0x3e0293ee, v31
v_cvt_pk_bf16_f32 v22, v30, v31
Mvvv 148,104,64
s_add_u32 m0, s78, 43008
s_add_u32 s70, s81, 35840
ds_read_b64_tr_b16 v[120:121], v27 offset:17920
Mvvv 148,108,68
buffer_load_dwordx4 v11, s[48:51], s70 offen sc1 lds
Mvvv 180,104,72
ds_read_b64_tr_b16 v[122:123], v27 offset:18176
v_accvgpr_read_b32 v30, a238
Mvvv 180,108,76
v_accvgpr_read_b32 v31, a239
v_mul_f32 v30, 0x3e0293ee, v30
Mvvv 212,104,80
s_add_u32 m0, s78, 44032
s_add_u32 s70, s81, 35840
ds_read_b64_tr_b16 v[124:125], v27 offset:26112
Mvvv 212,108,84
buffer_load_dwordx4 v12, s[48:51], s70 offen sc1 lds
Mvvv 244,104,88
ds_read_b64_tr_b16 v[126:127], v27 offset:26368
v_mul_f32 v31, 0x3e0293ee, v31
Mvvv 244,108,92
s_waitcnt lgkmcnt(4)
v_cvt_pk_bf16_f32 v23, v30, v31
v_accvgpr_read_b32 v30, a208
Mvvv 152,112,64
s_add_u32 s72, s79, 48128
buffer_store_dwordx4 v[20:23], v0, s[48:51], s72 offen sc1
Mvvv 152,116,68
v_accvgpr_read_b32 v31, a209
v_cvt_pk_bf16_f32 v16, v30, v31
Mvvv 184,112,72
v_accvgpr_read_b32 v30, a210
v_accvgpr_read_b32 v31, a211
Mvvv 184,116,76
v_cvt_pk_bf16_f32 v17, v30, v31
v_accvgpr_read_b32 v30, a240
Mvvv 216,112,80
v_accvgpr_read_b32 v31, a241
v_cvt_pk_bf16_f32 v18, v30, v31
Mvvv 216,116,84
s_waitcnt lgkmcnt(0)
v_accvgpr_read_b32 v30, a242
v_accvgpr_read_b32 v31, a243
Mvvv 248,112,88
s_barrier
Mvvv 248,116,92
ds_read_b64_tr_b16 v[96:97], v26 offset:32768
v_cvt_pk_bf16_f32 v19, v30, v31
Mvvv 156,120,64
ds_read_b64_tr_b16 v[98:99], v26 offset:33024
v_accvgpr_read_b32 v30, a212
Mvvv 156,124,68
ds_read_b64_tr_b16 v[100:101], v26 offset:40960
v_accvgpr_read_b32 v31, a213
Mvvv 188,120,72
s_add_u32 m0, s77, 114688
s_add_u32 s70, s68, 4718592
ds_read_b64_tr_b16 v[102:103], v26 offset:41216
Mvvv 188,124,76
buffer_load_dwordx4 v3, s[16:19], s70 offen lds
Mvvv 220,120,80
ds_read_b64_tr_b16 v[104:105], v27 offset:32768
v_cvt_pk_bf16_f32 v20, v30, v31
Mvvv 220,124,84
ds_read_b64_tr_b16 v[106:107], v27 offset:33024
v_accvgpr_read_b32 v30, a214
Mvvv 252,120,88
ds_read_b64_tr_b16 v[108:109], v27 offset:40960
v_accvgpr_read_b32 v31, a215
Mvvv 252,124,92
s_waitcnt lgkmcnt(5)
ds_read_b64_tr_b16 v[110:111], v27 offset:41216
v_cvt_pk_bf16_f32 v21, v30, v31
Mvvv 128,96,32
s_waitcnt lgkmcnt(4)
s_add_u32 m0, s77, 115584
s_add_u32 s70, s68, 4718592
ds_read_b64_tr_b16 v[112:113], v26 offset:33280
Mvvv 128,100,36
buffer_load_dwordx4 v3, s[16:19], s70 offen offset:128 lds
Mvvv 160,96,40
s_add_u32 m0, s77, 122880
s_add_u32 s70, s68, 4980736
ds_read_b64_tr_b16 v[114:115], v26 offset:33536
Mvvv 160,100,44
buffer_load_dwordx4 v3, s[16:19], s70 offen lds
Mvvv 192,96,48
s_add_u32 m0, s77, 123776
s_add_u32 s70, s68, 4980736
ds_read_b64_tr_b16 v[116:117], v26 offset:41472
Mvvv 192,100,52
buffer_load_dwordx4 v3, s[16:19], s70 offen offset:128 lds
Mvvv 224,96,56
s_waitcnt vmcnt(19) lgkmcnt(5)
ds_read_b64_tr_b16 v[118:119], v26 offset:41728
v_accvgpr_read_b32 v30, a244
Mvvv 224,100,60
s_add_u32 s72, s80, 35840
ds_read_b128 v[64:67], v5 offset:0
buffer_store_dwordx4 v[16:19], v0, s[48:51], s72 offen sc1
Mvvv 132,104,32
s_waitcnt lgkmcnt(5)
ds_read_b64_tr_b16 v[120:121], v27 offset:33280
v_accvgpr_read_b32 v31, a245
Mvvv 132,108,36
ds_read_b128 v[68:71], v6 offset:0
v_cvt_pk_bf16_f32 v22, v30, v31
Mvvv 164,104,40
ds_read_b64_tr_b16 v[122:123], v27 offset:33536
v_accvgpr_read_b32 v30, a246
Mvvv 164,108,44
ds_read_b128 v[72:75], v5 offset:2048
v_accvgpr_read_b32 v31, a247
Mvvv 196,104,48
ds_read_b64_tr_b16 v[124:125], v27 offset:41472
v_cvt_pk_bf16_f32 v23, v30, v31
Mvvv 196,108,52
ds_read_b128 v[76:79], v6 offset:2048
v_accvgpr_read_b32 v30, a216
Mvvv 228,104,56
ds_read_b64_tr_b16 v[126:127], v27 offset:41728
v_accvgpr_read_b32 v31, a217
Mvvv 228,108,60
s_waitcnt lgkmcnt(10)
ds_read_b128 v[80:83], v5 offset:8192
v_cvt_pk_bf16_f32 v16, v30, v31
Mvvv 136,112,32
s_waitcnt lgkmcnt(9)
ds_read_b64_tr_b16 v[96:97], v26 offset:33792
v_accvgpr_read_b32 v30, a218
Mvvv 136,116,36
ds_read_b128 v[84:87], v6 offset:8192
v_accvgpr_read_b32 v31, a219
Mvvv 168,112,40
ds_read_b64_tr_b16 v[98:99], v26 offset:34048
v_cvt_pk_bf16_f32 v17, v30, v31
Mvvv 168,116,44
ds_read_b128 v[88:91], v5 offset:10240
v_accvgpr_read_b32 v30, a248
Mvvv 200,112,48
ds_read_b64_tr_b16 v[100:101], v26 offset:41984
v_accvgpr_read_b32 v31, a249
Mvvv 200,116,52
s_waitcnt lgkmcnt(11)
ds_read_b128 v[92:95], v6 offset:10240
v_cvt_pk_bf16_f32 v18, v30, v31
Mvvv 232,112,56
s_add_u32 m0, s78, 65536
s_add_u32 s70, s82, 1152
ds_read_b64_tr_b16 v[102:103], v26 offset:42240
Mvvv 232,116,60
s_waitcnt lgkmcnt(8)
buffer_load_dwordx4 v11, s[48:51], s70 offen sc1 lds
Mvvv 140,120,32
s_add_u32 m0, s78, 66560
s_add_u32 s70, s82, 1152
ds_read_b64_tr_b16 v[104:105], v27 offset:33792
Mvvv 140,124,36
buffer_load_dwordx4 v12, s[48:51], s70 offen sc1 lds
Mvvv 172,120,40
s_add_u32 m0, s78, 67584
s_add_u32 s70, s82, 35968
ds_read_b64_tr_b16 v[106:107], v27 offset:34048
Mvvv 172,124,44
buffer_load_dwordx4 v11, s[48:51], s70 offen sc1 lds
Mvvv 204,120,48
s_add_u32 m0, s78, 68608
s_add_u32 s70, s82, 35968
ds_read_b64_tr_b16 v[108:109], v27 offset:41984
Mvvv 204,124,52
buffer_load_dwordx4 v12, s[48:51], s70 offen sc1 lds
Mvvv 236,120,56
s_waitcnt lgkmcnt(3)
ds_read_b64_tr_b16 v[110:111], v27 offset:42240
v_accvgpr_read_b32 v30, a250
Mvvv 236,124,60
s_add_u32 s72, s80, 37888
buffer_store_dwordx4 v[20:23], v0, s[48:51], s72 offen sc1
Mvvv 144,96,32
s_add_u32 m0, s78, 73728
s_add_u32 s70, s81, 1152
ds_read_b64_tr_b16 v[112:113], v26 offset:34304
Mvvv 144,100,36
buffer_load_dwordx4 v11, s[48:51], s70 offen sc1 lds
Mvvv 176,96,40
ds_read_b64_tr_b16 v[114:115], v26 offset:34560
v_accvgpr_read_b32 v31, a251
Mvvv 176,100,44
v_cvt_pk_bf16_f32 v19, v30, v31
v_accvgpr_read_b32 v30, a220
Mvvv 208,96,48
s_add_u32 m0, s78, 74752
s_add_u32 s70, s81, 1152
ds_read_b64_tr_b16 v[116:117], v26 offset:42496
Mvvv 208,100,52
buffer_load_dwordx4 v12, s[48:51], s70 offen sc1 lds
Mvvv 240,96,56
ds_read_b64_tr_b16 v[118:119], v26 offset:42752
v_accvgpr_read_b32 v31, a221
Mvvv 240,100,60
s_waitcnt lgkmcnt(4)
v_cvt_pk_bf16_f32 v20, v30, v31
v_accvgpr_read_b32 v30, a222
Mvvv 148,104,32
s_add_u32 m0, s78, 75776
s_add_u32 s70, s81, 35968
ds_read_b64_tr_b16 v[120:121], v27 offset:34304
Mvvv 148,108,36
buffer_load_dwordx4 v11, s[48:51], s70 offen sc1 lds
Mvvv 180,104,40
ds_read_b64_tr_b16 v[122:123], v27 offset:34560
v_accvgpr_read_b32 v31, a223
Mvvv 180,108,44
v_cvt_pk_bf16_f32 v21, v30, v31
v_accvgpr_read_b32 v30, a252
Mvvv 212,104,48
s_add_u32 m0, s78, 76800
s_add_u32 s70, s81, 35968
ds_read_b64_tr_b16 v[124:125], v27 offset:42496
Mvvv 212,108,52
buffer_load_dwordx4 v12, s[48:51], s70 offen sc1 lds
Mvvv 244,104,56
ds_read_b64_tr_b16 v[126:127], v27 offset:42752
v_accvgpr_read_b32 v31, a253
Mvvv 244,108,60
s_waitcnt lgkmcnt(6)
v_cvt_pk_bf16_f32 v22, v30, v31
v_accvgpr_read_b32 v30, a254
Mvvv 152,112,32
s_waitcnt lgkmcnt(4)
v_accvgpr_read_b32 v31, a255
v_cvt_pk_bf16_f32 v23, v30, v31
Mvvv 152,116,36
v_cmp_eq_u32 vcc, 2, v2
v_accvgpr_write_b32 a0, 0
Mvvv 184,112,40
v_accvgpr_write_b32 a1, 0
v_accvgpr_write_b32 a2, 0
Mvvv 184,116,44
s_add_u32 s72, s80, 39936
buffer_store_dwordx4 v[16:19], v0, s[48:51], s72 offen sc1
)"
R"(
Mvvv 216,112,48
v_cndmask_b32 v4, v4, v9, vcc
v_sub_u32 v9, 32, v1
Mvvv 216,116,52
s_waitcnt lgkmcnt(0)
v_cmp_eq_u32 vcc, 1, v2
v_accvgpr_write_b32 a3, 0
Mvvv 248,112,56
s_barrier
Mvvv 248,116,60
ds_read_b64_tr_b16 v[96:97], v26 offset:49152
v_cndmask_b32 v4, v4, v9, vcc
Mvvv 156,120,32
ds_read_b64_tr_b16 v[98:99], v26 offset:49408
v_cmp_gt_i32 vcc, 1, v2
Mvvv 156,124,36
ds_read_b64_tr_b16 v[100:101], v26 offset:57344
v_accvgpr_write_b32 a4, 0
Mvvv 188,120,40
s_add_u32 m0, s77, 131072
s_add_u32 s70, s68, 5242880
ds_read_b64_tr_b16 v[102:103], v26 offset:57600
Mvvv 188,124,44
buffer_load_dwordx4 v3, s[16:19], s70 offen lds
Mvvv 220,120,48
ds_read_b64_tr_b16 v[104:105], v27 offset:49152
v_cndmask_b32 v4, v4, v1, vcc
Mvvv 220,124,52
ds_read_b64_tr_b16 v[106:107], v27 offset:49408
v_mul_u32_u24 v4, 34816, v4
Mvvv 252,120,56
s_add_u32 s58, s60, 0
ds_read_b64_tr_b16 v[108:109], v27 offset:57344
v_add_u32 v4, s58, v4
Mvvv 252,124,60
s_waitcnt lgkmcnt(5)
s_and_b32 s58, s65, 6
s_lshl_b32 s58, s58, 2
ds_read_b64_tr_b16 v[110:111], v27 offset:57600
Mvvv 128,96,64
s_waitcnt lgkmcnt(4)
s_add_u32 m0, s77, 131968
s_add_u32 s70, s68, 5242880
ds_read_b64_tr_b16 v[112:113], v26 offset:49664
Mvvv 128,100,68
s_mul_i32 s66, s58, 36864
buffer_load_dwordx4 v3, s[16:19], s70 offen offset:128 lds
Mvvv 160,96,72
s_add_u32 m0, s77, 139264
s_add_u32 s70, s68, 5505024
ds_read_b64_tr_b16 v[114:115], v26 offset:49920
Mvvv 160,100,76
s_lshl_b32 s67, s58, 18
buffer_load_dwordx4 v3, s[16:19], s70 offen lds
Mvvv 192,96,80
s_add_u32 m0, s77, 140160
s_add_u32 s70, s68, 5505024
ds_read_b64_tr_b16 v[116:117], v26 offset:57856
Mvvv 192,100,84
s_lshl_b32 s58, s63, 2
buffer_load_dwordx4 v3, s[16:19], s70 offen offset:128 lds
Mvvv 224,96,88
s_waitcnt vmcnt(20) lgkmcnt(3)
s_lshr_b32 s58, 0x76043251, s58
ds_read_b64_tr_b16 v[118:119], v26 offset:58112
Mvvv 224,100,92
s_add_u32 s72, s80, 41984
ds_read_b128 v[32:35], v5 offset:32768
buffer_store_dwordx4 v[20:23], v0, s[48:51], s72 offen sc1
Mvvv 132,104,64
s_and_b32 s58, s58, 7
s_lshl_b32 s58, s58, 8
ds_read_b64_tr_b16 v[120:121], v27 offset:49664
Mvvv 132,108,68
s_waitcnt lgkmcnt(4)
s_add_u32 s67, s67, s58
s_lshl3_add_u32 s67, s69, s67
ds_read_b128 v[36:39], v6 offset:32768
Mvvv 164,104,72
s_and_b32 s71, s64, 7
ds_read_b64_tr_b16 v[122:123], v27 offset:49920
v_mov_b32 v16, s86
Mvvv 164,108,76
s_lshl_b32 s58, s71, 12
ds_read_b128 v[40:43], v5 offset:34816
v_mov_b32 v17, s87
Mvvv 196,104,80
s_add_u32 s66, s66, s58
s_add_u32 s66, s66, 54525952
ds_read_b64_tr_b16 v[124:125], v27 offset:57856
Mvvv 196,108,84
s_waitcnt lgkmcnt(6)
s_lshr_b32 s95, s65, 1
ds_read_b128 v[44:47], v6 offset:34816
v_accvgpr_write_b32 a5, 0
Mvvv 228,104,88
s_cmp_le_u32 s95, 0
s_cselect_b32 s92, 1, 0
ds_read_b64_tr_b16 v[126:127], v27 offset:58112
Mvvv 228,108,92
s_cmp_le_u32 s95, 1
s_cselect_b32 s93, 2, 1
ds_read_b128 v[48:51], v5 offset:40960
Mvvv 136,112,64
s_cmp_le_u32 s95, 2
s_cselect_b32 s94, 3, 2
ds_read_b64_tr_b16 v[96:97], v26 offset:50176
Mvvv 136,116,68
s_cmp_le_u32 s95, s57
s_addc_u32 s58, s57, 0
ds_read_b128 v[52:55], v6 offset:40960
Mvvv 168,112,72
s_cmp_eq_u32 s57, 3
s_cselect_b32 s58, s95, s58
ds_read_b64_tr_b16 v[98:99], v26 offset:50432
Mvvv 168,116,76
s_mul_i32 s58, s58, 1179648
s_add_u32 s73, s66, s58
ds_read_b128 v[56:59], v5 offset:43008
Mvvv 200,112,80
s_lshl3_add_u32 s74, s63, s65
s_sub_u32 s74, s74, 8
ds_read_b64_tr_b16 v[100:101], v26 offset:58368
Mvvv 200,116,84
s_waitcnt lgkmcnt(11)
ds_read_b128 v[60:63], v6 offset:43008
v_add_u32 v2, s61, v1
Mvvv 232,112,88
s_add_u32 m0, s78, 0
s_add_u32 s70, s82, 1280
ds_read_b64_tr_b16 v[102:103], v26 offset:58624
Mvvv 232,116,92
s_waitcnt lgkmcnt(8)
buffer_load_dwordx4 v11, s[48:51], s70 offen sc1 lds
Mvvv 140,120,64
s_add_u32 m0, s78, 1024
s_add_u32 s70, s82, 1280
ds_read_b64_tr_b16 v[104:105], v27 offset:50176
Mvvv 140,124,68
buffer_load_dwordx4 v12, s[48:51], s70 offen sc1 lds
Mvvv 172,120,72
s_add_u32 m0, s78, 2048
s_add_u32 s70, s82, 36096
ds_read_b64_tr_b16 v[106:107], v27 offset:50432
Mvvv 172,124,76
buffer_load_dwordx4 v11, s[48:51], s70 offen sc1 lds
Mvvv 204,120,80
s_add_u32 m0, s78, 3072
s_add_u32 s70, s82, 36096
ds_read_b64_tr_b16 v[108:109], v27 offset:58368
Mvvv 204,124,84
buffer_load_dwordx4 v12, s[48:51], s70 offen sc1 lds
Mvvv 236,120,88
ds_read_b64_tr_b16 v[110:111], v27 offset:58624
v_add_u32 v9, -32, v2
Mvvv 236,124,92
s_waitcnt lgkmcnt(4)
v_mul_u32_u24 v9, 36864, v9
v_add_u32 v9, s73, v9
Mvvv 144,96,64
s_add_u32 m0, s78, 8192
s_add_u32 s70, s81, 1280
ds_read_b64_tr_b16 v[112:113], v26 offset:50688
Mvvv 144,100,68
buffer_load_dwordx4 v11, s[48:51], s70 offen sc1 lds
Mvvv 176,96,72
ds_read_b64_tr_b16 v[114:115], v26 offset:50944
v_cmp_lt_u32 vcc, 31, v2
Mvvv 176,100,76
v_accvgpr_write_b32 a6, 0
v_accvgpr_write_b32 a7, 0
Mvvv 208,96,80
s_add_u32 m0, s78, 9216
s_add_u32 s70, s81, 1280
ds_read_b64_tr_b16 v[116:117], v26 offset:58880
Mvvv 208,100,84
buffer_load_dwordx4 v12, s[48:51], s70 offen sc1 lds
Mvvv 240,96,88
ds_read_b64_tr_b16 v[118:119], v26 offset:59136
v_cndmask_b32 v4, v4, v9, vcc
Mvvv 240,100,92
s_waitcnt lgkmcnt(4)
v_lshrrev_b32 v1, 4, v0
v_add_u32 v1, 2, v1
Mvvv 148,104,64
s_add_u32 m0, s78, 10240
s_add_u32 s70, s81, 36096
ds_read_b64_tr_b16 v[120:121], v27 offset:50688
Mvvv 148,108,68
buffer_load_dwordx4 v11, s[48:51], s70 offen sc1 lds
Mvvv 180,104,72
ds_read_b64_tr_b16 v[122:123], v27 offset:50944
v_and_b32 v1, 63, v1
Mvvv 180,108,76
v_subrev_u32 v2, s61, v1
v_add_u32 v14, -2, v1
Mvvv 212,104,80
s_add_u32 m0, s78, 11264
s_add_u32 s70, s81, 36096
ds_read_b64_tr_b16 v[124:125], v27 offset:58880
Mvvv 212,108,84
buffer_load_dwordx4 v12, s[48:51], s70 offen sc1 lds
Mvvv 244,104,88
ds_read_b64_tr_b16 v[126:127], v27 offset:59136
v_sub_u32 v9, 33, v1
Mvvv 244,108,92
s_waitcnt lgkmcnt(6)
v_cmp_eq_u32 vcc, 3, v2
v_accvgpr_write_b32 a8, 0
Mvvv 152,112,64
s_waitcnt lgkmcnt(4)
v_accvgpr_write_b32 a9, 0
v_accvgpr_write_b32 a10, 0
Mvvv 152,116,68
v_cndmask_b32 v14, v14, v9, vcc
v_add_u32 v9, -1, v1
Mvvv 184,112,72
v_cmp_eq_u32 vcc, 2, v2
v_accvgpr_write_b32 a11, 0
Mvvv 184,116,76
v_accvgpr_write_b32 a12, 0
v_accvgpr_write_b32 a13, 0
Mvvv 216,112,80
v_cndmask_b32 v14, v14, v9, vcc
v_sub_u32 v9, 32, v1
Mvvv 216,116,84
s_waitcnt lgkmcnt(0)
v_cmp_eq_u32 vcc, 1, v2
v_accvgpr_write_b32 a14, 0
Mvvv 248,112,88
s_barrier
Mvvv 248,116,92
ds_read_b64_tr_b16 v[96:97], v26 offset:0
v_cndmask_b32 v14, v14, v9, vcc
Mvvv 156,120,64
ds_read_b64_tr_b16 v[98:99], v26 offset:256
v_cmp_gt_i32 vcc, 1, v2
Mvvv 156,124,68
ds_read_b64_tr_b16 v[100:101], v26 offset:8192
v_accvgpr_write_b32 a15, 0
Mvvv 188,120,72
s_add_u32 m0, s77, 147456
s_add_u32 s70, s68, 5767168
ds_read_b64_tr_b16 v[102:103], v26 offset:8448
Mvvv 188,124,76
buffer_load_dwordx4 v3, s[16:19], s70 offen lds
Mvvv 220,120,80
ds_read_b64_tr_b16 v[104:105], v27 offset:0
v_cndmask_b32 v14, v14, v1, vcc
Mvvv 220,124,84
ds_read_b64_tr_b16 v[106:107], v27 offset:256
v_mul_u32_u24 v14, 34816, v14
Mvvv 252,120,88
s_add_u32 s58, s60, 16384
ds_read_b64_tr_b16 v[108:109], v27 offset:8192
v_add_u32 v14, s58, v14
Mvvv 252,124,92
s_waitcnt lgkmcnt(5)
ds_read_b64_tr_b16 v[110:111], v27 offset:8448
v_add_u32 v2, s61, v1
Mvvv 128,96,32
s_waitcnt lgkmcnt(4)
s_add_u32 m0, s77, 148352
s_add_u32 s70, s68, 5767168
ds_read_b64_tr_b16 v[112:113], v26 offset:512
Mvvv 128,100,36
buffer_load_dwordx4 v3, s[16:19], s70 offen offset:128 lds
Mvvv 160,96,40
s_add_u32 m0, s77, 155648
s_add_u32 s70, s68, 6029312
ds_read_b64_tr_b16 v[114:115], v26 offset:768
Mvvv 160,100,44
buffer_load_dwordx4 v3, s[16:19], s70 offen lds
Mvvv 192,96,48
s_add_u32 m0, s77, 156544
s_add_u32 s70, s68, 6029312
ds_read_b64_tr_b16 v[116:117], v26 offset:8704
Mvvv 192,100,52
buffer_load_dwordx4 v3, s[16:19], s70 offen offset:128 lds
Mvvv 224,96,56
s_waitcnt vmcnt(18)
ds_read_b64_tr_b16 v[118:119], v26 offset:8960
v_add_u32 v9, -32, v2
Mvvv 224,100,60
s_waitcnt lgkmcnt(4)
ds_read_b128 v[64:67], v7 offset:0
v_mul_u32_u24 v9, 131072, v9
Mvvv 132,104,32
s_add_u32 s58, s67, 262144
ds_read_b64_tr_b16 v[120:121], v27 offset:512
v_add_u32 v9, s58, v9
Mvvv 132,108,36
ds_read_b128 v[68:71], v8 offset:0
v_cmp_lt_u32 vcc, 31, v2
Mvvv 164,104,40
ds_read_b64_tr_b16 v[122:123], v27 offset:768
v_accvgpr_write_b32 a16, 0
Mvvv 164,108,44
s_waitcnt lgkmcnt(6)
ds_read_b128 v[72:75], v7 offset:2048
v_cndmask_b32 v14, v14, v9, vcc
Mvvv 196,104,48
s_and_b32 s75, s61, 2
s_lshl_b32 s75, s75, 14
ds_read_b64_tr_b16 v[124:125], v27 offset:8704
Mvvv 196,108,52
s_mov_b64 s[12:13], s[48:49]
s_mov_b64 s[14:15], s[50:51]
ds_read_b128 v[76:79], v8 offset:2048
Mvvv 228,104,56
s_mov_b32 s24, 0
s_mov_b32 s25, 0
ds_read_b64_tr_b16 v[126:127], v27 offset:8960
Mvvv 228,108,60
s_mov_b32 s26, 0
ds_read_b128 v[80:83], v7 offset:8192
v_accvgpr_write_b32 a17, 0
Mvvv 136,112,32
s_waitcnt lgkmcnt(9)
ds_read_b64_tr_b16 v[96:97], v26 offset:1024
v_accvgpr_write_b32 a18, 0
Mvvv 136,116,36
ds_read_b128 v[84:87], v8 offset:8192
v_accvgpr_write_b32 a19, 0
Mvvv 168,112,40
ds_read_b64_tr_b16 v[98:99], v26 offset:1280
v_accvgpr_write_b32 a20, 0
Mvvv 168,116,44
ds_read_b128 v[88:91], v7 offset:10240
v_accvgpr_write_b32 a21, 0
Mvvv 200,112,48
ds_read_b64_tr_b16 v[100:101], v26 offset:9216
v_accvgpr_write_b32 a22, 0
Mvvv 200,116,52
s_waitcnt lgkmcnt(11)
ds_read_b128 v[92:95], v8 offset:10240
v_accvgpr_write_b32 a23, 0
Mvvv 232,112,56
s_add_u32 m0, s78, 32768
s_add_u32 s70, s82, 1408
ds_read_b64_tr_b16 v[102:103], v26 offset:9472
Mvvv 232,116,60
s_waitcnt lgkmcnt(8)
buffer_load_dwordx4 v11, s[48:51], s70 offen sc1 lds
Mvvv 140,120,32
s_add_u32 m0, s78, 33792
s_add_u32 s70, s82, 1408
ds_read_b64_tr_b16 v[104:105], v27 offset:1024
Mvvv 140,124,36
buffer_load_dwordx4 v12, s[48:51], s70 offen sc1 lds
Mvvv 172,120,40
s_add_u32 m0, s78, 34816
s_add_u32 s70, s82, 36224
ds_read_b64_tr_b16 v[106:107], v27 offset:1280
Mvvv 172,124,44
buffer_load_dwordx4 v11, s[48:51], s70 offen sc1 lds
Mvvv 204,120,48
s_add_u32 m0, s78, 35840
s_add_u32 s70, s82, 36224
ds_read_b64_tr_b16 v[108:109], v27 offset:9216
Mvvv 204,124,52
buffer_load_dwordx4 v12, s[48:51], s70 offen sc1 lds
Mvvv 236,120,56
s_waitcnt lgkmcnt(3)
ds_read_b64_tr_b16 v[110:111], v27 offset:9472
v_accvgpr_write_b32 a24, 0
Mvvv 236,124,60
s_bitcmp1_b32 0x60, s63
s_cselect_b64 s[20:21], s[28:29], s[44:45]
s_cselect_b64 s[22:23], s[30:31], s[46:47]
Mvvv 144,96,32
s_add_u32 m0, s78, 40960
s_add_u32 s70, s81, 1408
ds_read_b64_tr_b16 v[112:113], v26 offset:1536
Mvvv 144,100,36
s_waitcnt lgkmcnt(3)
s_lshl2_add_u32 s58, s64, s57
buffer_load_dwordx4 v11, s[48:51], s70 offen sc1 lds
Mvvv 176,96,40
s_mul_i32 s58, s58, 3072
s_lshl_b32 s59, s63, 2
ds_read_b64_tr_b16 v[114:115], v26 offset:1792
Mvvv 176,100,44
s_lshr_b32 s59, 0x8a000000, s59
s_and_b32 s59, s59, 15
s_sub_u32 s59, s59, 1
Mvvv 208,96,48
s_add_u32 m0, s78, 41984
s_add_u32 s70, s81, 1408
ds_read_b64_tr_b16 v[116:117], v26 offset:9728
Mvvv 208,100,52
s_waitcnt lgkmcnt(3)
s_mul_i32 s59, s59, 0x60000
buffer_load_dwordx4 v12, s[48:51], s70 offen sc1 lds
Mvvv 240,96,56
s_add_u32 s58, s58, s59
ds_read_b64_tr_b16 v[118:119], v26 offset:9984
v_accvgpr_write_b32 a25, 0
Mvvv 240,100,60
s_bitcmp1_b32 0xf0, s63
s_cselect_b32 s88, s58, 0x80000000
s_lshl_b32 s58, s63, 5
Mvvv 148,104,32
s_add_u32 m0, s78, 43008
s_add_u32 s70, s81, 36224
ds_read_b64_tr_b16 v[120:121], v27 offset:1536
Mvvv 148,108,36
s_add_u32 s58, s58, s64
buffer_load_dwordx4 v11, s[48:51], s70 offen sc1 lds
Mvvv 180,104,40
s_mul_i32 s89, s58, 36864
s_add_u32 s89, s89, s76
ds_read_b64_tr_b16 v[122:123], v27 offset:1792
Mvvv 180,108,44
s_add_u32 s89, s89, 54525952
s_xor_b32 s58, s58, 0x1e0
s_mul_i32 s96, s58, 36864
Mvvv 212,104,48
s_add_u32 m0, s78, 44032
s_add_u32 s70, s81, 36224
ds_read_b64_tr_b16 v[124:125], v27 offset:9728
Mvvv 212,108,52
s_sub_u32 s96, s96, s76
buffer_load_dwordx4 v12, s[48:51], s70 offen sc1 lds
Mvvv 244,104,56
s_add_u32 s96, s96, 54550528
s_lshl2_add_u32 s58, s57, s95
ds_read_b64_tr_b16 v[126:127], v27 offset:9984
Mvvv 244,108,60
s_waitcnt vmcnt(20) lgkmcnt(4)
buffer_store_dwordx2 v[16:17], v13, s[48:51], s84 offen sc1
Mvvv 152,112,32
)"
R"(
s_lshl4_add_u32 s58, s58, s63
s_lshl3_add_u32 s58, s58, s71
s_lshl3_add_u32 s97, s58, 32768
Mvvv 152,116,36
s_and_b32 s58, s57, 2
s_lshl_b32 s98, s58, 13
s_lshl2_add_u32 s99, s63, s58
Mvvv 184,112,40
s_bitcmp1_b32 s57, 0
s_cselect_b64 s[4:5], -1, 0
v_accvgpr_write_b32 a26, 0
Mvvv 184,116,44
s_getpc_b64 s[6:7]
.Lgb:
v_accvgpr_write_b32 a27, 0
v_accvgpr_write_b32 a28, 0
Mvvv 216,112,48
s_add_u32 s6, s6, .Lsb-.Lgb
s_addc_u32 s7, s7, 0
s_mov_b32 s32, 0x91000000
Mvvv 216,116,52
s_waitcnt lgkmcnt(0)
s_mov_b32 s33, 0
v_accvgpr_write_b32 a29, 0
Mvvv 248,112,56
s_barrier
Mvvv 248,116,60
s_cmp_ge_u32 s63, 1
s_cselect_b32 s58, 0x100, 0
ds_read_b64_tr_b16 v[96:97], v26 offset:16384
Mvvv 156,120,32
s_or_b32 s32, s32, s58
ds_read_b64_tr_b16 v[98:99], v26 offset:16640
v_accvgpr_write_b32 a30, 0
Mvvv 156,124,36
s_cmp_ge_u32 s63, 3
s_cselect_b32 s58, 0x4000, 0
ds_read_b64_tr_b16 v[100:101], v26 offset:24576
Mvvv 188,120,40
s_add_u32 m0, s77, 98304
s_add_u32 s70, s68, 6291456
ds_read_b64_tr_b16 v[102:103], v26 offset:24832
Mvvv 188,124,44
s_or_b32 s32, s32, s58
buffer_load_dwordx4 v3, s[16:19], s70 offen lds
Mvvv 220,120,48
s_cmp_ge_u32 s63, 6
s_cselect_b32 s58, 0x100000, 0
ds_read_b64_tr_b16 v[104:105], v27 offset:16384
Mvvv 220,124,52
s_or_b32 s32, s32, s58
s_lshl2_add_u32 s58, s63, 5
ds_read_b64_tr_b16 v[106:107], v27 offset:16640
Mvvv 252,120,56
s_bitset1_b64 s[32:33], s58
ds_read_b64_tr_b16 v[108:109], v27 offset:24576
v_lshrrev_b32 v1, 1, v0
Mvvv 252,124,60
s_waitcnt lgkmcnt(5)
s_sub_u32 s58, 15, s63
s_lshr_b32 s58, s58, 1
ds_read_b64_tr_b16 v[110:111], v27 offset:24832
Mvvv 128,96,64
s_waitcnt lgkmcnt(4)
s_add_u32 m0, s77, 99200
s_add_u32 s70, s68, 6291456
ds_read_b64_tr_b16 v[112:113], v26 offset:16896
Mvvv 128,100,68
s_lshl_b32 s58, s58, 5
buffer_load_dwordx4 v3, s[16:19], s70 offen offset:128 lds
Mvvv 160,96,72
s_add_u32 m0, s77, 106496
s_add_u32 s70, s68, 6553600
ds_read_b64_tr_b16 v[114:115], v26 offset:17152
Mvvv 160,100,76
s_add_u32 s58, s58, 24
buffer_load_dwordx4 v3, s[16:19], s70 offen lds
Mvvv 192,96,80
s_add_u32 m0, s77, 107392
s_add_u32 s70, s68, 6553600
ds_read_b64_tr_b16 v[116:117], v26 offset:25088
Mvvv 192,100,84
buffer_load_dwordx4 v3, s[16:19], s70 offen offset:128 lds
Mvvv 224,96,88
s_waitcnt vmcnt(17)
ds_read_b64_tr_b16 v[118:119], v26 offset:25344
v_min_u32 v1, s58, v1
Mvvv 224,100,92
s_waitcnt lgkmcnt(6)
ds_read_b128 v[32:35], v5 offset:0
v_bfe_u32 v25, v0, 6, 3
Mvvv 132,104,64
s_waitcnt lgkmcnt(5)
ds_read_b64_tr_b16 v[120:121], v27 offset:16896
v_mul_u32_u24 v25, 7168, v25
Mvvv 132,108,68
ds_read_b128 v[36:39], v6 offset:0
v_lshrrev_b32 v24, 9, v0
Mvvv 164,104,72
ds_read_b64_tr_b16 v[122:123], v27 offset:17152
v_lshl_add_u32 v25, v24, 6, v25
Mvvv 164,108,76
ds_read_b128 v[40:43], v5 offset:2048
v_and_b32 v24, 48, v0
Mvvv 196,104,80
ds_read_b64_tr_b16 v[124:125], v27 offset:25088
v_add_u32 v25, v25, v24
Mvvv 196,108,84
ds_read_b128 v[44:47], v6 offset:2048
v_bfe_u32 v24, v0, 6, 2
Mvvv 228,104,88
ds_read_b64_tr_b16 v[126:127], v27 offset:25344
v_lshlrev_b32 v24, 6, v24
Mvvv 228,108,92
s_waitcnt lgkmcnt(10)
ds_read_b128 v[48:51], v5 offset:8192
v_bfe_u32 v2, v0, 4, 2
Mvvv 136,112,64
s_waitcnt lgkmcnt(9)
ds_read_b64_tr_b16 v[96:97], v26 offset:17408
v_lshl_add_u32 v24, v2, 3, v24
Mvvv 136,116,68
ds_read_b128 v[52:55], v6 offset:8192
v_lshrrev_b32 v2, 8, v0
Mvvv 168,112,72
ds_read_b64_tr_b16 v[98:99], v26 offset:17664
v_lshl_add_u32 v24, v2, 10, v24
Mvvv 168,116,76
s_add_u32 s59, s76, 131072
ds_read_b128 v[56:59], v5 offset:10240
v_add_u32 v24, s59, v24
Mvvv 200,112,80
ds_read_b64_tr_b16 v[100:101], v26 offset:25600
v_accvgpr_write_b32 a31, 0
Mvvv 200,116,84
s_waitcnt lgkmcnt(11)
ds_read_b128 v[60:63], v6 offset:10240
v_accvgpr_write_b32 a32, 0
Mvvv 232,112,88
s_add_u32 m0, s78, 65536
s_add_u32 s70, s82, 1536
ds_read_b64_tr_b16 v[102:103], v26 offset:25856
Mvvv 232,116,92
s_waitcnt lgkmcnt(8)
buffer_load_dwordx4 v11, s[48:51], s70 offen sc1 lds
Mvvv 140,120,64
s_add_u32 m0, s78, 66560
s_add_u32 s70, s82, 1536
ds_read_b64_tr_b16 v[104:105], v27 offset:17408
Mvvv 140,124,68
buffer_load_dwordx4 v12, s[48:51], s70 offen sc1 lds
Mvvv 172,120,72
s_add_u32 m0, s78, 67584
s_add_u32 s70, s82, 36352
ds_read_b64_tr_b16 v[106:107], v27 offset:17664
Mvvv 172,124,76
buffer_load_dwordx4 v11, s[48:51], s70 offen sc1 lds
Mvvv 204,120,80
s_add_u32 m0, s78, 68608
s_add_u32 s70, s82, 36352
ds_read_b64_tr_b16 v[108:109], v27 offset:25600
Mvvv 204,124,84
buffer_load_dwordx4 v12, s[48:51], s70 offen sc1 lds
Mvvv 236,120,88
ds_read_b64_tr_b16 v[110:111], v27 offset:25856
v_accvgpr_write_b32 a33, 0
Mvvv 236,124,92
s_waitcnt lgkmcnt(4)
v_accvgpr_write_b32 a34, 0
v_accvgpr_write_b32 a35, 0
Mvvv 144,96,64
s_add_u32 m0, s78, 73728
s_add_u32 s70, s81, 1536
ds_read_b64_tr_b16 v[112:113], v26 offset:17920
Mvvv 144,100,68
buffer_load_dwordx4 v11, s[48:51], s70 offen sc1 lds
Mvvv 176,96,72
ds_read_b64_tr_b16 v[114:115], v26 offset:18176
v_accvgpr_write_b32 a36, 0
Mvvv 176,100,76
v_accvgpr_write_b32 a37, 0
v_accvgpr_write_b32 a38, 0
Mvvv 208,96,80
s_add_u32 m0, s78, 74752
s_add_u32 s70, s81, 1536
ds_read_b64_tr_b16 v[116:117], v26 offset:26112
Mvvv 208,100,84
buffer_load_dwordx4 v12, s[48:51], s70 offen sc1 lds
Mvvv 240,96,88
ds_read_b64_tr_b16 v[118:119], v26 offset:26368
v_accvgpr_write_b32 a39, 0
Mvvv 240,100,92
s_waitcnt lgkmcnt(4)
v_accvgpr_write_b32 a40, 0
v_accvgpr_write_b32 a41, 0
Mvvv 148,104,64
s_add_u32 m0, s78, 75776
s_add_u32 s70, s81, 36352
ds_read_b64_tr_b16 v[120:121], v27 offset:17920
Mvvv 148,108,68
buffer_load_dwordx4 v11, s[48:51], s70 offen sc1 lds
Mvvv 180,104,72
ds_read_b64_tr_b16 v[122:123], v27 offset:18176
v_accvgpr_write_b32 a42, 0
Mvvv 180,108,76
v_accvgpr_write_b32 a43, 0
v_accvgpr_write_b32 a44, 0
Mvvv 212,104,80
s_add_u32 m0, s78, 76800
s_add_u32 s70, s81, 36352
ds_read_b64_tr_b16 v[124:125], v27 offset:26112
Mvvv 212,108,84
buffer_load_dwordx4 v12, s[48:51], s70 offen sc1 lds
Mvvv 244,104,88
ds_read_b64_tr_b16 v[126:127], v27 offset:26368
v_accvgpr_write_b32 a45, 0
Mvvv 244,108,92
s_waitcnt lgkmcnt(6)
v_accvgpr_write_b32 a46, 0
v_accvgpr_write_b32 a47, 0
Mvvv 152,112,64
s_waitcnt lgkmcnt(4)
v_accvgpr_write_b32 a48, 0
v_accvgpr_write_b32 a49, 0
Mvvv 152,116,68
v_accvgpr_write_b32 a50, 0
v_accvgpr_write_b32 a51, 0
Mvvv 184,112,72
v_accvgpr_write_b32 a52, 0
v_accvgpr_write_b32 a53, 0
Mvvv 184,116,76
v_accvgpr_write_b32 a54, 0
v_accvgpr_write_b32 a55, 0
Mvvv 216,112,80
v_accvgpr_write_b32 a56, 0
v_accvgpr_write_b32 a57, 0
Mvvv 216,116,84
s_waitcnt lgkmcnt(0)
v_accvgpr_write_b32 a58, 0
v_accvgpr_write_b32 a59, 0
Mvvv 248,112,88
s_barrier
Mvvv 248,116,92
ds_read_b64_tr_b16 v[96:97], v26 offset:32768
v_accvgpr_write_b32 a60, 0
Mvvv 156,120,64
ds_read_b64_tr_b16 v[98:99], v26 offset:33024
v_accvgpr_write_b32 a61, 0
Mvvv 156,124,68
ds_read_b64_tr_b16 v[100:101], v26 offset:40960
v_accvgpr_write_b32 a62, 0
Mvvv 188,120,72
s_add_u32 m0, s77, 114688
s_add_u32 s70, s68, 6815744
ds_read_b64_tr_b16 v[102:103], v26 offset:41216
Mvvv 188,124,76
buffer_load_dwordx4 v3, s[16:19], s70 offen lds
Mvvv 220,120,80
ds_read_b64_tr_b16 v[104:105], v27 offset:32768
v_accvgpr_write_b32 a63, 0
Mvvv 220,124,84
ds_read_b64_tr_b16 v[106:107], v27 offset:33024
v_accvgpr_write_b32 a64, 0
Mvvv 252,120,88
ds_read_b64_tr_b16 v[108:109], v27 offset:40960
v_accvgpr_write_b32 a65, 0
Mvvv 252,124,92
s_waitcnt lgkmcnt(5)
ds_read_b64_tr_b16 v[110:111], v27 offset:41216
v_accvgpr_write_b32 a66, 0
Mvvv 128,96,32
s_waitcnt lgkmcnt(4)
s_add_u32 m0, s77, 115584
s_add_u32 s70, s68, 6815744
ds_read_b64_tr_b16 v[112:113], v26 offset:33280
Mvvv 128,100,36
buffer_load_dwordx4 v3, s[16:19], s70 offen offset:128 lds
Mvvv 160,96,40
s_add_u32 m0, s77, 122880
s_add_u32 s70, s68, 7077888
ds_read_b64_tr_b16 v[114:115], v26 offset:33536
Mvvv 160,100,44
buffer_load_dwordx4 v3, s[16:19], s70 offen lds
Mvvv 192,96,48
s_add_u32 m0, s77, 123776
s_add_u32 s70, s68, 7077888
ds_read_b64_tr_b16 v[116:117], v26 offset:41472
Mvvv 192,100,52
buffer_load_dwordx4 v3, s[16:19], s70 offen offset:128 lds
Mvvv 224,96,56
s_waitcnt vmcnt(17)
ds_read_b64_tr_b16 v[118:119], v26 offset:41728
v_accvgpr_write_b32 a67, 0
Mvvv 224,100,60
s_waitcnt lgkmcnt(6)
ds_read_b128 v[64:67], v5 offset:32768
v_accvgpr_write_b32 a68, 0
Mvvv 132,104,32
s_waitcnt lgkmcnt(5)
ds_read_b64_tr_b16 v[120:121], v27 offset:33280
v_accvgpr_write_b32 a69, 0
Mvvv 132,108,36
ds_read_b128 v[68:71], v6 offset:32768
v_accvgpr_write_b32 a70, 0
Mvvv 164,104,40
ds_read_b64_tr_b16 v[122:123], v27 offset:33536
v_accvgpr_write_b32 a71, 0
Mvvv 164,108,44
ds_read_b128 v[72:75], v5 offset:34816
v_accvgpr_write_b32 a72, 0
Mvvv 196,104,48
ds_read_b64_tr_b16 v[124:125], v27 offset:41472
v_accvgpr_write_b32 a73, 0
Mvvv 196,108,52
ds_read_b128 v[76:79], v6 offset:34816
v_accvgpr_write_b32 a74, 0
Mvvv 228,104,56
ds_read_b64_tr_b16 v[126:127], v27 offset:41728
v_accvgpr_write_b32 a75, 0
Mvvv 228,108,60
s_waitcnt lgkmcnt(10)
ds_read_b128 v[80:83], v5 offset:40960
v_accvgpr_write_b32 a76, 0
Mvvv 136,112,32
s_waitcnt lgkmcnt(9)
ds_read_b64_tr_b16 v[96:97], v26 offset:33792
v_accvgpr_write_b32 a77, 0
Mvvv 136,116,36
ds_read_b128 v[84:87], v6 offset:40960
v_accvgpr_write_b32 a78, 0
Mvvv 168,112,40
ds_read_b64_tr_b16 v[98:99], v26 offset:34048
v_accvgpr_write_b32 a79, 0
Mvvv 168,116,44
ds_read_b128 v[88:91], v5 offset:43008
v_accvgpr_write_b32 a80, 0
Mvvv 200,112,48
ds_read_b64_tr_b16 v[100:101], v26 offset:41984
v_accvgpr_write_b32 a81, 0
Mvvv 200,116,52
s_waitcnt lgkmcnt(11)
ds_read_b128 v[92:95], v6 offset:43008
v_accvgpr_write_b32 a82, 0
Mvvv 232,112,56
s_add_u32 m0, s78, 0
s_add_u32 s70, s82, 1664
ds_read_b64_tr_b16 v[102:103], v26 offset:42240
Mvvv 232,116,60
s_waitcnt lgkmcnt(8)
buffer_load_dwordx4 v11, s[48:51], s70 offen sc1 lds
Mvvv 140,120,32
s_add_u32 m0, s78, 1024
s_add_u32 s70, s82, 1664
ds_read_b64_tr_b16 v[104:105], v27 offset:33792
Mvvv 140,124,36
buffer_load_dwordx4 v12, s[48:51], s70 offen sc1 lds
Mvvv 172,120,40
s_add_u32 m0, s78, 2048
s_add_u32 s70, s82, 36480
ds_read_b64_tr_b16 v[106:107], v27 offset:34048
Mvvv 172,124,44
buffer_load_dwordx4 v11, s[48:51], s70 offen sc1 lds
Mvvv 204,120,48
s_add_u32 m0, s78, 3072
s_add_u32 s70, s82, 36480
ds_read_b64_tr_b16 v[108:109], v27 offset:41984
Mvvv 204,124,52
buffer_load_dwordx4 v12, s[48:51], s70 offen sc1 lds
Mvvv 236,120,56
ds_read_b64_tr_b16 v[110:111], v27 offset:42240
v_accvgpr_write_b32 a83, 0
Mvvv 236,124,60
s_waitcnt lgkmcnt(4)
v_accvgpr_write_b32 a84, 0
v_accvgpr_write_b32 a85, 0
Mvvv 144,96,32
s_add_u32 m0, s78, 8192
s_add_u32 s70, s81, 1664
ds_read_b64_tr_b16 v[112:113], v26 offset:34304
Mvvv 144,100,36
buffer_load_dwordx4 v11, s[48:51], s70 offen sc1 lds
Mvvv 176,96,40
ds_read_b64_tr_b16 v[114:115], v26 offset:34560
v_accvgpr_write_b32 a86, 0
Mvvv 176,100,44
v_accvgpr_write_b32 a87, 0
v_accvgpr_write_b32 a88, 0
Mvvv 208,96,48
s_add_u32 m0, s78, 9216
s_add_u32 s70, s81, 1664
ds_read_b64_tr_b16 v[116:117], v26 offset:42496
Mvvv 208,100,52
buffer_load_dwordx4 v12, s[48:51], s70 offen sc1 lds
Mvvv 240,96,56
ds_read_b64_tr_b16 v[118:119], v26 offset:42752
v_accvgpr_write_b32 a89, 0
Mvvv 240,100,60
s_waitcnt lgkmcnt(4)
v_accvgpr_write_b32 a90, 0
v_accvgpr_write_b32 a91, 0
Mvvv 148,104,32
s_add_u32 m0, s78, 10240
s_add_u32 s70, s81, 36480
ds_read_b64_tr_b16 v[120:121], v27 offset:34304
Mvvv 148,108,36
buffer_load_dwordx4 v11, s[48:51], s70 offen sc1 lds
Mvvv 180,104,40
ds_read_b64_tr_b16 v[122:123], v27 offset:34560
v_accvgpr_write_b32 a92, 0
Mvvv 180,108,44
v_accvgpr_write_b32 a93, 0
v_accvgpr_write_b32 a94, 0
Mvvv 212,104,48
s_add_u32 m0, s78, 11264
s_add_u32 s70, s81, 36480
ds_read_b64_tr_b16 v[124:125], v27 offset:42496
Mvvv 212,108,52
buffer_load_dwordx4 v12, s[48:51], s70 offen sc1 lds
Mvvv 244,104,56
ds_read_b64_tr_b16 v[126:127], v27 offset:42752
v_accvgpr_write_b32 a95, 0
Mvvv 244,108,60
s_waitcnt lgkmcnt(6)
v_accvgpr_write_b32 a96, 0
v_accvgpr_write_b32 a97, 0
Mvvv 152,112,32
s_waitcnt lgkmcnt(4)
v_accvgpr_write_b32 a98, 0
v_accvgpr_write_b32 a99, 0
Mvvv 152,116,36
v_accvgpr_write_b32 a100, 0
v_accvgpr_write_b32 a101, 0
Mvvv 184,112,40
)"
R"(
v_accvgpr_write_b32 a102, 0
v_accvgpr_write_b32 a103, 0
Mvvv 184,116,44
v_accvgpr_write_b32 a104, 0
v_accvgpr_write_b32 a105, 0
Mvvv 216,112,48
v_accvgpr_write_b32 a106, 0
v_accvgpr_write_b32 a107, 0
Mvvv 216,116,52
s_waitcnt lgkmcnt(0)
v_accvgpr_write_b32 a108, 0
v_accvgpr_write_b32 a109, 0
Mvvv 248,112,56
s_barrier
Mvvv 248,116,60
ds_read_b64_tr_b16 v[96:97], v26 offset:49152
v_accvgpr_write_b32 a110, 0
Mvvv 156,120,32
ds_read_b64_tr_b16 v[98:99], v26 offset:49408
v_accvgpr_write_b32 a111, 0
Mvvv 156,124,36
ds_read_b64_tr_b16 v[100:101], v26 offset:57344
v_accvgpr_write_b32 a112, 0
Mvvv 188,120,40
s_add_u32 m0, s77, 131072
s_add_u32 s70, s68, 7340032
ds_read_b64_tr_b16 v[102:103], v26 offset:57600
Mvvv 188,124,44
buffer_load_dwordx4 v3, s[16:19], s70 offen lds
Mvvv 220,120,48
ds_read_b64_tr_b16 v[104:105], v27 offset:49152
v_accvgpr_write_b32 a113, 0
Mvvv 220,124,52
ds_read_b64_tr_b16 v[106:107], v27 offset:49408
v_accvgpr_write_b32 a114, 0
Mvvv 252,120,56
ds_read_b64_tr_b16 v[108:109], v27 offset:57344
v_accvgpr_write_b32 a115, 0
Mvvv 252,124,60
s_waitcnt lgkmcnt(5)
ds_read_b64_tr_b16 v[110:111], v27 offset:57600
v_accvgpr_write_b32 a116, 0
Mvvv 128,96,64
s_waitcnt lgkmcnt(4)
s_add_u32 m0, s77, 131968
s_add_u32 s70, s68, 7340032
ds_read_b64_tr_b16 v[112:113], v26 offset:49664
Mvvv 128,100,68
buffer_load_dwordx4 v3, s[16:19], s70 offen offset:128 lds
Mvvv 160,96,72
s_add_u32 m0, s77, 139264
s_add_u32 s70, s68, 7602176
ds_read_b64_tr_b16 v[114:115], v26 offset:49920
Mvvv 160,100,76
buffer_load_dwordx4 v3, s[16:19], s70 offen lds
Mvvv 192,96,80
s_add_u32 m0, s77, 140160
s_add_u32 s70, s68, 7602176
ds_read_b64_tr_b16 v[116:117], v26 offset:57856
Mvvv 192,100,84
buffer_load_dwordx4 v3, s[16:19], s70 offen offset:128 lds
Mvvv 224,96,88
s_waitcnt vmcnt(16)
ds_read_b64_tr_b16 v[118:119], v26 offset:58112
v_accvgpr_write_b32 a117, 0
Mvvv 224,100,92
s_waitcnt lgkmcnt(6)
ds_read_b128 v[32:35], v7 offset:0
v_accvgpr_write_b32 a118, 0
Mvvv 132,104,64
s_waitcnt lgkmcnt(5)
ds_read_b64_tr_b16 v[120:121], v27 offset:49664
v_accvgpr_write_b32 a119, 0
Mvvv 132,108,68
ds_read_b128 v[36:39], v8 offset:0
v_accvgpr_write_b32 a120, 0
Mvvv 164,104,72
ds_read_b64_tr_b16 v[122:123], v27 offset:49920
v_accvgpr_write_b32 a121, 0
Mvvv 164,108,76
ds_read_b128 v[40:43], v7 offset:2048
v_accvgpr_write_b32 a122, 0
Mvvv 196,104,80
ds_read_b64_tr_b16 v[124:125], v27 offset:57856
v_accvgpr_write_b32 a123, 0
Mvvv 196,108,84
ds_read_b128 v[44:47], v8 offset:2048
v_accvgpr_write_b32 a124, 0
Mvvv 228,104,88
ds_read_b64_tr_b16 v[126:127], v27 offset:58112
v_accvgpr_write_b32 a125, 0
Mvvv 228,108,92
s_waitcnt lgkmcnt(10)
ds_read_b128 v[48:51], v7 offset:8192
v_accvgpr_write_b32 a126, 0
Mvvv 136,112,64
s_waitcnt lgkmcnt(9)
ds_read_b64_tr_b16 v[96:97], v26 offset:50176
v_accvgpr_write_b32 a127, 0
Mvvv 136,116,68
ds_read_b128 v[52:55], v8 offset:8192
Mvvv 168,112,72
ds_read_b64_tr_b16 v[98:99], v26 offset:50432
Mvvv 168,116,76
ds_read_b128 v[56:59], v7 offset:10240
Mvvv 200,112,80
ds_read_b64_tr_b16 v[100:101], v26 offset:58368
Mvvv 200,116,84
s_waitcnt lgkmcnt(11)
ds_read_b128 v[60:63], v8 offset:10240
Mvvv 232,112,88
s_add_u32 m0, s78, 32768
s_add_u32 s70, s82, 1792
ds_read_b64_tr_b16 v[102:103], v26 offset:58624
Mvvv 232,116,92
s_waitcnt lgkmcnt(8)
buffer_load_dwordx4 v11, s[48:51], s70 offen sc1 lds
Mvvv 140,120,64
s_add_u32 m0, s78, 33792
s_add_u32 s70, s82, 1792
ds_read_b64_tr_b16 v[104:105], v27 offset:50176
Mvvv 140,124,68
buffer_load_dwordx4 v12, s[48:51], s70 offen sc1 lds
Mvvv 172,120,72
s_add_u32 m0, s78, 34816
s_add_u32 s70, s82, 36608
ds_read_b64_tr_b16 v[106:107], v27 offset:50432
Mvvv 172,124,76
buffer_load_dwordx4 v11, s[48:51], s70 offen sc1 lds
Mvvv 204,120,80
s_add_u32 m0, s78, 35840
s_add_u32 s70, s82, 36608
ds_read_b64_tr_b16 v[108:109], v27 offset:58368
Mvvv 204,124,84
buffer_load_dwordx4 v12, s[48:51], s70 offen sc1 lds
Mvvv 236,120,88
ds_read_b64_tr_b16 v[110:111], v27 offset:58624
Mvvv 236,124,92
s_waitcnt lgkmcnt(4)
Mvvv 144,96,64
s_add_u32 m0, s78, 40960
s_add_u32 s70, s81, 1792
ds_read_b64_tr_b16 v[112:113], v26 offset:50688
Mvvv 144,100,68
buffer_load_dwordx4 v11, s[48:51], s70 offen sc1 lds
Mvvv 176,96,72
ds_read_b64_tr_b16 v[114:115], v26 offset:50944
Mvvv 176,100,76
Mvvv 208,96,80
s_add_u32 m0, s78, 41984
s_add_u32 s70, s81, 1792
ds_read_b64_tr_b16 v[116:117], v26 offset:58880
Mvvv 208,100,84
buffer_load_dwordx4 v12, s[48:51], s70 offen sc1 lds
Mvvv 240,96,88
ds_read_b64_tr_b16 v[118:119], v26 offset:59136
Mvvv 240,100,92
s_waitcnt lgkmcnt(4)
Mvvv 148,104,64
s_add_u32 m0, s78, 43008
s_add_u32 s70, s81, 36608
ds_read_b64_tr_b16 v[120:121], v27 offset:50688
Mvvv 148,108,68
buffer_load_dwordx4 v11, s[48:51], s70 offen sc1 lds
Mvvv 180,104,72
ds_read_b64_tr_b16 v[122:123], v27 offset:50944
Mvvv 180,108,76
Mvvv 212,104,80
s_add_u32 m0, s78, 44032
s_add_u32 s70, s81, 36608
ds_read_b64_tr_b16 v[124:125], v27 offset:58880
Mvvv 212,108,84
buffer_load_dwordx4 v12, s[48:51], s70 offen sc1 lds
Mvvv 244,104,88
ds_read_b64_tr_b16 v[126:127], v27 offset:59136
Mvvv 244,108,92
s_waitcnt lgkmcnt(6)
Mvvv 152,112,64
s_waitcnt lgkmcnt(4)
Mvvv 152,116,68
Mvvv 184,112,72
Mvvv 184,116,76
Mvvv 216,112,80
Mvvv 216,116,84
s_waitcnt lgkmcnt(0)
Mvvv 248,112,88
s_barrier
Mvvv 248,116,92
ds_read_b64_tr_b16 v[96:97], v26 offset:0
Mvvv 156,120,64
ds_read_b64_tr_b16 v[98:99], v26 offset:256
Mvvv 156,124,68
ds_read_b64_tr_b16 v[100:101], v26 offset:8192
Mvvv 188,120,72
s_add_u32 m0, s77, 147456
s_add_u32 s70, s68, 7864320
ds_read_b64_tr_b16 v[102:103], v26 offset:8448
Mvvv 188,124,76
buffer_load_dwordx4 v3, s[16:19], s70 offen lds
Mvvv 220,120,80
ds_read_b64_tr_b16 v[104:105], v27 offset:0
Mvvv 220,124,84
ds_read_b64_tr_b16 v[106:107], v27 offset:256
Mvvv 252,120,88
ds_read_b64_tr_b16 v[108:109], v27 offset:8192
Mvvv 252,124,92
s_waitcnt lgkmcnt(5)
ds_read_b64_tr_b16 v[110:111], v27 offset:8448
Mvvv 128,96,32
s_waitcnt lgkmcnt(4)
s_add_u32 m0, s77, 148352
s_add_u32 s70, s68, 7864320
ds_read_b64_tr_b16 v[112:113], v26 offset:512
Mvvv 128,100,36
buffer_load_dwordx4 v3, s[16:19], s70 offen offset:128 lds
Mvvv 160,96,40
s_add_u32 m0, s77, 155648
s_add_u32 s70, s68, 8126464
ds_read_b64_tr_b16 v[114:115], v26 offset:768
Mvvv 160,100,44
buffer_load_dwordx4 v3, s[16:19], s70 offen lds
Mvvv 192,96,48
s_add_u32 m0, s77, 156544
s_add_u32 s70, s68, 8126464
ds_read_b64_tr_b16 v[116:117], v26 offset:8704
Mvvv 192,100,52
buffer_load_dwordx4 v3, s[16:19], s70 offen offset:128 lds
Mvvv 224,96,56
s_waitcnt vmcnt(16)
ds_read_b64_tr_b16 v[118:119], v26 offset:8960
Mvvv 224,100,60
s_waitcnt lgkmcnt(6)
ds_read_b128 v[64:67], v5 offset:0
Mvvv 132,104,32
s_waitcnt lgkmcnt(5)
ds_read_b64_tr_b16 v[120:121], v27 offset:512
Mvvv 132,108,36
ds_read_b128 v[68:71], v6 offset:0
Mvvv 164,104,40
ds_read_b64_tr_b16 v[122:123], v27 offset:768
Mvvv 164,108,44
ds_read_b128 v[72:75], v5 offset:2048
Mvvv 196,104,48
ds_read_b64_tr_b16 v[124:125], v27 offset:8704
Mvvv 196,108,52
ds_read_b128 v[76:79], v6 offset:2048
Mvvv 228,104,56
ds_read_b64_tr_b16 v[126:127], v27 offset:8960
Mvvv 228,108,60
s_waitcnt lgkmcnt(10)
ds_read_b128 v[80:83], v5 offset:8192
Mvvv 136,112,32
s_waitcnt lgkmcnt(9)
ds_read_b64_tr_b16 v[96:97], v26 offset:1024
Mvvv 136,116,36
ds_read_b128 v[84:87], v6 offset:8192
Mvvv 168,112,40
ds_read_b64_tr_b16 v[98:99], v26 offset:1280
Mvvv 168,116,44
ds_read_b128 v[88:91], v5 offset:10240
Mvvv 200,112,48
ds_read_b64_tr_b16 v[100:101], v26 offset:9216
Mvvv 200,116,52
s_waitcnt lgkmcnt(11)
ds_read_b128 v[92:95], v6 offset:10240
Mvvv 232,112,56
s_add_u32 m0, s78, 65536
s_add_u32 s70, s82, 1920
ds_read_b64_tr_b16 v[102:103], v26 offset:9472
Mvvv 232,116,60
s_waitcnt lgkmcnt(8)
buffer_load_dwordx4 v11, s[48:51], s70 offen sc1 lds
Mvvv 140,120,32
s_add_u32 m0, s78, 66560
s_add_u32 s70, s82, 1920
ds_read_b64_tr_b16 v[104:105], v27 offset:1024
Mvvv 140,124,36
buffer_load_dwordx4 v12, s[48:51], s70 offen sc1 lds
Mvvv 172,120,40
s_add_u32 m0, s78, 67584
s_add_u32 s70, s82, 36736
ds_read_b64_tr_b16 v[106:107], v27 offset:1280
Mvvv 172,124,44
buffer_load_dwordx4 v11, s[48:51], s70 offen sc1 lds
Mvvv 204,120,48
s_add_u32 m0, s78, 68608
s_add_u32 s70, s82, 36736
ds_read_b64_tr_b16 v[108:109], v27 offset:9216
Mvvv 204,124,52
buffer_load_dwordx4 v12, s[48:51], s70 offen sc1 lds
Mvvv 236,120,56
ds_read_b64_tr_b16 v[110:111], v27 offset:9472
Mvvv 236,124,60
s_waitcnt lgkmcnt(4)
buffer_load_dwordx2 v[20:21], v1, s[48:51], s83 offen sc1
Mvvv 144,96,32
s_add_u32 m0, s78, 73728
s_add_u32 s70, s81, 1920
ds_read_b64_tr_b16 v[112:113], v26 offset:1536
Mvvv 144,100,36
buffer_load_dwordx4 v11, s[48:51], s70 offen sc1 lds
Mvvv 176,96,40
ds_read_b64_tr_b16 v[114:115], v26 offset:1792
Mvvv 176,100,44
Mvvv 208,96,48
s_add_u32 m0, s78, 74752
s_add_u32 s70, s81, 1920
ds_read_b64_tr_b16 v[116:117], v26 offset:9728
Mvvv 208,100,52
buffer_load_dwordx4 v12, s[48:51], s70 offen sc1 lds
Mvvv 240,96,56
ds_read_b64_tr_b16 v[118:119], v26 offset:9984
Mvvv 240,100,60
s_waitcnt lgkmcnt(4)
Mvvv 148,104,32
s_add_u32 m0, s78, 75776
s_add_u32 s70, s81, 36736
ds_read_b64_tr_b16 v[120:121], v27 offset:1536
Mvvv 148,108,36
buffer_load_dwordx4 v11, s[48:51], s70 offen sc1 lds
Mvvv 180,104,40
ds_read_b64_tr_b16 v[122:123], v27 offset:1792
Mvvv 180,108,44
Mvvv 212,104,48
s_add_u32 m0, s78, 76800
s_add_u32 s70, s81, 36736
ds_read_b64_tr_b16 v[124:125], v27 offset:9728
Mvvv 212,108,52
buffer_load_dwordx4 v12, s[48:51], s70 offen sc1 lds
Mvvv 244,104,56
ds_read_b64_tr_b16 v[126:127], v27 offset:9984
Mvvv 244,108,60
s_waitcnt lgkmcnt(6)
Mvvv 152,112,32
s_waitcnt lgkmcnt(4)
Mvvv 152,116,36
Mvvv 184,112,40
Mvvv 184,116,44
Mvvv 216,112,48
Mvvv 216,116,52
s_waitcnt lgkmcnt(0)
Mvvv 248,112,56
s_barrier
Mvvv 248,116,60
ds_read_b64_tr_b16 v[96:97], v26 offset:16384
Mvvv 156,120,32
ds_read_b64_tr_b16 v[98:99], v26 offset:16640
Mvvv 156,124,36
ds_read_b64_tr_b16 v[100:101], v26 offset:24576
Mvvv 188,120,40
ds_read_b64_tr_b16 v[102:103], v26 offset:24832
Mvvv 188,124,44
Mvvv 220,120,48
ds_read_b64_tr_b16 v[104:105], v27 offset:16384
Mvvv 220,124,52
ds_read_b64_tr_b16 v[106:107], v27 offset:16640
Mvvv 252,120,56
ds_read_b64_tr_b16 v[108:109], v27 offset:24576
Mvvv 252,124,60
s_waitcnt lgkmcnt(5)
ds_read_b64_tr_b16 v[110:111], v27 offset:24832
Mvvv 128,96,64
s_waitcnt lgkmcnt(4)
ds_read_b64_tr_b16 v[112:113], v26 offset:16896
Mvvv 128,100,68
Mvvv 160,96,72
ds_read_b64_tr_b16 v[114:115], v26 offset:17152
Mvvv 160,100,76
Mvvv 192,96,80
s_lshl2_add_u32 s81, s62, s57
s_lshl_b32 s81, s81, 10
ds_read_b64_tr_b16 v[116:117], v26 offset:25088
Mvvv 192,100,84
s_mov_b32 s82, .Lhot_end-.Lstart
s_min_u32 s81, s81, s82
s_lshl3_add_u32 s59, s57, 0x18000
Mvvv 224,96,88
s_waitcnt vmcnt(13) lgkmcnt(3)
s_getpc_b64 s[16:17]
.Lcp2:
ds_read_b64_tr_b16 v[118:119], v26 offset:25344
Mvvv 224,100,92
s_sub_u32 s16, s16, .Lcp2-.Lstart
s_subb_u32 s17, s17, 0
ds_read_b128 v[32:35], v5 offset:32768
Mvvv 132,104,64
s_add_u32 s16, s16, s81
s_addc_u32 s17, s17, 0
ds_read_b64_tr_b16 v[120:121], v27 offset:16896
Mvvv 132,108,68
s_and_b32 s16, s16, -16
ds_read_b128 v[36:39], v6 offset:32768
v_mov_b32 v9, s59
Mvvv 164,104,72
ds_read_b64_tr_b16 v[122:123], v27 offset:17152
v_mov_b32 v22, s90
Mvvv 164,108,76
ds_read_b128 v[40:43], v5 offset:34816
v_mov_b32 v23, s91
Mvvv 196,104,80
ds_read_b64_tr_b16 v[124:125], v27 offset:25088
Mvvv 196,108,84
ds_read_b128 v[44:47], v6 offset:34816
Mvvv 228,104,88
ds_read_b64_tr_b16 v[126:127], v27 offset:25344
Mvvv 228,108,92
s_waitcnt lgkmcnt(10)
ds_read_b128 v[48:51], v5 offset:40960
Mvvv 136,112,64
s_waitcnt lgkmcnt(9)
ds_read_b64_tr_b16 v[96:97], v26 offset:17408
Mvvv 136,116,68
ds_read_b128 v[52:55], v6 offset:40960
Mvvv 168,112,72
ds_read_b64_tr_b16 v[98:99], v26 offset:17664
Mvvv 168,116,76
ds_read_b128 v[56:59], v5 offset:43008
Mvvv 200,112,80
ds_read_b64_tr_b16 v[100:101], v26 offset:25600
Mvvv 200,116,84
ds_read_b128 v[60:63], v6 offset:43008
Mvvv 232,112,88
s_waitcnt lgkmcnt(12)
ds_read_b64_tr_b16 v[102:103], v26 offset:25856
Mvvv 232,116,92
s_waitcnt lgkmcnt(12)
global_load_dwordx4 v[28:31], v0, s[16:17]
Mvvv 140,120,64
)"
R"(
s_waitcnt lgkmcnt(8)
ds_read_b64_tr_b16 v[104:105], v27 offset:17408
Mvvv 140,124,68
ds_write_b64 v9, v[22:23]
Mvvv 172,120,72
ds_read_b64_tr_b16 v[106:107], v27 offset:17664
v_bfe_u32 v9, v0, 4, 3
Mvvv 172,124,76
v_lshlrev_b32 v9, 2, v9
v_add_u32 v9, 0x18000, v9
Mvvv 204,120,80
s_waitcnt vmcnt(5)
ds_read_b64_tr_b16 v[108:109], v27 offset:25600
v_xor_b32 v20, s86, v20
Mvvv 204,124,84
v_bitop3_b32 v20, v21, s87, v20 bitop3:0xbe
v_cmp_ne_u32 vcc, 0, v20
Mvvv 236,120,88
ds_read_b64_tr_b16 v[110:111], v27 offset:25856
Mvvv 236,124,92
s_waitcnt lgkmcnt(9)
s_cbranch_vccnz .Lkvf
.Lkvf_r:
Mvvv 144,96,64
s_waitcnt lgkmcnt(5)
ds_read_b64_tr_b16 v[112:113], v26 offset:17920
Mvvv 144,100,68
Mvvv 176,96,72
ds_read_b64_tr_b16 v[114:115], v26 offset:18176
Mvvv 176,100,76
Mvvv 208,96,80
ds_read_b64_tr_b16 v[116:117], v26 offset:26112
Mvvv 208,100,84
Mvvv 240,96,88
ds_read_b64_tr_b16 v[118:119], v26 offset:26368
Mvvv 240,100,92
s_waitcnt lgkmcnt(6)
Mvvv 148,104,64
s_waitcnt lgkmcnt(4)
ds_read_b64_tr_b16 v[120:121], v27 offset:17920
Mvvv 148,108,68
Mvvv 180,104,72
ds_read_b64_tr_b16 v[122:123], v27 offset:18176
Mvvv 180,108,76
Mvvv 212,104,80
ds_read_b64_tr_b16 v[124:125], v27 offset:26112
Mvvv 212,108,84
Mvvv 244,104,88
ds_read_b64_tr_b16 v[126:127], v27 offset:26368
Mvvv 244,108,92
s_waitcnt lgkmcnt(6)
Mvvv 152,112,64
s_waitcnt lgkmcnt(4)
Mvvv 152,116,68
Mvvv 184,112,72
v_readlane_b32 s35, v4, 60
Mvvv 184,116,76
s_add_u32 s58, s75, 0
Mvvv 216,112,80
s_and_b32 s58, s58, 0xc000
Mvvv 216,116,84
s_waitcnt lgkmcnt(0)
s_add_u32 s58, s58, s69
Mvvv 248,112,88
s_barrier
s_add_u32 m0, s58, 0
Mvvv 248,116,92
ds_read_b64_tr_b16 v[96:97], v26 offset:32768
buffer_load_dwordx4 v0, s[48:51], s35 offen sc1 lds
Mvvv 156,120,64
ds_read_b64_tr_b16 v[98:99], v26 offset:33024
Mvvv 156,124,68
ds_read_b64_tr_b16 v[100:101], v26 offset:40960
Mvvv 188,120,72
ds_read_b64_tr_b16 v[102:103], v26 offset:41216
Mvvv 188,124,76
buffer_load_dwordx4 v0, s[48:51], s35 offen offset:1024 sc1 lds
Mvvv 220,120,80
ds_read_b64_tr_b16 v[104:105], v27 offset:32768
Mvvv 220,124,84
ds_read_b64_tr_b16 v[106:107], v27 offset:33024
Mvvv 252,120,88
ds_read_b64_tr_b16 v[108:109], v27 offset:40960
buffer_load_dwordx4 v0, s[48:51], s35 offen offset:2048 sc1 lds
Mvvv 252,124,92
s_waitcnt lgkmcnt(5)
ds_read_b64_tr_b16 v[110:111], v27 offset:41216
Mvvv 128,96,32
s_waitcnt lgkmcnt(4)
s_mov_b32 s27, 0
ds_read_b64_tr_b16 v[112:113], v26 offset:33280
v_add_u32 v1, s75, v0
Mvvv 128,100,36
s_or_b32 s80, s75, 0x10000
buffer_load_dwordx4 v0, s[48:51], s35 offen offset:3072 sc1 lds
Mvvv 160,96,40
ds_read_b64_tr_b16 v[114:115], v26 offset:33536
v_add_u32 v2, s80, v0
Mvvv 160,100,44
s_cmp_eq_u32 s98, 0
s_cselect_b32 s84, 0x30000, s69
ds_read_b32 v22, v9
Mvvv 192,96,48
ds_read_b64_tr_b16 v[116:117], v26 offset:41472
v_readlane_b32 s35, v4, 61
Mvvv 192,100,52
s_add_u32 s58, s75, 16384
s_cmp_eq_u32 s99, 2
s_cselect_b32 s79, s98, 0
Mvvv 224,96,56
s_waitcnt vmcnt(5) lgkmcnt(4)
s_and_b32 s58, s58, 0xc000
ds_read_b64_tr_b16 v[118:119], v26 offset:41728
Mvvv 224,100,60
s_add_u32 s58, s58, s69
s_add_u32 s79, s79, s75
ds_read_b128 v[64:67], v7 offset:0
Mvvv 132,104,32
s_add_u32 m0, s58, 0
s_add_u32 s79, s79, 0x4000
ds_read_b64_tr_b16 v[120:121], v27 offset:33280
Mvvv 132,108,36
ds_read_b128 v[68:71], v8 offset:0
buffer_load_dwordx4 v0, s[48:51], s35 offen sc1 lds
Mvvv 164,104,40
s_and_b32 s79, s79, 0xc000
ds_read_b64_tr_b16 v[122:123], v27 offset:33536
Mvvv 164,108,44
s_waitcnt lgkmcnt(6)
ds_read_b128 v[72:75], v7 offset:2048
v_mov_b32 v10, v22
Mvvv 196,104,48
ds_read_b64_tr_b16 v[124:125], v27 offset:41472
buffer_load_dwordx4 v0, s[48:51], s35 offen offset:1024 sc1 lds
Mvvv 196,108,52
ds_read_b128 v[76:79], v8 offset:2048
v_max_u32_dpp v10, v10, v10 quad_perm:[1,0,3,2] row_mask:0xf bank_mask:0xf bound_ctrl:1
Mvvv 228,104,56
ds_read_b64_tr_b16 v[126:127], v27 offset:41728
Mvvv 228,108,60
ds_read_b128 v[80:83], v7 offset:8192
buffer_load_dwordx4 v0, s[48:51], s35 offen offset:2048 sc1 lds
Mvvv 136,112,32
s_waitcnt lgkmcnt(9)
ds_read_b64_tr_b16 v[96:97], v26 offset:33792
v_max_u32_dpp v10, v10, v10 quad_perm:[2,3,0,1] row_mask:0xf bank_mask:0xf bound_ctrl:1
Mvvv 136,116,36
ds_read_b128 v[84:87], v8 offset:8192
Mvvv 168,112,40
ds_read_b64_tr_b16 v[98:99], v26 offset:34048
buffer_load_dwordx4 v0, s[48:51], s35 offen offset:3072 sc1 lds
Mvvv 168,116,44
ds_read_b128 v[88:91], v7 offset:10240
v_max_u32_dpp v10, v10, v10 row_half_mirror row_mask:0xf bank_mask:0xf bound_ctrl:1
Mvvv 200,112,48
ds_read_b64_tr_b16 v[100:101], v26 offset:41984
v_cmp_eq_u32 vcc, v10, v22
Mvvv 200,116,52
s_waitcnt lgkmcnt(11)
ds_read_b128 v[92:95], v8 offset:10240
v_readlane_b32 s35, v4, 62
Mvvv 232,112,56
s_add_u32 s58, s75, 32768
ds_read_b64_tr_b16 v[102:103], v26 offset:42240
v_cndmask_b32 v22, v22, 0, vcc
Mvvv 232,116,60
s_waitcnt lgkmcnt(8)
s_and_b32 s58, s58, 0xc000
Mvvv 140,120,32
s_add_u32 s58, s58, s69
ds_read_b64_tr_b16 v[104:105], v27 offset:33792
v_max_u32_dpp v22, v22, v22 quad_perm:[1,0,3,2] row_mask:0xf bank_mask:0xf bound_ctrl:1
Mvvv 140,124,36
s_add_u32 m0, s58, 0
Mvvv 172,120,40
ds_read_b64_tr_b16 v[106:107], v27 offset:34048
v_max_u32_dpp v22, v22, v22 quad_perm:[2,3,0,1] row_mask:0xf bank_mask:0xf bound_ctrl:1
Mvvv 172,124,44
buffer_load_dwordx4 v0, s[48:51], s35 offen sc1 lds
Mvvv 204,120,48
ds_read_b64_tr_b16 v[108:109], v27 offset:41984
v_max_u32_dpp v22, v22, v22 row_half_mirror row_mask:0xf bank_mask:0xf bound_ctrl:1
Mvvv 204,124,52
v_readfirstlane_b32 s90, v10
v_readfirstlane_b32 s91, v22
Mvvv 236,120,56
ds_read_b64_tr_b16 v[110:111], v27 offset:42240
v_mov_b32 v9, s90
Mvvv 236,124,60
s_waitcnt lgkmcnt(8)
buffer_load_dwordx4 v0, s[48:51], s35 offen offset:1024 sc1 lds
Mvvv 144,96,32
s_waitcnt lgkmcnt(4)
ds_read_b64_tr_b16 v[112:113], v26 offset:34304
v_mov_b32 v10, s91
Mvvv 144,100,36
s_mov_b32 s18, 0xaaaaaaaa
s_mov_b32 s19, 0xaaaaaaaa
v_cndmask_b32 v9, v9, v10, s[18:19]
Mvvv 176,96,40
ds_read_b64_tr_b16 v[114:115], v26 offset:34560
v_not_b32 v9, v9
Mvvv 176,100,44
buffer_load_dwordx4 v0, s[48:51], s35 offen offset:2048 sc1 lds
Mvvv 208,96,48
ds_read_b64_tr_b16 v[116:117], v26 offset:42496
v_and_b32 v9, 0x7ff, v9
Mvvv 208,100,52
v_mul_u32_u24 v9, 2176, v9
v_lshrrev_b32 v10, 8, v0
Mvvv 240,96,56
s_waitcnt lgkmcnt(3)
ds_read_b64_tr_b16 v[118:119], v26 offset:42752
v_lshl_add_u32 v9, v10, 4, v9
Mvvv 240,100,60
s_lshr_b32 s59, s62, 3
buffer_load_dwordx4 v0, s[48:51], s35 offen offset:3072 sc1 lds
Mvvv 148,104,32
s_lshl2_add_u32 s59, s59, s57
s_lshl_b32 s59, s59, 7
ds_read_b64_tr_b16 v[120:121], v27 offset:34304
Mvvv 148,108,36
s_add_u32 s59, s59, 4194304
buffer_load_dwordx4 v[16:19], v9, s[48:51], s59 offen sc1
Mvvv 180,104,40
ds_read_b64_tr_b16 v[122:123], v27 offset:34560
Mvvv 180,108,44
s_waitcnt lgkmcnt(4)
buffer_load_dwordx4 v[20:23], v9, s[48:51], s59 offen offset:64 sc1
Mvvv 212,104,48
s_mov_b32 s34, 1
s_lshl_b32 s59, s63, 2
ds_read_b64_tr_b16 v[124:125], v27 offset:42496
Mvvv 212,108,52
s_waitcnt lgkmcnt(3)
s_lshr_b32 s64, 0xb8642100, s59
s_and_b32 s64, s64, 15
s_xor_b64 s[40:41], s[36:37], s[40:41]
Mvvv 244,104,56
s_bitcmp1_b32 s64, 0
s_cselect_b64 s[18:19], s[40:41], 0
ds_read_b64_tr_b16 v[126:127], v27 offset:42752
Mvvv 244,108,60
s_xor_b64 s[36:37], s[36:37], s[18:19]
s_lshr_b32 s59, s62, 3
s_lshl2_add_u32 s59, s59, s57
Mvvv 152,112,32
s_mul_i32 s59, s59, 458752
s_lshr_b32 s68, s64, 1
s_lshl3_add_u32 s68, s68, s65
Mvvv 152,116,36
s_lshl_b32 s68, s68, 7
s_add_u32 s68, s68, s59
v_readlane_b32 s35, v14, 62
Mvvv 184,112,40
s_add_u32 s58, s75, 0
s_add_u32 s77, s76, 0x20000
s_add_u32 s72, s34, 4
Mvvv 184,116,44
s_and_b32 s58, s58, 0xc000
s_mov_b32 s60, 57344
s_mov_b32 s61, 114688
Mvvv 216,112,48
s_add_u32 s58, s58, s69
s_bitcmp1_b32 s32, 8
s_cselect_b32 s72, s72, 0
Mvvv 216,116,52
s_waitcnt vmcnt(10) lgkmcnt(0)
s_add_u32 m0, s58, 65536
Mvvv 248,112,56
s_barrier
Mvvv 248,116,60
ds_read_b64_tr_b16 v[96:97], v26 offset:49152
buffer_load_dwordx4 v0, s[48:51], s35 offen sc1 lds
Mvvv 156,120,32
ds_read_b64_tr_b16 v[98:99], v26 offset:49408
Mvvv 156,124,36
ds_read_b64_tr_b16 v[100:101], v26 offset:57344
Mvvv 188,120,40
ds_read_b64_tr_b16 v[102:103], v26 offset:57600
Mvvv 188,124,44
buffer_load_dwordx4 v0, s[48:51], s35 offen offset:1024 sc1 lds
Mvvv 220,120,48
ds_read_b64_tr_b16 v[104:105], v27 offset:49152
Mvvv 220,124,52
ds_read_b64_tr_b16 v[106:107], v27 offset:49408
Mvvv 252,120,56
ds_read_b64_tr_b16 v[108:109], v27 offset:57344
buffer_load_dwordx4 v0, s[48:51], s35 offen offset:2048 sc1 lds
Mvvv 252,124,60
s_waitcnt lgkmcnt(5)
ds_read_b64_tr_b16 v[110:111], v27 offset:57600
Mvvv 128,96,64
s_waitcnt lgkmcnt(4)
ds_read_b64_tr_b16 v[112:113], v26 offset:49664
Mvvv 128,100,68
ds_read_b128 a[128:131], v1 offset:0
buffer_load_dwordx4 v0, s[48:51], s35 offen offset:3072 sc1 lds
Mvvv 160,96,72
ds_read_b64_tr_b16 v[114:115], v26 offset:49920
Mvvv 160,100,76
ds_read_b128 a[132:135], v1 offset:1024
Mvvv 192,96,80
ds_read_b64_tr_b16 v[116:117], v26 offset:57856
v_readlane_b32 s35, v4, 63
Mvvv 192,100,84
s_add_u32 s58, s75, 49152
ds_read_b128 a[136:139], v1 offset:2048
Mvvv 224,96,88
s_and_b32 s58, s58, 0xc000
ds_read_b64_tr_b16 v[118:119], v26 offset:58112
Mvvv 224,100,92
s_waitcnt lgkmcnt(9)
s_add_u32 s58, s58, s69
ds_read_b128 a[140:143], v1 offset:3072
Mvvv 132,104,64
s_waitcnt lgkmcnt(8)
s_add_u32 m0, s58, 0
ds_read_b64_tr_b16 v[120:121], v27 offset:49664
Mvvv 132,108,68
ds_read_b128 a[144:147], v1 offset:4096
buffer_load_dwordx4 v0, s[48:51], s35 offen sc1 lds
Mvvv 164,104,72
ds_read_b64_tr_b16 v[122:123], v27 offset:49920
Mvvv 164,108,76
ds_read_b128 a[148:151], v1 offset:5120
Mvvv 196,104,80
ds_read_b64_tr_b16 v[124:125], v27 offset:57856
buffer_load_dwordx4 v0, s[48:51], s35 offen offset:1024 sc1 lds
Mvvv 196,108,84
ds_read_b128 a[152:155], v1 offset:6144
Mvvv 228,104,88
s_waitcnt lgkmcnt(11)
ds_read_b64_tr_b16 v[126:127], v27 offset:58112
Mvvv 228,108,92
ds_read_b128 a[156:159], v1 offset:7168
buffer_load_dwordx4 v0, s[48:51], s35 offen offset:2048 sc1 lds
Mvvv 136,112,64
s_waitcnt lgkmcnt(9)
ds_read_b64_tr_b16 v[96:97], v26 offset:50176
Mvvv 136,116,68
ds_read_b128 a[160:163], v1 offset:8192
Mvvv 168,112,72
ds_read_b64_tr_b16 v[98:99], v26 offset:50432
buffer_load_dwordx4 v0, s[48:51], s35 offen offset:3072 sc1 lds
v_cvt_pk_bf16_f32 v128, v128, v129
Mvvv 168,116,76
ds_read_b128 a[164:167], v1 offset:9216
v_cvt_pk_bf16_f32 v129, v130, v131
v_cvt_pk_bf16_f32 v130, v132, v133
Mvvv 200,112,80
s_waitcnt lgkmcnt(9)
ds_read_b64_tr_b16 v[100:101], v26 offset:58368
v_cvt_pk_bf16_f32 v131, v134, v135
Mvvv 200,116,84
s_waitcnt lgkmcnt(6)
ds_read_b128 a[168:171], v1 offset:10240
v_cvt_pk_bf16_f32 v160, v160, v161
v_cvt_pk_bf16_f32 v161, v162, v163
Mvvv 232,112,88
ds_read_b64_tr_b16 v[102:103], v26 offset:58624
v_cvt_pk_bf16_f32 v162, v164, v165
v_readlane_b32 s35, v14, 63
Mvvv 232,116,92
s_add_u32 s58, s75, 16384
ds_read_b128 a[172:175], v1 offset:11264
v_cvt_pk_bf16_f32 v163, v166, v167
v_cvt_pk_bf16_f32 v192, v192, v193
Mvvv 140,120,64
s_and_b32 s58, s58, 0xc000
ds_read_b64_tr_b16 v[104:105], v27 offset:50176
v_cvt_pk_bf16_f32 v193, v194, v195
Mvvv 140,124,68
s_add_u32 s58, s58, s69
ds_read_b128 a[176:179], v1 offset:12288
v_cvt_pk_bf16_f32 v194, v196, v197
v_cvt_pk_bf16_f32 v195, v198, v199
Mvvv 172,120,72
s_add_u32 m0, s58, 65536
ds_read_b64_tr_b16 v[106:107], v27 offset:50432
v_cvt_pk_bf16_f32 v224, v224, v225
Mvvv 172,124,76
ds_read_b128 a[180:183], v1 offset:13312
v_cvt_pk_bf16_f32 v225, v226, v227
v_cvt_pk_bf16_f32 v226, v228, v229
Mvvv 204,120,80
ds_read_b64_tr_b16 v[108:109], v27 offset:58368
v_cvt_pk_bf16_f32 v227, v230, v231
Mvvv 204,124,84
ds_read_b128 a[184:187], v1 offset:14336
buffer_load_dwordx4 v0, s[48:51], s35 offen sc1 lds
Mvvv 236,120,88
s_waitcnt lgkmcnt(12)
ds_read_b64_tr_b16 v[110:111], v27 offset:58624
Mvvv 236,124,92
s_waitcnt lgkmcnt(12)
ds_read_b128 a[188:191], v1 offset:15360
Mvvv 144,96,64
s_waitcnt lgkmcnt(9)
ds_read_b64_tr_b16 v[112:113], v26 offset:50688
Mvvv 144,100,68
buffer_load_dwordx4 v0, s[48:51], s35 offen offset:1024 sc1 lds
Mvvv 176,96,72
)"
R"(
ds_read_b64_tr_b16 v[114:115], v26 offset:50944
v_cvt_pk_bf16_f32 v136, v136, v137
Mvvv 176,100,76
v_cvt_pk_bf16_f32 v137, v138, v139
v_cvt_pk_bf16_f32 v138, v140, v141
Mvvv 208,96,80
ds_read_b64_tr_b16 v[116:117], v26 offset:58880
buffer_load_dwordx4 v0, s[48:51], s35 offen offset:2048 sc1 lds
v_cvt_pk_bf16_f32 v139, v142, v143
Mvvv 208,100,84
v_cvt_pk_bf16_f32 v168, v168, v169
v_cvt_pk_bf16_f32 v169, v170, v171
Mvvv 240,96,88
s_waitcnt lgkmcnt(8)
ds_read_b64_tr_b16 v[118:119], v26 offset:59136
v_cvt_pk_bf16_f32 v170, v172, v173
Mvvv 240,100,92
buffer_load_dwordx4 v0, s[48:51], s35 offen offset:3072 sc1 lds
v_cvt_pk_bf16_f32 v171, v174, v175
v_cvt_pk_bf16_f32 v200, v200, v201
Mvvv 148,104,64
s_waitcnt lgkmcnt(5)
ds_read_b64_tr_b16 v[120:121], v27 offset:50688
v_cvt_pk_bf16_f32 v201, v202, v203
Mvvv 148,108,68
v_cvt_pk_bf16_f32 v202, v204, v205
v_cvt_pk_bf16_f32 v203, v206, v207
Mvvv 180,104,72
ds_read_b64_tr_b16 v[122:123], v27 offset:50944
v_cvt_pk_bf16_f32 v232, v232, v233
Mvvv 180,108,76
v_cvt_pk_bf16_f32 v233, v234, v235
v_cvt_pk_bf16_f32 v234, v236, v237
Mvvv 212,104,80
ds_read_b64_tr_b16 v[124:125], v27 offset:58880
v_cvt_pk_bf16_f32 v235, v238, v239
Mvvv 212,108,84
Mvvv 244,104,88
ds_read_b64_tr_b16 v[126:127], v27 offset:59136
Mvvv 244,108,92
s_waitcnt lgkmcnt(6)
Mvvv 152,112,64
s_waitcnt lgkmcnt(4)
Mvvv 152,116,68
Mvvv 184,112,72
v_cvt_pk_bf16_f32 v144, v144, v145
v_cvt_pk_bf16_f32 v145, v146, v147
Mvvv 184,116,76
v_cvt_pk_bf16_f32 v146, v148, v149
v_cvt_pk_bf16_f32 v147, v150, v151
Mvvv 216,112,80
v_cvt_pk_bf16_f32 v176, v176, v177
v_cvt_pk_bf16_f32 v177, v178, v179
Mvvv 216,116,84
s_waitcnt lgkmcnt(0)
v_cvt_pk_bf16_f32 v178, v180, v181
v_cvt_pk_bf16_f32 v179, v182, v183
Mvvv 248,112,88
s_barrier
v_cvt_pk_bf16_f32 v208, v208, v209
v_cvt_pk_bf16_f32 v209, v210, v211
Mvvv 248,116,92
v_cvt_pk_bf16_f32 v210, v212, v213
v_cvt_pk_bf16_f32 v211, v214, v215
Mvvv 156,120,64
v_cvt_pk_bf16_f32 v240, v240, v241
v_cvt_pk_bf16_f32 v241, v242, v243
Mvvv 156,124,68
v_cvt_pk_bf16_f32 v242, v244, v245
v_cvt_pk_bf16_f32 v243, v246, v247
Mvvv 188,120,72
Mvvv 188,124,76
Mvvv 220,120,80
Mvvv 220,124,84
Mvvv 252,120,88
Mvvv 252,124,92
v_cvt_pk_bf16_f32 v152, v152, v153
v_cvt_pk_bf16_f32 v153, v154, v155
Zvav 32,128,128
v_cvt_pk_bf16_f32 v154, v156, v157
v_cvt_pk_bf16_f32 v155, v158, v159
Mvav 32,132,136
v_mov_b32 v12, v4
v_mov_b32 v3, v0
Mvav 32,136,144
v_and_b32 v26, 48, v0
v_mov_b32 v27, 0x3f803f80
Mvav 32,140,152
v_cvt_pk_bf16_f32 v184, v184, v185
v_cvt_pk_bf16_f32 v185, v186, v187
Zvav 48,144,128
v_cvt_pk_bf16_f32 v186, v188, v189
v_cvt_pk_bf16_f32 v187, v190, v191
Mvav 48,148,136
v_cmp_eq_u32 vcc, 0, v26
v_mov_b32 v112, 0
Mvav 48,152,144
v_cvt_pk_bf16_f32 v216, v216, v217
v_cvt_pk_bf16_f32 v217, v218, v219
Mvav 48,156,152
v_cvt_pk_bf16_f32 v218, v220, v221
v_cvt_pk_bf16_f32 v219, v222, v223
Zvav 36,128,160
v_cvt_pk_bf16_f32 v248, v248, v249
v_cvt_pk_bf16_f32 v249, v250, v251
Mvav 36,132,168
v_cvt_pk_bf16_f32 v250, v252, v253
v_cvt_pk_bf16_f32 v251, v254, v255
Mvav 36,136,176
v_cndmask_b32 v96, 0, v27, vcc
v_cndmask_b32 v97, 0, v27, vcc
Mvav 36,140,184
v_cndmask_b32 v98, 0, v27, vcc
v_cndmask_b32 v99, 0, v27, vcc
Zvav 52,144,160
v_max3_f32 v4, v32, v33, v34
v_max3_f32 v4, v4, v35, v48
Mvav 52,148,168
v_max3_f32 v4, v4, v49, v50
v_max_f32 v4, v4, v51
Mvav 52,152,176
v_mov_b32 v64, v4
v_cmp_eq_u32 vcc, 16, v26
Mvav 52,156,184
v_mov_b32 v113, 0
v_mov_b32 v114, 0
Zvav 40,128,192
v_permlane16_swap_b32 v4, v64
v_max_f32 v4, v4, v64
Mvav 40,132,200
v_mov_b32 v64, v4
v_cndmask_b32 v100, 0, v27, vcc
Mvav 40,136,208
v_cndmask_b32 v101, 0, v27, vcc
v_cndmask_b32 v102, 0, v27, vcc
Mvav 40,140,216
v_permlane32_swap_b32 v4, v64
v_max_f32 v4, v4, v64
Zvav 56,144,192
v_sub_f32 v7, 0, v4
v_sub_f32 v6, 0, v4
Mvav 56,148,200
v_sub_f32 v5, 0, v4
v_sub_f32 v4, 0, v4
Mvav 56,152,208
v_add_f32 v32, v32, v4
v_add_f32 v33, v33, v4
Mvav 56,156,216
v_exp_f32 v120, v32
Zvav 44,128,224
v_exp_f32 v121, v33
Mvav 44,132,232
v_add_f32 v34, v34, v4
v_add_f32 v35, v35, v4
Mvav 44,136,240
v_exp_f32 v122, v34
Mvav 44,140,248
v_exp_f32 v123, v35
Zvav 60,144,224
v_cvt_pk_bf16_f32 v196, v120, v121
v_cvt_pk_bf16_f32 v197, v122, v123
Mvav 60,148,232
v_add_f32 v48, v48, v4
v_add_f32 v49, v49, v4
Mvav 60,152,240
v_exp_f32 v80, v48
Mvav 60,156,248
v_exp_f32 v81, v49
v_add_f32 v50, v50, v4
v_add_f32 v51, v51, v4
v_exp_f32 v82, v50
v_exp_f32 v83, v51
v_cvt_pk_bf16_f32 v198, v80, v81
v_readlane_b32 s35, v12, s27
v_readlane_b32 s81, v14, s27
s_or_b32 m0, s69, s75
s_nop 2
buffer_load_dwordx4 v0, s[48:51], s35 offen sc1 lds
v_cvt_pk_bf16_f32 v199, v82, v83
v_max3_f32 v8, v36, v37, v38
v_max3_f32 v8, v8, v39, v52
v_max3_f32 v8, v8, v53, v54
v_max_f32 v8, v8, v55
v_mov_b32 v65, v8
s_nop 1
v_permlane16_swap_b32 v8, v65
v_max_f32 v8, v8, v65
v_mov_b32 v65, v8
buffer_load_dwordx4 v0, s[48:51], s35 offen offset:1024 sc1 lds
s_nop 0
v_permlane32_swap_b32 v8, v65
v_max_f32 v8, v8, v65
v_sub_f32 v11, 0, v8
v_sub_f32 v10, 0, v8
v_sub_f32 v9, 0, v8
v_sub_f32 v8, 0, v8
v_add_f32 v36, v36, v8
v_add_f32 v37, v37, v8
v_add_f32 v38, v38, v8
buffer_load_dwordx4 v0, s[48:51], s35 offen offset:2048 sc1 lds
v_add_f32 v39, v39, v8
v_exp_f32 v68, v36
v_exp_f32 v69, v37
v_exp_f32 v70, v38
v_exp_f32 v71, v39
v_cvt_pk_bf16_f32 v204, v68, v69
v_cvt_pk_bf16_f32 v205, v70, v71
v_add_f32 v52, v52, v8
v_add_f32 v53, v53, v8
buffer_load_dwordx4 v0, s[48:51], s35 offen offset:3072 sc1 lds
v_add_f32 v54, v54, v8
v_add_f32 v55, v55, v8
v_exp_f32 v84, v52
v_exp_f32 v85, v53
v_exp_f32 v86, v54
v_exp_f32 v87, v55
v_cvt_pk_bf16_f32 v206, v84, v85
v_cvt_pk_bf16_f32 v207, v86, v87
v_cndmask_b32 v103, 0, v27, vcc
s_xor_b32 s58, s75, 0x18000
s_or_b32 m0, s69, s58
s_nop 0
buffer_load_dwordx4 v3, s[12:15], s81 offen sc1 lds
v_cmp_eq_u32 vcc, 32, v26
s_nop 1
v_cndmask_b32 v104, 0, v27, vcc
v_cndmask_b32 v105, 0, v27, vcc
v_cndmask_b32 v106, 0, v27, vcc
v_cndmask_b32 v107, 0, v27, vcc
v_cmp_eq_u32 vcc, 48, v26
v_mov_b32 v115, 0
s_nop 0
v_cndmask_b32 v108, 0, v27, vcc
v_cndmask_b32 v109, 0, v27, vcc
s_add_u32 s70, s81, s24
buffer_load_dwordx4 v3, s[12:15], s70 offen offset:1024 sc1 lds
v_cndmask_b32 v110, 0, v27, vcc
v_cndmask_b32 v111, 0, v27, vcc
s_lshl2_add_u32 s59, s63, s57
s_cmp_eq_u32 s59, 0
s_cbranch_scc1 .Lm0
.Lm0_r:
v_max3_f32 v132, v40, v41, v42
v_max3_f32 v140, v44, v45, v46
v_max3_f32 v132, v132, v43, v56
s_add_u32 s70, s81, s25
buffer_load_dwordx4 v3, s[12:15], s70 offen offset:2048 sc1 lds
v_max3_f32 v140, v140, v47, v60
v_max3_f32 v132, v132, v57, v58
v_max3_f32 v140, v140, v61, v62
v_max_f32 v132, v132, v59
v_max_f32 v140, v140, v63
v_mov_b32 v66, v132
v_mov_b32 v67, v140
s_nop 0
v_permlane16_swap_b32 v132, v66
v_permlane16_swap_b32 v140, v67
s_add_u32 s70, s81, s26
buffer_load_dwordx4 v3, s[12:15], s70 offen offset:3072 sc1 lds
v_max_f32 v132, v132, v66
v_max_f32 v140, v140, v67
v_mov_b32 v66, v132
v_mov_b32 v67, v140
s_nop 0
v_permlane32_swap_b32 v132, v66
v_permlane32_swap_b32 v140, v67
v_max_f32 v132, v132, v66
v_max_f32 v140, v140, v67
v_sub_f32 v135, 0, v132
s_add_u32 s88, s88, 0x60000
buffer_load_dwordx4 v[28:31], v0, s[20:23], s88 offen
v_sub_f32 v143, 0, v140
v_sub_f32 v134, 0, v132
v_sub_f32 v142, 0, v140
v_sub_f32 v133, 0, v132
v_sub_f32 v141, 0, v140
v_sub_f32 v132, 0, v132
v_sub_f32 v140, 0, v140
v_add_f32 v40, v40, v132
v_add_f32 v44, v44, v140
buffer_load_dwordx4 v[28:31], v0, s[20:23], s88 offen offset:1024
v_add_f32 v41, v41, v132
v_add_f32 v45, v45, v140
v_add_f32 v42, v42, v132
v_add_f32 v46, v46, v140
v_add_f32 v43, v43, v132
v_add_f32 v47, v47, v140
v_exp_f32 v72, v40
v_exp_f32 v76, v44
v_exp_f32 v73, v41
buffer_load_dwordx4 v[28:31], v0, s[20:23], s88 offen offset:2048
v_exp_f32 v77, v45
v_exp_f32 v74, v42
v_exp_f32 v78, v46
v_exp_f32 v75, v43
v_exp_f32 v79, v47
v_cvt_pk_bf16_f32 v212, v72, v73
v_cvt_pk_bf16_f32 v220, v76, v77
v_cvt_pk_bf16_f32 v213, v74, v75
v_cvt_pk_bf16_f32 v221, v78, v79
v_add_f32 v56, v56, v132
v_add_f32 v60, v60, v140
v_add_f32 v57, v57, v132
v_add_f32 v61, v61, v140
v_add_f32 v58, v58, v132
v_add_f32 v62, v62, v140
v_add_f32 v59, v59, v132
v_add_f32 v63, v63, v140
v_exp_f32 v88, v56
v_exp_f32 v92, v60
v_exp_f32 v89, v57
v_exp_f32 v93, v61
v_exp_f32 v90, v58
v_exp_f32 v94, v62
v_exp_f32 v91, v59
v_exp_f32 v95, v63
v_cvt_pk_bf16_f32 v214, v88, v89
v_cvt_pk_bf16_f32 v222, v92, v93
v_cvt_pk_bf16_f32 v215, v90, v91
v_cvt_pk_bf16_f32 v223, v94, v95
s_waitcnt lgkmcnt(0)
s_waitcnt vmcnt(11)
s_cmp_eq_u32 s99, 0
s_cbranch_scc1 .Lm1
s_branch .Lloop1
.p2align 6
.Lloop0:
Cvav 32,128,128,4
s_waitcnt vmcnt(28)
s_barrier
Mvav 32,132,136
v_readlane_b32 s35, v12, s27
v_readlane_b32 s81, v14, s27
Mvav 32,136,144
Mvav 32,140,152
Cvvv 112,96,228,116
Mvvv 112,100,236
s_or_b32 m0, s69, s75
Mvvv 112,104,244
buffer_load_dwordx4 v0, s[48:51], s35 offen sc1 lds
Mvvv 112,108,252
buffer_load_dwordx4 v0, s[48:51], s35 offen offset:1024 sc1 lds
Cvav 36,128,160,8
Mvav 36,132,168
buffer_load_dwordx4 v0, s[48:51], s35 offen offset:2048 sc1 lds
Mvav 36,136,176
v_exp_f32 v120, v32
Mvav 36,140,184
Cvav 40,128,192,132
v_exp_f32 v121, v33
Mvav 40,132,200
v_add_f32 v26, v112, v113
v_add_f32 v27, v114, v115
Mvav 40,136,208
v_exp_f32 v122, v34
Mvav 40,140,216
v_add_f32 v26, v26, v27
v_cmp_nge_f32 vcc, 0x53800000, v26
Cvav 44,128,224,140
v_exp_f32 v123, v35
Mvav 44,132,232
buffer_load_dwordx4 v0, s[48:51], s35 offen offset:3072 sc1 lds
Mvav 44,136,240
v_exp_f32 v124, v36
Mvav 44,140,248
s_xor_b32 s58, s75, 0x18000
v_cvt_pk_bf16_f32 v196, v120, v121
v_cvt_pk_bf16_f32 v197, v122, v123
Cvav 48,144,128,4
s_or_b32 m0, s69, s58
v_exp_f32 v125, v37
Mvav 48,148,136
s_or_b32 s80, s75, 0x10000
buffer_load_dwordx4 v3, s[12:15], s81 offen sc1 lds
Mvav 48,152,144
v_exp_f32 v126, v38
Mvav 48,156,152
v_cvt_pk_bf16_f32 v204, v124, v125
Cvav 52,144,160,8
s_add_u32 s70, s81, s24
v_exp_f32 v127, v39
Mvav 52,148,168
buffer_load_dwordx4 v3, s[12:15], s70 offen offset:1024 sc1 lds
Mvav 52,152,176
v_exp_f32 v120, v40
Mvav 52,156,184
v_cvt_pk_bf16_f32 v205, v126, v127
Cvav 56,144,192,132
s_add_u32 s70, s81, s25
v_exp_f32 v121, v41
Mvav 56,148,200
buffer_load_dwordx4 v3, s[12:15], s70 offen offset:2048 sc1 lds
Mvav 56,152,208
v_exp_f32 v122, v42
Mvav 56,156,216
v_cvt_pk_bf16_f32 v212, v120, v121
v_add_u32 v2, s80, v0
Cvav 60,144,224,140
s_add_u32 s70, s81, s26
v_exp_f32 v123, v43
Mvav 60,148,232
buffer_load_dwordx4 v3, s[12:15], s70 offen offset:3072 sc1 lds
Mvav 60,152,240
v_exp_f32 v124, v44
Mvav 60,156,248
s_cbranch_vccnz .Lab0
.Lab0_r:
Maav 0,196,228
v_exp_f32 v125, v45
Maav 4,196,236
s_cmp_lt_u32 s34, s72
s_cselect_b32 m0, s77, 0x30000
v_cvt_pk_bf16_f32 v213, v122, v123
Maav 64,196,244
v_exp_f32 v126, v46
Maav 68,196,252
buffer_load_dwordx4 v25, s[36:39], s68 offen nt lds
Maav 8,204,228
v_exp_f32 v127, v47
Maav 12,204,236
s_add_u32 m0, m0, 1024
v_cvt_pk_bf16_f32 v220, v124, v125
v_cvt_pk_bf16_f32 v221, v126, v127
Maav 72,204,244
s_add_u32 s70, s68, s60
v_exp_f32 v120, v48
Maav 76,204,252
buffer_load_dwordx4 v25, s[36:39], s70 offen nt lds
Maav 16,212,228
v_exp_f32 v121, v49
Maav 20,212,236
v_cvt_pk_bf16_f32 v198, v120, v121
Maav 80,212,244
s_add_u32 s88, s88, 0x60000
v_exp_f32 v122, v50
Maav 84,212,252
buffer_load_dwordx4 v[28:31], v0, s[20:23], s88 offen
Maav 24,220,228
v_exp_f32 v123, v51
Maav 28,220,236
v_cvt_pk_bf16_f32 v199, v122, v123
Maav 88,220,244
s_add_u32 s59, s34, 2
v_exp_f32 v124, v52
Maav 92,220,252
buffer_load_dwordx4 v[28:31], v0, s[20:23], s88 offen offset:1024
Maav 32,228,228
v_exp_f32 v125, v53
Maav 36,228,236
s_cmp_eq_u32 s59, s99
s_cselect_b32 s59, s98, 0
v_cvt_pk_bf16_f32 v206, v124, v125
Maav 96,228,244
s_add_u32 s79, s75, 0x4000
v_exp_f32 v126, v54
Maav 100,228,252
s_add_u32 s79, s79, s59
buffer_load_dwordx4 v[28:31], v0, s[20:23], s88 offen offset:2048
Maav 40,236,228
s_and_b32 s79, s79, 0xc000
v_exp_f32 v127, v55
Maav 44,236,236
s_add_u32 s34, s34, 1
s_add_u32 s68, s68, s61
v_cvt_pk_bf16_f32 v207, v126, v127
Maav 104,236,244
s_add_u32 s77, s77, 2048
v_exp_f32 v120, v56
Maav 108,236,252
v_exp_f32 v121, v57
Maav 48,244,228
v_exp_f32 v122, v58
Maav 52,244,236
)"
R"(
v_exp_f32 v123, v59
Maav 112,244,244
v_exp_f32 v124, v60
Maav 116,244,252
v_exp_f32 v125, v61
Maav 56,252,228
v_exp_f32 v126, v62
Maav 60,252,236
v_exp_f32 v127, v63
Maav 120,252,244
s_waitcnt lgkmcnt(0)
v_cvt_pk_bf16_f32 v214, v120, v121
v_cvt_pk_bf16_f32 v215, v122, v123
Maav 124,252,252
v_cvt_pk_bf16_f32 v222, v124, v125
v_cvt_pk_bf16_f32 v223, v126, v127
.Lloop1:
Cvav 64,160,128,4
s_waitcnt vmcnt(28)
s_barrier
Mvav 64,164,136
ds_read_b128 a[192:195], v2 offset:0
v_add_u32 v1, s79, v0
Mvav 64,168,144
ds_read_b128 a[200:203], v2 offset:2048
Mvav 64,172,152
ds_read_b128 a[208:211], v2 offset:4096
Cvvv 116,96,196,112
ds_read_b128 a[216:219], v2 offset:6144
Mvvv 116,100,204
ds_read_b128 a[224:227], v2 offset:8192
Mvvv 116,104,212
ds_read_b128 a[232:235], v2 offset:10240
Mvvv 116,108,220
ds_read_b128 a[240:243], v2 offset:12288
Cvav 68,160,160,8
v_exp_f32 v120, v64
Mvav 68,164,168
ds_read_b128 a[248:251], v2 offset:14336
Mvav 68,168,176
v_exp_f32 v121, v65
Mvav 68,172,184
ds_read_b128 a[196:199], v2 offset:1024
v_cvt_pk_bf16_f32 v228, v120, v121
Cvav 72,160,192,132
v_exp_f32 v122, v66
Mvav 72,164,200
ds_read_b128 a[204:207], v2 offset:3072
v_add_f32 v26, v116, v117
Mvav 72,168,208
v_exp_f32 v123, v67
Mvav 72,172,216
ds_read_b128 a[212:215], v2 offset:5120
v_add_f32 v27, v118, v119
Cvav 76,160,224,140
v_exp_f32 v124, v68
Mvav 76,164,232
ds_read_b128 a[220:223], v2 offset:7168
v_add_f32 v26, v26, v27
Mvav 76,168,240
v_exp_f32 v125, v69
Mvav 76,172,248
ds_read_b128 a[228:231], v2 offset:9216
v_cmp_nge_f32 vcc, 0x53800000, v26
Cvav 80,176,128,4
s_or_b32 s80, s75, 0x10000
v_exp_f32 v126, v70
Mvav 80,180,136
ds_read_b128 a[236:239], v2 offset:11264
v_cvt_pk_bf16_f32 v229, v122, v123
Mvav 80,184,144
v_exp_f32 v127, v71
Mvav 80,188,152
s_cmp_lt_u32 s34, s72
s_cselect_b32 m0, s77, 0x30000
ds_read_b128 a[244:247], v2 offset:13312
Cvav 84,176,160,8
s_waitcnt lgkmcnt(12)
v_exp_f32 v120, v72
Mvav 84,180,168
ds_read_b128 a[252:255], v2 offset:15360
buffer_load_dwordx4 v25, s[36:39], s68 offen nt lds
Mvav 84,184,176
v_exp_f32 v121, v73
Mvav 84,188,184
ds_read_b128 a[128:131], v1 offset:0
v_cvt_pk_bf16_f32 v236, v124, v125
Cvav 88,176,192,132
s_waitcnt lgkmcnt(11)
v_exp_f32 v122, v74
Mvav 88,180,200
s_add_u32 m0, m0, 1024
ds_read_b128 a[132:135], v1 offset:1024
v_cvt_pk_bf16_f32 v237, v126, v127
Mvav 88,184,208
s_add_u32 s70, s68, s60
v_exp_f32 v123, v75
Mvav 88,188,216
ds_read_b128 a[136:139], v1 offset:2048
buffer_load_dwordx4 v25, s[36:39], s70 offen nt lds
Cvav 92,176,224,140
v_exp_f32 v124, v76
Mvav 92,180,232
ds_read_b128 a[140:143], v1 offset:3072
v_cvt_pk_bf16_f32 v244, v120, v121
Mvav 92,184,240
v_exp_f32 v125, v77
Mvav 92,188,248
s_cbranch_vccnz .Lab1
.Lab1_r:
Maav 0,192,196
v_exp_f32 v126, v78
Maav 4,192,204
ds_read_b128 a[144:147], v1 offset:4096
v_cvt_pk_bf16_f32 v245, v122, v123
Maav 64,192,212
v_exp_f32 v127, v79
Maav 68,192,220
s_waitcnt lgkmcnt(12)
ds_read_b128 a[148:151], v1 offset:5120
v_cvt_pk_bf16_f32 v252, v124, v125
Maav 8,200,196
v_exp_f32 v120, v80
Maav 12,200,204
ds_read_b128 a[152:155], v1 offset:6144
v_cvt_pk_bf16_f32 v253, v126, v127
Maav 72,200,212
v_exp_f32 v121, v81
Maav 76,200,220
ds_read_b128 a[156:159], v1 offset:7168
v_cvt_pk_bf16_f32 v230, v120, v121
Maav 16,208,196
v_exp_f32 v122, v82
Maav 20,208,204
s_waitcnt lgkmcnt(12)
ds_read_b128 a[160:163], v1 offset:8192
v_add_u32 v2, s80, v0
Maav 80,208,212
v_exp_f32 v123, v83
Maav 84,208,220
ds_read_b128 a[164:167], v1 offset:9216
v_cvt_pk_bf16_f32 v231, v122, v123
Maav 24,216,196
v_exp_f32 v124, v84
Maav 28,216,204
s_waitcnt lgkmcnt(11)
ds_read_b128 a[168:171], v1 offset:10240
Maav 88,216,212
s_add_u32 s27, s27, 1
v_exp_f32 v125, v85
Maav 92,216,220
s_add_u32 s75, s75, 0x4000
ds_read_b128 a[172:175], v1 offset:11264
v_cvt_pk_bf16_f32 v238, v124, v125
Maav 32,224,196
s_and_b32 s75, s75, 0xc000
v_exp_f32 v126, v86
Maav 36,224,204
s_add_u32 s79, s75, 0x4000
s_and_b32 s79, s79, 0xc000
ds_read_b128 a[176:179], v1 offset:12288
Maav 96,224,212
s_add_u32 s34, s34, 1
v_exp_f32 v127, v87
Maav 100,224,220
s_add_u32 s68, s68, s61
ds_read_b128 a[180:183], v1 offset:13312
v_cvt_pk_bf16_f32 v239, v126, v127
Maav 40,232,196
s_add_u32 s77, s77, 2048
v_exp_f32 v120, v88
Maav 44,232,204
s_waitcnt lgkmcnt(12)
s_cmp_lt_u32 s34, s99
ds_read_b128 a[184:187], v1 offset:14336
Maav 104,232,212
s_cselect_b32 s59, s32, -1
ds_read_b128 a[188:191], v1 offset:15360
Maav 108,232,220
s_bitcmp1_b32 s59, s34
v_exp_f32 v121, v89
Maav 48,240,196
v_exp_f32 v122, v90
Maav 52,240,204
v_exp_f32 v123, v91
Maav 112,240,212
v_exp_f32 v124, v92
Maav 116,240,220
v_exp_f32 v125, v93
Maav 56,248,196
v_exp_f32 v126, v94
Maav 60,248,204
v_exp_f32 v127, v95
Maav 120,248,212
s_waitcnt lgkmcnt(0)
v_cvt_pk_bf16_f32 v246, v120, v121
v_cvt_pk_bf16_f32 v247, v122, v123
Maav 124,248,220
v_cvt_pk_bf16_f32 v254, v124, v125
v_cvt_pk_bf16_f32 v255, v126, v127
s_cbranch_scc0 .Lloop0
s_bitcmp1_b64 s[32:33], s34
s_cbranch_scc1 .Lhab1
.Lhab1_r:
s_cmp_lt_u32 s34, s99
s_cbranch_scc1 .Lloop0
Cvav 32,128,128,4
s_waitcnt vmcnt(20)
s_barrier
Mvav 32,132,136
v_bfe_u32 v172, v0, 4, 4
v_lshrrev_b32 v173, 8, v0
Mvav 32,136,144
v_lshlrev_b32 v173, 2, v173
v_sub_u32 v173, v172, v173
Mvav 32,140,152
v_mov_b32 v174, 0xff800000
v_cndmask_b32 v172, v173, 64, s[4:5]
Cvvv 112,96,228,116
v_cmp_gt_i32 vcc, 0, v172
v_cndmask_b32 v156, v174, v132, s[4:5]
Mvvv 112,100,236
v_readlane_b32 s35, v12, s27
v_readlane_b32 s81, v14, s27
Mvvv 112,104,244
v_cndmask_b32 v148, v132, v174, vcc
v_cndmask_b32 v164, v140, v174, vcc
Mvvv 112,108,252
v_cmp_gt_i32 vcc, 1, v172
v_cndmask_b32 v157, v174, v132, s[4:5]
Cvav 36,128,160,8
Mvav 36,132,168
v_cndmask_b32 v149, v132, v174, vcc
v_cndmask_b32 v165, v140, v174, vcc
Mvav 36,136,176
v_exp_f32 v120, v32
Mvav 36,140,184
v_cmp_gt_i32 vcc, 2, v172
v_cndmask_b32 v158, v174, v132, s[4:5]
Cvav 44,128,224,140
v_exp_f32 v121, v33
Mvav 44,132,232
v_cndmask_b32 v150, v132, v174, vcc
v_cndmask_b32 v166, v140, v174, vcc
v_cvt_pk_bf16_f32 v196, v120, v121
Mvav 44,136,240
v_exp_f32 v122, v34
Mvav 44,140,248
v_cmp_gt_i32 vcc, 3, v172
v_cndmask_b32 v159, v174, v132, s[4:5]
Cvav 48,144,128,4
v_exp_f32 v123, v35
Mvav 48,148,136
v_cndmask_b32 v151, v132, v174, vcc
v_cndmask_b32 v167, v140, v174, vcc
Mvav 48,152,144
v_exp_f32 v124, v36
Mvav 48,156,152
v_add_f32 v26, v112, v113
v_add_f32 v27, v114, v115
Cvav 52,144,160,8
s_add_u32 s80, s75, s98
v_exp_f32 v125, v37
Mvav 52,148,168
s_and_b32 s80, s80, 0xc000
v_add_f32 v26, v26, v27
v_cmp_nge_f32 vcc, 0x53800000, v26
Mvav 52,152,176
s_or_b32 m0, s69, s75
v_exp_f32 v126, v38
Mvav 52,156,184
s_or_b32 s80, s80, 0x10000
buffer_load_dwordx4 v0, s[48:51], s35 offen sc1 lds
Cvav 40,128,192,148
v_exp_f32 v127, v39
Mvav 40,132,200
buffer_load_dwordx4 v0, s[48:51], s35 offen offset:1024 sc1 lds
Mvav 40,136,208
v_exp_f32 v120, v44
Mvav 40,140,216
v_cvt_pk_bf16_f32 v197, v122, v123
v_cvt_pk_bf16_f32 v204, v124, v125
Cvav 56,144,192,156
v_exp_f32 v121, v45
Mvav 56,148,200
buffer_load_dwordx4 v0, s[48:51], s35 offen offset:2048 sc1 lds
Mvav 56,152,208
v_exp_f32 v122, v46
Mvav 56,156,216
buffer_load_dwordx4 v0, s[48:51], s35 offen offset:3072 sc1 lds
Cvav 60,144,224,164
v_exp_f32 v123, v47
Mvav 60,148,232
v_cvt_pk_bf16_f32 v205, v126, v127
v_cvt_pk_bf16_f32 v220, v120, v121
Mvav 60,152,240
v_exp_f32 v124, v48
Mvav 60,156,248
s_xor_b32 s58, s75, 0x18000
s_cbranch_vccnz .Ld0
.Ld0_r:
Maav 0,196,228
s_or_b32 m0, s69, s58
v_exp_f32 v125, v49
v_bfe_u32 v64, v0, 4, 4
v_lshrrev_b32 v65, 8, v0
Maav 4,196,236
buffer_load_dwordx4 v3, s[12:15], s81 offen sc1 lds
Maav 64,196,244
s_add_u32 s70, s81, s24
v_exp_f32 v126, v50
v_lshlrev_b32 v65, 2, v65
v_sub_u32 v65, v64, v65
Maav 68,196,252
buffer_load_dwordx4 v3, s[12:15], s70 offen offset:1024 sc1 lds
Maav 8,204,228
v_exp_f32 v127, v51
v_mov_b32 v66, 0xff800000
v_cndmask_b32 v65, -16, v65, s[4:5]
Maav 12,204,236
v_cvt_pk_bf16_f32 v221, v122, v123
v_cvt_pk_bf16_f32 v198, v124, v125
v_cmp_gt_i32 vcc, 0, v65
v_cndmask_b32 v180, v66, v140, s[4:5]
Maav 72,204,244
s_add_u32 s70, s81, s25
v_exp_f32 v120, v52
v_cndmask_b32 v172, v132, v66, vcc
v_cndmask_b32 v188, v140, v66, vcc
Maav 76,204,252
buffer_load_dwordx4 v3, s[12:15], s70 offen offset:2048 sc1 lds
Maav 16,212,228
s_add_u32 s70, s81, s26
v_exp_f32 v121, v53
v_cmp_gt_i32 vcc, 1, v65
v_cndmask_b32 v181, v66, v140, s[4:5]
Maav 20,212,236
buffer_load_dwordx4 v3, s[12:15], s70 offen offset:3072 sc1 lds
Maav 80,212,244
v_exp_f32 v122, v54
v_cndmask_b32 v173, v132, v66, vcc
v_cndmask_b32 v189, v140, v66, vcc
Maav 84,212,252
s_cmp_lt_u32 s34, s72
s_cselect_b32 m0, s77, 0x30000
v_cvt_pk_bf16_f32 v199, v126, v127
v_cmp_gt_i32 vcc, 2, v65
v_cndmask_b32 v182, v66, v140, s[4:5]
Maav 24,220,228
v_exp_f32 v123, v55
v_cndmask_b32 v174, v132, v66, vcc
v_cndmask_b32 v190, v140, v66, vcc
Maav 28,220,236
buffer_load_dwordx4 v25, s[36:39], s68 offen nt lds
Maav 88,220,244
s_add_u32 m0, m0, 1024
s_add_u32 s70, s68, s60
v_exp_f32 v124, v40
v_cmp_gt_i32 vcc, 3, v65
v_cndmask_b32 v183, v66, v140, s[4:5]
Maav 92,220,252
s_add_u32 s79, s75, 0x4000
buffer_load_dwordx4 v25, s[36:39], s70 offen nt lds
Maav 32,228,228
s_add_u32 s79, s79, s98
v_exp_f32 v125, v41
Maav 36,228,236
s_and_b32 s79, s79, 0xc000
v_cvt_pk_bf16_f32 v206, v120, v121
v_cvt_pk_bf16_f32 v207, v122, v123
Maav 96,228,244
s_add_u32 s34, s34, 1
v_exp_f32 v126, v42
Maav 100,228,252
s_add_u32 s68, s68, s61
v_cvt_pk_bf16_f32 v212, v124, v125
v_add_u32 v2, s80, v0
Maav 40,236,228
s_add_u32 s77, s77, 2048
v_exp_f32 v127, v43
Maav 44,236,236
v_cvt_pk_bf16_f32 v213, v126, v127
v_cndmask_b32 v175, v132, v66, vcc
v_cndmask_b32 v191, v140, v66, vcc
Maav 104,236,244
v_exp_f32 v120, v56
Maav 108,236,252
s_bitcmp1_b64 s[32:33], s34
v_exp_f32 v121, v57
Maav 48,244,228
v_exp_f32 v122, v58
Maav 52,244,236
v_exp_f32 v123, v59
Maav 112,244,244
v_exp_f32 v124, v60
Maav 116,244,252
v_exp_f32 v125, v61
Maav 56,252,228
v_exp_f32 v126, v62
Maav 60,252,236
v_exp_f32 v127, v63
Maav 120,252,244
s_waitcnt lgkmcnt(0)
v_cvt_pk_bf16_f32 v214, v120, v121
v_cvt_pk_bf16_f32 v215, v122, v123
Maav 124,252,252
v_cvt_pk_bf16_f32 v222, v124, v125
v_cvt_pk_bf16_f32 v223, v126, v127
s_cbranch_scc1 .Lhd0
.Lhd0_r:
.Ldiag1:
Cvav 64,160,128,4
s_waitcnt vmcnt(15)
s_barrier
Mvav 64,164,136
s_lshr_b32 s58, s98, 14
ds_read_b128 a[192:195], v2 offset:0
v_add_u32 v1, s79, v0
Mvav 64,168,144
s_add_u32 s27, s27, s58
s_add_u32 s75, s75, s98
ds_read_b128 a[200:203], v2 offset:2048
v_mov_b32 v88, 0xff800000
v_mov_b32 v89, 0xff800000
Mvav 64,172,152
s_and_b32 s75, s75, 0xc000
ds_read_b128 a[208:211], v2 offset:4096
v_mov_b32 v90, 0xff800000
v_mov_b32 v91, 0xff800000
Cvvv 116,96,196,112
ds_read_b128 a[216:219], v2 offset:6144
v_readlane_b32 s35, v12, s27
v_mov_b32 v246, 0
v_mov_b32 v247, 0
Mvvv 116,100,204
ds_read_b128 a[224:227], v2 offset:8192
v_readlane_b32 s81, v14, s27
Mvvv 116,104,212
ds_read_b128 a[232:235], v2 offset:10240
Mvvv 116,108,220
ds_read_b128 a[240:243], v2 offset:12288
Cvav 68,160,160,8
v_exp_f32 v120, v64
Mvav 68,164,168
ds_read_b128 a[248:251], v2 offset:14336
Mvav 68,168,176
s_or_b32 m0, s84, s75
v_exp_f32 v121, v65
Mvav 68,172,184
ds_read_b128 a[196:199], v2 offset:1024
buffer_load_dwordx4 v0, s[48:51], s35 offen sc1 lds
Cvav 72,160,192,172
v_exp_f32 v122, v66
Mvav 72,164,200
ds_read_b128 a[204:207], v2 offset:3072
v_add_f32 v26, v116, v117
Mvav 72,168,208
v_exp_f32 v123, v67
Mvav 72,172,216
ds_read_b128 a[212:215], v2 offset:5120
v_add_f32 v27, v118, v119
v_cvt_pk_bf16_f32 v229, v122, v123
Cvav 76,160,224,180
v_exp_f32 v124, v68
Mvav 76,164,232
ds_read_b128 a[220:223], v2 offset:7168
v_add_f32 v26, v26, v27
Mvav 76,168,240
v_exp_f32 v125, v69
Mvav 76,172,248
ds_read_b128 a[228:231], v2 offset:9216
v_cmp_nge_f32 vcc, 0x53800000, v26
Cvav 80,176,128,4
s_or_b32 s80, s75, 0x10000
v_exp_f32 v126, v70
Mvav 80,180,136
ds_read_b128 a[236:239], v2 offset:11264
buffer_load_dwordx4 v0, s[48:51], s35 offen offset:1024 sc1 lds
Mvav 80,184,144
v_exp_f32 v127, v71
Mvav 80,188,152
ds_read_b128 a[244:247], v2 offset:13312
v_cvt_pk_bf16_f32 v228, v120, v121
v_cvt_pk_bf16_f32 v237, v126, v127
)"
R"(
Cvav 84,176,160,8
s_waitcnt lgkmcnt(12)
v_exp_f32 v120, v72
Mvav 84,180,168
ds_read_b128 a[252:255], v2 offset:15360
buffer_load_dwordx4 v0, s[48:51], s35 offen offset:2048 sc1 lds
Mvav 84,184,176
v_exp_f32 v121, v73
Mvav 84,188,184
ds_read_b128 a[128:131], v1 offset:0
buffer_load_dwordx4 v0, s[48:51], s35 offen offset:3072 sc1 lds
Cvav 92,176,224,188
v_exp_f32 v122, v74
Mvav 92,180,232
ds_read_b128 a[132:135], v1 offset:1024
v_cvt_pk_bf16_f32 v236, v124, v125
Mvav 92,184,240
s_waitcnt lgkmcnt(3)
v_exp_f32 v123, v75
Mvav 92,188,248
s_xor_b32 s58, s75, 0x18000
s_cbranch_vccnz .Ld1
.Ld1_r:
Maav 0,192,196
s_or_b32 m0, s84, s58
v_exp_f32 v124, v76
v_bfe_u32 v32, v0, 4, 4
Maav 4,192,204
ds_read_b128 a[136:139], v1 offset:2048
buffer_load_dwordx4 v3, s[12:15], s81 offen sc1 lds
Maav 64,192,212
s_add_u32 s70, s81, s24
v_exp_f32 v125, v77
v_lshrrev_b32 v33, 8, v0
v_lshlrev_b32 v33, 2, v33
Maav 68,192,220
ds_read_b128 a[140:143], v1 offset:3072
buffer_load_dwordx4 v3, s[12:15], s70 offen offset:1024 sc1 lds
Maav 8,200,196
s_waitcnt lgkmcnt(2)
v_exp_f32 v126, v78
v_cvt_pk_bf16_f32 v252, v124, v125
v_sub_u32 v33, v32, v33
v_mov_b32 v34, 0xff800000
Maav 12,200,204
ds_read_b128 a[144:147], v1 offset:4096
v_cvt_pk_bf16_f32 v244, v120, v121
v_cndmask_b32 v32, 64, v33, s[4:5]
v_cndmask_b32 v33, v33, -16, s[4:5]
Maav 72,200,212
s_add_u32 s70, s81, s25
v_exp_f32 v127, v79
v_cmp_gt_i32 vcc, 0, v32
v_cndmask_b32 v156, v4, v34, s[4:5]
Maav 76,200,220
ds_read_b128 a[148:151], v1 offset:5120
buffer_load_dwordx4 v3, s[12:15], s70 offen offset:2048 sc1 lds
Maav 16,208,196
s_add_u32 s70, s81, s26
v_exp_f32 v120, v80
v_cndmask_b32 v148, v4, v34, vcc
v_cndmask_b32 v164, v8, v34, vcc
Maav 20,208,204
ds_read_b128 a[152:155], v1 offset:6144
buffer_load_dwordx4 v3, s[12:15], s70 offen offset:3072 sc1 lds
Maav 80,208,212
v_exp_f32 v121, v81
v_cmp_gt_i32 vcc, 1, v32
v_cndmask_b32 v157, v4, v34, s[4:5]
Maav 84,208,220
s_cmp_lt_u32 s34, s72
s_cselect_b32 m0, s77, 0x30000
ds_read_b128 a[156:159], v1 offset:7168
v_cvt_pk_bf16_f32 v245, v122, v123
v_cndmask_b32 v149, v4, v34, vcc
v_cndmask_b32 v165, v8, v34, vcc
Maav 24,216,196
v_exp_f32 v122, v82
v_cmp_gt_i32 vcc, 2, v32
v_cndmask_b32 v158, v4, v34, s[4:5]
Maav 28,216,204
ds_read_b128 a[160:163], v1 offset:8192
buffer_load_dwordx4 v25, s[36:39], s68 offen nt lds
Maav 88,216,212
s_add_u32 m0, m0, 1024
s_add_u32 s70, s68, s60
v_exp_f32 v123, v83
v_cndmask_b32 v150, v4, v34, vcc
v_cndmask_b32 v166, v8, v34, vcc
Maav 92,216,220
ds_read_b128 a[164:167], v1 offset:9216
buffer_load_dwordx4 v25, s[36:39], s70 offen nt lds
Maav 32,224,196
v_exp_f32 v124, v84
v_cmp_gt_i32 vcc, 3, v32
v_cndmask_b32 v159, v4, v34, s[4:5]
Maav 36,224,204
ds_read_b128 a[168:171], v1 offset:10240
v_cvt_pk_bf16_f32 v253, v126, v127
v_cndmask_b32 v151, v4, v34, vcc
v_cndmask_b32 v167, v8, v34, vcc
Maav 96,224,212
v_exp_f32 v125, v85
v_cmp_gt_i32 vcc, 0, v33
v_cndmask_b32 v180, v8, v34, s[4:5]
Maav 100,224,220
ds_read_b128 a[172:175], v1 offset:11264
v_cvt_pk_bf16_f32 v230, v120, v121
v_cndmask_b32 v172, v4, v34, vcc
v_cndmask_b32 v188, v8, v34, vcc
Maav 40,232,196
v_exp_f32 v126, v86
v_cmp_gt_i32 vcc, 1, v33
v_cndmask_b32 v181, v8, v34, s[4:5]
Maav 44,232,204
ds_read_b128 a[176:179], v1 offset:12288
v_cvt_pk_bf16_f32 v231, v122, v123
v_cndmask_b32 v173, v4, v34, vcc
v_cndmask_b32 v189, v8, v34, vcc
Maav 104,232,212
v_exp_f32 v127, v87
v_cmp_gt_i32 vcc, 2, v33
v_cndmask_b32 v182, v8, v34, s[4:5]
Maav 108,232,220
ds_read_b128 a[180:183], v1 offset:13312
v_cvt_pk_bf16_f32 v238, v124, v125
v_cndmask_b32 v174, v4, v34, vcc
v_cndmask_b32 v190, v8, v34, vcc
Maav 48,240,196
v_exp_f32 v120, v92
v_cmp_gt_i32 vcc, 3, v33
v_cndmask_b32 v183, v8, v34, s[4:5]
Maav 52,240,204
ds_read_b128 a[184:187], v1 offset:14336
v_cvt_pk_bf16_f32 v239, v126, v127
v_cndmask_b32 v175, v4, v34, vcc
v_cndmask_b32 v191, v8, v34, vcc
Maav 112,240,212
s_add_u32 s27, s27, 1
ds_read_b128 a[188:191], v1 offset:15360
v_add_u32 v2, s80, v0
Maav 116,240,220
s_add_u32 s75, s75, 0x4000
v_exp_f32 v121, v93
Maav 56,248,196
s_and_b32 s75, s75, 0xc000
v_exp_f32 v122, v94
Maav 60,248,204
s_add_u32 s79, s75, 0x4000
v_exp_f32 v123, v95
Maav 120,248,212
s_and_b32 s79, s79, 0xc000
s_waitcnt lgkmcnt(0)
v_cvt_pk_bf16_f32 v254, v120, v121
v_cvt_pk_bf16_f32 v255, v122, v123
Maav 124,248,220
s_add_u32 s34, s34, 1
s_add_u32 s68, s68, s61
s_add_u32 s77, s77, 2048
s_bitcmp1_b64 s[32:33], s34
s_cbranch_scc1 .Lhd1
.Lhd1_r:
Cvav 32,128,128,148
s_waitcnt vmcnt(20)
s_barrier
Mvav 32,132,136
s_lshl3_add_u32 s18, s95, s71
v_add_u32 v1, s79, v0
v_readlane_b32 s35, v12, s27
Mvav 32,136,144
s_and_b32 s19, s67, 0x700
s_lshr_b32 s99, s69, 6
v_readlane_b32 s81, v14, s27
v_lshrrev_b32 v141, 6, v0
v_mov_b32 v56, 0xff800000
v_mov_b32 v57, 0xff800000
Mvav 32,140,152
s_add_u32 s19, s19, s99
s_lshl_b32 s99, s18, 15
s_add_u32 s99, s99, s19
v_and_b32 v142, 48, v0
v_mov_b32 v58, 0xff800000
v_mov_b32 v59, 0xff800000
Cvvv 112,96,228,116
s_mul_i32 s18, s18, 34816
s_add_u32 s18, s18, s19
v_lshl_add_u32 v140, v141, 11, v142
v_mov_b32 v214, 0
v_mov_b32 v215, 0
Mvvv 112,100,236
s_mov_b32 s84, 0x80000000
s_mov_b32 s98, 0x80000000
v_mul_u32_u24 v141, 2176, v141
Mvvv 112,104,244
s_or_b32 m0, s69, s75
v_add_u32 v141, v141, v142
Mvvv 112,108,252
buffer_load_dwordx4 v0, s[48:51], s35 offen sc1 lds
Cvav 36,128,160,8
buffer_load_dwordx4 v0, s[48:51], s35 offen offset:1024 sc1 lds
Mvav 36,132,168
buffer_load_dwordx4 v0, s[48:51], s35 offen offset:2048 sc1 lds
Mvav 36,136,176
s_add_u32 s19, s18, 4194304
v_exp_f32 v120, v32
Mvav 36,140,184
buffer_load_dwordx4 v[192:195], v141, s[48:51], s19 offen sc1
Cvav 40,144,128,156
v_exp_f32 v121, v33
Mvav 40,148,136
v_add_f32 v26, v112, v113
v_add_f32 v27, v114, v115
Mvav 40,152,144
v_exp_f32 v122, v34
Mvav 40,156,152
v_add_f32 v26, v26, v27
v_cmp_nge_f32 vcc, 0x53800000, v26
Cvav 44,144,160,164
v_exp_f32 v123, v35
Mvav 44,148,168
buffer_load_dwordx4 v0, s[48:51], s35 offen offset:3072 sc1 lds
Mvav 44,152,176
v_exp_f32 v124, v36
Mvav 44,156,184
v_cvt_pk_bf16_f32 v196, v120, v121
v_cvt_pk_bf16_f32 v197, v122, v123
Cvav 48,160,128,172
s_add_u32 s19, s18, 5308416
v_exp_f32 v125, v37
Mvav 48,164,136
s_or_b32 s80, s75, 0x10000
buffer_load_dwordx4 v[200:203], v141, s[48:51], s19 offen sc1
Mvav 48,168,144
s_xor_b32 s58, s75, 0x18000
s_or_b32 m0, s69, s58
v_exp_f32 v126, v38
Mvav 48,172,152
buffer_load_dwordx4 v3, s[12:15], s81 offen sc1 lds
Cvav 52,160,160,180
s_add_u32 s19, s18, 6422528
v_exp_f32 v127, v39
Mvav 52,164,168
ds_read_b128 a[128:131], v1 offset:0
buffer_load_dwordx4 v[208:211], v141, s[48:51], s19 offen sc1
Mvav 52,168,176
s_add_u32 s70, s81, s24
v_exp_f32 v120, v40
v_cvt_pk_bf16_f32 v205, v126, v127
Mvav 52,172,184
ds_read_b128 a[132:135], v1 offset:1024
buffer_load_dwordx4 v3, s[12:15], s70 offen offset:1024 sc1 lds
Cvav 60,176,160,188
v_exp_f32 v121, v41
v_add_u32 v2, s80, v0
Mvav 60,180,168
ds_read_b128 a[136:139], v1 offset:2048
v_cvt_pk_bf16_f32 v204, v124, v125
v_cvt_pk_bf16_f32 v198, v120, v121
Mvav 60,184,176
v_exp_f32 v122, v42
Mvav 60,188,184
s_cbranch_vccnz .Ly
.Ly_r:
Maav 0,196,228
s_add_u32 s19, s18, 7536640
v_exp_f32 v123, v43
Maav 4,196,236
ds_read_b128 a[140:143], v1 offset:3072
buffer_load_dwordx4 v[216:219], v141, s[48:51], s19 offen sc1
Maav 64,196,244
s_add_u32 s70, s81, s25
v_exp_f32 v124, v44
Maav 68,196,252
ds_read_b128 a[144:147], v1 offset:4096
buffer_load_dwordx4 v3, s[12:15], s70 offen offset:2048 sc1 lds
Maav 8,204,228
s_add_u32 s19, s99, 0
v_exp_f32 v125, v45
Maav 12,204,236
ds_read_b128 a[148:151], v1 offset:5120
buffer_load_dwordx4 v[224:227], v140, s[8:11], s19 offen
Maav 72,204,244
s_add_u32 s70, s81, s26
v_exp_f32 v126, v46
v_cvt_pk_bf16_f32 v206, v124, v125
Maav 76,204,252
ds_read_b128 a[152:155], v1 offset:6144
buffer_load_dwordx4 v3, s[12:15], s70 offen offset:3072 sc1 lds
Maav 16,212,228
s_add_u32 s19, s99, 1048576
v_exp_f32 v127, v47
Maav 20,212,236
ds_read_b128 a[156:159], v1 offset:7168
buffer_load_dwordx4 v[232:235], v140, s[8:11], s19 offen
Maav 80,212,244
v_exp_f32 v120, v48
Maav 84,212,252
s_cmp_lt_u32 s34, s72
s_cselect_b32 m0, s77, 0x30000
ds_read_b128 a[160:163], v1 offset:8192
v_cvt_pk_bf16_f32 v199, v122, v123
Maav 24,220,228
s_waitcnt lgkmcnt(6)
v_exp_f32 v121, v49
Maav 28,220,236
ds_read_b128 a[164:167], v1 offset:9216
buffer_load_dwordx4 v25, s[36:39], s68 offen nt lds
Maav 88,220,244
s_add_u32 s19, s99, 2097152
v_exp_f32 v122, v50
Maav 92,220,252
ds_read_b128 a[168:171], v1 offset:10240
buffer_load_dwordx4 v[240:243], v140, s[8:11], s19 offen
Maav 32,228,228
s_add_u32 m0, m0, 1024
s_add_u32 s70, s68, s60
v_exp_f32 v123, v51
Maav 36,228,236
ds_read_b128 a[172:175], v1 offset:11264
buffer_load_dwordx4 v25, s[36:39], s70 offen nt lds
Maav 96,228,244
s_add_u32 s19, s99, 3145728
v_exp_f32 v124, v52
Maav 100,228,252
ds_read_b128 a[176:179], v1 offset:12288
buffer_load_dwordx4 v[248:251], v140, s[8:11], s19 offen
Maav 40,236,228
s_add_u32 s27, s27, 1
v_exp_f32 v125, v53
Maav 44,236,236
s_add_u32 s75, s75, 0x4000
ds_read_b128 a[180:183], v1 offset:13312
v_cvt_pk_bf16_f32 v207, v126, v127
Maav 104,236,244
s_and_b32 s75, s75, 0xc000
v_exp_f32 v126, v54
Maav 108,236,252
s_add_u32 s79, s75, 0x4000
ds_read_b128 a[184:187], v1 offset:14336
v_cvt_pk_bf16_f32 v212, v120, v121
Maav 48,244,228
s_and_b32 s79, s79, 0xc000
v_exp_f32 v127, v55
Maav 52,244,236
s_add_u32 s34, s34, 1
ds_read_b128 a[188:191], v1 offset:15360
v_cvt_pk_bf16_f32 v213, v122, v123
Maav 112,244,244
s_add_u32 s68, s68, s61
v_exp_f32 v120, v60
Maav 116,244,252
s_add_u32 s77, s77, 2048
v_exp_f32 v121, v61
Maav 56,252,228
v_exp_f32 v122, v62
Maav 60,252,236
v_exp_f32 v123, v63
Maav 120,252,244
s_waitcnt lgkmcnt(0)
v_cvt_pk_bf16_f32 v220, v124, v125
v_cvt_pk_bf16_f32 v221, v126, v127
Maav 124,252,252
v_cvt_pk_bf16_f32 v222, v120, v121
v_cvt_pk_bf16_f32 v223, v122, v123
s_bitcmp1_b64 s[32:33], s34
s_cbranch_scc1 .Lhy
.Lhy_r:
s_cmp_ge_u32 s34, 33
s_cbranch_scc1 .Lend
.Laa:
Cvav 64,128,128,4
s_waitcnt vmcnt(20)
s_barrier
Mvav 64,132,136
ds_read_b128 a[192:195], v2 offset:0
v_add_u32 v1, s79, v0
Mvav 64,136,144
ds_read_b128 a[196:199], v2 offset:1024
v_readlane_b32 s35, v12, s27
Mvav 64,140,152
ds_read_b128 a[200:203], v2 offset:2048
v_readlane_b32 s81, v14, s27
Cvvv 116,96,196,112
ds_read_b128 a[204:207], v2 offset:3072
Mvvv 116,100,204
ds_read_b128 a[208:211], v2 offset:4096
Mvvv 116,96,212
ds_read_b128 a[212:215], v2 offset:5120
Mvvv 116,100,220
ds_read_b128 a[216:219], v2 offset:6144
Cvav 68,128,160,8
s_or_b32 m0, s69, s75
v_exp_f32 v120, v64
Mvav 68,132,168
ds_read_b128 a[220:223], v2 offset:7168
buffer_load_dwordx4 v0, s[48:51], s35 offen sc1 lds
Mvav 68,136,176
v_exp_f32 v121, v65
Mvav 68,140,184
ds_read_b128 a[224:227], v2 offset:8192
v_cvt_pk_bf16_f32 v228, v120, v121
Cvav 72,144,128,4
v_exp_f32 v122, v66
Mvav 72,148,136
ds_read_b128 a[228:231], v2 offset:9216
v_add_f32 v26, v116, v117
Mvav 72,152,144
v_exp_f32 v123, v67
Mvav 72,156,152
ds_read_b128 a[232:235], v2 offset:10240
v_cmp_nge_f32 vcc, 0x53800000, v26
Cvav 76,144,160,8
v_exp_f32 v124, v68
Mvav 76,148,168
ds_read_b128 a[236:239], v2 offset:11264
buffer_load_dwordx4 v0, s[48:51], s35 offen offset:1024 sc1 lds
Mvav 76,152,176
v_exp_f32 v125, v69
Mvav 76,156,184
ds_read_b128 a[240:243], v2 offset:12288
v_cvt_pk_bf16_f32 v229, v122, v123
Cvav 80,160,128,4
s_or_b32 s80, s75, 0x10000
v_exp_f32 v126, v70
Mvav 80,164,136
ds_read_b128 a[244:247], v2 offset:13312
buffer_load_dwordx4 v0, s[48:51], s35 offen offset:2048 sc1 lds
Mvav 80,168,144
v_exp_f32 v127, v71
Mvav 80,172,152
ds_read_b128 a[248:251], v2 offset:14336
v_cvt_pk_bf16_f32 v236, v124, v125
Cvav 84,160,160,8
s_waitcnt lgkmcnt(12)
v_exp_f32 v120, v72
Mvav 84,164,168
ds_read_b128 a[252:255], v2 offset:15360
buffer_load_dwordx4 v0, s[48:51], s35 offen offset:3072 sc1 lds
Mvav 84,168,176
v_exp_f32 v121, v73
Mvav 84,172,184
s_xor_b32 s58, s75, 0x18000
ds_read_b128 a[128:131], v1 offset:0
v_cvt_pk_bf16_f32 v237, v126, v127
Cvav 88,176,128,4
s_or_b32 m0, s69, s58
v_exp_f32 v122, v74
Mvav 88,180,136
ds_read_b128 a[132:135], v1 offset:1024
)"
R"(
buffer_load_dwordx4 v3, s[12:15], s81 offen sc1 lds
Mvav 88,184,144
v_exp_f32 v123, v75
Mvav 88,188,152
s_waitcnt lgkmcnt(12)
ds_read_b128 a[136:139], v1 offset:2048
v_cvt_pk_bf16_f32 v230, v120, v121
Cvav 92,176,160,8
s_add_u32 s70, s81, s24
v_exp_f32 v124, v76
Mvav 92,180,168
ds_read_b128 a[140:143], v1 offset:3072
buffer_load_dwordx4 v3, s[12:15], s70 offen offset:1024 sc1 lds
Mvav 92,184,176
v_exp_f32 v125, v77
Mvav 92,188,184
s_cbranch_vccnz .Laa1
.Laa1_r:
Maav 0,192,196
v_exp_f32 v126, v78
Maav 0,196,212
s_waitcnt lgkmcnt(11)
ds_read_b128 a[144:147], v1 offset:4096
v_cvt_pk_bf16_f32 v231, v122, v123
Maav 4,192,204
s_add_u32 s70, s81, s25
v_exp_f32 v127, v79
Maav 4,196,220
ds_read_b128 a[148:151], v1 offset:5120
buffer_load_dwordx4 v3, s[12:15], s70 offen offset:2048 sc1 lds
Maav 8,200,196
v_exp_f32 v120, v80
Maav 8,204,212
ds_read_b128 a[152:155], v1 offset:6144
v_cvt_pk_bf16_f32 v238, v124, v125
Maav 12,200,204
s_add_u32 s70, s81, s26
v_exp_f32 v121, v81
Maav 12,204,220
ds_read_b128 a[156:159], v1 offset:7168
buffer_load_dwordx4 v3, s[12:15], s70 offen offset:3072 sc1 lds
Maav 16,208,196
s_waitcnt lgkmcnt(12)
v_exp_f32 v122, v82
Maav 16,212,212
s_cmp_lt_u32 s34, s72
s_cselect_b32 m0, s77, 0x30000
ds_read_b128 a[160:163], v1 offset:8192
v_add_u32 v2, s80, v0
Maav 20,208,204
v_exp_f32 v123, v83
Maav 20,212,220
ds_read_b128 a[164:167], v1 offset:9216
buffer_load_dwordx4 v25, s[36:39], s68 offen nt lds
Maav 24,216,196
s_waitcnt lgkmcnt(8)
v_exp_f32 v124, v84
Maav 24,220,212
s_add_u32 m0, m0, 1024
ds_read_b128 a[168:171], v1 offset:10240
v_cvt_pk_bf16_f32 v239, v126, v127
Maav 28,216,204
s_add_u32 s70, s68, s60
v_exp_f32 v125, v85
Maav 28,220,220
ds_read_b128 a[172:175], v1 offset:11264
buffer_load_dwordx4 v25, s[36:39], s70 offen nt lds
Maav 32,224,196
s_add_u32 s27, s27, 1
v_exp_f32 v126, v86
Maav 32,228,212
s_add_u32 s75, s75, 0x4000
ds_read_b128 a[176:179], v1 offset:12288
v_cvt_pk_bf16_f32 v244, v120, v121
Maav 36,224,204
s_and_b32 s75, s75, 0xc000
v_exp_f32 v127, v87
Maav 36,228,220
s_add_u32 s79, s75, 0x4000
ds_read_b128 a[180:183], v1 offset:13312
v_cvt_pk_bf16_f32 v245, v122, v123
Maav 40,232,196
s_and_b32 s79, s79, 0xc000
v_exp_f32 v120, v88
Maav 40,236,212
s_add_u32 s34, s34, 1
ds_read_b128 a[184:187], v1 offset:14336
v_cvt_pk_bf16_f32 v252, v124, v125
Maav 44,232,204
s_add_u32 s68, s68, s61
ds_read_b128 a[188:191], v1 offset:15360
v_cvt_pk_bf16_f32 v253, v126, v127
Maav 44,236,220
s_add_u32 s77, s77, 2048
v_exp_f32 v121, v89
Maav 48,240,196
v_exp_f32 v122, v90
Maav 48,244,212
v_exp_f32 v123, v91
Maav 52,240,204
s_bitcmp1_b64 s[32:33], s34
v_exp_f32 v124, v92
Maav 52,244,220
v_exp_f32 v125, v93
Maav 56,248,196
v_exp_f32 v126, v94
Maav 56,252,212
v_exp_f32 v127, v95
Maav 60,248,204
s_waitcnt lgkmcnt(0)
v_cvt_pk_bf16_f32 v246, v120, v121
v_cvt_pk_bf16_f32 v247, v122, v123
Maav 60,252,220
v_cvt_pk_bf16_f32 v254, v124, v125
v_cvt_pk_bf16_f32 v255, v126, v127
s_cbranch_scc1 .Lhaa1
.Lhaa1_r:
Cvav 32,128,128,4
s_waitcnt vmcnt(20)
s_barrier
Mvav 32,132,136
ds_read_b128 a[192:195], v2 offset:0
v_add_u32 v1, s79, v0
Mvav 32,136,144
ds_read_b128 a[196:199], v2 offset:1024
v_readlane_b32 s35, v12, s27
Mvav 32,140,152
ds_read_b128 a[200:203], v2 offset:2048
v_readlane_b32 s81, v14, s27
Cvvv 112,96,228,116
ds_read_b128 a[204:207], v2 offset:3072
Mvvv 112,100,236
ds_read_b128 a[208:211], v2 offset:4096
Mvvv 112,96,244
ds_read_b128 a[212:215], v2 offset:5120
Mvvv 112,100,252
ds_read_b128 a[216:219], v2 offset:6144
Cvav 36,128,160,8
s_or_b32 m0, s69, s75
v_exp_f32 v120, v32
Mvav 36,132,168
ds_read_b128 a[220:223], v2 offset:7168
buffer_load_dwordx4 v0, s[48:51], s35 offen sc1 lds
Mvav 36,136,176
v_exp_f32 v121, v33
Mvav 36,140,184
ds_read_b128 a[224:227], v2 offset:8192
v_cvt_pk_bf16_f32 v196, v120, v121
Cvav 40,144,128,4
v_exp_f32 v122, v34
Mvav 40,148,136
ds_read_b128 a[228:231], v2 offset:9216
v_add_f32 v26, v112, v113
Mvav 40,152,144
v_exp_f32 v123, v35
Mvav 40,156,152
ds_read_b128 a[232:235], v2 offset:10240
v_cmp_nge_f32 vcc, 0x53800000, v26
Cvav 44,144,160,8
v_exp_f32 v124, v36
Mvav 44,148,168
ds_read_b128 a[236:239], v2 offset:11264
buffer_load_dwordx4 v0, s[48:51], s35 offen offset:1024 sc1 lds
Mvav 44,152,176
v_exp_f32 v125, v37
Mvav 44,156,184
ds_read_b128 a[240:243], v2 offset:12288
v_cvt_pk_bf16_f32 v197, v122, v123
Cvav 48,160,128,4
s_or_b32 s80, s75, 0x10000
v_exp_f32 v126, v38
Mvav 48,164,136
ds_read_b128 a[244:247], v2 offset:13312
buffer_load_dwordx4 v0, s[48:51], s35 offen offset:2048 sc1 lds
Mvav 48,168,144
v_exp_f32 v127, v39
Mvav 48,172,152
ds_read_b128 a[248:251], v2 offset:14336
v_cvt_pk_bf16_f32 v204, v124, v125
Cvav 52,160,160,8
s_waitcnt lgkmcnt(12)
v_exp_f32 v120, v40
Mvav 52,164,168
ds_read_b128 a[252:255], v2 offset:15360
buffer_load_dwordx4 v0, s[48:51], s35 offen offset:3072 sc1 lds
Mvav 52,168,176
v_exp_f32 v121, v41
Mvav 52,172,184
s_xor_b32 s58, s75, 0x18000
ds_read_b128 a[128:131], v1 offset:0
v_cvt_pk_bf16_f32 v205, v126, v127
Cvav 56,176,128,4
s_or_b32 m0, s69, s58
v_exp_f32 v122, v42
Mvav 56,180,136
ds_read_b128 a[132:135], v1 offset:1024
buffer_load_dwordx4 v3, s[12:15], s81 offen sc1 lds
Mvav 56,184,144
v_exp_f32 v123, v43
Mvav 56,188,152
s_waitcnt lgkmcnt(12)
ds_read_b128 a[136:139], v1 offset:2048
v_cvt_pk_bf16_f32 v198, v120, v121
Cvav 60,176,160,8
s_add_u32 s70, s81, s24
v_exp_f32 v124, v44
Mvav 60,180,168
ds_read_b128 a[140:143], v1 offset:3072
buffer_load_dwordx4 v3, s[12:15], s70 offen offset:1024 sc1 lds
Mvav 60,184,176
v_exp_f32 v125, v45
Mvav 60,188,184
s_cbranch_vccnz .Laa0
.Laa0_r:
Maav 0,192,228
s_waitcnt lgkmcnt(11)
v_exp_f32 v126, v46
Maav 0,196,244
ds_read_b128 a[144:147], v1 offset:4096
buffer_store_dwordx2 v[132:133], v13, s[48:51], s84 offen sc1
v_cvt_pk_bf16_f32 v199, v122, v123
Maav 4,192,236
s_add_u32 s70, s81, s25
v_exp_f32 v127, v47
Maav 4,196,252
ds_read_b128 a[148:151], v1 offset:5120
buffer_load_dwordx4 v3, s[12:15], s70 offen offset:2048 sc1 lds
Maav 8,200,228
s_mov_b32 s84, s98
v_exp_f32 v120, v48
Maav 8,204,244
s_mov_b32 s98, 0x80000000
ds_read_b128 a[152:155], v1 offset:6144
v_cvt_pk_bf16_f32 v206, v124, v125
Maav 12,200,236
s_add_u32 s70, s81, s26
v_exp_f32 v121, v49
Maav 12,204,252
ds_read_b128 a[156:159], v1 offset:7168
buffer_load_dwordx4 v3, s[12:15], s70 offen offset:3072 sc1 lds
Maav 16,208,228
s_waitcnt lgkmcnt(12)
v_exp_f32 v122, v50
Maav 16,212,244
s_cmp_lt_u32 s34, s72
s_cselect_b32 m0, s77, 0x30000
ds_read_b128 a[160:163], v1 offset:8192
v_add_u32 v2, s80, v0
Maav 20,208,236
v_exp_f32 v123, v51
Maav 20,212,252
ds_read_b128 a[164:167], v1 offset:9216
buffer_load_dwordx4 v25, s[36:39], s68 offen nt lds
Maav 24,216,228
s_waitcnt lgkmcnt(8)
v_exp_f32 v124, v52
Maav 24,220,244
s_add_u32 m0, m0, 1024
ds_read_b128 a[168:171], v1 offset:10240
v_cvt_pk_bf16_f32 v207, v126, v127
Maav 28,216,236
s_add_u32 s70, s68, s60
v_exp_f32 v125, v53
Maav 28,220,252
ds_read_b128 a[172:175], v1 offset:11264
buffer_load_dwordx4 v25, s[36:39], s70 offen nt lds
Maav 32,224,228
s_add_u32 s27, s27, 1
v_exp_f32 v126, v54
Maav 32,228,244
s_add_u32 s75, s75, 0x4000
ds_read_b128 a[176:179], v1 offset:12288
v_cvt_pk_bf16_f32 v212, v120, v121
Maav 36,224,236
s_and_b32 s75, s75, 0xc000
v_exp_f32 v127, v55
Maav 36,228,252
s_add_u32 s79, s75, 0x4000
ds_read_b128 a[180:183], v1 offset:13312
v_cvt_pk_bf16_f32 v213, v122, v123
Maav 40,232,228
s_and_b32 s79, s79, 0xc000
v_exp_f32 v120, v56
Maav 40,236,244
s_add_u32 s34, s34, 1
ds_read_b128 a[184:187], v1 offset:14336
v_cvt_pk_bf16_f32 v220, v124, v125
Maav 44,232,236
s_add_u32 s68, s68, s61
ds_read_b128 a[188:191], v1 offset:15360
v_cvt_pk_bf16_f32 v221, v126, v127
Maav 44,236,252
s_add_u32 s77, s77, 2048
v_exp_f32 v121, v57
Maav 48,240,228
s_cmp_lt_u32 s34, 33
v_exp_f32 v122, v58
Maav 48,244,244
s_cselect_b32 s59, s32, -1
v_exp_f32 v123, v59
Maav 52,240,236
s_bitcmp1_b32 s59, s34
v_exp_f32 v124, v60
Maav 52,244,252
v_exp_f32 v125, v61
Maav 56,248,228
v_exp_f32 v126, v62
Maav 56,252,244
v_exp_f32 v127, v63
Maav 60,248,236
s_waitcnt lgkmcnt(0)
v_cvt_pk_bf16_f32 v214, v120, v121
v_cvt_pk_bf16_f32 v215, v122, v123
Maav 60,252,252
v_cvt_pk_bf16_f32 v222, v124, v125
v_cvt_pk_bf16_f32 v223, v126, v127
s_cbranch_scc0 .Laa
s_bitcmp1_b64 s[32:33], s34
s_cbranch_scc1 .Lhaa0
.Lhaa0_r:
s_cmp_lt_u32 s34, 33
s_cbranch_scc1 .Laa
.Lend:
v_lshrrev_b32 v26, 9, v0
v_bfe_u32 v27, v0, 4, 5
v_lshlrev_b32 v27, 4, v27
v_lshl_add_u32 v26, v26, 11, v27
s_lshr_b32 s58, s69, 3
s_add_u32 s58, s58, 0x20000
v_add_u32 v3, s58, v26
ds_read_b64_tr_b16 a[64:65], v3 offset:0
ds_read_b64_tr_b16 a[66:67], v3 offset:4096
ds_read_b64_tr_b16 a[72:73], v3 offset:8192
ds_read_b64_tr_b16 a[74:75], v3 offset:12288
ds_read_b64_tr_b16 a[80:81], v3 offset:16384
ds_read_b64_tr_b16 a[82:83], v3 offset:20480
ds_read_b64_tr_b16 a[88:89], v3 offset:24576
ds_read_b64_tr_b16 a[90:91], v3 offset:28672
Cvvv 116,96,196,112
s_waitcnt lgkmcnt(0)
s_waitcnt vmcnt(20)
s_barrier
Mvvv 116,100,204
s_add_u32 m0, s69, 0
ds_read_b64_tr_b16 a[68:69], v3 offset:8
ds_read_b64_tr_b16 a[70:71], v3 offset:4104
Mvvv 116,96,212
s_add_u32 s70, s67, 524288
ds_read_b64_tr_b16 a[76:77], v3 offset:8200
ds_read_b64_tr_b16 a[78:79], v3 offset:12296
Mvvv 116,100,220
ds_read_b64_tr_b16 a[84:85], v3 offset:16392
ds_read_b64_tr_b16 a[86:87], v3 offset:20488
buffer_load_dwordx4 v25, s[28:31], s70 offen lds
Zvaa 64,64,128
s_xor_b32 s38, s97, 0x33c0
ds_read_b64_tr_b16 a[92:93], v3 offset:24584
ds_read_b64_tr_b16 a[94:95], v3 offset:28680
Mvaa 64,72,132
s_add_u32 m0, s69, 896
ds_read_b128 a[192:195], v2 offset:0
ds_read_b128 a[196:199], v2 offset:1024
Mvaa 64,80,136
s_add_u32 s70, s67, 524288
ds_read_b128 a[200:203], v2 offset:2048
ds_read_b128 a[204:207], v2 offset:3072
Mvaa 64,88,140
s_waitcnt lgkmcnt(10)
ds_read_b128 a[208:211], v2 offset:4096
buffer_load_dwordx4 v25, s[28:31], s70 offen offset:128 lds
v_subrev_u32 v1, 0x10000, v3
Zvaa 68,68,128
s_waitcnt lgkmcnt(9)
s_add_u32 m0, s69, 2048
ds_read_b128 a[212:215], v2 offset:5120
v_subrev_u32 v10, 0x20000, v3
Mvaa 68,76,132
s_waitcnt lgkmcnt(8)
s_add_u32 s70, s67, 540672
ds_read_b128 a[216:219], v2 offset:6144
v_add_f32 v26, v116, v117
Mvaa 68,84,136
s_waitcnt lgkmcnt(7)
ds_read_b128 a[220:223], v2 offset:7168
buffer_load_dwordx4 v25, s[28:31], s70 offen lds
v_cmp_nge_f32 vcc, 0x53800000, v26
Mvaa 68,92,140
s_lshl_b32 s21, s74, 14
ds_read_b128 a[224:227], v2 offset:8192
v_add_u32 v12, 0x10000, v0
Zvaa 72,64,144
s_add_u32 m0, s69, 2944
ds_read_b128 a[228:231], v2 offset:9216
v_add_u32 v14, 0x20000, v0
Mvaa 72,72,148
s_waitcnt lgkmcnt(7)
s_add_u32 s70, s67, 540672
s_lshl_b32 s59, s57, 7
ds_read_b128 a[232:235], v2 offset:10240
Mvaa 72,80,152
ds_read_b128 a[236:239], v2 offset:11264
buffer_load_dwordx4 v25, s[28:31], s70 offen offset:128 lds
Mvaa 72,88,156
s_add_u32 m0, s69, 98304
s_add_u32 s21, s21, s59
ds_read_b128 a[240:243], v2 offset:12288
Zvaa 76,68,144
s_add_u32 s70, s67, 655360
s_add_u32 s21, s21, 1048576
ds_read_b128 a[244:247], v2 offset:13312
Mvaa 76,76,148
ds_read_b128 a[248:251], v2 offset:14336
buffer_load_dwordx4 v25, s[28:31], s70 offen lds
Mvaa 76,84,152
s_mul_i32 s58, s57, 36864
s_add_u32 s66, s66, s58
ds_read_b128 a[252:255], v2 offset:15360
Mvaa 76,92,156
s_cbranch_vccnz .Ldr
.Ldr_r:
s_add_u32 m0, s69, 99200
Maav 0,192,196
s_add_u32 s70, s67, 655360
s_mul_i32 s58, s92, 1179648
s_add_u32 s12, s66, s58
Maav 0,196,212
s_mul_i32 s58, s93, 1179648
buffer_load_dwordx4 v25, s[28:31], s70 offen offset:128 lds
v_rcp_f32 v32, v116
Maav 4,192,204
s_add_u32 m0, s69, 100352
s_add_u32 s13, s66, s58
s_mul_i32 s58, s94, 1179648
v_rcp_f32 v33, v117
Maav 4,196,220
s_add_u32 s70, s67, 671744
s_add_u32 s14, s66, s58
s_mul_i32 s58, s95, 1179648
s_add_u32 s83, s96, 4096
Maav 8,200,196
s_waitcnt lgkmcnt(0)
buffer_load_dwordx4 v25, s[28:31], s70 offen lds
Maav 8,204,212
s_barrier
s_add_u32 s15, s66, s58
Maav 12,200,204
s_add_u32 m0, s69, 101248
s_lshl3_add_u32 s58, s71, s63
s_lshl_b32 s58, s58, 19
Maav 12,204,220
s_add_u32 s70, s67, 671744
s_lshr_b32 s59, s69, 1
s_add_u32 s58, s58, s59
Maav 16,208,196
s_add_u32 s58, s58, 75497472
)"
R"(
buffer_load_dwordx4 v25, s[28:31], s70 offen offset:128 lds
Maav 16,212,212
s_add_u32 m0, s69, 114688
s_lshl_b32 s59, s95, 13
s_add_u32 s22, s58, s59
Maav 20,208,204
s_add_u32 s70, s67, 786432
s_lshl_b32 s59, s95, 17
s_add_u32 s23, s58, s59
Maav 20,212,220
s_and_b32 s59, s63, 1
buffer_load_dwordx4 v25, s[28:31], s70 offen lds
Maav 24,216,196
s_lshl_b32 s32, s59, 12
s_lshr_b32 s59, s69, 2
s_sub_u32 s32, s32, s59
v_accvgpr_read_b32 v34, a0
v_accvgpr_read_b32 v35, a1
v_mul_f32 v34, v34, v32
v_mul_f32 v35, v35, v32
v_cvt_pk_bf16_f32 v40, v34, v35
v_accvgpr_read_b32 v34, a2
Maav 24,220,212
s_add_u32 m0, s69, 115584
s_lshl3_add_u32 s58, s71, s63
s_lshl2_add_u32 s58, s58, s57
v_accvgpr_read_b32 v35, a3
v_mul_f32 v34, v34, v32
v_mul_f32 v35, v35, v32
v_cvt_pk_bf16_f32 v41, v34, v35
v_accvgpr_read_b32 v34, a8
v_accvgpr_read_b32 v35, a9
Maav 28,216,204
s_add_u32 s70, s67, 786432
s_lshl_b32 s58, s58, 7
s_add_u32 s33, s58, 49152
v_mul_f32 v34, v34, v32
v_mul_f32 v35, v35, v32
v_cvt_pk_bf16_f32 v42, v34, v35
v_accvgpr_read_b32 v34, a10
v_accvgpr_read_b32 v35, a11
v_mul_f32 v34, v34, v32
Maav 28,220,220
s_lshl3_add_u32 s34, s95, s33
buffer_load_dwordx4 v25, s[28:31], s70 offen offset:128 lds
v_mul_f32 v35, v35, v32
v_cvt_pk_bf16_f32 v43, v34, v35
v_accvgpr_read_b32 v34, a4
v_accvgpr_read_b32 v35, a5
Maav 32,224,196
s_add_u32 m0, s69, 116736
s_and_b32 s24, s86, 0xff
s_bfe_u32 s25, s86, 0x80008
buffer_store_dwordx4 v[40:43], v0, s[48:51], s96 offen sc1
v_mul_f32 v34, v34, v33
v_mul_f32 v35, v35, v33
v_cvt_pk_bf16_f32 v44, v34, v35
v_accvgpr_read_b32 v34, a6
Maav 32,228,212
s_add_u32 s70, s67, 802816
s_bfe_u32 s26, s86, 0x80010
s_lshr_b32 s27, s86, 24
v_accvgpr_read_b32 v35, a7
v_mul_f32 v34, v34, v33
v_mul_f32 v35, v35, v33
v_cvt_pk_bf16_f32 v45, v34, v35
v_accvgpr_read_b32 v34, a12
v_accvgpr_read_b32 v35, a13
Maav 36,224,204
s_lshr_b32 s58, s62, 3
buffer_load_dwordx4 v25, s[28:31], s70 offen lds
v_mul_f32 v34, v34, v33
v_mul_f32 v35, v35, v33
v_cvt_pk_bf16_f32 v46, v34, v35
v_accvgpr_read_b32 v34, a14
Maav 36,228,220
s_lshl2_add_u32 s58, s58, s57
s_lshl_b32 s59, s58, 7
s_lshl_b32 s20, s74, 17
v_accvgpr_read_b32 v35, a15
v_mul_f32 v34, v34, v33
v_mul_f32 v35, v35, v33
v_cvt_pk_bf16_f32 v47, v34, v35
Maav 40,232,196
s_add_u32 m0, s69, 117632
s_add_u32 s20, s20, s59
s_mul_i32 s35, s58, 14336
buffer_load_dwordx4 v[16:19], v20, s[48:51], s82 offen sc1
v_accvgpr_read_b32 v34, a16
v_accvgpr_read_b32 v35, a17
v_mul_f32 v34, v34, v32
v_mul_f32 v35, v35, v32
Maav 40,236,212
s_add_u32 s70, s67, 802816
s_lshl_b32 s59, s74, 7
s_add_u32 s35, s35, s59
buffer_store_dwordx4 v[44:47], v0, s[48:51], s83 offen sc1
v_cvt_pk_bf16_f32 v48, v34, v35
v_accvgpr_read_b32 v34, a18
v_accvgpr_read_b32 v35, a19
v_mul_f32 v34, v34, v32
Maav 44,232,204
s_add_u32 s35, s35, 2097152
buffer_load_dwordx4 v25, s[28:31], s70 offen offset:128 lds
v_mul_f32 v35, v35, v32
v_cvt_pk_bf16_f32 v49, v34, v35
v_accvgpr_read_b32 v34, a24
v_accvgpr_read_b32 v35, a25
Maav 44,236,220
s_cmp_eq_u32 s63, 0
s_cselect_b32 s20, 0x80000000, s20
v_mul_f32 v34, v34, v32
v_mul_f32 v35, v35, v32
v_cvt_pk_bf16_f32 v50, v34, v35
v_accvgpr_read_b32 v34, a26
v_accvgpr_read_b32 v35, a27
v_mul_f32 v34, v34, v32
Maav 48,240,196
s_add_u32 m0, s69, 131072
s_cmp_eq_u32 s63, 0
s_cselect_b32 s35, 0x80000000, s35
v_mul_f32 v35, v35, v32
v_cvt_pk_bf16_f32 v51, v34, v35
v_accvgpr_read_b32 v34, a20
v_accvgpr_read_b32 v35, a21
v_mul_f32 v34, v34, v33
v_mul_f32 v35, v35, v33
Maav 48,244,212
s_waitcnt vmcnt(15)
s_barrier
s_add_u32 s70, s67, 917504
Maav 52,240,204
ds_read_b128 v[128:131], v0 offset:16384
buffer_load_dwordx4 v25, s[28:31], s70 offen lds
v_cvt_pk_bf16_f32 v52, v34, v35
v_accvgpr_read_b32 v34, a22
v_accvgpr_read_b32 v35, a23
Maav 52,244,220
s_add_u32 m0, s69, 131968
ds_read_b128 v[136:139], v0 offset:17408
buffer_store_dwordx4 v[48:51], v0, s[48:51], s96 offen offset:1024 sc1
v_mul_f32 v34, v34, v33
v_mul_f32 v35, v35, v33
v_cvt_pk_bf16_f32 v53, v34, v35
Maav 56,248,196
s_add_u32 s70, s67, 917504
ds_read_b128 v[144:147], v0 offset:18432
v_accvgpr_read_b32 v34, a28
v_accvgpr_read_b32 v35, a29
v_mul_f32 v34, v34, v33
v_mul_f32 v35, v35, v33
v_cvt_pk_bf16_f32 v54, v34, v35
Maav 56,252,212
ds_read_b128 v[152:155], v0 offset:19456
buffer_load_dwordx4 v25, s[28:31], s70 offen offset:128 lds
v_accvgpr_read_b32 v34, a30
v_accvgpr_read_b32 v35, a31
v_mul_f32 v34, v34, v33
Maav 60,248,204
s_add_u32 m0, s69, 133120
ds_read_b64_tr_b16 a[96:97], v1 offset:0
v_mul_f32 v35, v35, v33
v_cvt_pk_bf16_f32 v55, v34, v35
v_accvgpr_read_b32 v34, a32
v_accvgpr_read_b32 v35, a33
v_mul_f32 v34, v34, v32
Maav 60,252,220
s_add_u32 s70, s67, 933888
ds_read_b64_tr_b16 a[98:99], v1 offset:4096
buffer_store_dwordx4 v[52:55], v0, s[48:51], s83 offen offset:1024 sc1
v_mul_f32 v35, v35, v32
v_cvt_pk_bf16_f32 v228, v34, v35
v_accvgpr_read_b32 v34, a34
Zvaa 80,64,160
ds_read_b64_tr_b16 a[104:105], v1 offset:8192
buffer_load_dwordx4 v25, s[28:31], s70 offen lds
v_accvgpr_read_b32 v35, a35
v_mul_f32 v34, v34, v32
v_mul_f32 v35, v35, v32
Mvaa 80,72,164
s_cmp_eq_u32 s63, 0
s_cselect_b64 s[36:37], 0, -1
ds_read_b64_tr_b16 a[106:107], v1 offset:12288
v_cvt_pk_bf16_f32 v229, v34, v35
v_accvgpr_read_b32 v34, a40
v_accvgpr_read_b32 v35, a41
v_mul_f32 v34, v34, v32
v_mul_f32 v35, v35, v32
Mvaa 80,80,168
s_add_u32 m0, s69, 134016
ds_read_b64_tr_b16 a[112:113], v1 offset:16384
v_cvt_pk_bf16_f32 v230, v34, v35
v_accvgpr_read_b32 v34, a42
v_accvgpr_read_b32 v35, a43
v_mul_f32 v34, v34, v32
v_mul_f32 v35, v35, v32
Mvaa 80,88,172
s_waitcnt lgkmcnt(6)
s_add_u32 s70, s67, 933888
ds_read_b64_tr_b16 a[114:115], v1 offset:20480
v_cvt_pk_bf16_f32 v231, v34, v35
v_accvgpr_read_b32 v34, a36
v_accvgpr_read_b32 v35, a37
v_mul_f32 v34, v34, v33
v_mul_f32 v35, v35, v33
Zvaa 84,68,160
ds_read_b64_tr_b16 a[120:121], v1 offset:24576
buffer_load_dwordx4 v25, s[28:31], s70 offen offset:128 lds
v_cvt_pk_bf16_f32 v236, v34, v35
v_accvgpr_read_b32 v34, a38
v_accvgpr_read_b32 v35, a39
Mvaa 84,76,164
s_add_u32 m0, s69, 147456
ds_read_b64_tr_b16 a[122:123], v1 offset:28672
buffer_store_dwordx4 v[228:231], v0, s[48:51], s96 offen offset:2048 sc1
v_mul_f32 v34, v34, v33
v_mul_f32 v35, v35, v33
v_cvt_pk_bf16_f32 v237, v34, v35
Mvaa 84,84,168
s_waitcnt lgkmcnt(6)
s_add_u32 s70, s67, 1048576
ds_read_b64_tr_b16 a[100:101], v1 offset:8
v_accvgpr_read_b32 v34, a44
v_accvgpr_read_b32 v35, a45
v_mul_f32 v34, v34, v33
v_mul_f32 v35, v35, v33
v_cvt_pk_bf16_f32 v238, v34, v35
Mvaa 84,92,172
ds_read_b64_tr_b16 a[102:103], v1 offset:4104
buffer_load_dwordx4 v25, s[28:31], s70 offen lds
v_accvgpr_read_b32 v34, a46
v_accvgpr_read_b32 v35, a47
v_mul_f32 v34, v34, v33
Zvaa 88,64,176
s_cmp_eq_u32 s63, 0
s_cselect_b32 s21, 0x80000000, s21
ds_read_b64_tr_b16 a[108:109], v1 offset:8200
v_mul_f32 v35, v35, v33
v_cvt_pk_bf16_f32 v239, v34, v35
v_accvgpr_read_b32 v34, a48
v_accvgpr_read_b32 v35, a49
v_mul_f32 v34, v34, v32
Mvaa 88,72,180
s_waitcnt lgkmcnt(6)
s_add_u32 m0, s69, 148352
ds_read_b64_tr_b16 a[110:111], v1 offset:12296
buffer_store_dwordx4 v[236:239], v0, s[48:51], s83 offen offset:2048 sc1
v_mul_f32 v35, v35, v32
v_cvt_pk_bf16_f32 v244, v34, v35
v_accvgpr_read_b32 v34, a50
Mvaa 88,80,184
s_waitcnt lgkmcnt(6)
s_add_u32 s70, s67, 1048576
ds_read_b64_tr_b16 a[116:117], v1 offset:16392
v_accvgpr_read_b32 v35, a51
v_mul_f32 v34, v34, v32
v_mul_f32 v35, v35, v32
v_cvt_pk_bf16_f32 v245, v34, v35
v_accvgpr_read_b32 v34, a56
Mvaa 88,88,188
ds_read_b64_tr_b16 a[118:119], v1 offset:20488
buffer_load_dwordx4 v25, s[28:31], s70 offen offset:128 lds
v_accvgpr_read_b32 v35, a57
v_mul_f32 v34, v34, v32
v_mul_f32 v35, v35, v32
Zvaa 92,68,176
s_waitcnt lgkmcnt(6)
s_add_u32 m0, s69, 149504
ds_read_b64_tr_b16 a[124:125], v1 offset:24584
v_cvt_pk_bf16_f32 v246, v34, v35
v_accvgpr_read_b32 v34, a58
v_accvgpr_read_b32 v35, a59
v_mul_f32 v34, v34, v32
v_mul_f32 v35, v35, v32
Mvaa 92,76,180
s_add_u32 s70, s67, 1064960
ds_read_b64_tr_b16 a[126:127], v1 offset:28680
v_cvt_pk_bf16_f32 v247, v34, v35
v_accvgpr_read_b32 v34, a52
v_accvgpr_read_b32 v35, a53
v_mul_f32 v34, v34, v33
v_mul_f32 v35, v35, v33
Mvaa 92,84,184
s_waitcnt lgkmcnt(6)
ds_read_b128 v[160:163], v0 offset:20480
buffer_load_dwordx4 v25, s[28:31], s70 offen lds
v_cvt_pk_bf16_f32 v252, v34, v35
v_accvgpr_read_b32 v34, a54
v_accvgpr_read_b32 v35, a55
Mvaa 92,92,188
ds_read_b128 v[168:171], v0 offset:21504
buffer_store_dwordx4 v[244:247], v0, s[48:51], s96 offen offset:3072 sc1
v_mul_f32 v34, v34, v33
v_mul_f32 v35, v35, v33
v_cvt_pk_bf16_f32 v253, v34, v35
Mvav 64,96,128
s_add_u32 m0, s69, 150400
ds_read_b128 v[176:179], v0 offset:22528
v_accvgpr_read_b32 v34, a60
v_accvgpr_read_b32 v35, a61
v_mul_f32 v34, v34, v33
v_mul_f32 v35, v35, v33
v_cvt_pk_bf16_f32 v254, v34, v35
Mvav 64,104,136
s_add_u32 s70, s67, 1064960
ds_read_b128 v[184:187], v0 offset:23552
v_accvgpr_read_b32 v34, a62
v_accvgpr_read_b32 v35, a63
v_mul_f32 v34, v34, v33
v_mul_f32 v35, v35, v33
v_cvt_pk_bf16_f32 v255, v34, v35
Mvav 64,112,144
ds_read_b128 v[132:135], v0 offset:24576
buffer_load_dwordx4 v25, s[28:31], s70 offen offset:128 lds
Mvav 64,120,152
ds_read_b128 v[140:143], v0 offset:25600
buffer_store_dwordx4 v[252:255], v0, s[48:51], s83 offen offset:3072 sc1
Mvav 68,100,128
s_waitcnt lgkmcnt(10)
ds_read_b128 v[148:151], v0 offset:26624
Mvav 68,108,136
s_waitcnt lgkmcnt(9)
ds_read_b128 v[156:159], v0 offset:27648
Mvav 68,116,144
s_waitcnt lgkmcnt(6)
ds_read_b128 v[164:167], v0 offset:28672
Mvav 68,124,152
s_add_u32 s60, s69, 32768
ds_read_b128 v[172:175], v0 offset:29696
v_lshrrev_b32 v11, 8, v0
Mvav 72,96,160
s_add_u32 m0, s69, 65536
ds_read_b128 v[180:183], v0 offset:30720
ds_read_b128 v[188:191], v0 offset:31744
Mvav 72,104,168
s_waitcnt vmcnt(21) lgkmcnt(7)
s_barrier
s_add_u32 s70, s67, 1179648
Mvav 72,112,176
s_waitcnt lgkmcnt(6)
ds_read_b128 v[128:131], v0 offset:32768
buffer_load_dwordx4 v25, s[28:31], s70 offen lds
Mvav 72,120,184
s_waitcnt lgkmcnt(6)
s_add_u32 m0, s69, 66432
ds_read_b128 v[136:139], v0 offset:33792
v_mov_b32 v26, s86
Mvav 76,100,160
s_waitcnt lgkmcnt(6)
s_add_u32 s70, s67, 1179648
ds_read_b128 v[144:147], v0 offset:34816
v_mov_b32 v27, s87
Mvav 76,108,168
ds_read_b128 v[152:155], v0 offset:35840
buffer_load_dwordx4 v25, s[28:31], s70 offen offset:128 lds
Mvav 76,116,176
s_add_u32 m0, s69, 67584
ds_read_b64_tr_b16 a[128:129], v10 offset:0
v_mov_b32 v28, 0x80000000
Mvav 76,124,184
s_waitcnt lgkmcnt(6)
s_add_u32 s70, s67, 1196032
s_cmp_eq_u32 s63, 7
s_cselect_b32 s97, s97, 0x80000000
ds_read_b64_tr_b16 a[130:131], v10 offset:4096
Mvav 80,96,132
ds_read_b64_tr_b16 a[136:137], v10 offset:8192
buffer_load_dwordx4 v25, s[28:31], s70 offen lds
Mvav 80,104,140
s_add_u32 m0, s69, 68480
ds_read_b64_tr_b16 a[138:139], v10 offset:12288
v_writelane_b32 v28, s97, 0
Mvav 80,112,148
s_waitcnt lgkmcnt(6)
s_add_u32 s70, s67, 1196032
ds_read_b64_tr_b16 a[144:145], v1 offset:32768
v_writelane_b32 v28, s38, 1
Mvav 80,120,156
ds_read_b64_tr_b16 a[146:147], v1 offset:36864
buffer_load_dwordx4 v25, s[28:31], s70 offen offset:128 lds
Mvav 84,100,132
s_lshl3_add_u32 s64, s95, s71
ds_read_b64_tr_b16 a[152:153], v1 offset:40960
v_lshlrev_b32 v11, 12, v11
Mvav 84,108,140
s_add_u32 m0, s69, 81920
ds_read_b64_tr_b16 a[154:155], v1 offset:45056
v_and_b32 v56, 128, v0
Mvav 84,116,148
s_add_u32 s70, s73, 147456
ds_read_b64_tr_b16 a[132:133], v10 offset:8
v_lshl_add_u32 v11, v56, 2, v11
Mvav 84,124,156
ds_read_b64_tr_b16 a[134:135], v10 offset:4104
buffer_load_dwordx4 v0, s[48:51], s70 offen sc1 lds
Mvav 88,96,164
s_and_b32 s39, s67, 0x700
ds_read_b64_tr_b16 a[140:141], v10 offset:8200
v_and_b32 v56, 112, v0
Mvav 88,104,172
s_waitcnt vmcnt(21) lgkmcnt(7)
s_lshr_b32 s61, s69, 6
ds_read_b64_tr_b16 a[142:143], v10 offset:12296
v_xor_b32 v16, s86, v16
Mvav 88,112,180
ds_read_b64_tr_b16 a[148:149], v1 offset:32776
buffer_load_dwordx4 v0, s[48:51], s70 offen offset:1024 sc1 lds
Mvav 88,120,188
s_add_u32 s39, s39, s61
ds_read_b64_tr_b16 a[150:151], v1 offset:36872
v_bitop3_b32 v16, v17, s87, v16 bitop3:0xbe
Mvav 92,100,164
s_lshl_b32 s61, s64, 15
ds_read_b64_tr_b16 a[156:157], v1 offset:40968
)"
R"(
v_bitop3_b32 v16, v18, s86, v16 bitop3:0xbe
Mvav 92,108,172
ds_read_b64_tr_b16 a[158:159], v1 offset:45064
buffer_load_dwordx4 v0, s[48:51], s70 offen offset:2048 sc1 lds
Mvav 92,116,180
s_add_u32 s61, s61, s39
ds_read_b128 v[160:163], v0 offset:36864
v_bitop3_b32 v16, v19, s87, v16 bitop3:0xbe
Mvav 92,124,188
s_lshr_b32 s68, s39, 6
ds_read_b128 v[168:171], v0 offset:37888
v_and_b32 v16, v16, v21
Mvav 64,128,128
s_waitcnt lgkmcnt(10)
ds_read_b128 v[176:179], v0 offset:38912
v_cmp_ne_u32 vcc, 0, v16
Mvav 64,136,136
ds_read_b128 v[184:187], v0 offset:39936
buffer_load_dwordx4 v0, s[48:51], s70 offen offset:3072 sc1 lds
Mvav 64,144,144
s_cbranch_vccnz .Lcf2
.Lcf2_r:
ds_read_b128 v[132:135], v0 offset:40960
Mvav 64,152,152
s_waitcnt lgkmcnt(11)
ds_read_b128 v[140:143], v0 offset:41984
v_add_u32 v11, v56, v11
Mvav 68,132,128
s_waitcnt lgkmcnt(10)
ds_read_b128 v[148:151], v0 offset:43008
v_bfe_u32 v60, v0, 6, 2
Mvav 68,140,136
s_waitcnt lgkmcnt(9)
ds_read_b128 v[156:159], v0 offset:44032
v_lshlrev_b32 v60, 6, v60
Mvav 68,148,144
s_waitcnt lgkmcnt(8)
ds_read_b128 v[164:167], v0 offset:45056
v_bfe_u32 v58, v0, 4, 2
Mvav 68,156,152
s_waitcnt lgkmcnt(7)
ds_read_b128 v[172:175], v0 offset:46080
v_lshl_add_u32 v60, v58, 3, v60
Mvav 72,128,160
s_add_u32 m0, s69, 0
ds_read_b128 v[180:183], v0 offset:47104
ds_read_b128 v[188:191], v0 offset:48128
Mvav 72,136,168
s_waitcnt vmcnt(16) lgkmcnt(8)
s_barrier
s_add_u32 s70, s12, 4718592
Mvav 72,144,176
ds_read_b128 v[128:131], v0 offset:49152
buffer_load_dwordx4 v0, s[48:51], s70 offen sc1 lds
Mvav 72,152,184
ds_read_b128 v[136:139], v0 offset:50176
v_add_u32 v21, s69, v0
Mvav 76,132,160
ds_read_b128 v[144:147], v0 offset:51200
v_subrev_u32 v21, 448, v21
Mvav 76,140,168
ds_read_b128 v[152:155], v0 offset:52224
buffer_load_dwordx4 v0, s[48:51], s70 offen offset:1024 sc1 lds
Mvav 76,148,176
ds_read_b64_tr_b16 a[160:161], v1 offset:49152
v_cmp_gt_u32 vcc, 576, v21
Mvav 76,156,184
s_waitcnt lgkmcnt(11)
ds_read_b64_tr_b16 a[162:163], v1 offset:53248
v_lshrrev_b32 v58, 8, v0
Mvav 80,128,132
ds_read_b64_tr_b16 a[168:169], v1 offset:57344
buffer_load_dwordx4 v0, s[48:51], s70 offen offset:2048 sc1 lds
Mvav 80,136,140
s_waitcnt lgkmcnt(12)
ds_read_b64_tr_b16 a[170:171], v1 offset:61440
v_cndmask_b32 v21, 0, -1, vcc
Mvav 80,144,148
s_waitcnt lgkmcnt(9)
ds_read_b64_tr_b16 a[176:177], v3 offset:0
v_not_b32 v20, v21
Mvav 80,152,156
ds_read_b64_tr_b16 a[178:179], v3 offset:4096
buffer_load_dwordx4 v0, s[48:51], s70 offen offset:3072 sc1 lds
Mvav 84,132,132
s_add_u32 m0, s69, 16384
ds_read_b64_tr_b16 a[184:185], v3 offset:8192
v_lshl_or_b32 v20, v20, 31, v0
Mvav 84,140,140
s_add_u32 s70, s67, 1310720
ds_read_b64_tr_b16 a[186:187], v3 offset:12288
v_lshl_add_u32 v60, v58, 10, v60
Mvav 84,148,148
ds_read_b64_tr_b16 a[164:165], v1 offset:49160
buffer_load_dwordx4 v25, s[28:31], s70 offen lds
Mvav 84,156,156
s_waitcnt lgkmcnt(8)
ds_read_b64_tr_b16 a[166:167], v1 offset:53256
v_add_u32 v60, s60, v60
Mvav 88,128,164
s_waitcnt lgkmcnt(6)
s_add_u32 m0, s69, 17280
ds_read_b64_tr_b16 a[172:173], v1 offset:57352
v_add_u32 v61, 81920, v60
Mvav 88,136,172
s_add_u32 s70, s67, 1310720
ds_read_b64_tr_b16 a[174:175], v1 offset:61448
v_lshrrev_b32 v5, 6, v0
Mvav 88,144,180
ds_read_b64_tr_b16 a[180:181], v3 offset:8
buffer_load_dwordx4 v25, s[28:31], s70 offen offset:128 lds
Mvav 88,152,188
s_waitcnt lgkmcnt(6)
s_add_u32 m0, s69, 18432
ds_read_b64_tr_b16 a[182:183], v3 offset:4104
v_and_b32 v6, 48, v0
Mvav 92,132,164
s_waitcnt lgkmcnt(6)
s_add_u32 s70, s67, 1327104
ds_read_b64_tr_b16 a[188:189], v3 offset:8200
v_lshl_add_u32 v4, v5, 11, v6
Mvav 92,140,172
ds_read_b64_tr_b16 a[190:191], v3 offset:12296
buffer_load_dwordx4 v25, s[28:31], s70 offen lds
Mvav 92,148,180
s_waitcnt lgkmcnt(6)
s_add_u32 m0, s69, 19328
ds_read_b128 v[160:163], v0 offset:53248
v_add_u32 v5, v5, v6
Mvav 92,156,188
s_add_u32 s70, s67, 1327104
ds_read_b128 v[168:171], v0 offset:54272
v_lshlrev_b32 v5, 2, v5
Mvav 64,160,128
ds_read_b128 v[176:179], v0 offset:55296
buffer_load_dwordx4 v25, s[28:31], s70 offen offset:128 lds
Mvav 64,168,136
s_add_u32 m0, s69, 98304
ds_read_b128 v[184:187], v0 offset:56320
v_bfe_u32 v7, v0, 4, 3
Mvav 64,176,144
s_add_u32 s70, s67, 1441792
ds_read_b128 v[132:135], v0 offset:57344
v_lshrrev_b32 v9, 8, v0
Mvav 64,184,152
ds_read_b128 v[140:143], v0 offset:58368
buffer_load_dwordx4 v25, s[28:31], s70 offen lds
Mvav 68,164,128
s_waitcnt lgkmcnt(8)
ds_read_b128 v[148:151], v0 offset:59392
v_lshl_add_u32 v7, v7, 2, v9
Mvav 68,172,136
s_add_u32 m0, s69, 99200
ds_read_b128 v[156:159], v0 offset:60416
v_lshlrev_b32 v7, 1, v7
Mvav 68,180,144
s_waitcnt lgkmcnt(6)
s_add_u32 s70, s67, 1441792
ds_read_b128 v[164:167], v0 offset:61440
Mvav 68,188,152
ds_read_b128 v[172:175], v0 offset:62464
buffer_load_dwordx4 v25, s[28:31], s70 offen offset:128 lds
Mvav 72,160,160
s_add_u32 m0, s69, 100352
ds_read_b128 v[180:183], v0 offset:63488
ds_read_b128 v[188:191], v0 offset:64512
Mvav 72,168,168
s_waitcnt vmcnt(10) lgkmcnt(8)
s_barrier
s_add_u32 s70, s67, 1458176
Mvav 72,176,176
ds_read_b128 v[128:131], v12 offset:16384
buffer_load_dwordx4 v25, s[28:31], s70 offen lds
Mvav 72,184,184
s_add_u32 m0, s69, 101248
ds_read_b128 v[136:139], v12 offset:17408
buffer_store_dwordx2 v[26:27], v28, s[48:51], 0 offen sc1
Mvav 76,164,160
s_add_u32 s70, s67, 1458176
ds_read_b128 v[144:147], v12 offset:18432
Mvav 76,172,168
ds_read_b128 v[152:155], v12 offset:19456
buffer_load_dwordx4 v25, s[28:31], s70 offen offset:128 lds
Mvav 76,180,176
s_add_u32 m0, s69, 32768
ds_read_b64_tr_b16 a[192:193], v3 offset:16384
Mvav 76,188,184
s_waitcnt lgkmcnt(11)
s_add_u32 s70, s73, 184320
ds_read_b64_tr_b16 a[194:195], v3 offset:20480
Mvav 80,160,132
ds_read_b64_tr_b16 a[200:201], v3 offset:24576
buffer_load_dwordx4 v0, s[48:51], s70 offen sc1 lds
Mvav 80,168,140
s_waitcnt lgkmcnt(12)
ds_read_b64_tr_b16 a[202:203], v3 offset:28672
Mvav 80,176,148
s_waitcnt lgkmcnt(12)
ds_read_b64_tr_b16 a[208:209], v1 offset:0
Mvav 80,184,156
ds_read_b64_tr_b16 a[210:211], v1 offset:4096
buffer_load_dwordx4 v0, s[48:51], s70 offen offset:1024 sc1 lds
Mvav 84,164,132
ds_read_b64_tr_b16 a[216:217], v1 offset:8192
Mvav 84,172,140
s_waitcnt lgkmcnt(12)
ds_read_b64_tr_b16 a[218:219], v1 offset:12288
Mvav 84,180,148
ds_read_b64_tr_b16 a[196:197], v3 offset:16392
Mvav 84,188,156
ds_read_b64_tr_b16 a[198:199], v3 offset:20488
buffer_load_dwordx4 v0, s[48:51], s70 offen offset:2048 sc1 lds
Mvav 88,160,164
s_waitcnt lgkmcnt(12)
ds_read_b64_tr_b16 a[204:205], v3 offset:24584
Mvav 88,168,172
ds_read_b64_tr_b16 a[206:207], v3 offset:28680
Mvav 88,176,180
ds_read_b64_tr_b16 a[212:213], v1 offset:8
buffer_load_dwordx4 v0, s[48:51], s70 offen offset:3072 sc1 lds
Mvav 88,184,188
s_waitcnt lgkmcnt(12)
s_add_u32 m0, s69, 114688
ds_read_b64_tr_b16 a[214:215], v1 offset:4104
Mvav 92,164,164
s_add_u32 s70, s67, 1572864
ds_read_b64_tr_b16 a[220:221], v1 offset:8200
Mvav 92,172,172
ds_read_b64_tr_b16 a[222:223], v1 offset:12296
buffer_load_dwordx4 v25, s[28:31], s70 offen lds
Mvav 92,180,180
s_waitcnt lgkmcnt(12)
s_add_u32 m0, s69, 115584
ds_read_b128 v[160:163], v12 offset:20480
Mvav 92,188,188
s_add_u32 s70, s67, 1572864
ds_read_b128 v[168:171], v12 offset:21504
Mvav 64,192,128
ds_read_b128 v[176:179], v12 offset:22528
buffer_load_dwordx4 v25, s[28:31], s70 offen offset:128 lds
Mvav 64,200,136
s_waitcnt lgkmcnt(12)
ds_read_b128 v[184:187], v12 offset:23552
Mvav 64,208,144
s_waitcnt lgkmcnt(12)
s_add_u32 m0, s69, 116736
ds_read_b128 v[132:135], v12 offset:24576
Mvav 64,216,152
s_waitcnt lgkmcnt(9)
s_add_u32 s70, s67, 1589248
ds_read_b128 v[140:143], v12 offset:25600
Mvav 68,196,128
ds_read_b128 v[148:151], v12 offset:26624
buffer_load_dwordx4 v25, s[28:31], s70 offen lds
Mvav 68,204,136
s_waitcnt lgkmcnt(9)
s_add_u32 m0, s69, 117632
ds_read_b128 v[156:159], v12 offset:27648
Mvav 68,212,144
s_waitcnt lgkmcnt(7)
s_add_u32 s70, s67, 1589248
ds_read_b128 v[164:167], v12 offset:28672
Mvav 68,220,152
ds_read_b128 v[172:175], v12 offset:29696
buffer_load_dwordx4 v25, s[28:31], s70 offen offset:128 lds
Mvav 72,192,160
s_waitcnt lgkmcnt(8)
ds_read_b128 v[180:183], v12 offset:30720
ds_read_b128 v[188:191], v12 offset:31744
Mvav 72,200,168
s_waitcnt vmcnt(17) lgkmcnt(9)
s_barrier
s_add_u32 m0, s69, 131072
Mvav 72,208,176
s_waitcnt lgkmcnt(8)
s_add_u32 s70, s67, 1703936
ds_read_b128 v[128:131], v0 offset:0
Mvav 72,216,184
buffer_load_dwordx4 v25, s[28:31], s70 offen lds
Mvav 76,196,160
s_add_u32 m0, s69, 131968
ds_read_b128 v[136:139], v0 offset:1024
Mvav 76,204,168
s_add_u32 s70, s67, 1703936
buffer_load_dwordx4 v25, s[28:31], s70 offen offset:128 lds
Mvav 76,212,176
ds_read_b128 v[144:147], v0 offset:2048
Mvav 76,220,184
s_waitcnt lgkmcnt(10)
s_add_u32 m0, s69, 133120
Mvav 80,192,132
s_waitcnt lgkmcnt(9)
s_add_u32 s70, s67, 1720320
ds_read_b128 v[152:155], v0 offset:3072
Mvav 80,200,140
s_waitcnt lgkmcnt(9)
buffer_load_dwordx4 v25, s[28:31], s70 offen lds
Mvav 80,208,148
s_waitcnt lgkmcnt(8)
ds_read_b128 v[160:163], v0 offset:4096
Mvav 80,216,156
s_add_u32 m0, s69, 134016
Mvav 84,196,132
s_add_u32 s70, s67, 1720320
ds_read_b128 v[168:171], v0 offset:5120
Mvav 84,204,140
buffer_load_dwordx4 v25, s[28:31], s70 offen offset:128 lds
Mvav 84,212,148
s_waitcnt lgkmcnt(9)
s_add_u32 m0, s69, 49152
ds_read_b128 v[176:179], v0 offset:6144
Mvav 84,220,156
s_add_u32 s70, s73, 221184
buffer_load_dwordx4 v0, s[48:51], s70 offen sc1 lds
Mvav 88,192,164
s_waitcnt lgkmcnt(9)
ds_read_b128 v[184:187], v0 offset:7168
Mvav 88,200,172
s_waitcnt lgkmcnt(9)
Mvav 88,208,180
s_waitcnt lgkmcnt(8)
ds_read_b128 v[132:135], v0 offset:8192
Mvav 88,216,188
buffer_load_dwordx4 v0, s[48:51], s70 offen offset:1024 sc1 lds
Mvav 92,196,164
ds_read_b128 v[140:143], v0 offset:9216
Mvav 92,204,172
buffer_load_dwordx4 v0, s[48:51], s70 offen offset:2048 sc1 lds
Mvav 92,212,180
ds_read_b128 v[148:151], v0 offset:10240
Mvav 92,220,188
s_waitcnt lgkmcnt(10)
Zvav 96,64,128
s_waitcnt lgkmcnt(9)
ds_read_b128 v[156:159], v0 offset:11264
Mvav 96,72,136
s_waitcnt lgkmcnt(9)
buffer_load_dwordx4 v0, s[48:51], s70 offen offset:3072 sc1 lds
Mvav 96,80,144
s_waitcnt lgkmcnt(8)
ds_read_b128 v[164:167], v0 offset:12288
Mvav 96,88,152
s_add_u32 m0, s69, 65536
Zvav 100,68,128
s_add_u32 s70, s13, 4718592
ds_read_b128 v[172:175], v0 offset:13312
Mvav 100,76,136
buffer_load_dwordx4 v0, s[48:51], s70 offen sc1 lds
Mvav 100,84,144
ds_read_b128 v[180:183], v0 offset:14336
Mvav 100,92,152
s_waitcnt lgkmcnt(10)
Mvav 100,100,160
s_waitcnt lgkmcnt(9)
ds_read_b128 v[188:191], v0 offset:15360
Mvav 100,108,168
s_waitcnt vmcnt(13) lgkmcnt(8)
s_barrier
Mvav 100,116,176
ds_read_b128 v[128:131], v0 offset:32768
buffer_load_dwordx4 v0, s[48:51], s70 offen offset:1024 sc1 lds
Mvav 100,124,184
ds_read_b128 v[136:139], v0 offset:33792
Mvav 96,96,160
ds_read_b128 v[144:147], v0 offset:34816
Mvav 96,104,168
ds_read_b128 v[152:155], v0 offset:35840
buffer_load_dwordx4 v0, s[48:51], s70 offen offset:2048 sc1 lds
Mvav 96,112,176
ds_read_b64_tr_b16 a[224:225], v10 offset:16384
Mvav 96,120,184
s_waitcnt lgkmcnt(12)
ds_read_b64_tr_b16 a[226:227], v10 offset:20480
Mvav 96,128,132
s_waitcnt lgkmcnt(11)
ds_read_b64_tr_b16 a[232:233], v10 offset:24576
Mvav 96,136,140
ds_read_b64_tr_b16 a[234:235], v10 offset:28672
buffer_load_dwordx4 v0, s[48:51], s70 offen offset:3072 sc1 lds
Mvav 96,144,148
s_waitcnt lgkmcnt(12)
ds_read_b64_tr_b16 a[240:241], v1 offset:32768
Mvav 96,152,156
s_add_u32 m0, s69, 147456
ds_read_b64_tr_b16 a[242:243], v1 offset:36864
Mvav 100,132,132
s_waitcnt lgkmcnt(11)
s_add_u32 s70, s67, 1835008
ds_read_b64_tr_b16 a[248:249], v1 offset:40960
Mvav 100,140,140
ds_read_b64_tr_b16 a[250:251], v1 offset:45056
buffer_load_dwordx4 v25, s[28:31], s70 offen lds
Mvav 100,148,148
ds_read_b64_tr_b16 a[228:229], v10 offset:16392
Mvav 100,156,156
s_add_u32 m0, s69, 148352
ds_read_b64_tr_b16 a[230:231], v10 offset:20488
Mvav 100,164,164
s_waitcnt lgkmcnt(12)
s_add_u32 s70, s67, 1835008
ds_read_b64_tr_b16 a[236:237], v10 offset:24584
Mvav 100,172,172
ds_read_b64_tr_b16 a[238:239], v10 offset:28680
)"
R"(
buffer_load_dwordx4 v25, s[28:31], s70 offen offset:128 lds
Mvav 100,180,180
ds_read_b64_tr_b16 a[244:245], v1 offset:32776
Mvav 100,188,188
s_waitcnt lgkmcnt(12)
s_add_u32 m0, s69, 149504
ds_read_b64_tr_b16 a[246:247], v1 offset:36872
Mvav 96,160,164
s_add_u32 s70, s67, 1851392
ds_read_b64_tr_b16 a[252:253], v1 offset:40968
Mvav 96,168,172
ds_read_b64_tr_b16 a[254:255], v1 offset:45064
buffer_load_dwordx4 v25, s[28:31], s70 offen lds
Mvav 96,176,180
s_waitcnt lgkmcnt(12)
ds_read_b128 v[160:163], v0 offset:36864
Mvav 96,184,188
s_add_u32 m0, s69, 150400
ds_read_b128 v[168:171], v0 offset:37888
Mvav 64,224,128
s_waitcnt lgkmcnt(11)
s_add_u32 s70, s67, 1851392
ds_read_b128 v[176:179], v0 offset:38912
Mvav 64,232,136
ds_read_b128 v[184:187], v0 offset:39936
buffer_load_dwordx4 v25, s[28:31], s70 offen offset:128 lds
Mvav 64,240,144
s_waitcnt lgkmcnt(12)
ds_read_b128 v[132:135], v0 offset:40960
Mvav 64,248,152
s_waitcnt lgkmcnt(11)
s_add_u32 m0, s69, 81920
ds_read_b128 v[140:143], v0 offset:41984
Mvav 68,228,128
s_waitcnt lgkmcnt(8)
s_add_u32 s70, s67, 1966080
ds_read_b128 v[148:151], v0 offset:43008
Mvav 68,236,136
ds_read_b128 v[156:159], v0 offset:44032
buffer_load_dwordx4 v25, s[28:31], s70 offen lds
Mvav 68,244,144
s_waitcnt lgkmcnt(8)
ds_read_b128 v[164:167], v0 offset:45056
Mvav 68,252,152
s_waitcnt lgkmcnt(7)
ds_read_b128 v[172:175], v0 offset:46080
Mvav 72,224,160
s_add_u32 m0, s69, 82816
ds_read_b128 v[180:183], v0 offset:47104
ds_read_b128 v[188:191], v0 offset:48128
Mvav 72,232,168
s_waitcnt vmcnt(9) lgkmcnt(8)
s_barrier
s_add_u32 s70, s67, 1966080
Mvav 72,240,176
ds_read_b128 v[128:131], v0 offset:49152
buffer_load_dwordx4 v25, s[28:31], s70 offen offset:128 lds
Mvav 72,248,184
s_add_u32 m0, s69, 83968
ds_read_b128 v[136:139], v0 offset:50176
Mvav 76,228,160
s_add_u32 s70, s67, 1982464
ds_read_b128 v[144:147], v0 offset:51200
Mvav 76,236,168
ds_read_b128 v[152:155], v0 offset:52224
buffer_load_dwordx4 v25, s[28:31], s70 offen lds
Mvav 76,244,176
ds_read_b64_tr_b16 a[0:1], v1 offset:49152
Mvav 76,252,184
s_waitcnt lgkmcnt(12)
s_add_u32 m0, s69, 84864
ds_read_b64_tr_b16 a[2:3], v1 offset:53248
Mvav 80,224,132
s_waitcnt lgkmcnt(11)
s_add_u32 s70, s67, 1982464
ds_read_b64_tr_b16 a[8:9], v1 offset:57344
Mvav 80,232,140
ds_read_b64_tr_b16 a[10:11], v1 offset:61440
buffer_load_dwordx4 v25, s[28:31], s70 offen offset:128 lds
Mvav 80,240,148
s_waitcnt lgkmcnt(12)
ds_read_b64_tr_b16 a[16:17], v3 offset:0
Mvav 80,248,156
s_add_u32 m0, s69, 0
ds_read_b64_tr_b16 a[18:19], v3 offset:4096
Mvav 84,228,132
s_waitcnt lgkmcnt(11)
s_add_u32 s70, s73, 258048
ds_read_b64_tr_b16 a[24:25], v3 offset:8192
Mvav 84,236,140
ds_read_b64_tr_b16 a[26:27], v3 offset:12288
buffer_load_dwordx4 v0, s[48:51], s70 offen sc1 lds
Mvav 84,244,148
ds_read_b64_tr_b16 a[4:5], v1 offset:49160
Mvav 84,252,156
ds_read_b64_tr_b16 a[6:7], v1 offset:53256
Mvav 88,224,164
s_waitcnt lgkmcnt(12)
ds_read_b64_tr_b16 a[12:13], v1 offset:57352
Mvav 88,232,172
ds_read_b64_tr_b16 a[14:15], v1 offset:61448
buffer_load_dwordx4 v0, s[48:51], s70 offen offset:1024 sc1 lds
Mvav 88,240,180
ds_read_b64_tr_b16 a[20:21], v3 offset:8
Mvav 88,248,188
s_waitcnt lgkmcnt(12)
ds_read_b64_tr_b16 a[22:23], v3 offset:4104
Mvav 92,228,164
ds_read_b64_tr_b16 a[28:29], v3 offset:8200
Mvav 92,236,172
ds_read_b64_tr_b16 a[30:31], v3 offset:12296
buffer_load_dwordx4 v0, s[48:51], s70 offen offset:2048 sc1 lds
Mvav 92,244,180
s_waitcnt lgkmcnt(12)
ds_read_b128 v[160:163], v0 offset:53248
Mvav 92,252,188
ds_read_b128 v[168:171], v0 offset:54272
Mvav 64,0,128
s_waitcnt lgkmcnt(11)
ds_read_b128 v[176:179], v0 offset:55296
Mvav 64,8,136
ds_read_b128 v[184:187], v0 offset:56320
buffer_load_dwordx4 v0, s[48:51], s70 offen offset:3072 sc1 lds
Mvav 64,16,144
s_waitcnt lgkmcnt(12)
ds_read_b128 v[132:135], v0 offset:57344
Mvav 64,24,152
s_waitcnt lgkmcnt(11)
s_add_u32 m0, s69, 16384
ds_read_b128 v[140:143], v0 offset:58368
Mvav 68,4,128
s_waitcnt lgkmcnt(8)
s_add_u32 s70, s12, 4866048
ds_read_b128 v[148:151], v0 offset:59392
Mvav 68,12,136
ds_read_b128 v[156:159], v0 offset:60416
buffer_load_dwordx4 v0, s[48:51], s70 offen sc1 lds
Mvav 68,20,144
s_waitcnt lgkmcnt(8)
ds_read_b128 v[164:167], v0 offset:61440
Mvav 68,28,152
s_waitcnt lgkmcnt(8)
ds_read_b128 v[172:175], v0 offset:62464
Mvav 72,0,160
s_waitcnt lgkmcnt(8)
ds_read_b128 v[180:183], v0 offset:63488
ds_read_b128 v[188:191], v0 offset:64512
Mvav 72,8,168
s_waitcnt vmcnt(13) lgkmcnt(8)
s_barrier
Mvav 72,16,176
ds_read_b128 v[128:131], v12 offset:0
buffer_load_dwordx4 v0, s[48:51], s70 offen offset:1024 sc1 lds
Mvav 72,24,184
Mvav 76,4,160
ds_read_b128 v[136:139], v12 offset:1024
Mvav 76,12,168
buffer_load_dwordx4 v0, s[48:51], s70 offen offset:2048 sc1 lds
Mvav 76,20,176
ds_read_b128 v[144:147], v12 offset:2048
buffer_load_dwordx4 v[16:19], v20, s[48:51], s82 offen sc1
Mvav 76,28,184
s_waitcnt lgkmcnt(10)
Mvav 80,0,132
s_waitcnt lgkmcnt(9)
ds_read_b128 v[152:155], v12 offset:3072
Mvav 80,8,140
s_waitcnt lgkmcnt(9)
buffer_load_dwordx4 v0, s[48:51], s70 offen offset:3072 sc1 lds
Mvav 80,16,148
s_waitcnt lgkmcnt(8)
ds_read_b128 v[160:163], v12 offset:4096
Mvav 80,24,156
s_add_u32 m0, s69, 98304
Mvav 84,4,132
s_add_u32 s70, s13, 4866048
ds_read_b128 v[168:171], v12 offset:5120
Mvav 84,12,140
buffer_load_dwordx4 v0, s[48:51], s70 offen sc1 lds
Mvav 84,20,148
ds_read_b128 v[176:179], v12 offset:6144
Mvav 84,28,156
s_waitcnt lgkmcnt(10)
Mvav 88,0,164
s_waitcnt lgkmcnt(9)
ds_read_b128 v[184:187], v12 offset:7168
Mvav 88,8,172
s_waitcnt lgkmcnt(9)
buffer_load_dwordx4 v0, s[48:51], s70 offen offset:1024 sc1 lds
Mvav 88,16,180
s_waitcnt lgkmcnt(8)
ds_read_b128 v[132:135], v12 offset:8192
Mvav 88,24,188
Mvav 92,4,164
ds_read_b128 v[140:143], v12 offset:9216
Mvav 92,12,172
buffer_load_dwordx4 v0, s[48:51], s70 offen offset:2048 sc1 lds
Mvav 92,20,180
ds_read_b128 v[148:151], v12 offset:10240
Mvav 92,28,188
s_waitcnt lgkmcnt(10)
Zvav 104,64,128
s_waitcnt lgkmcnt(9)
ds_read_b128 v[156:159], v12 offset:11264
Mvav 104,72,136
s_waitcnt lgkmcnt(9)
buffer_load_dwordx4 v0, s[48:51], s70 offen offset:3072 sc1 lds
Mvav 104,80,144
s_waitcnt lgkmcnt(8)
ds_read_b128 v[164:167], v12 offset:12288
Mvav 104,88,152
s_add_u32 m0, s69, 32768
buffer_load_dwordx4 v[40:43], v11, s[48:51], s21 offen sc1
Zvav 108,68,128
s_add_u32 s70, s20, 0
ds_read_b128 v[172:175], v12 offset:13312
Mvav 108,76,136
buffer_load_dwordx4 v25, s[44:47], s70 offen lds
Mvav 108,84,144
ds_read_b128 v[180:183], v12 offset:14336
Mvav 108,92,152
s_waitcnt lgkmcnt(10)
Mvav 108,100,160
s_waitcnt lgkmcnt(9)
s_add_u32 m0, s69, 33792
ds_read_b128 v[188:191], v12 offset:15360
Mvav 108,108,168
s_waitcnt vmcnt(11) lgkmcnt(8)
s_barrier
s_add_u32 s70, s20, 16384
Mvav 108,116,176
ds_read_b128 v[128:131], v0 offset:0
buffer_load_dwordx4 v25, s[44:47], s70 offen lds
Mvav 108,124,184
s_add_u32 m0, s69, 34816
ds_read_b128 v[136:139], v0 offset:1024
Mvav 104,96,160
s_add_u32 s70, s20, 32768
ds_read_b128 v[144:147], v0 offset:2048
Mvav 104,104,168
ds_read_b128 v[152:155], v0 offset:3072
buffer_load_dwordx4 v25, s[44:47], s70 offen lds
Mvav 104,112,176
ds_read_b64_tr_b16 a[32:33], v3 offset:16384
Mvav 104,120,184
s_waitcnt lgkmcnt(12)
s_add_u32 m0, s69, 35840
ds_read_b64_tr_b16 a[34:35], v3 offset:20480
Mvav 104,128,132
s_waitcnt lgkmcnt(11)
s_add_u32 s70, s20, 49152
ds_read_b64_tr_b16 a[40:41], v3 offset:24576
Mvav 104,136,140
ds_read_b64_tr_b16 a[42:43], v3 offset:28672
buffer_load_dwordx4 v25, s[44:47], s70 offen lds
Mvav 104,144,148
s_waitcnt lgkmcnt(12)
ds_read_b64_tr_b16 a[48:49], v1 offset:16384
Mvav 104,152,156
s_add_u32 m0, s69, 114688
ds_read_b64_tr_b16 a[50:51], v1 offset:20480
Mvav 108,132,132
s_waitcnt lgkmcnt(11)
s_add_u32 s70, s20, 65536
ds_read_b64_tr_b16 a[56:57], v1 offset:24576
Mvav 108,140,140
ds_read_b64_tr_b16 a[58:59], v1 offset:28672
buffer_load_dwordx4 v25, s[44:47], s70 offen lds
Mvav 108,148,148
ds_read_b64_tr_b16 a[36:37], v3 offset:16392
Mvav 108,156,156
s_add_u32 m0, s69, 115712
ds_read_b64_tr_b16 a[38:39], v3 offset:20488
Mvav 108,164,164
s_waitcnt lgkmcnt(12)
s_add_u32 s70, s20, 81920
ds_read_b64_tr_b16 a[44:45], v3 offset:24584
Mvav 108,172,172
ds_read_b64_tr_b16 a[46:47], v3 offset:28680
buffer_load_dwordx4 v25, s[44:47], s70 offen lds
Mvav 108,180,180
s_waitcnt vmcnt(12) lgkmcnt(11)
ds_read_b64_tr_b16 a[52:53], v1 offset:16392
v_xor_b32 v16, s86, v16
Mvav 108,188,188
s_add_u32 m0, s69, 116736
ds_read_b64_tr_b16 a[54:55], v1 offset:20488
v_bitop3_b32 v16, v17, s87, v16 bitop3:0xbe
Mvav 104,160,164
s_add_u32 s70, s20, 98304
ds_read_b64_tr_b16 a[60:61], v1 offset:24584
v_bitop3_b32 v16, v18, s86, v16 bitop3:0xbe
Mvav 104,168,172
ds_read_b64_tr_b16 a[62:63], v1 offset:28680
buffer_load_dwordx4 v25, s[44:47], s70 offen lds
Mvav 104,176,180
s_waitcnt lgkmcnt(8)
ds_read_b128 v[160:163], v0 offset:4096
v_bitop3_b32 v16, v19, s87, v16 bitop3:0xbe
Mvav 104,184,188
s_add_u32 m0, s69, 117760
ds_read_b128 v[168:171], v0 offset:5120
v_and_b32 v16, v16, v21
Mvav 64,32,128
s_add_u32 s70, s20, 114688
ds_read_b128 v[176:179], v0 offset:6144
v_cmp_ne_u32 vcc, 0, v16
Mvav 64,40,136
ds_read_b128 v[184:187], v0 offset:7168
buffer_load_dwordx4 v25, s[44:47], s70 offen lds
Mvav 64,48,144
s_cbranch_vccnz .Lcf3
.Lcf3_r:
ds_read_b128 v[132:135], v0 offset:8192
Mvav 64,56,152
s_waitcnt lgkmcnt(11)
ds_read_b128 v[140:143], v0 offset:9216
Mvav 68,36,128
s_waitcnt lgkmcnt(10)
ds_read_b128 v[148:151], v0 offset:10240
Mvav 68,44,136
s_waitcnt lgkmcnt(9)
ds_read_b128 v[156:159], v0 offset:11264
Mvav 68,52,144
s_waitcnt lgkmcnt(8)
ds_read_b128 v[164:167], v0 offset:12288
Mvav 68,60,152
s_waitcnt lgkmcnt(8)
ds_read_b128 v[172:175], v0 offset:13312
Mvav 72,32,160
s_waitcnt lgkmcnt(8)
ds_read_b128 v[180:183], v0 offset:14336
ds_read_b128 v[188:191], v0 offset:15360
Mvav 72,40,168
s_waitcnt vmcnt(13) lgkmcnt(9)
s_barrier
s_add_u32 m0, s69, 131072
Mvav 72,48,176
s_waitcnt lgkmcnt(8)
s_add_u32 s70, s14, 4718592
ds_read_b128 v[128:131], v0 offset:16384
Mvav 72,56,184
buffer_load_dwordx4 v0, s[48:51], s70 offen sc1 lds
Mvav 76,36,160
ds_read_b128 v[136:139], v0 offset:17408
Mvav 76,44,168
buffer_load_dwordx4 v0, s[48:51], s70 offen offset:1024 sc1 lds
Mvav 76,52,176
ds_read_b128 v[144:147], v0 offset:18432
v_lshlrev_b32 v16, 16, v216
Mvav 76,60,184
s_waitcnt lgkmcnt(10)
buffer_load_dwordx4 v[44:47], v11, s[48:51], s21 offen offset:1024 sc1
Mvav 80,32,132
s_waitcnt lgkmcnt(9)
ds_read_b128 v[152:155], v0 offset:19456
v_and_b32 v17, 0xffff0000, v216
Mvav 80,40,140
s_waitcnt lgkmcnt(9)
buffer_load_dwordx4 v0, s[48:51], s70 offen offset:2048 sc1 lds
Mvav 80,48,148
s_waitcnt lgkmcnt(8)
ds_read_b128 v[160:163], v0 offset:20480
v_lshlrev_b32 v18, 16, v217
Mvav 80,56,156
buffer_load_dwordx4 v0, s[48:51], s70 offen offset:3072 sc1 lds
Mvav 84,36,132
ds_read_b128 v[168:171], v0 offset:21504
v_and_b32 v19, 0xffff0000, v217
Mvav 84,44,140
s_add_u32 m0, s69, 49152
v_lshlrev_b32 v20, 16, v218
v_and_b32 v21, 0xffff0000, v218
Mvav 84,52,148
s_add_u32 s70, s14, 4866048
ds_read_b128 v[176:179], v0 offset:22528
v_lshlrev_b32 v22, 16, v219
Mvav 84,60,156
s_waitcnt lgkmcnt(10)
buffer_load_dwordx4 v0, s[48:51], s70 offen sc1 lds
Mvav 88,32,164
s_waitcnt lgkmcnt(9)
ds_read_b128 v[184:187], v0 offset:23552
v_and_b32 v23, 0xffff0000, v219
Mvav 88,40,172
s_waitcnt lgkmcnt(9)
buffer_load_dwordx4 v[48:51], v11, s[48:51], s21 offen offset:2048 sc1
Mvav 88,48,180
s_waitcnt lgkmcnt(8)
ds_read_b128 v[132:135], v0 offset:24576
Mvav 88,56,188
buffer_load_dwordx4 v0, s[48:51], s70 offen offset:1024 sc1 lds
Mvav 92,36,164
ds_read_b128 v[140:143], v0 offset:25600
Mvav 92,44,172
buffer_load_dwordx4 v0, s[48:51], s70 offen offset:2048 sc1 lds
Mvav 92,52,180
ds_read_b128 v[148:151], v0 offset:26624
Mvav 92,60,188
s_waitcnt vmcnt(17) lgkmcnt(10)
v_xor_b32 v40, s24, v40
v_xor_b32 v41, s25, v41
Mvav 96,192,128
s_waitcnt lgkmcnt(7)
ds_read_b128 v[156:159], v0 offset:27648
v_mov_b32 v62, s86
Mvav 96,200,136
s_lshl_b32 s58, s92, 17
buffer_load_dwordx4 v0, s[48:51], s70 offen offset:3072 sc1 lds
Mvav 96,208,144
s_add_u32 s58, s58, s22
)"
R"(
ds_read_b128 v[164:167], v0 offset:28672
v_mov_b32 v2, 0xffffff00
Mvav 96,216,152
s_add_u32 s58, s58, 0
buffer_load_dwordx4 v[52:55], v11, s[48:51], s21 offen offset:3072 sc1
Mvav 100,196,128
s_add_u32 s58, s58, s32
ds_read_b128 v[172:175], v0 offset:29696
v_cvt_pk_bf16_f32 v64, v64, v65
Mvav 100,204,136
s_add_u32 m0, s69, 65536
v_cvt_pk_bf16_f32 v65, v66, v67
v_cvt_pk_bf16_f32 v66, v68, v69
Mvav 100,212,144
s_add_u32 s70, s12, 9437184
ds_read_b128 v[180:183], v0 offset:30720
v_cvt_pk_bf16_f32 v67, v70, v71
Mvav 100,220,152
s_waitcnt lgkmcnt(10)
buffer_load_dwordx4 v0, s[48:51], s70 offen sc1 lds
Mvav 100,228,160
s_waitcnt lgkmcnt(8)
ds_read_b128 v[188:191], v0 offset:31744
v_cvt_pk_bf16_f32 v72, v72, v73
Mvav 100,236,168
s_barrier
Mvav 100,244,176
s_waitcnt lgkmcnt(8)
ds_read_b128 v[128:131], v12 offset:32768
v_cvt_pk_bf16_f32 v73, v74, v75
Mvav 100,252,184
buffer_load_dwordx4 v0, s[48:51], s70 offen offset:1024 sc1 lds
Mvav 96,224,160
ds_read_b128 v[136:139], v12 offset:33792
v_cvt_pk_bf16_f32 v74, v76, v77
Mvav 96,232,168
buffer_store_dwordx4 v[64:67], v0, s[48:51], s58 offen sc1
Mvav 96,240,176
s_waitcnt lgkmcnt(6)
ds_read_b128 v[144:147], v12 offset:34816
v_cvt_pk_bf16_f32 v75, v78, v79
Mvav 96,248,184
s_lshl_b32 s58, s93, 17
v_cvt_pk_bf16_f32 v80, v80, v81
v_cvt_pk_bf16_f32 v81, v82, v83
Mvav 96,0,132
s_add_u32 s58, s58, s22
ds_read_b128 v[152:155], v12 offset:35840
v_cvt_pk_bf16_f32 v82, v84, v85
Mvav 96,8,140
s_add_u32 s58, s58, 0
buffer_load_dwordx4 v0, s[48:51], s70 offen offset:2048 sc1 lds
Mvav 96,16,148
s_add_u32 s58, s58, s32
ds_read_b128 v[160:163], v12 offset:36864
v_cvt_pk_bf16_f32 v83, v86, v87
Mvav 96,24,156
v_mov_b32 v63, s87
v_xor_b32 v42, s26, v42
Mvav 100,4,132
ds_read_b128 v[168:171], v12 offset:37888
v_xor_b32 v43, s27, v43
Mvav 100,12,140
buffer_load_dwordx4 v0, s[48:51], s70 offen offset:3072 sc1 lds
Mvav 100,20,148
ds_read_b128 v[176:179], v12 offset:38912
v_or3_b32 v56, v40, v41, v42
Mvav 100,28,156
s_waitcnt lgkmcnt(10)
buffer_store_dwordx4 v[72:75], v0, s[48:51], s58 offen sc1
Mvav 100,36,164
s_waitcnt lgkmcnt(7)
ds_read_b128 v[184:187], v12 offset:39936
v_lshlrev_b32 v64, 16, v192
Mvav 100,44,172
s_add_u32 m0, s69, 81920
v_mul_f32 v24, 0xbfb8aa3b, v64
v_and_b32 v65, 0xffff0000, v192
Mvav 100,52,180
s_add_u32 s70, s12, 9584640
ds_read_b128 v[132:135], v12 offset:40960
v_mul_f32 v25, 0xbfb8aa3b, v65
Mvav 100,60,188
s_lshl_b32 s58, s94, 17
buffer_load_dwordx4 v0, s[48:51], s70 offen sc1 lds
Mvav 96,32,164
s_add_u32 s58, s58, s22
ds_read_b128 v[140:143], v12 offset:41984
v_lshlrev_b32 v66, 16, v193
Mvav 96,40,172
s_add_u32 s58, s58, 0
v_exp_f32 v24, v24
Mvav 96,48,180
s_add_u32 s58, s58, s32
ds_read_b128 v[148:151], v12 offset:43008
v_add_f32 v24, 1.0, v24
Mvav 96,56,188
s_waitcnt lgkmcnt(10)
buffer_load_dwordx4 v0, s[48:51], s70 offen offset:1024 sc1 lds
Mvav 104,192,128
s_waitcnt lgkmcnt(9)
ds_read_b128 v[156:159], v12 offset:44032
v_mul_f32 v26, 0xbfb8aa3b, v66
Mvav 104,200,136
s_waitcnt lgkmcnt(9)
buffer_store_dwordx4 v[80:83], v0, s[48:51], s58 offen sc1
Mvav 104,208,144
s_waitcnt lgkmcnt(8)
ds_read_b128 v[164:167], v12 offset:45056
v_and_b32 v67, 0xffff0000, v193
Mvav 104,216,152
v_rcp_f32 v24, v24
Mvav 108,196,128
ds_read_b128 v[172:175], v12 offset:46080
v_mul_f32 v64, v64, v24
Mvav 108,204,136
s_waitcnt vmcnt(20)
buffer_load_dwordx4 v0, s[48:51], s70 offen offset:2048 sc1 lds
Mvav 108,212,144
ds_read_b128 v[180:183], v12 offset:47104
v_mul_f32 v27, 0xbfb8aa3b, v67
Mvav 108,220,152
s_waitcnt lgkmcnt(9)
ds_read_b64_tr_b16 v[196:197], v60 offset:0
v_lshlrev_b32 v68, 16, v194
Mvav 108,228,160
s_lshl_b32 s58, s92, 17
ds_read_b128 v[188:191], v12 offset:48128
v_cvt_pk_bf16_f32 v96, v96, v97
Mvav 108,236,168
s_waitcnt vmcnt(11) lgkmcnt(8)
s_barrier
Mvav 108,244,176
ds_read_b128 v[128:131], v14 offset:0
buffer_load_dwordx4 v0, s[48:51], s70 offen offset:3072 sc1 lds
Mvav 108,252,184
s_add_u32 s58, s58, s22
ds_read_b64_tr_b16 v[198:199], v60 offset:256
v_cvt_pk_bf16_f32 v97, v98, v99
Mvav 104,224,160
s_add_u32 s58, s58, 32768
ds_read_b128 v[136:139], v14 offset:1024
v_cvt_pk_bf16_f32 v98, v100, v101
Mvav 104,232,168
s_add_u32 m0, s69, 147456
ds_read_b64_tr_b16 v[204:205], v61 offset:0
v_cvt_pk_bf16_f32 v99, v102, v103
Mvav 104,240,176
s_add_u32 s70, s13, 9437184
ds_read_b128 v[144:147], v14 offset:2048
v_xor_b32 v44, s24, v44
Mvav 104,248,184
s_add_u32 s58, s58, s32
buffer_load_dwordx4 v0, s[48:51], s70 offen sc1 lds
Mvav 104,0,132
s_waitcnt lgkmcnt(12)
ds_read_b128 v[152:155], v14 offset:3072
v_xor_b32 v45, s25, v45
Mvav 104,8,140
s_waitcnt lgkmcnt(12)
buffer_store_dwordx4 v[96:99], v0, s[48:51], s58 offen sc1
Mvav 104,16,148
s_waitcnt lgkmcnt(11)
ds_read_b128 v[160:163], v14 offset:4096
v_xor_b32 v46, s26, v46
Mvav 104,24,156
buffer_load_dwordx4 v0, s[48:51], s70 offen offset:1024 sc1 lds
Mvav 108,4,132
ds_read_b128 v[168:171], v14 offset:5120
v_xor_b32 v47, s27, v47
Mvav 108,12,140
ds_read_b64_tr_b16 v[206:207], v61 offset:256
v_xor_b32 v48, s24, v48
Mvav 108,20,148
ds_read_b128 v[176:179], v14 offset:6144
v_xor_b32 v49, s25, v49
Mvav 108,28,156
s_waitcnt lgkmcnt(12)
ds_read_b64_tr_b16 v[212:213], v60 offset:32
v_xor_b32 v50, s26, v50
Mvav 108,36,164
ds_read_b128 v[184:187], v14 offset:7168
v_xor_b32 v51, s27, v51
Mvav 108,44,172
buffer_load_dwordx4 v0, s[48:51], s70 offen offset:2048 sc1 lds
Mvav 108,52,180
s_waitcnt lgkmcnt(12)
ds_read_b128 v[132:135], v14 offset:8192
v_or3_b32 v56, v56, v43, v44
Mvav 108,60,188
ds_read_b64_tr_b16 v[214:215], v60 offset:288
v_or3_b32 v56, v56, v45, v46
Mvav 104,32,164
ds_read_b128 v[140:143], v14 offset:9216
v_or3_b32 v56, v56, v47, v48
Mvav 104,40,172
buffer_load_dwordx4 v0, s[48:51], s70 offen offset:3072 sc1 lds
Mvav 104,48,180
s_waitcnt lgkmcnt(12)
ds_read_b128 v[148:151], v14 offset:10240
v_or3_b32 v56, v56, v49, v50
Mvav 104,56,188
s_waitcnt vmcnt(16)
ds_read_b64_tr_b16 v[220:221], v61 offset:32
v_xor_b32 v52, s24, v52
Zvav 96,64,128
s_waitcnt lgkmcnt(11)
ds_read_b128 v[156:159], v14 offset:11264
v_xor_b32 v53, s25, v53
Mvav 96,72,136
s_add_u32 m0, s69, 0
ds_read_b64_tr_b16 v[222:223], v61 offset:288
v_xor_b32 v54, s26, v54
Mvav 96,80,144
s_add_u32 s70, s13, 9584640
ds_read_b128 v[164:167], v14 offset:12288
v_xor_b32 v55, s27, v55
Mvav 96,88,152
buffer_load_dwordx4 v0, s[48:51], s70 offen sc1 lds
Zvav 100,68,128
ds_read_b128 v[172:175], v14 offset:13312
v_or3_b32 v56, v56, v51, v52
Mvav 100,76,136
s_waitcnt lgkmcnt(12)
ds_read_b64_tr_b16 v[228:229], v60 offset:512
v_or3_b32 v56, v56, v53, v54
Mvav 100,84,144
ds_read_b128 v[180:183], v14 offset:14336
v_or_b32 v56, v56, v55
Mvav 100,92,152
s_waitcnt lgkmcnt(11)
buffer_load_dwordx4 v0, s[48:51], s70 offen offset:1024 sc1 lds
Mvav 100,100,160
s_lshl_b32 s58, s93, 17
ds_read_b128 v[188:191], v14 offset:15360
v_cvt_pk_bf16_f32 v104, v104, v105
Mvav 100,108,168
s_add_u32 s58, s58, s22
ds_read_b64_tr_b16 v[230:231], v60 offset:768
v_cvt_pk_bf16_f32 v105, v106, v107
Mvav 100,116,176
s_add_u32 s58, s58, 32768
ds_read_b128 v[128:131], v0 offset:49152
v_cvt_pk_bf16_f32 v106, v108, v109
Mvav 100,124,184
s_add_u32 s58, s58, s32
ds_read_b128 v[136:139], v0 offset:50176
v_cvt_pk_bf16_f32 v107, v110, v111
Mvav 96,96,160
buffer_load_dwordx4 v0, s[48:51], s70 offen offset:2048 sc1 lds
Mvav 96,104,168
s_waitcnt lgkmcnt(12)
ds_read_b128 v[144:147], v0 offset:51200
v_and_b32 v56, 0xff, v56
Mvav 96,112,176
ds_read_b128 v[152:155], v0 offset:52224
v_cmp_ne_u32 vcc, 0, v56
Mvav 96,120,184
s_and_b64 vcc, vcc, s[36:37]
buffer_store_dwordx4 v[104:107], v0, s[48:51], s58 offen sc1
Mvav 96,128,132
ds_read_b128 v[160:163], v0 offset:53248
v_mul_f32 v28, 0xbfb8aa3b, v68
Mvav 96,136,140
s_waitcnt lgkmcnt(12)
ds_read_b128 v[168:171], v0 offset:54272
v_and_b32 v69, 0xffff0000, v194
Mvav 96,144,148
buffer_load_dwordx4 v0, s[48:51], s70 offen offset:3072 sc1 lds
Mvav 96,152,156
s_waitcnt lgkmcnt(10)
ds_read_b128 v[176:179], v0 offset:55296
v_mul_f32 v29, 0xbfb8aa3b, v69
Mvav 100,132,132
s_add_u32 m0, s69, 16384
ds_read_b128 v[184:187], v0 offset:56320
v_lshlrev_b32 v70, 16, v195
Mvav 100,140,140
s_add_u32 s70, s14, 9437184
s_cbranch_vccnz .Lgu
.Lgu_r:
Mvav 100,148,148
ds_read_b128 v[132:135], v0 offset:57344
buffer_load_dwordx4 v0, s[48:51], s70 offen sc1 lds
Mvav 100,156,156
s_lshl_b32 s59, s57, 5
ds_read_b128 v[140:143], v0 offset:58368
v_add_f32 v40, v40, v44
Mvav 100,164,164
s_add_u32 s59, s59, 32768
ds_read_b64_tr_b16 v[236:237], v61 offset:512
v_add_f32 v41, v41, v45
Mvav 100,172,172
s_waitcnt lgkmcnt(12)
ds_read_b128 v[148:151], v0 offset:59392
v_add_f32 v42, v42, v46
Mvav 100,180,180
ds_read_b128 v[156:159], v0 offset:60416
v_add_f32 v43, v43, v47
Mvav 100,188,188
buffer_load_dwordx4 v0, s[48:51], s70 offen offset:1024 sc1 lds
Mvav 96,160,164
ds_read_b128 v[164:167], v0 offset:61440
v_add_f32 v48, v48, v52
Mvav 96,168,172
s_waitcnt lgkmcnt(12)
ds_read_b128 v[172:175], v0 offset:62464
v_add_f32 v49, v49, v53
Mvav 96,176,180
ds_read_b64_tr_b16 v[238:239], v61 offset:768
v_add_f32 v50, v50, v54
Mvav 96,184,188
ds_read_b128 v[180:183], v0 offset:63488
v_add_f32 v51, v51, v55
Mvav 96,192,128
s_waitcnt lgkmcnt(12)
ds_read_b128 v[188:191], v0 offset:64512
v_add_f32 v40, v40, v48
Mvav 96,200,136
buffer_load_dwordx4 v0, s[48:51], s70 offen offset:2048 sc1 lds
Mvav 96,208,144
ds_read_b64_tr_b16 v[244:245], v60 offset:544
v_add_f32 v41, v41, v49
Mvav 96,216,152
ds_read_b64_tr_b16 v[246:247], v60 offset:800
v_add_f32 v42, v42, v50
Mvav 100,196,128
s_waitcnt vmcnt(15)
buffer_store_dwordx2 v[62:63], v13, s[48:51], s34 offen sc1
Mvav 100,204,136
buffer_load_dwordx4 v0, s[48:51], s70 offen offset:3072 sc1 lds
Mvav 100,212,144
s_waitcnt lgkmcnt(12)
ds_read_b64_tr_b16 v[252:253], v61 offset:544
v_add_f32 v43, v43, v51
Mvav 100,220,152
ds_read_b64_tr_b16 v[254:255], v61 offset:800
v_permlane16_swap_b32 v40, v41
Mvav 100,228,160
v_exp_f32 v25, v25
Mvav 100,236,168
s_waitcnt vmcnt(15) lgkmcnt(0)
s_barrier
s_add_u32 m0, s69, 98304
Mvav 100,244,176
s_add_u32 s70, s14, 9584640
ds_read_b128 v[128:131], v12 offset:0
v_permlane16_swap_b32 v42, v43
Mvav 100,252,184
buffer_load_dwordx4 v0, s[48:51], s70 offen sc1 lds
Mvav 96,224,160
ds_read_b128 v[136:139], v12 offset:1024
v_add_f32 v40, v40, v41
Mvav 96,232,168
v_exp_f32 v26, v26
Mvav 96,240,176
ds_read_b128 v[144:147], v12 offset:2048
v_add_f32 v42, v42, v43
Mvav 96,248,184
buffer_load_dwordx4 v0, s[48:51], s70 offen offset:1024 sc1 lds
Mvav 96,0,132
ds_read_b128 v[152:155], v12 offset:3072
v_permlane32_swap_b32 v40, v42
Mvav 96,8,140
v_exp_f32 v27, v27
Mvav 96,16,148
ds_read_b128 v[160:163], v12 offset:4096
v_add_f32 v40, v40, v42
Mvav 96,24,156
v_exp_f32 v28, v28
Mvav 100,4,132
ds_read_b128 v[168:171], v12 offset:5120
v_cvt_pk_bf16_f32 v40, 0, v40
Mvav 100,12,140
buffer_load_dwordx4 v0, s[48:51], s70 offen offset:2048 sc1 lds
Mvav 100,20,148
ds_read_b128 v[176:179], v12 offset:6144
v_mov_b32_dpp v41, v40 row_ror:8 row_mask:0xf bank_mask:0xf bound_ctrl:1
Mvav 100,28,156
v_exp_f32 v29, v29
Mvav 100,36,164
ds_read_b128 v[184:187], v12 offset:7168
v_mul_f32 v42, 0xbfb8aa3b, v40
Mvav 100,44,172
buffer_load_dwordx4 v0, s[48:51], s70 offen offset:3072 sc1 lds
Mvav 100,52,180
ds_read_b128 v[132:135], v12 offset:8192
v_bfe_u32 v56, v0, 4, 3
Mvav 100,60,188
v_exp_f32 v42, v42
Mvav 96,32,164
ds_read_b128 v[140:143], v12 offset:9216
v_add_f32 v42, 1.0, v42
Mvav 96,40,172
v_rcp_f32 v42, v42
Mvav 96,48,180
ds_read_b128 v[148:151], v12 offset:10240
v_mul_f32 v40, v40, v42
Mvav 96,56,188
s_waitcnt lgkmcnt(10)
v_cvt_pk_bf16_f32 v40, 0, v40
v_mul_f32 v40, v40, v41
Zvav 104,64,128
s_waitcnt lgkmcnt(9)
ds_read_b128 v[156:159], v12 offset:11264
v_cvt_pk_bf16_f32 v40, v40, v40
Mvav 104,72,136
s_waitcnt lgkmcnt(9)
v_lshrrev_b32 v57, 9, v0
v_lshl_add_u32 v56, v56, 1, v57
Mvav 104,80,144
s_waitcnt lgkmcnt(8)
ds_read_b128 v[164:167], v12 offset:12288
v_bfe_u32 v57, v0, 8, 1
Mvav 104,88,152
v_lshl_add_u32 v56, v57, 6, v56
v_and_b32 v57, 128, v0
Zvav 108,68,128
ds_read_b128 v[172:175], v12 offset:13312
v_add_u32 v56, v56, v57
Mvav 108,76,136
v_lshl_add_u32 v56, v56, 1, s59
v_lshrrev_b32 v57, 8, v0
)"
R"(
Mvav 108,84,144
ds_read_b128 v[180:183], v12 offset:14336
v_lshlrev_b32 v57, 4, v57
Mvav 108,92,152
s_waitcnt lgkmcnt(7)
ds_write_b16 v56, v40
Mvav 108,100,160
s_lshl_b32 s58, s94, 17
ds_read_b128 v[188:191], v12 offset:15360
v_cvt_pk_bf16_f32 v96, v96, v97
Mvav 108,108,168
s_add_u32 s58, s58, s22
v_cvt_pk_bf16_f32 v97, v98, v99
v_cvt_pk_bf16_f32 v98, v100, v101
Mvav 108,116,176
s_add_u32 s58, s58, 32768
ds_read_b128 v[128:131], v12 offset:16384
v_cvt_pk_bf16_f32 v99, v102, v103
Mvav 108,124,184
s_add_u32 s58, s58, s32
ds_read_b128 v[136:139], v12 offset:17408
v_and_b32 v56, 16, v0
Mvav 104,96,160
buffer_store_dwordx4 v[96:99], v0, s[48:51], s58 offen sc1
Mvav 104,104,168
ds_read_b128 v[144:147], v12 offset:18432
v_lshl_add_u32 v56, v56, 3, v57
Mvav 104,112,176
ds_read_b128 v[152:155], v12 offset:19456
v_add_u32 v56, 32768, v56
Mvav 104,120,184
s_waitcnt lgkmcnt(12)
v_add_f32 v25, 1.0, v25
v_add_f32 v26, 1.0, v26
Mvav 104,128,132
s_waitcnt lgkmcnt(9)
ds_read_b128 v[160:163], v12 offset:20480
v_add_f32 v27, 1.0, v27
Mvav 104,136,140
s_add_u32 m0, s69, 131072
ds_read_b128 v[168:171], v12 offset:21504
v_add_f32 v28, 1.0, v28
Mvav 104,144,148
s_add_u32 s70, s12, 14155776
v_rcp_f32 v25, v25
Mvav 104,152,156
ds_read_b128 v[176:179], v12 offset:22528
buffer_load_dwordx4 v0, s[48:51], s70 offen sc1 lds
Mvav 108,132,132
ds_read_b128 v[184:187], v12 offset:23552
v_mul_f32 v65, v65, v25
Mvav 108,140,140
v_rcp_f32 v26, v26
Mvav 108,148,148
ds_read_b128 v[132:135], v12 offset:24576
v_mul_f32 v66, v66, v26
Mvav 108,156,156
s_waitcnt lgkmcnt(13)
ds_read_b128 v[140:143], v12 offset:25600
v_add_f32 v29, 1.0, v29
Mvav 108,164,164
s_waitcnt lgkmcnt(13)
buffer_load_dwordx4 v0, s[48:51], s70 offen offset:1024 sc1 lds
Mvav 108,172,172
s_waitcnt lgkmcnt(12)
ds_read_b128 v[148:151], v12 offset:26624
v_mul_f32 v30, 0xbfb8aa3b, v70
Mvav 108,180,180
s_waitcnt lgkmcnt(11)
ds_read_b128 v[156:159], v12 offset:27648
v_and_b32 v71, 0xffff0000, v195
Mvav 108,188,188
v_rcp_f32 v27, v27
Mvav 104,160,164
ds_read_b128 v[164:167], v12 offset:28672
v_mul_f32 v67, v67, v27
Mvav 104,168,172
ds_read_b128 v[172:175], v12 offset:29696
v_mul_f32 v31, 0xbfb8aa3b, v71
Mvav 104,176,180
buffer_load_dwordx4 v0, s[48:51], s70 offen offset:2048 sc1 lds
Mvav 104,184,188
s_waitcnt lgkmcnt(13)
ds_read_b128 v[180:183], v12 offset:30720
v_lshlrev_b32 v72, 16, v200
Mvav 104,192,128
s_waitcnt lgkmcnt(13)
ds_read_b128 v[188:191], v12 offset:31744
v_mul_f32 v24, 0xbfb8aa3b, v72
Mvav 104,200,136
s_waitcnt lgkmcnt(13)
v_rcp_f32 v28, v28
Mvav 104,208,144
s_waitcnt lgkmcnt(12)
buffer_load_dwordx4 v0, s[48:51], s70 offen offset:3072 sc1 lds
Mvav 104,216,152
v_rcp_f32 v29, v29
Mvav 108,196,128
v_exp_f32 v30, v30
Mvav 108,204,136
v_exp_f32 v31, v31
Mvav 108,212,144
v_exp_f32 v24, v24
Mvav 108,220,152
s_waitcnt lgkmcnt(11)
v_mul_f32 v68, v68, v28
v_mul_f32 v69, v69, v29
Mvav 108,228,160
s_waitcnt lgkmcnt(10)
v_add_f32 v30, 1.0, v30
v_add_f32 v31, 1.0, v31
Mvav 108,236,168
s_waitcnt vmcnt(14) lgkmcnt(0)
s_barrier
s_add_u32 m0, s69, 49152
Mvav 108,244,176
s_add_u32 s70, s12, 14303232
ds_read_b128 v[128:131], v14 offset:16384
v_lshrrev_b32 v57, 9, v0
Mvav 108,252,184
s_mov_b32 vcc_lo, 0xffff0000
buffer_load_dwordx4 v0, s[48:51], s70 offen sc1 lds
Mvav 104,224,160
s_mov_b32 vcc_hi, 0xffff0000
ds_read_b128 v[136:139], v14 offset:17408
v_mul_u32_u24 v57, 7040, v57
Mvav 104,232,168
ds_read_b128 v[32:35], v56 offset:0
v_lshrrev_b32 v58, 2, v0
Mvav 104,240,176
ds_read_b128 v[144:147], v14 offset:18432
v_add_u32 v57, v57, v58
Mvav 104,248,184
buffer_load_dwordx4 v0, s[48:51], s70 offen offset:1024 sc1 lds
Mvav 104,0,132
ds_read_b128 v[152:155], v14 offset:19456
v_add_f32 v24, 1.0, v24
Mvav 104,8,140
ds_read_b128 v[36:39], v56 offset:64
v_bfe_u32 v56, v0, 4, 2
Mvav 104,16,148
ds_read_b128 v[160:163], v14 offset:20480
v_lshlrev_b32 v56, 3, v56
Mvav 104,24,156
v_rcp_f32 v30, v30
Mvav 108,4,132
ds_read_b128 v[168:171], v14 offset:21504
v_lshrrev_b32 v56, v56, s86
Mvav 108,12,140
s_waitcnt lgkmcnt(5)
buffer_load_dwordx4 v0, s[48:51], s70 offen offset:2048 sc1 lds
Zvvv 40,32,196
Mvav 108,20,148
ds_read_b128 v[176:179], v14 offset:22528
v_and_b32 v56, 0xff, v56
Zvvv 44,32,212
Mvav 108,28,156
v_rcp_f32 v31, v31
Zvvv 48,32,228
Mvav 108,36,164
ds_read_b128 v[184:187], v14 offset:23552
v_mul_f32 v70, v70, v30
Zvvv 52,32,244
Mvav 108,44,172
s_waitcnt lgkmcnt(4)
buffer_load_dwordx4 v0, s[48:51], s70 offen offset:3072 sc1 lds
Mvvv 40,36,204
Mvav 108,52,180
ds_read_b128 v[132:135], v14 offset:24576
v_mul_f32 v71, v71, v31
Mvvv 44,36,220
Mvav 108,60,188
v_rcp_f32 v24, v24
Mvvv 48,36,236
Mvav 104,32,164
ds_read_b128 v[140:143], v14 offset:25600
v_mul_f32 v72, v72, v24
Mvvv 52,36,252
Mvav 104,40,172
v_and_b32 v73, 0xffff0000, v200
v_mul_f32 v25, 0xbfb8aa3b, v73
Mvav 104,48,180
ds_read_b128 v[148:151], v14 offset:26624
v_lshlrev_b32 v74, 16, v201
Mvav 104,56,188
v_exp_f32 v25, v25
Zvav 96,64,128
ds_read_b128 v[156:159], v14 offset:27648
v_add_f32 v25, 1.0, v25
Mvav 96,72,136
v_rcp_f32 v25, v25
Mvav 96,80,144
ds_read_b128 v[164:167], v14 offset:28672
v_mul_f32 v73, v73, v25
Mvav 96,88,152
v_cndmask_b32 v40, v40, v44, vcc
v_cndmask_b32 v41, v41, v45, vcc
Zvav 100,68,128
ds_read_b128 v[172:175], v14 offset:29696
v_mul_f32 v26, 0xbfb8aa3b, v74
Mvav 100,76,136
v_exp_f32 v26, v26
Mvav 100,84,144
ds_read_b128 v[180:183], v14 offset:30720
v_cndmask_b32 v48, v48, v52, vcc
Mvav 100,92,152
s_waitcnt lgkmcnt(7)
v_cndmask_b32 v49, v49, v53, vcc
v_add_f32 v26, 1.0, v26
Mvav 100,100,160
s_lshl_b32 s58, s92, 17
ds_read_b128 v[188:191], v14 offset:31744
v_cvt_pk_bf16_f32 v104, v104, v105
Mvav 100,108,168
s_add_u32 s58, s58, s22
v_rcp_f32 v26, v26
Mvav 100,116,176
s_add_u32 s58, s58, 65536
ds_read_b128 v[128:131], v0 offset:0
v_cvt_pk_bf16_f32 v105, v106, v107
Mvav 100,124,184
s_add_u32 s58, s58, s32
v_cvt_pk_bf16_f32 v106, v108, v109
v_cvt_pk_bf16_f32 v107, v110, v111
Mvav 96,96,160
s_mov_b64 vcc, 0xffffffff
ds_read_b128 v[136:139], v0 offset:1024
v_cndmask_b32 v58, v48, v40, vcc
Mvav 96,104,168
buffer_store_dwordx4 v[104:107], v0, s[48:51], s58 offen sc1
Mvav 96,112,176
ds_read_b128 v[144:147], v0 offset:2048
v_cndmask_b32 v59, v49, v41, vcc
Mvav 96,120,184
s_waitcnt lgkmcnt(10)
v_and_or_b32 v58, v58, v2, v56
v_and_or_b32 v59, v59, v2, v56
Mvav 96,128,132
s_waitcnt lgkmcnt(7)
ds_read_b128 v[152:155], v0 offset:3072
v_mul_f32 v74, v74, v26
Mvav 96,136,140
s_add_u32 m0, s69, 65536
buffer_store_dword v58, v57, s[48:51], s35 offen sc1
Mvav 96,144,148
s_add_u32 s70, s13, 14155776
ds_read_b128 v[160:163], v0 offset:4096
v_and_b32 v75, 0xffff0000, v201
Mvav 96,152,156
s_add_u32 s35, s35, 229376
buffer_load_dwordx4 v0, s[48:51], s70 offen sc1 lds
Mvav 100,132,132
ds_read_b128 v[168:171], v0 offset:5120
v_mul_f32 v27, 0xbfb8aa3b, v75
Mvav 100,140,140
buffer_store_dword v59, v57, s[48:51], s35 offen sc1
Mvav 100,148,148
ds_read_b128 v[176:179], v0 offset:6144
v_lshlrev_b32 v76, 16, v202
Mvav 100,156,156
s_waitcnt lgkmcnt(10)
v_exp_f32 v27, v27
Mvav 100,164,164
s_waitcnt lgkmcnt(9)
ds_read_b128 v[184:187], v0 offset:7168
v_add_f32 v27, 1.0, v27
Mvav 100,172,172
s_waitcnt lgkmcnt(9)
buffer_load_dwordx4 v0, s[48:51], s70 offen offset:1024 sc1 lds
Mvav 100,180,180
s_waitcnt lgkmcnt(8)
ds_read_b128 v[132:135], v0 offset:8192
v_mul_f32 v28, 0xbfb8aa3b, v76
Mvav 100,188,188
v_rcp_f32 v27, v27
Mvav 96,160,164
ds_read_b128 v[140:143], v0 offset:9216
v_mul_f32 v75, v75, v27
Mvav 96,168,172
buffer_load_dwordx4 v0, s[48:51], s70 offen offset:2048 sc1 lds
Mvav 96,176,180
ds_read_b128 v[148:151], v0 offset:10240
v_and_b32 v77, 0xffff0000, v202
Mvav 96,184,188
s_waitcnt lgkmcnt(10)
v_exp_f32 v28, v28
Mvav 96,192,128
s_waitcnt vmcnt(14) lgkmcnt(9)
ds_read_b128 v[156:159], v0 offset:11264
v_add_f32 v28, 1.0, v28
Mvav 96,200,136
s_waitcnt lgkmcnt(9)
buffer_store_dwordx2 v[62:63], v13, s[48:51], s34 offen offset:32 sc1
Mvav 96,208,144
s_waitcnt lgkmcnt(8)
ds_read_b128 v[164:167], v0 offset:12288
v_mul_f32 v29, 0xbfb8aa3b, v77
Mvav 96,216,152
buffer_load_dwordx4 v0, s[48:51], s70 offen offset:3072 sc1 lds
Mvav 100,196,128
ds_read_b128 v[172:175], v0 offset:13312
v_lshlrev_b32 v78, 16, v203
Mvav 100,204,136
v_rcp_f32 v28, v28
Mvav 100,212,144
ds_read_b128 v[180:183], v0 offset:14336
v_mul_f32 v76, v76, v28
Mvav 100,220,152
s_waitcnt lgkmcnt(10)
v_exp_f32 v29, v29
Mvav 100,228,160
s_waitcnt lgkmcnt(7)
ds_read_b128 v[188:191], v0 offset:15360
v_add_f32 v29, 1.0, v29
Mvav 100,236,168
s_barrier
s_add_u32 m0, s69, 32768
Mvav 100,244,176
s_add_u32 s70, s13, 14303232
ds_read_b128 v[128:131], v0 offset:16384
v_mul_f32 v30, 0xbfb8aa3b, v78
Mvav 100,252,184
buffer_load_dwordx4 v0, s[48:51], s70 offen sc1 lds
Mvav 96,224,160
ds_read_b128 v[136:139], v0 offset:17408
v_and_b32 v79, 0xffff0000, v203
Mvav 96,232,168
v_rcp_f32 v29, v29
Mvav 96,240,176
ds_read_b128 v[144:147], v0 offset:18432
v_mul_f32 v77, v77, v29
Mvav 96,248,184
s_waitcnt lgkmcnt(10)
buffer_load_dwordx4 v0, s[48:51], s70 offen offset:1024 sc1 lds
Mvav 96,0,132
s_waitcnt lgkmcnt(9)
ds_read_b128 v[152:155], v0 offset:19456
v_mul_f32 v31, 0xbfb8aa3b, v79
Mvav 96,8,140
s_waitcnt lgkmcnt(9)
v_exp_f32 v30, v30
Mvav 96,16,148
s_waitcnt lgkmcnt(8)
ds_read_b128 v[160:163], v0 offset:20480
v_add_f32 v30, 1.0, v30
Mvav 96,24,156
v_rcp_f32 v30, v30
Mvav 100,4,132
ds_read_b128 v[168:171], v0 offset:21504
v_mul_f32 v78, v78, v30
Mvav 100,12,140
buffer_load_dwordx4 v0, s[48:51], s70 offen offset:2048 sc1 lds
Mvav 100,20,148
ds_read_b128 v[176:179], v0 offset:22528
v_lshlrev_b32 v80, 16, v208
Mvav 100,28,156
s_waitcnt lgkmcnt(10)
v_exp_f32 v31, v31
Mvav 100,36,164
s_waitcnt lgkmcnt(9)
ds_read_b128 v[184:187], v0 offset:23552
v_add_f32 v31, 1.0, v31
Mvav 100,44,172
s_waitcnt lgkmcnt(9)
buffer_load_dwordx4 v0, s[48:51], s70 offen offset:3072 sc1 lds
Mvav 100,52,180
s_waitcnt lgkmcnt(8)
ds_read_b128 v[132:135], v0 offset:24576
v_mul_f32 v24, 0xbfb8aa3b, v80
Mvav 100,60,188
v_rcp_f32 v31, v31
Mvav 96,32,164
ds_read_b128 v[140:143], v0 offset:25600
v_mul_f32 v79, v79, v31
Mvav 96,40,172
v_exp_f32 v24, v24
Mvav 96,48,180
ds_read_b128 v[148:151], v0 offset:26624
v_add_f32 v24, 1.0, v24
Mvav 96,56,188
s_waitcnt lgkmcnt(10)
v_rcp_f32 v24, v24
Zvav 104,64,128
s_waitcnt lgkmcnt(9)
ds_read_b128 v[156:159], v0 offset:27648
v_mul_f32 v80, v80, v24
Mvav 104,72,136
s_waitcnt lgkmcnt(9)
v_and_b32 v81, 0xffff0000, v208
v_mul_f32 v25, 0xbfb8aa3b, v81
Mvav 104,80,144
s_waitcnt lgkmcnt(8)
ds_read_b128 v[164:167], v0 offset:28672
v_lshlrev_b32 v82, 16, v209
Mvav 104,88,152
v_exp_f32 v25, v25
Zvav 108,68,128
ds_read_b128 v[172:175], v0 offset:29696
v_add_f32 v25, 1.0, v25
Mvav 108,76,136
v_rcp_f32 v25, v25
Mvav 108,84,144
ds_read_b128 v[180:183], v0 offset:30720
v_mul_f32 v81, v81, v25
Mvav 108,92,152
s_waitcnt lgkmcnt(7)
v_mul_f32 v26, 0xbfb8aa3b, v82
v_and_b32 v83, 0xffff0000, v209
Mvav 108,100,160
s_lshl_b32 s58, s93, 17
ds_read_b128 v[188:191], v0 offset:31744
v_cvt_pk_bf16_f32 v96, v96, v97
Mvav 108,108,168
s_add_u32 s58, s58, s22
v_exp_f32 v26, v26
Mvav 108,116,176
s_add_u32 s58, s58, 65536
ds_read_b128 v[128:131], v12 offset:32768
v_cvt_pk_bf16_f32 v97, v98, v99
Mvav 108,124,184
s_add_u32 s58, s58, s32
v_cvt_pk_bf16_f32 v98, v100, v101
v_cvt_pk_bf16_f32 v99, v102, v103
Mvav 104,96,160
ds_read_b128 v[136:139], v12 offset:33792
v_add_f32 v26, 1.0, v26
Mvav 104,104,168
buffer_store_dwordx4 v[96:99], v0, s[48:51], s58 offen sc1
Mvav 104,112,176
ds_read_b128 v[144:147], v12 offset:34816
v_mul_f32 v27, 0xbfb8aa3b, v83
Mvav 104,120,184
s_waitcnt lgkmcnt(10)
v_rcp_f32 v26, v26
Mvav 104,128,132
s_waitcnt lgkmcnt(7)
ds_read_b128 v[152:155], v12 offset:35840
v_mul_f32 v82, v82, v26
Mvav 104,136,140
s_add_u32 m0, s69, 81920
v_exp_f32 v27, v27
Mvav 104,144,148
s_add_u32 s70, s14, 14155776
ds_read_b128 v[160:163], v12 offset:36864
v_add_f32 v27, 1.0, v27
Mvav 104,152,156
buffer_load_dwordx4 v0, s[48:51], s70 offen sc1 lds
Mvav 108,132,132
ds_read_b128 v[168:171], v12 offset:37888
v_lshlrev_b32 v84, 16, v210
Mvav 108,140,140
v_rcp_f32 v27, v27
Mvav 108,148,148
ds_read_b128 v[176:179], v12 offset:38912
v_mul_f32 v83, v83, v27
)"
R"(
Mvav 108,156,156
s_waitcnt lgkmcnt(10)
v_mul_f32 v28, 0xbfb8aa3b, v84
v_and_b32 v85, 0xffff0000, v210
Mvav 108,164,164
s_waitcnt lgkmcnt(9)
ds_read_b128 v[184:187], v12 offset:39936
v_mul_f32 v29, 0xbfb8aa3b, v85
Mvav 108,172,172
s_waitcnt lgkmcnt(9)
buffer_load_dwordx4 v0, s[48:51], s70 offen offset:1024 sc1 lds
Mvav 108,180,180
s_waitcnt lgkmcnt(8)
ds_read_b128 v[132:135], v12 offset:40960
v_lshlrev_b32 v86, 16, v211
Mvav 108,188,188
v_exp_f32 v28, v28
Mvav 104,160,164
ds_read_b128 v[140:143], v12 offset:41984
v_bfe_u32 v8, v0, 4, 4
Mvav 104,168,172
buffer_load_dwordx4 v0, s[48:51], s70 offen offset:2048 sc1 lds
Mvav 104,176,180
ds_read_b128 v[148:151], v12 offset:43008
v_lshlrev_b32 v8, 3, v8
Mvav 104,184,188
s_waitcnt lgkmcnt(10)
buffer_load_dwordx2 v[58:59], v8, s[48:51], s33 offen sc1
Mvav 104,192,128
s_waitcnt lgkmcnt(9)
ds_read_b128 v[156:159], v12 offset:44032
v_add_f32 v28, 1.0, v28
Mvav 104,200,136
s_waitcnt lgkmcnt(9)
v_rcp_f32 v28, v28
Mvav 104,208,144
s_waitcnt lgkmcnt(8)
ds_read_b128 v[164:167], v12 offset:45056
v_mul_f32 v84, v84, v28
Mvav 104,216,152
buffer_load_dwordx4 v0, s[48:51], s70 offen offset:3072 sc1 lds
Mvav 108,196,128
ds_read_b128 v[172:175], v12 offset:46080
v_mul_f32 v30, 0xbfb8aa3b, v86
Mvav 108,204,136
s_add_u32 m0, s69, 114688
v_exp_f32 v29, v29
Mvav 108,212,144
s_add_u32 s70, s14, 14303232
ds_read_b128 v[180:183], v12 offset:47104
v_add_f32 v29, 1.0, v29
Mvav 108,220,152
s_waitcnt lgkmcnt(10)
buffer_load_dwordx4 v0, s[48:51], s70 offen sc1 lds
Mvav 108,228,160
s_waitcnt lgkmcnt(9)
ds_read_b128 v[188:191], v12 offset:48128
v_and_b32 v87, 0xffff0000, v211
Mvav 108,236,168
s_waitcnt vmcnt(19) lgkmcnt(9)
s_barrier
Mvav 108,244,176
s_waitcnt lgkmcnt(8)
ds_read_b128 v[128:131], v14 offset:0
v_mul_f32 v31, 0xbfb8aa3b, v87
Mvav 108,252,184
v_rcp_f32 v29, v29
Mvav 104,224,160
ds_read_b128 v[136:139], v14 offset:1024
v_mul_f32 v85, v85, v29
Mvav 104,232,168
buffer_load_dwordx4 v0, s[48:51], s70 offen offset:1024 sc1 lds
Mvav 104,240,176
ds_read_b128 v[144:147], v14 offset:2048
v_mul_f32 v24, 0xbfb8aa3b, v16
Mvav 104,248,184
s_waitcnt lgkmcnt(10)
v_exp_f32 v30, v30
Mvav 104,0,132
s_waitcnt lgkmcnt(9)
ds_read_b128 v[152:155], v14 offset:3072
v_add_f32 v30, 1.0, v30
Mvav 104,8,140
s_waitcnt lgkmcnt(9)
buffer_load_dwordx4 v0, s[48:51], s70 offen offset:2048 sc1 lds
Mvav 104,16,148
s_waitcnt lgkmcnt(8)
ds_read_b128 v[160:163], v14 offset:4096
v_mul_f32 v25, 0xbfb8aa3b, v17
Mvav 104,24,156
v_rcp_f32 v30, v30
Mvav 108,4,132
ds_read_b128 v[168:171], v14 offset:5120
v_mul_f32 v86, v86, v30
Mvav 108,12,140
v_exp_f32 v31, v31
Mvav 108,20,148
ds_read_b128 v[176:179], v14 offset:6144
v_add_f32 v31, 1.0, v31
Mvav 108,28,156
s_waitcnt lgkmcnt(10)
buffer_load_dwordx4 v0, s[48:51], s70 offen offset:3072 sc1 lds
Mvav 108,36,164
s_waitcnt lgkmcnt(9)
ds_read_b128 v[184:187], v14 offset:7168
v_mul_f32 v26, 0xbfb8aa3b, v18
Mvav 108,44,172
s_waitcnt lgkmcnt(9)
v_rcp_f32 v31, v31
Mvav 108,52,180
s_waitcnt lgkmcnt(8)
ds_read_b128 v[132:135], v14 offset:8192
v_mul_f32 v87, v87, v31
Mvav 108,60,188
v_exp_f32 v24, v24
Mvav 104,32,164
ds_read_b128 v[140:143], v14 offset:9216
v_add_f32 v24, 1.0, v24
Mvav 104,40,172
v_rcp_f32 v24, v24
Mvav 104,48,180
ds_read_b128 v[148:151], v14 offset:10240
v_mul_f32 v16, v16, v24
Mvav 104,56,188
s_waitcnt lgkmcnt(10)
v_exp_f32 v25, v25
Zvav 96,64,128
s_waitcnt lgkmcnt(9)
ds_read_b128 v[156:159], v14 offset:11264
v_add_f32 v25, 1.0, v25
Mvav 96,72,136
s_waitcnt lgkmcnt(9)
v_rcp_f32 v25, v25
Mvav 96,80,144
s_waitcnt lgkmcnt(8)
ds_read_b128 v[164:167], v14 offset:12288
v_mul_f32 v17, v17, v25
Mvav 96,88,152
v_exp_f32 v26, v26
Zvav 100,68,128
ds_read_b128 v[172:175], v14 offset:13312
v_add_f32 v26, 1.0, v26
Mvav 100,76,136
v_rcp_f32 v26, v26
Mvav 100,84,144
ds_read_b128 v[180:183], v14 offset:14336
v_mul_f32 v18, v18, v26
Mvav 100,92,152
s_waitcnt lgkmcnt(7)
v_mul_f32 v27, 0xbfb8aa3b, v19
v_mul_f32 v28, 0xbfb8aa3b, v20
Mvav 100,100,160
s_lshl_b32 s58, s94, 17
ds_read_b128 v[188:191], v14 offset:15360
v_cvt_pk_bf16_f32 v104, v104, v105
Mvav 100,108,168
s_add_u32 s58, s58, s22
v_exp_f32 v27, v27
Mvav 100,116,176
s_add_u32 s58, s58, 65536
ds_read_b128 v[128:131], v0 offset:49152
v_cvt_pk_bf16_f32 v105, v106, v107
Mvav 100,124,184
s_add_u32 s58, s58, s32
v_exp_f32 v28, v28
Mvav 96,96,160
ds_read_b128 v[136:139], v0 offset:50176
v_cvt_pk_bf16_f32 v106, v108, v109
Mvav 96,104,168
v_cvt_pk_bf16_f32 v107, v110, v111
v_add_f32 v27, 1.0, v27
Mvav 96,112,176
ds_read_b128 v[144:147], v0 offset:51200
v_add_f32 v28, 1.0, v28
Mvav 96,120,184
s_waitcnt lgkmcnt(10)
buffer_store_dwordx4 v[104:107], v0, s[48:51], s58 offen sc1
Mvav 96,128,132
s_waitcnt lgkmcnt(7)
ds_read_b128 v[152:155], v0 offset:52224
v_mul_f32 v29, 0xbfb8aa3b, v21
Mvav 96,136,140
s_add_u32 m0, s69, 147456
v_rcp_f32 v27, v27
Mvav 96,144,148
s_add_u32 s70, s15, 4718592
ds_read_b128 v[160:163], v0 offset:53248
v_mul_f32 v19, v19, v27
Mvav 96,152,156
buffer_load_dwordx4 v0, s[48:51], s70 offen sc1 lds
Mvav 100,132,132
ds_read_b128 v[168:171], v0 offset:54272
v_mul_f32 v30, 0xbfb8aa3b, v22
Mvav 100,140,140
v_rcp_f32 v28, v28
Mvav 100,148,148
ds_read_b128 v[176:179], v0 offset:55296
v_mul_f32 v20, v20, v28
Mvav 100,156,156
s_waitcnt lgkmcnt(10)
v_exp_f32 v29, v29
Mvav 100,164,164
s_waitcnt lgkmcnt(9)
ds_read_b128 v[184:187], v0 offset:56320
v_add_f32 v29, 1.0, v29
Mvav 100,172,172
s_waitcnt lgkmcnt(9)
buffer_load_dwordx4 v0, s[48:51], s70 offen offset:1024 sc1 lds
Mvav 100,180,180
s_waitcnt lgkmcnt(8)
ds_read_b128 v[132:135], v0 offset:57344
v_mul_f32 v31, 0xbfb8aa3b, v23
Mvav 100,188,188
v_rcp_f32 v29, v29
Mvav 96,160,164
s_waitcnt vmcnt(8)
ds_read_b128 v[140:143], v0 offset:58368
v_xor_b32 v58, s86, v58
Mvav 96,168,172
buffer_load_dwordx4 v0, s[48:51], s70 offen offset:2048 sc1 lds
Mvav 96,176,180
ds_read_b128 v[148:151], v0 offset:59392
v_bitop3_b32 v58, v59, s87, v58 bitop3:0xbe
Mvav 96,184,188
s_waitcnt lgkmcnt(10)
v_exp_f32 v30, v30
Mvav 96,192,128
s_waitcnt lgkmcnt(6)
ds_read_b128 v[156:159], v0 offset:60416
v_cmp_ne_u32 vcc, 0, v58
Mvav 96,200,136
s_and_b64 vcc, vcc, 0xff
s_cbranch_vccnz .Lpf1
.Lpf1_r:
Mvav 96,208,144
s_lshl_b32 s60, s92, 13
ds_read_b128 v[164:167], v0 offset:61440
v_mul_f32 v21, v21, v29
Mvav 96,216,152
s_add_u32 s60, s60, s23
buffer_load_dwordx4 v0, s[48:51], s70 offen offset:3072 sc1 lds
Mvav 100,196,128
s_add_u32 s60, s60, s32
ds_read_b128 v[172:175], v0 offset:62464
v_add_f32 v30, 1.0, v30
Mvav 100,204,136
s_add_u32 m0, s69, 0
v_rcp_f32 v30, v30
Mvav 100,212,144
s_add_u32 s70, s15, 4866048
ds_read_b128 v[180:183], v0 offset:63488
v_mul_f32 v22, v22, v30
Mvav 100,220,152
s_add_u32 s60, s60, 32768
buffer_load_dwordx4 v0, s[48:51], s70 offen sc1 lds
Mvav 100,228,160
s_waitcnt lgkmcnt(8)
ds_read_b128 v[188:191], v0 offset:64512
Mvav 100,236,168
s_barrier
Mvav 100,244,176
s_waitcnt lgkmcnt(8)
ds_read_b128 v[128:131], v12 offset:0
Mvav 100,252,184
buffer_load_dwordx4 v[44:47], v0, s[48:51], s60 offen sc1
Mvav 96,224,160
s_lshl_b32 s60, s93, 13
ds_read_b128 v[136:139], v12 offset:1024
Mvav 96,232,168
s_add_u32 s60, s60, s23
buffer_load_dwordx4 v0, s[48:51], s70 offen offset:1024 sc1 lds
Mvav 96,240,176
s_waitcnt lgkmcnt(9)
s_add_u32 s60, s60, s32
ds_read_b128 v[144:147], v12 offset:2048
Mvav 96,248,184
s_add_u32 s60, s60, 32768
v_exp_f32 v31, v31
Mvav 96,0,132
s_waitcnt lgkmcnt(9)
ds_read_b128 v[152:155], v12 offset:3072
v_add_f32 v31, 1.0, v31
Mvav 96,8,140
s_waitcnt lgkmcnt(9)
buffer_load_dwordx4 v0, s[48:51], s70 offen offset:2048 sc1 lds
Mvav 96,16,148
s_waitcnt lgkmcnt(8)
ds_read_b128 v[160:163], v12 offset:4096
Mvav 96,24,156
v_rcp_f32 v31, v31
Mvav 100,4,132
ds_read_b128 v[168:171], v12 offset:5120
v_mul_f32 v23, v23, v31
Mvav 100,12,140
buffer_load_dwordx4 v[48:51], v0, s[48:51], s60 offen sc1
Mvav 100,20,148
s_waitcnt lgkmcnt(9)
s_lshl_b32 s60, s94, 13
ds_read_b128 v[176:179], v12 offset:6144
Mvav 100,28,156
s_add_u32 s60, s60, s23
buffer_load_dwordx4 v0, s[48:51], s70 offen offset:3072 sc1 lds
Mvav 100,36,164
s_waitcnt lgkmcnt(9)
s_add_u32 s60, s60, s32
ds_read_b128 v[184:187], v12 offset:7168
Mvav 100,44,172
s_waitcnt lgkmcnt(9)
s_add_u32 s60, s60, 32768
Mvav 100,52,180
s_waitcnt lgkmcnt(8)
ds_read_b128 v[132:135], v12 offset:8192
Mvav 100,60,188
Mvav 96,32,164
ds_read_b128 v[140:143], v12 offset:9216
Mvav 96,40,172
Mvav 96,48,180
ds_read_b128 v[148:151], v12 offset:10240
Mvav 96,56,188
s_waitcnt lgkmcnt(10)
buffer_load_dwordx4 v[52:55], v0, s[48:51], s60 offen sc1
Zvav 104,64,128
s_waitcnt lgkmcnt(9)
s_lshl_b32 s60, s92, 13
ds_read_b128 v[156:159], v12 offset:11264
Mvav 104,72,136
s_waitcnt lgkmcnt(9)
s_add_u32 s60, s60, s23
Mvav 104,80,144
s_waitcnt lgkmcnt(8)
s_add_u32 s60, s60, s32
ds_read_b128 v[164:167], v12 offset:12288
Mvav 104,88,152
s_add_u32 s60, s60, 0
Zvav 108,68,128
s_not_b32 s59, s91
ds_read_b128 v[172:175], v12 offset:13312
Mvav 108,76,136
s_bfe_u32 s59, s59, 0x50004
Mvav 108,84,144
s_cmp_eq_u32 s59, s64
s_cselect_b32 s75, 2, 0
ds_read_b128 v[180:183], v12 offset:14336
Mvav 108,92,152
s_waitcnt lgkmcnt(7)
s_not_b32 s59, s90
Mvav 108,100,160
s_lshl_b32 s58, s92, 17
ds_read_b128 v[188:191], v12 offset:15360
v_and_or_b32 v96, v96, v2, s24
Mvav 108,108,168
s_add_u32 s58, s58, s22
buffer_load_dwordx4 v[32:35], v0, s[48:51], s60 offen sc1
Mvav 108,116,176
s_add_u32 s58, s58, 98304
ds_read_b128 v[128:131], v0 offset:32768
v_and_or_b32 v97, v97, v2, s25
Mvav 108,124,184
s_lshl_b32 s60, s93, 13
v_and_or_b32 v98, v98, v2, s26
v_and_or_b32 v99, v99, v2, s27
Mvav 104,96,160
s_add_u32 s60, s60, s23
ds_read_b128 v[136:139], v0 offset:33792
v_and_or_b32 v100, v100, v2, s24
Mvav 104,104,168
s_add_u32 s60, s60, s32
v_and_or_b32 v101, v101, v2, s25
v_and_or_b32 v102, v102, v2, s26
Mvav 104,112,176
s_add_u32 s60, s60, 0
ds_read_b128 v[144:147], v0 offset:34816
v_and_or_b32 v103, v103, v2, s27
Mvav 104,120,184
s_waitcnt lgkmcnt(9)
s_bfe_u32 s59, s59, 0x50004
Mvav 104,128,132
s_cmp_eq_u32 s59, s64
s_addc_u32 s75, s75, 0
ds_read_b128 v[152:155], v0 offset:35840
Mvav 104,136,140
s_waitcnt lgkmcnt(9)
s_add_u32 m0, s69, 16384
Mvav 104,144,148
s_waitcnt lgkmcnt(8)
s_add_u32 s70, s15, 9437184
ds_read_b128 v[160:163], v0 offset:36864
Mvav 104,152,156
buffer_load_dwordx4 v0, s[48:51], s70 offen sc1 lds
Mvav 108,132,132
ds_read_b128 v[168:171], v0 offset:37888
Mvav 108,140,140
buffer_store_dwordx4 v[96:99], v0, s[48:51], s58 offen sc1
Mvav 108,148,148
s_waitcnt lgkmcnt(9)
ds_read_b128 v[176:179], v0 offset:38912
Mvav 108,156,156
s_cmp_lg_u32 s75, 0
s_cbranch_scc1 .Lrt0
.Lrt0_r:
Mvav 108,164,164
s_waitcnt lgkmcnt(9)
ds_read_b128 v[184:187], v0 offset:39936
Mvav 108,172,172
s_waitcnt lgkmcnt(9)
buffer_load_dwordx4 v0, s[48:51], s70 offen offset:1024 sc1 lds
Mvav 108,180,180
s_waitcnt lgkmcnt(8)
ds_read_b128 v[132:135], v0 offset:40960
Mvav 108,188,188
Mvav 104,160,164
ds_read_b128 v[140:143], v0 offset:41984
Mvav 104,168,172
buffer_load_dwordx4 v0, s[48:51], s70 offen offset:2048 sc1 lds
Mvav 104,176,180
ds_read_b128 v[148:151], v0 offset:43008
Mvav 104,184,188
s_waitcnt lgkmcnt(10)
buffer_store_dwordx4 v[100:103], v0, s[48:51], s58 offen offset:1024 sc1
Mvav 104,192,128
s_waitcnt vmcnt(17) lgkmcnt(9)
ds_read_b128 v[156:159], v0 offset:44032
Mvav 104,200,136
s_waitcnt lgkmcnt(9)
buffer_store_dwordx2 v[62:63], v13, s[48:51], s34 offen offset:64 sc1
Mvav 104,208,144
s_waitcnt lgkmcnt(8)
ds_read_b128 v[164:167], v0 offset:45056
Mvav 104,216,152
buffer_load_dwordx4 v0, s[48:51], s70 offen offset:3072 sc1 lds
Mvav 108,196,128
ds_read_b128 v[172:175], v0 offset:46080
Mvav 108,204,136
s_add_u32 m0, s69, 98304
Mvav 108,212,144
s_add_u32 s70, s15, 9584640
ds_read_b128 v[180:183], v0 offset:47104
Mvav 108,220,152
s_waitcnt lgkmcnt(10)
buffer_load_dwordx4 v0, s[48:51], s70 offen sc1 lds
Mvav 108,228,160
s_waitcnt vmcnt(14) lgkmcnt(8)
ds_read_b128 v[188:191], v0 offset:48128
v_lshlrev_b32 v112, 16, v44
Mvav 108,236,168
s_barrier
Mvav 108,244,176
s_waitcnt lgkmcnt(8)
ds_read_b128 v[128:131], v12 offset:16384
v_and_b32 v113, 0xffff0000, v44
Mvav 108,252,184
s_waitcnt lgkmcnt(8)
buffer_load_dwordx4 v[36:39], v0, s[48:51], s60 offen sc1
)"
R"(
Mvav 104,224,160
s_lshl_b32 s60, s94, 13
ds_read_b128 v[136:139], v12 offset:17408
v_lshlrev_b32 v114, 16, v45
Mvav 104,232,168
s_add_u32 s60, s60, s23
buffer_load_dwordx4 v0, s[48:51], s70 offen offset:1024 sc1 lds
Mvav 104,240,176
s_add_u32 s60, s60, s32
ds_read_b128 v[144:147], v12 offset:18432
v_and_b32 v115, 0xffff0000, v45
Mvav 104,248,184
s_add_u32 s60, s60, 0
v_lshlrev_b32 v116, 16, v46
v_and_b32 v117, 0xffff0000, v46
Mvav 104,0,132
s_waitcnt lgkmcnt(9)
ds_read_b128 v[152:155], v12 offset:19456
v_lshlrev_b32 v118, 16, v47
Mvav 104,8,140
s_waitcnt lgkmcnt(9)
buffer_load_dwordx4 v0, s[48:51], s70 offen offset:2048 sc1 lds
Mvav 104,16,148
s_waitcnt vmcnt(14) lgkmcnt(8)
ds_read_b128 v[160:163], v12 offset:20480
v_lshlrev_b32 v56, 16, v48
Mvav 104,24,156
v_add_f32 v112, v112, v56
v_and_b32 v57, 0xffff0000, v48
Mvav 108,4,132
ds_read_b128 v[168:171], v12 offset:21504
v_add_f32 v113, v113, v57
Mvav 108,12,140
buffer_load_dwordx4 v[40:43], v0, s[48:51], s60 offen sc1
Mvav 108,20,148
ds_read_b128 v[176:179], v12 offset:22528
v_lshlrev_b32 v60, 16, v49
Mvav 108,28,156
s_waitcnt lgkmcnt(10)
buffer_load_dwordx4 v0, s[48:51], s70 offen offset:3072 sc1 lds
Mvav 108,36,164
s_waitcnt lgkmcnt(9)
ds_read_b128 v[184:187], v12 offset:23552
v_add_f32 v114, v114, v60
Mvav 108,44,172
s_waitcnt lgkmcnt(9)
v_and_b32 v61, 0xffff0000, v49
v_add_f32 v115, v115, v61
Mvav 108,52,180
s_waitcnt lgkmcnt(8)
ds_read_b128 v[132:135], v12 offset:24576
v_and_b32 v119, 0xffff0000, v47
Mvav 108,60,188
s_waitcnt vmcnt(14)
v_lshlrev_b32 v56, 16, v52
v_add_f32 v112, v112, v56
Mvav 104,32,164
ds_read_b128 v[140:143], v12 offset:25600
v_and_b32 v57, 0xffff0000, v52
Mvav 104,40,172
v_add_f32 v113, v113, v57
v_lshlrev_b32 v60, 16, v53
Mvav 104,48,180
ds_read_b128 v[148:151], v12 offset:26624
v_add_f32 v114, v114, v60
Mvav 104,56,188
s_waitcnt lgkmcnt(10)
v_and_b32 v61, 0xffff0000, v53
v_add_f32 v115, v115, v61
Zvav 96,64,128
s_waitcnt lgkmcnt(9)
ds_read_b128 v[156:159], v12 offset:27648
v_lshlrev_b32 v56, 16, v50
Mvav 96,72,136
s_waitcnt lgkmcnt(9)
v_add_f32 v116, v116, v56
v_lshlrev_b32 v56, 16, v54
Mvav 96,80,144
s_waitcnt lgkmcnt(8)
ds_read_b128 v[164:167], v12 offset:28672
v_add_f32 v116, v116, v56
Mvav 96,88,152
v_and_b32 v57, 0xffff0000, v50
v_add_f32 v117, v117, v57
Zvav 100,68,128
ds_read_b128 v[172:175], v12 offset:29696
v_and_b32 v57, 0xffff0000, v54
Mvav 100,76,136
v_add_f32 v117, v117, v57
v_lshlrev_b32 v60, 16, v51
Mvav 100,84,144
ds_read_b128 v[180:183], v12 offset:30720
v_add_f32 v118, v118, v60
Mvav 100,92,152
s_waitcnt lgkmcnt(7)
v_lshlrev_b32 v60, 16, v55
v_add_f32 v118, v118, v60
Mvav 100,100,160
s_lshl_b32 s58, s93, 17
ds_read_b128 v[188:191], v12 offset:31744
v_and_or_b32 v104, v104, v2, s24
Mvav 100,108,168
s_add_u32 s58, s58, s22
v_and_or_b32 v105, v105, v2, s25
v_and_or_b32 v106, v106, v2, s26
Mvav 100,116,176
s_add_u32 s58, s58, 98304
ds_read_b128 v[128:131], v12 offset:49152
v_and_or_b32 v107, v107, v2, s27
Mvav 100,124,184
buffer_store_dwordx4 v[104:107], v0, s[48:51], s58 offen sc1
Mvav 96,96,160
ds_read_b128 v[136:139], v12 offset:50176
v_and_or_b32 v108, v108, v2, s24
Mvav 96,104,168
v_and_or_b32 v109, v109, v2, s25
v_and_or_b32 v110, v110, v2, s26
Mvav 96,112,176
ds_read_b128 v[144:147], v12 offset:51200
v_and_or_b32 v111, v111, v2, s27
Mvav 96,120,184
s_waitcnt lgkmcnt(10)
v_and_b32 v61, 0xffff0000, v51
v_add_f32 v119, v119, v61
Mvav 96,128,132
s_waitcnt lgkmcnt(9)
ds_read_b128 v[152:155], v12 offset:52224
v_and_b32 v61, 0xffff0000, v55
Mvav 96,136,140
s_waitcnt vmcnt(14) lgkmcnt(8)
s_add_u32 m0, s69, 131072
v_add_f32 v119, v119, v61
v_lshlrev_b32 v120, 16, v32
Mvav 96,144,148
s_add_u32 s70, s15, 14155776
ds_read_b128 v[160:163], v12 offset:53248
v_and_b32 v121, 0xffff0000, v32
Mvav 96,152,156
buffer_load_dwordx4 v0, s[48:51], s70 offen sc1 lds
Mvav 100,132,132
ds_read_b128 v[168:171], v12 offset:54272
v_lshlrev_b32 v122, 16, v33
Mvav 100,140,140
buffer_store_dwordx4 v[108:111], v0, s[48:51], s58 offen offset:1024 sc1
Mvav 100,148,148
ds_read_b128 v[176:179], v12 offset:55296
v_and_b32 v123, 0xffff0000, v33
Mvav 100,156,156
s_waitcnt lgkmcnt(10)
v_lshlrev_b32 v124, 16, v34
v_and_b32 v125, 0xffff0000, v34
Mvav 100,164,164
s_waitcnt lgkmcnt(9)
ds_read_b128 v[184:187], v12 offset:56320
v_lshlrev_b32 v126, 16, v35
Mvav 100,172,172
s_waitcnt lgkmcnt(9)
buffer_load_dwordx4 v0, s[48:51], s70 offen offset:1024 sc1 lds
Mvav 100,180,180
s_waitcnt lgkmcnt(8)
ds_read_b128 v[132:135], v12 offset:57344
v_and_b32 v127, 0xffff0000, v35
Mvav 100,188,188
v_lshlrev_b32 v200, 16, v224
v_and_b32 v201, 0xffff0000, v224
Mvav 96,160,164
ds_read_b128 v[140:143], v12 offset:58368
v_lshlrev_b32 v202, 16, v225
Mvav 96,168,172
buffer_load_dwordx4 v0, s[48:51], s70 offen offset:2048 sc1 lds
Mvav 96,176,180
ds_read_b128 v[148:151], v12 offset:59392
v_and_b32 v203, 0xffff0000, v225
Mvav 96,184,188
s_waitcnt lgkmcnt(10)
buffer_load_dwordx2 v[30:31], v8, s[48:51], s33 offen sc1
Mvav 96,192,128
s_waitcnt lgkmcnt(9)
ds_read_b128 v[156:159], v12 offset:60416
v_lshlrev_b32 v208, 16, v226
Mvav 96,200,136
s_waitcnt lgkmcnt(9)
v_and_b32 v209, 0xffff0000, v226
v_lshlrev_b32 v210, 16, v227
Mvav 96,208,144
s_waitcnt lgkmcnt(8)
ds_read_b128 v[164:167], v12 offset:61440
v_and_b32 v211, 0xffff0000, v227
Mvav 96,216,152
buffer_load_dwordx4 v0, s[48:51], s70 offen offset:3072 sc1 lds
Mvav 100,196,128
ds_read_b128 v[172:175], v12 offset:62464
v_lshlrev_b32 v58, 16, v233
Mvav 100,204,136
s_add_u32 m0, s69, 49152
v_and_b32 v59, 0xffff0000, v233
v_lshlrev_b32 v24, 16, v234
Mvav 100,212,144
s_add_u32 s70, s15, 14303232
ds_read_b128 v[180:183], v12 offset:63488
v_and_b32 v25, 0xffff0000, v234
Mvav 100,220,152
s_waitcnt lgkmcnt(10)
buffer_load_dwordx4 v0, s[48:51], s70 offen sc1 lds
Mvav 100,228,160
s_waitcnt vmcnt(12) lgkmcnt(8)
ds_read_b128 v[188:191], v12 offset:64512
v_lshlrev_b32 v56, 16, v36
Mvav 100,236,168
s_barrier
Mvav 100,244,176
s_waitcnt lgkmcnt(8)
ds_read_b128 v[128:131], v14 offset:16384
v_add_f32 v120, v120, v56
Mvav 100,252,184
v_and_b32 v57, 0xffff0000, v36
v_add_f32 v121, v121, v57
Mvav 96,224,160
ds_read_b128 v[136:139], v14 offset:17408
v_lshlrev_b32 v60, 16, v37
Mvav 96,232,168
buffer_load_dwordx4 v0, s[48:51], s70 offen offset:1024 sc1 lds
Mvav 96,240,176
ds_read_b128 v[144:147], v14 offset:18432
v_add_f32 v122, v122, v60
Mvav 96,248,184
s_waitcnt lgkmcnt(10)
v_and_b32 v61, 0xffff0000, v37
v_add_f32 v123, v123, v61
Mvav 96,0,132
s_waitcnt lgkmcnt(9)
ds_read_b128 v[152:155], v14 offset:19456
v_lshlrev_b32 v26, 16, v235
Mvav 96,8,140
s_waitcnt lgkmcnt(9)
buffer_load_dwordx4 v0, s[48:51], s70 offen offset:2048 sc1 lds
Mvav 96,16,148
s_waitcnt vmcnt(11) lgkmcnt(8)
ds_read_b128 v[160:163], v14 offset:20480
v_lshlrev_b32 v56, 16, v40
Mvav 96,24,156
s_cmp_lg_u32 s75, 0
s_cbranch_scc1 .Lrt1
.Lrt1_r:
Mvav 100,4,132
s_add_u32 s59, s61, 0
ds_read_b128 v[168:171], v14 offset:21504
v_add_f32 v120, v120, v56
Mvav 100,12,140
v_and_b32 v57, 0xffff0000, v40
v_add_f32 v121, v121, v57
Mvav 100,20,148
ds_read_b128 v[176:179], v14 offset:22528
v_lshlrev_b32 v60, 16, v41
Mvav 100,28,156
s_waitcnt lgkmcnt(10)
buffer_load_dwordx4 v0, s[48:51], s70 offen offset:3072 sc1 lds
Mvav 100,36,164
s_waitcnt lgkmcnt(9)
ds_read_b128 v[184:187], v14 offset:23552
v_add_f32 v122, v122, v60
Mvav 100,44,172
s_waitcnt lgkmcnt(9)
v_and_b32 v61, 0xffff0000, v41
v_add_f32 v123, v123, v61
Mvav 100,52,180
s_waitcnt lgkmcnt(8)
ds_read_b128 v[132:135], v14 offset:24576
v_lshlrev_b32 v56, 16, v38
Mvav 100,60,188
v_add_f32 v124, v124, v56
v_lshlrev_b32 v56, 16, v42
Mvav 96,32,164
ds_read_b128 v[140:143], v14 offset:25600
v_add_f32 v124, v124, v56
Mvav 96,40,172
v_and_b32 v57, 0xffff0000, v38
v_add_f32 v125, v125, v57
Mvav 96,48,180
ds_read_b128 v[148:151], v14 offset:26624
v_and_b32 v57, 0xffff0000, v42
Mvav 96,56,188
s_waitcnt lgkmcnt(10)
v_add_f32 v125, v125, v57
v_lshlrev_b32 v60, 16, v39
Mvav 112,64,128
s_waitcnt lgkmcnt(9)
ds_read_b128 v[156:159], v14 offset:27648
v_add_f32 v126, v126, v60
Mvav 112,72,136
s_waitcnt lgkmcnt(9)
v_lshlrev_b32 v60, 16, v43
v_add_f32 v126, v126, v60
Mvav 112,80,144
s_waitcnt lgkmcnt(8)
ds_read_b128 v[164:167], v14 offset:28672
v_and_b32 v61, 0xffff0000, v39
Mvav 112,88,152
v_add_f32 v127, v127, v61
v_and_b32 v61, 0xffff0000, v43
Mvav 116,68,128
ds_read_b128 v[172:175], v14 offset:29696
v_add_f32 v127, v127, v61
Mvav 116,76,136
v_add_f32 v88, v88, v120
v_add_f32 v89, v89, v121
Mvav 116,84,144
ds_read_b128 v[180:183], v14 offset:30720
v_add_f32 v90, v90, v122
Mvav 116,92,152
s_waitcnt lgkmcnt(7)
v_add_f32 v91, v91, v123
v_add_f32 v92, v92, v124
Mvav 116,100,160
s_lshl_b32 s58, s94, 17
ds_read_b128 v[188:191], v14 offset:31744
v_and_or_b32 v96, v96, v2, s24
Mvav 116,108,168
s_add_u32 s58, s58, s22
v_and_or_b32 v97, v97, v2, s25
v_and_or_b32 v98, v98, v2, s26
Mvav 116,116,176
s_add_u32 s58, s58, 98304
ds_read_b128 v[128:131], v0 offset:0
v_and_or_b32 v99, v99, v2, s27
Mvav 116,124,184
buffer_store_dwordx4 v[96:99], v0, s[48:51], s58 offen sc1
Mvav 112,96,160
ds_read_b128 v[136:139], v0 offset:1024
v_and_or_b32 v100, v100, v2, s24
Mvav 112,104,168
v_and_or_b32 v101, v101, v2, s25
v_and_or_b32 v102, v102, v2, s26
Mvav 112,112,176
ds_read_b128 v[144:147], v0 offset:2048
v_and_or_b32 v103, v103, v2, s27
Mvav 112,120,184
s_waitcnt lgkmcnt(10)
v_add_f32 v93, v93, v125
v_add_f32 v94, v94, v126
Mvav 112,128,132
s_waitcnt lgkmcnt(9)
ds_read_b128 v[152:155], v0 offset:3072
v_add_f32 v95, v95, v127
Mvav 112,136,140
s_waitcnt lgkmcnt(9)
v_cvt_pk_bf16_f32 v88, v88, v89
v_cvt_pk_bf16_f32 v89, v90, v91
Mvav 112,144,148
s_waitcnt lgkmcnt(8)
ds_read_b128 v[160:163], v0 offset:4096
v_cvt_pk_bf16_f32 v90, v92, v93
Mvav 112,152,156
ds_bpermute_b32 v216, v5, v88
v_cvt_pk_bf16_f32 v91, v94, v95
Mvav 116,132,132
ds_read_b128 v[168:171], v0 offset:5120
v_lshlrev_b32 v56, 16, v232
Mvav 116,140,140
buffer_store_dwordx4 v[100:103], v0, s[48:51], s58 offen offset:1024 sc1
Mvav 116,148,148
ds_read_b128 v[176:179], v0 offset:6144
v_and_b32 v57, 0xffff0000, v232
Mvav 116,156,156
s_waitcnt lgkmcnt(11)
ds_bpermute_b32 v217, v5, v89
v_and_b32 v27, 0xffff0000, v235
Mvav 116,164,164
s_waitcnt lgkmcnt(11)
ds_read_b128 v[184:187], v0 offset:7168
Mvav 116,172,172
s_waitcnt lgkmcnt(11)
ds_bpermute_b32 v218, v5, v90
Mvav 116,180,180
s_waitcnt lgkmcnt(11)
ds_read_b128 v[132:135], v0 offset:8192
Mvav 116,188,188
s_waitcnt vmcnt(7)
ds_bpermute_b32 v219, v5, v91
v_xor_b32 v30, s86, v30
Mvav 112,160,164
ds_read_b128 v[140:143], v0 offset:9216
v_bitop3_b32 v30, v31, s87, v30 bitop3:0xbe
Mvav 112,168,172
s_waitcnt lgkmcnt(8)
v_cmp_ne_u32 vcc, 0, v30
v_lshlrev_b32 v88, 16, v216
Mvav 112,176,180
s_waitcnt lgkmcnt(5)
s_and_b64 vcc, vcc, 0xf00
ds_read_b128 v[148:151], v0 offset:10240
v_and_b32 v89, 0xffff0000, v216
Mvav 112,184,188
s_cbranch_vccnz .Lpf2
.Lpf2_r:
s_lshl_b32 s60, s92, 13
Mvav 112,192,128
s_waitcnt lgkmcnt(4)
s_add_u32 s60, s60, s23
ds_read_b128 v[156:159], v0 offset:11264
v_lshlrev_b32 v90, 16, v217
Mvav 112,200,136
s_add_u32 s60, s60, s32
v_and_b32 v91, 0xffff0000, v217
v_lshlrev_b32 v92, 16, v218
Mvav 112,208,144
s_add_u32 s60, s60, 65536
ds_read_b128 v[164:167], v0 offset:12288
v_and_b32 v93, 0xffff0000, v218
Mvav 112,216,152
s_waitcnt lgkmcnt(4)
buffer_load_dwordx4 v[32:35], v0, s[48:51], s60 offen sc1
Mvav 116,196,128
s_lshl_b32 s60, s93, 13
ds_read_b128 v[172:175], v0 offset:13312
v_lshlrev_b32 v94, 16, v219
Mvav 116,204,136
s_add_u32 s60, s60, s23
v_and_b32 v95, 0xffff0000, v219
v_fmac_f32 v200, v88, v64
Mvav 116,212,144
s_add_u32 s60, s60, s32
ds_read_b128 v[180:183], v0 offset:14336
v_fmac_f32 v201, v89, v65
Mvav 116,220,152
s_add_u32 s60, s60, 65536
v_fmac_f32 v202, v90, v66
v_fmac_f32 v203, v91, v67
Mvav 116,228,160
ds_read_b128 v[188:191], v0 offset:15360
v_fmac_f32 v208, v92, v68
Mvav 116,236,168
s_barrier
Mvav 116,244,176
ds_read_b128 v[128:131], v0 offset:16384
v_fmac_f32 v209, v93, v69
Mvav 116,252,184
v_fmac_f32 v210, v94, v70
v_fmac_f32 v211, v95, v71
Mvav 112,224,160
ds_read_b128 v[136:139], v0 offset:17408
v_cvt_pk_bf16_f32 v216, v200, v201
Mvav 112,232,168
)"
R"(
s_waitcnt lgkmcnt(7)
buffer_load_dwordx4 v[36:39], v0, s[48:51], s60 offen sc1
Mvav 112,240,176
s_lshl_b32 s60, s94, 13
ds_read_b128 v[144:147], v0 offset:18432
v_cvt_pk_bf16_f32 v217, v202, v203
Mvav 112,248,184
s_add_u32 s60, s60, s23
v_cvt_pk_bf16_f32 v218, v208, v209
v_cvt_pk_bf16_f32 v219, v210, v211
Mvav 112,0,132
s_add_u32 s60, s60, s32
ds_read_b128 v[152:155], v0 offset:19456
v_lshlrev_b32 v200, 16, v240
Mvav 112,8,140
s_add_u32 s60, s60, 65536
v_and_b32 v201, 0xffff0000, v240
v_lshlrev_b32 v202, 16, v241
Mvav 112,16,148
s_waitcnt lgkmcnt(8)
ds_read_b128 v[160:163], v0 offset:20480
v_and_b32 v203, 0xffff0000, v241
Mvav 112,24,156
v_lshlrev_b32 v208, 16, v242
v_and_b32 v209, 0xffff0000, v242
Mvav 116,4,132
ds_read_b128 v[168:171], v0 offset:21504
v_lshlrev_b32 v210, 16, v243
Mvav 116,12,140
v_and_b32 v211, 0xffff0000, v243
Mvav 116,20,148
ds_read_b128 v[176:179], v0 offset:22528
Mvav 116,28,156
s_waitcnt lgkmcnt(10)
buffer_load_dwordx4 v[40:43], v0, s[48:51], s60 offen sc1
Mvav 116,36,164
s_waitcnt lgkmcnt(9)
ds_read_b128 v[184:187], v0 offset:23552
Mvav 116,44,172
s_waitcnt lgkmcnt(9)
Mvav 116,52,180
s_waitcnt lgkmcnt(8)
ds_read_b128 v[132:135], v0 offset:24576
Mvav 116,60,188
Mvav 112,32,164
ds_read_b128 v[140:143], v0 offset:25600
Mvav 112,40,172
Mvav 112,48,180
ds_read_b128 v[148:151], v0 offset:26624
Mvav 112,56,188
s_waitcnt lgkmcnt(10)
Zvav 120,64,128
s_waitcnt lgkmcnt(8)
s_lshl_b32 s4, s92, 13
ds_read_b128 v[156:159], v0 offset:27648
Mvav 120,72,136
s_add_u32 s4, s4, s23
buffer_store_dwordx4 v[216:219], v4, s[52:55], s59 offen
Mvav 120,80,144
s_waitcnt lgkmcnt(8)
s_add_u32 s4, s4, 0x18000
ds_read_b128 v[164:167], v0 offset:28672
Mvav 120,88,152
s_lshl_b32 s5, s93, 13
Zvav 124,68,128
s_add_u32 s5, s5, s23
ds_read_b128 v[172:175], v0 offset:29696
Mvav 124,76,136
s_add_u32 s5, s5, 0x18000
Mvav 124,84,144
s_lshl_b32 s6, s94, 13
ds_read_b128 v[180:183], v0 offset:30720
Mvav 124,92,152
s_waitcnt lgkmcnt(8)
s_add_u32 s6, s6, s23
Mvav 124,100,160
s_add_u32 s6, s6, 0x18000
ds_read_b128 v[188:191], v0 offset:31744
v_cvt_pk_bf16_f32 v112, v112, v113
Mvav 124,108,168
s_add_u32 s59, s61, 1048576
ds_bpermute_b32 v192, v5, v112
v_cvt_pk_bf16_f32 v113, v114, v115
Mvav 124,116,176
s_waitcnt lgkmcnt(9)
ds_read_b128 v[128:131], v12 offset:32768
v_cvt_pk_bf16_f32 v114, v116, v117
Mvav 124,124,184
ds_bpermute_b32 v193, v5, v113
v_cvt_pk_bf16_f32 v115, v118, v119
Mvav 120,96,160
ds_read_b128 v[136:139], v12 offset:33792
Mvav 120,104,168
ds_bpermute_b32 v194, v5, v114
Mvav 120,112,176
ds_read_b128 v[144:147], v12 offset:34816
Mvav 120,120,184
s_waitcnt lgkmcnt(13)
ds_bpermute_b32 v195, v5, v115
Mvav 120,128,132
s_waitcnt lgkmcnt(13)
ds_read_b128 v[152:155], v12 offset:35840
Mvav 120,136,140
s_waitcnt lgkmcnt(7)
v_lshlrev_b32 v112, 16, v192
v_and_b32 v113, 0xffff0000, v192
Mvav 120,144,148
ds_read_b128 v[160:163], v12 offset:36864
v_fmac_f32 v56, v112, v72
Mvav 120,152,156
s_waitcnt lgkmcnt(6)
v_lshlrev_b32 v114, 16, v193
v_and_b32 v115, 0xffff0000, v193
Mvav 124,132,132
ds_read_b128 v[168:171], v12 offset:37888
v_fmac_f32 v57, v113, v73
Mvav 124,140,140
s_waitcnt lgkmcnt(5)
v_lshlrev_b32 v116, 16, v194
v_and_b32 v117, 0xffff0000, v194
Mvav 124,148,148
ds_read_b128 v[176:179], v12 offset:38912
v_fmac_f32 v58, v114, v74
Mvav 124,156,156
s_waitcnt lgkmcnt(4)
v_lshlrev_b32 v118, 16, v195
v_and_b32 v119, 0xffff0000, v195
Mvav 124,164,164
ds_read_b128 v[184:187], v12 offset:39936
v_fmac_f32 v59, v115, v75
Mvav 124,172,172
v_fmac_f32 v24, v116, v76
v_fmac_f32 v25, v117, v77
Mvav 124,180,180
ds_read_b128 v[132:135], v12 offset:40960
v_fmac_f32 v26, v118, v78
Mvav 124,188,188
v_fmac_f32 v27, v119, v79
v_cvt_pk_bf16_f32 v192, v56, v57
Mvav 120,160,164
ds_read_b128 v[140:143], v12 offset:41984
v_cvt_pk_bf16_f32 v193, v58, v59
Mvav 120,168,172
buffer_load_dwordx4 v[196:199], v0, s[48:51], s4 offen sc1
Mvav 120,176,180
ds_read_b128 v[148:151], v12 offset:43008
v_cvt_pk_bf16_f32 v194, v24, v25
Mvav 120,184,188
buffer_load_dwordx4 v[204:207], v0, s[48:51], s4 offen offset:1024 sc1
Mvav 120,192,128
s_waitcnt vmcnt(5)
ds_read_b128 v[156:159], v12 offset:44032
v_lshlrev_b32 v44, 16, v32
Mvav 120,200,136
buffer_load_dwordx4 v[212:215], v0, s[48:51], s5 offen sc1
Mvav 120,208,144
s_waitcnt lgkmcnt(8)
ds_read_b128 v[164:167], v12 offset:45056
v_and_b32 v45, 0xffff0000, v32
Mvav 120,216,152
buffer_load_dwordx4 v[220:223], v0, s[48:51], s5 offen offset:1024 sc1
Mvav 124,196,128
ds_read_b128 v[172:175], v12 offset:46080
v_lshlrev_b32 v46, 16, v33
Mvav 124,204,136
buffer_load_dwordx4 v[228:231], v0, s[48:51], s6 offen sc1
Mvav 124,212,144
ds_read_b128 v[180:183], v12 offset:47104
v_and_b32 v47, 0xffff0000, v33
Mvav 124,220,152
s_waitcnt lgkmcnt(10)
buffer_load_dwordx4 v[236:239], v0, s[48:51], s6 offen offset:1024 sc1
Mvav 124,228,160
s_waitcnt lgkmcnt(8)
ds_read_b128 v[188:191], v12 offset:48128
v_lshlrev_b32 v48, 16, v34
Mvav 124,236,168
s_barrier
Mvav 124,244,176
s_waitcnt vmcnt(8) lgkmcnt(8)
ds_read_b128 v[128:131], v14 offset:0
v_lshlrev_b32 v52, 16, v36
Mvav 124,252,184
v_add_f32 v44, v44, v52
v_and_b32 v53, 0xffff0000, v36
Mvav 120,224,160
ds_read_b128 v[136:139], v14 offset:1024
v_add_f32 v45, v45, v53
Mvav 120,232,168
v_lshlrev_b32 v54, 16, v37
v_add_f32 v46, v46, v54
Mvav 120,240,176
ds_read_b128 v[144:147], v14 offset:2048
v_and_b32 v55, 0xffff0000, v37
Mvav 120,248,184
s_waitcnt lgkmcnt(10)
v_add_f32 v47, v47, v55
v_and_b32 v49, 0xffff0000, v34
Mvav 120,0,132
s_waitcnt lgkmcnt(9)
ds_read_b128 v[152:155], v14 offset:3072
v_lshlrev_b32 v50, 16, v35
Mvav 120,8,140
s_waitcnt lgkmcnt(9)
v_and_b32 v51, 0xffff0000, v35
v_cvt_pk_bf16_f32 v195, v26, v27
Mvav 120,16,148
s_waitcnt lgkmcnt(8)
ds_read_b128 v[160:163], v14 offset:4096
v_lshlrev_b32 v56, 16, v248
Mvav 120,24,156
buffer_store_dwordx4 v[192:195], v4, s[52:55], s59 offen
Mvav 124,4,132
s_waitcnt vmcnt(8)
ds_read_b128 v[168:171], v14 offset:5120
v_lshlrev_b32 v52, 16, v40
Mvav 124,12,140
s_add_u32 s59, s61, 2097152
v_add_f32 v44, v44, v52
v_and_b32 v53, 0xffff0000, v40
Mvav 124,20,148
ds_read_b128 v[176:179], v14 offset:6144
v_add_f32 v45, v45, v53
Mvav 124,28,156
s_waitcnt lgkmcnt(10)
v_lshlrev_b32 v54, 16, v41
v_add_f32 v46, v46, v54
Mvav 124,36,164
s_waitcnt lgkmcnt(9)
ds_read_b128 v[184:187], v14 offset:7168
v_and_b32 v55, 0xffff0000, v41
Mvav 124,44,172
s_waitcnt lgkmcnt(9)
v_add_f32 v47, v47, v55
v_lshlrev_b32 v52, 16, v38
Mvav 124,52,180
s_waitcnt lgkmcnt(8)
ds_read_b128 v[132:135], v14 offset:8192
v_add_f32 v48, v48, v52
Mvav 124,60,188
v_lshlrev_b32 v52, 16, v42
v_add_f32 v48, v48, v52
Mvav 120,32,164
ds_read_b128 v[140:143], v14 offset:9216
v_and_b32 v53, 0xffff0000, v38
Mvav 120,40,172
v_add_f32 v49, v49, v53
v_and_b32 v53, 0xffff0000, v42
Mvav 120,48,180
ds_read_b128 v[148:151], v14 offset:10240
v_add_f32 v49, v49, v53
Mvav 120,56,188
s_waitcnt lgkmcnt(10)
v_lshlrev_b32 v54, 16, v39
v_add_f32 v50, v50, v54
Zvav 96,64,128
s_waitcnt lgkmcnt(9)
ds_read_b128 v[156:159], v14 offset:11264
v_lshlrev_b32 v54, 16, v43
Mvav 96,72,136
s_waitcnt lgkmcnt(9)
v_add_f32 v50, v50, v54
v_and_b32 v55, 0xffff0000, v39
Mvav 96,80,144
s_waitcnt lgkmcnt(8)
ds_read_b128 v[164:167], v14 offset:12288
v_add_f32 v51, v51, v55
Mvav 96,88,152
v_and_b32 v55, 0xffff0000, v43
v_add_f32 v51, v51, v55
Zvav 100,68,128
ds_read_b128 v[172:175], v14 offset:13312
v_and_b32 v57, 0xffff0000, v248
Mvav 100,76,136
v_lshlrev_b32 v58, 16, v249
v_and_b32 v59, 0xffff0000, v249
Mvav 100,84,144
ds_read_b128 v[180:183], v14 offset:14336
v_lshlrev_b32 v24, 16, v250
Mvav 100,92,152
s_waitcnt lgkmcnt(10)
v_and_b32 v25, 0xffff0000, v250
v_lshlrev_b32 v26, 16, v251
Mvav 100,100,160
s_waitcnt lgkmcnt(9)
ds_read_b128 v[188:191], v14 offset:15360
v_add_f32 v120, v120, v44
Mvav 100,108,168
s_waitcnt lgkmcnt(9)
v_add_f32 v121, v121, v45
v_add_f32 v122, v122, v46
Mvav 100,116,176
s_waitcnt lgkmcnt(8)
ds_read_b128 v[128:131], v0 offset:49152
v_add_f32 v123, v123, v47
Mvav 100,124,184
v_add_f32 v124, v124, v48
v_add_f32 v125, v125, v49
Mvav 96,96,160
ds_read_b128 v[136:139], v0 offset:50176
v_add_f32 v126, v126, v50
Mvav 96,104,168
v_add_f32 v127, v127, v51
v_cvt_pk_bf16_f32 v120, v120, v121
Mvav 96,112,176
ds_read_b128 v[144:147], v0 offset:51200
v_cvt_pk_bf16_f32 v121, v122, v123
Mvav 96,120,184
s_waitcnt lgkmcnt(10)
ds_bpermute_b32 v216, v5, v120
v_cvt_pk_bf16_f32 v122, v124, v125
Mvav 96,128,132
s_waitcnt lgkmcnt(10)
ds_read_b128 v[152:155], v0 offset:52224
v_cvt_pk_bf16_f32 v123, v126, v127
Mvav 96,136,140
s_waitcnt lgkmcnt(10)
ds_bpermute_b32 v217, v5, v121
v_and_b32 v27, 0xffff0000, v251
Mvav 96,144,148
s_waitcnt lgkmcnt(10)
ds_read_b128 v[160:163], v0 offset:53248
Mvav 96,152,156
ds_bpermute_b32 v218, v5, v122
Mvav 100,132,132
ds_read_b128 v[168:171], v0 offset:54272
Mvav 100,140,140
ds_bpermute_b32 v219, v5, v123
Mvav 100,148,148
ds_read_b128 v[176:179], v0 offset:55296
Mvav 100,156,156
s_waitcnt lgkmcnt(7)
v_lshlrev_b32 v120, 16, v216
v_and_b32 v121, 0xffff0000, v216
Mvav 100,164,164
ds_read_b128 v[184:187], v0 offset:56320
v_fmac_f32 v200, v120, v80
Mvav 100,172,172
s_waitcnt vmcnt(6)
v_xor_b32 v196, s24, v196
v_xor_b32 v197, s25, v197
Mvav 100,180,180
ds_read_b128 v[132:135], v0 offset:57344
v_xor_b32 v198, s26, v198
Mvav 100,188,188
s_waitcnt vmcnt(5)
v_xor_b32 v199, s27, v199
v_xor_b32 v204, s24, v204
Mvav 96,160,164
ds_read_b128 v[140:143], v0 offset:58368
v_xor_b32 v205, s25, v205
Mvav 96,168,172
v_xor_b32 v206, s26, v206
v_xor_b32 v207, s27, v207
Mvav 96,176,180
s_waitcnt vmcnt(4)
ds_read_b128 v[148:151], v0 offset:59392
v_xor_b32 v212, s24, v212
Mvav 96,184,188
v_xor_b32 v213, s25, v213
v_xor_b32 v214, s26, v214
Mvav 96,192,128
ds_read_b128 v[156:159], v0 offset:60416
v_xor_b32 v215, s27, v215
Mvav 96,200,136
s_waitcnt vmcnt(3)
v_xor_b32 v220, s24, v220
v_xor_b32 v221, s25, v221
Mvav 96,208,144
s_waitcnt lgkmcnt(11)
ds_read_b128 v[164:167], v0 offset:61440
v_xor_b32 v222, s26, v222
Mvav 96,216,152
s_waitcnt vmcnt(2)
v_xor_b32 v223, s27, v223
v_xor_b32 v228, s24, v228
Mvav 100,196,128
ds_read_b128 v[172:175], v0 offset:62464
v_xor_b32 v229, s25, v229
Mvav 100,204,136
v_xor_b32 v230, s26, v230
v_xor_b32 v231, s27, v231
Mvav 100,212,144
s_waitcnt vmcnt(1)
ds_read_b128 v[180:183], v0 offset:63488
v_xor_b32 v236, s24, v236
Mvav 100,220,152
s_waitcnt lgkmcnt(12)
v_xor_b32 v237, s25, v237
v_xor_b32 v238, s26, v238
Mvav 100,228,160
s_waitcnt lgkmcnt(10)
ds_read_b128 v[188:191], v0 offset:64512
v_xor_b32 v239, s27, v239
Mvav 100,236,168
s_waitcnt lgkmcnt(9)
v_or3_b32 v9, v196, v197, v198
v_or3_b32 v9, v9, v199, v204
Mvav 100,244,176
s_waitcnt lgkmcnt(8)
v_or3_b32 v9, v9, v205, v206
v_or3_b32 v9, v9, v207, v212
Mvav 100,252,184
v_or3_b32 v9, v9, v213, v214
v_or3_b32 v9, v9, v215, v220
Mvav 96,224,160
v_or3_b32 v9, v9, v221, v222
v_or3_b32 v9, v9, v223, v228
Mvav 96,232,168
v_or3_b32 v9, v9, v229, v230
v_or3_b32 v9, v9, v231, v236
Mvav 96,240,176
v_or3_b32 v9, v9, v237, v238
v_or_b32 v9, v9, v239
Mvav 96,248,184
s_waitcnt lgkmcnt(7)
v_and_b32 v9, 0xff, v9
v_cmp_ne_u32 vcc, 0, v9
Mvav 96,0,132
s_waitcnt lgkmcnt(6)
s_cbranch_vccnz .Lpc3
.Lpc3_r:
Mvav 96,8,140
s_waitcnt lgkmcnt(5)
v_add_f32 v196, v196, v212
v_add_f32 v197, v197, v213
Mvav 96,16,148
s_waitcnt lgkmcnt(4)
v_add_f32 v198, v198, v214
v_add_f32 v199, v199, v215
Mvav 96,24,156
v_add_f32 v204, v204, v220
v_add_f32 v205, v205, v221
Mvav 100,4,132
v_add_f32 v206, v206, v222
v_add_f32 v207, v207, v223
Mvav 100,12,140
v_add_f32 v196, v196, v228
v_add_f32 v197, v197, v229
Mvav 100,20,148
v_add_f32 v198, v198, v230
v_add_f32 v199, v199, v231
Mvav 100,28,156
s_waitcnt lgkmcnt(3)
v_add_f32 v204, v204, v236
v_add_f32 v205, v205, v237
Mvav 100,36,164
s_waitcnt lgkmcnt(2)
v_add_f32 v206, v206, v238
v_add_f32 v207, v207, v239
Mvav 100,44,172
s_waitcnt lgkmcnt(1)
v_lshlrev_b32 v122, 16, v217
v_and_b32 v123, 0xffff0000, v217
Mvav 100,52,180
s_waitcnt lgkmcnt(0)
v_lshlrev_b32 v124, 16, v218
v_and_b32 v125, 0xffff0000, v218
Mvav 100,60,188
v_lshlrev_b32 v126, 16, v219
v_and_b32 v127, 0xffff0000, v219
Mvav 96,32,164
v_fmac_f32 v201, v121, v81
v_fmac_f32 v202, v122, v82
)"
R"(
Mvav 96,40,172
v_fmac_f32 v203, v123, v83
v_fmac_f32 v208, v124, v84
Mvav 96,48,180
v_fmac_f32 v209, v125, v85
v_fmac_f32 v210, v126, v86
Mvav 96,56,188
v_fmac_f32 v211, v127, v87
v_cvt_pk_bf16_f32 v216, v200, v201
s_nop 5
v_add_f32 v96, v96, v196
v_add_f32 v97, v97, v197
v_add_f32 v98, v98, v198
v_add_f32 v99, v99, v199
v_add_f32 v100, v100, v204
v_add_f32 v101, v101, v205
v_add_f32 v102, v102, v206
v_add_f32 v103, v103, v207
v_cvt_pk_bf16_f32 v217, v202, v203
v_cvt_pk_bf16_f32 v218, v208, v209
v_cvt_pk_bf16_f32 v219, v210, v211
buffer_store_dwordx4 v[216:219], v4, s[52:55], s59 offen
s_nop 1
s_add_u32 s59, s61, 3145728
v_cvt_pk_bf16_f32 v96, v96, v97
v_cvt_pk_bf16_f32 v97, v98, v99
v_cvt_pk_bf16_f32 v98, v100, v101
v_cvt_pk_bf16_f32 v99, v102, v103
ds_bpermute_b32 v192, v5, v96
ds_bpermute_b32 v193, v5, v97
ds_bpermute_b32 v194, v5, v98
ds_bpermute_b32 v195, v5, v99
s_waitcnt lgkmcnt(3)
v_lshlrev_b32 v96, 16, v192
v_and_b32 v97, 0xffff0000, v192
s_waitcnt lgkmcnt(2)
v_lshlrev_b32 v98, 16, v193
v_and_b32 v99, 0xffff0000, v193
s_waitcnt lgkmcnt(1)
v_lshlrev_b32 v100, 16, v194
v_and_b32 v101, 0xffff0000, v194
s_waitcnt lgkmcnt(0)
v_lshlrev_b32 v102, 16, v195
v_and_b32 v103, 0xffff0000, v195
v_fmac_f32 v56, v96, v16
v_fmac_f32 v57, v97, v17
v_fmac_f32 v58, v98, v18
v_fmac_f32 v59, v99, v19
v_fmac_f32 v24, v100, v20
v_fmac_f32 v25, v101, v21
v_fmac_f32 v26, v102, v22
v_fmac_f32 v27, v103, v23
v_cvt_pk_bf16_f32 v192, v56, v57
v_cvt_pk_bf16_f32 v193, v58, v59
v_cvt_pk_bf16_f32 v194, v24, v25
v_cvt_pk_bf16_f32 v195, v26, v27
buffer_store_dwordx4 v[192:195], v4, s[52:55], s59 offen
s_endpgm
.Lbepi:
s_mov_b32 s98, s97
v_mov_b32 v132, s86
v_mov_b32 v133, s87
v_rcp_f32 v64, v114
v_rcp_f32 v65, v115
s_add_u32 s83, s89, 4096
v_accvgpr_read_b32 v66, a64
v_accvgpr_read_b32 v67, a65
v_mul_f32 v66, v66, v64
v_mul_f32 v67, v67, v64
v_cvt_pk_bf16_f32 v68, v66, v67
v_accvgpr_read_b32 v66, a66
v_accvgpr_read_b32 v67, a67
v_mul_f32 v66, v66, v64
v_mul_f32 v67, v67, v64
v_cvt_pk_bf16_f32 v69, v66, v67
v_accvgpr_read_b32 v66, a72
v_accvgpr_read_b32 v67, a73
v_mul_f32 v66, v66, v64
v_mul_f32 v67, v67, v64
v_cvt_pk_bf16_f32 v70, v66, v67
v_accvgpr_read_b32 v66, a74
v_accvgpr_read_b32 v67, a75
v_mul_f32 v66, v66, v64
v_mul_f32 v67, v67, v64
v_cvt_pk_bf16_f32 v71, v66, v67
buffer_store_dwordx4 v[68:71], v0, s[48:51], s89 offen sc1
v_accvgpr_read_b32 v66, a68
v_accvgpr_read_b32 v67, a69
v_mul_f32 v66, v66, v65
v_mul_f32 v67, v67, v65
v_cvt_pk_bf16_f32 v72, v66, v67
v_accvgpr_read_b32 v66, a70
v_accvgpr_read_b32 v67, a71
v_mul_f32 v66, v66, v65
v_mul_f32 v67, v67, v65
v_cvt_pk_bf16_f32 v73, v66, v67
v_accvgpr_read_b32 v66, a76
v_accvgpr_read_b32 v67, a77
v_mul_f32 v66, v66, v65
v_mul_f32 v67, v67, v65
v_cvt_pk_bf16_f32 v74, v66, v67
v_accvgpr_read_b32 v66, a78
v_accvgpr_read_b32 v67, a79
v_mul_f32 v66, v66, v65
v_mul_f32 v67, v67, v65
v_cvt_pk_bf16_f32 v75, v66, v67
buffer_store_dwordx4 v[72:75], v0, s[48:51], s83 offen sc1
v_accvgpr_read_b32 v66, a80
v_accvgpr_read_b32 v67, a81
v_mul_f32 v66, v66, v64
v_mul_f32 v67, v67, v64
v_cvt_pk_bf16_f32 v68, v66, v67
v_accvgpr_read_b32 v66, a82
v_accvgpr_read_b32 v67, a83
v_mul_f32 v66, v66, v64
v_mul_f32 v67, v67, v64
v_cvt_pk_bf16_f32 v69, v66, v67
v_accvgpr_read_b32 v66, a88
v_accvgpr_read_b32 v67, a89
v_mul_f32 v66, v66, v64
v_mul_f32 v67, v67, v64
v_cvt_pk_bf16_f32 v70, v66, v67
v_accvgpr_read_b32 v66, a90
v_accvgpr_read_b32 v67, a91
v_mul_f32 v66, v66, v64
v_mul_f32 v67, v67, v64
v_cvt_pk_bf16_f32 v71, v66, v67
buffer_store_dwordx4 v[68:71], v0, s[48:51], s89 offen offset:1024 sc1
v_accvgpr_read_b32 v66, a84
v_accvgpr_read_b32 v67, a85
v_mul_f32 v66, v66, v65
v_mul_f32 v67, v67, v65
v_cvt_pk_bf16_f32 v72, v66, v67
v_accvgpr_read_b32 v66, a86
v_accvgpr_read_b32 v67, a87
v_mul_f32 v66, v66, v65
v_mul_f32 v67, v67, v65
v_cvt_pk_bf16_f32 v73, v66, v67
v_accvgpr_read_b32 v66, a92
v_accvgpr_read_b32 v67, a93
v_mul_f32 v66, v66, v65
v_mul_f32 v67, v67, v65
v_cvt_pk_bf16_f32 v74, v66, v67
v_accvgpr_read_b32 v66, a94
v_accvgpr_read_b32 v67, a95
v_mul_f32 v66, v66, v65
v_mul_f32 v67, v67, v65
v_cvt_pk_bf16_f32 v75, v66, v67
buffer_store_dwordx4 v[72:75], v0, s[48:51], s83 offen offset:1024 sc1
v_accvgpr_read_b32 v66, a96
v_accvgpr_read_b32 v67, a97
v_mul_f32 v66, v66, v64
v_mul_f32 v67, v67, v64
v_cvt_pk_bf16_f32 v68, v66, v67
v_accvgpr_read_b32 v66, a98
v_accvgpr_read_b32 v67, a99
v_mul_f32 v66, v66, v64
v_mul_f32 v67, v67, v64
v_cvt_pk_bf16_f32 v69, v66, v67
v_accvgpr_read_b32 v66, a104
v_accvgpr_read_b32 v67, a105
v_mul_f32 v66, v66, v64
v_mul_f32 v67, v67, v64
v_cvt_pk_bf16_f32 v70, v66, v67
v_accvgpr_read_b32 v66, a106
v_accvgpr_read_b32 v67, a107
v_mul_f32 v66, v66, v64
v_mul_f32 v67, v67, v64
v_cvt_pk_bf16_f32 v71, v66, v67
buffer_store_dwordx4 v[68:71], v0, s[48:51], s89 offen offset:2048 sc1
v_accvgpr_read_b32 v66, a100
v_accvgpr_read_b32 v67, a101
v_mul_f32 v66, v66, v65
v_mul_f32 v67, v67, v65
v_cvt_pk_bf16_f32 v72, v66, v67
v_accvgpr_read_b32 v66, a102
v_accvgpr_read_b32 v67, a103
v_mul_f32 v66, v66, v65
v_mul_f32 v67, v67, v65
v_cvt_pk_bf16_f32 v73, v66, v67
v_accvgpr_read_b32 v66, a108
v_accvgpr_read_b32 v67, a109
v_mul_f32 v66, v66, v65
v_mul_f32 v67, v67, v65
v_cvt_pk_bf16_f32 v74, v66, v67
v_accvgpr_read_b32 v66, a110
v_accvgpr_read_b32 v67, a111
v_mul_f32 v66, v66, v65
v_mul_f32 v67, v67, v65
v_cvt_pk_bf16_f32 v75, v66, v67
buffer_store_dwordx4 v[72:75], v0, s[48:51], s83 offen offset:2048 sc1
v_accvgpr_read_b32 v66, a112
v_accvgpr_read_b32 v67, a113
v_mul_f32 v66, v66, v64
v_mul_f32 v67, v67, v64
v_cvt_pk_bf16_f32 v68, v66, v67
v_accvgpr_read_b32 v66, a114
v_accvgpr_read_b32 v67, a115
v_mul_f32 v66, v66, v64
v_mul_f32 v67, v67, v64
v_cvt_pk_bf16_f32 v69, v66, v67
v_accvgpr_read_b32 v66, a120
v_accvgpr_read_b32 v67, a121
v_mul_f32 v66, v66, v64
v_mul_f32 v67, v67, v64
v_cvt_pk_bf16_f32 v70, v66, v67
v_accvgpr_read_b32 v66, a122
v_accvgpr_read_b32 v67, a123
v_mul_f32 v66, v66, v64
v_mul_f32 v67, v67, v64
v_cvt_pk_bf16_f32 v71, v66, v67
buffer_store_dwordx4 v[68:71], v0, s[48:51], s89 offen offset:3072 sc1
v_accvgpr_read_b32 v66, a116
v_accvgpr_read_b32 v67, a117
v_mul_f32 v66, v66, v65
v_mul_f32 v67, v67, v65
v_cvt_pk_bf16_f32 v72, v66, v67
v_accvgpr_read_b32 v66, a118
v_accvgpr_read_b32 v67, a119
v_mul_f32 v66, v66, v65
v_mul_f32 v67, v67, v65
v_cvt_pk_bf16_f32 v73, v66, v67
v_accvgpr_read_b32 v66, a124
v_accvgpr_read_b32 v67, a125
v_mul_f32 v66, v66, v65
v_mul_f32 v67, v67, v65
v_cvt_pk_bf16_f32 v74, v66, v67
v_accvgpr_read_b32 v66, a126
v_accvgpr_read_b32 v67, a127
v_mul_f32 v66, v66, v65
v_mul_f32 v67, v67, v65
v_cvt_pk_bf16_f32 v75, v66, v67
buffer_store_dwordx4 v[72:75], v0, s[48:51], s83 offen offset:3072 sc1
s_setpc_b64 s[16:17]
.Lsb:
s_cmp_eq_u32 s34, 24
s_cbranch_scc1 .Lsw
s_waitcnt vmcnt(15)
ds_read_b64_tr_b16 v[32:33], v24 offset:0
v_and_b32 v27, 16, v0
ds_read_b64_tr_b16 v[34:35], v24 offset:256
v_lshrrev_b32 v27, v27, s86
ds_read_b64_tr_b16 v[36:37], v24 offset:4096
v_lshrrev_b32 v120, 8, v27
ds_read_b64_tr_b16 v[38:39], v24 offset:4352
v_lshrrev_b32 v121, 1, v0
ds_read_b64_tr_b16 v[40:41], v24 offset:32
v_mov_b32 v124, 0xff
ds_read_b64_tr_b16 v[42:43], v24 offset:288
s_lshr_b32 s58, s62, 3
ds_read_b64_tr_b16 v[44:45], v24 offset:4128
s_lshl2_add_u32 s58, s65, s58
ds_read_b64_tr_b16 v[46:47], v24 offset:4384
s_lshl2_add_u32 s58, s58, s57
s_waitcnt lgkmcnt(6)
Zvvv 196,16,32
s_lshl_b32 s58, s58, 10
ds_read_b64_tr_b16 v[48:49], v24 offset:512
ds_read_b64_tr_b16 v[50:51], v24 offset:768
s_waitcnt lgkmcnt(6)
Mvvv 196,20,36
s_mul_i32 s59, s64, 0x10200
ds_read_b64_tr_b16 v[52:53], v24 offset:4608
ds_read_b64_tr_b16 v[54:55], v24 offset:4864
s_waitcnt lgkmcnt(6)
Zvvv 204,16,40
s_and_b32 s59, s59, 0xe0200
ds_read_b64_tr_b16 v[56:57], v24 offset:544
ds_read_b64_tr_b16 v[58:59], v24 offset:800
s_waitcnt lgkmcnt(6)
Mvvv 204,20,44
s_add_u32 s58, s58, s59
ds_read_b64_tr_b16 v[60:61], v24 offset:4640
ds_read_b64_tr_b16 v[62:63], v24 offset:4896
s_waitcnt lgkmcnt(6)
Zvvv 212,16,48
s_add_u32 s58, s58, 1048576
s_add_u32 s64, s64, 1
s_mov_b32 vcc_lo, 0xffff0000
s_waitcnt lgkmcnt(4)
Mvvv 212,20,52
s_mov_b32 vcc_hi, 0xffff0000
s_lshr_b32 s59, s62, 3
s_lshl2_add_u32 s59, s59, s57
s_waitcnt lgkmcnt(2)
Zvvv 220,16,56
s_mul_i32 s59, s59, 458752
s_lshr_b32 s68, s64, 1
s_lshl3_add_u32 s68, s68, s65
s_waitcnt lgkmcnt(0)
Mvvv 220,20,60
s_lshl_b32 s68, s68, 7
s_add_u32 s68, s68, s59
s_xor_b64 s[36:37], s[36:37], s[40:41]
s_add_u32 s77, s76, 0x20000
s_add_u32 s72, s34, 4
s_add_u32 s59, s34, 6
s_and_b32 s70, s32, 0x104000
s_bitcmp1_b32 s70, s59
s_cselect_b32 s72, s72, 0
v_cndmask_b32 v196, v196, v204, vcc
v_cndmask_b32 v197, v197, v205, vcc
v_cndmask_b32 v212, v212, v220, vcc
v_cndmask_b32 v213, v213, v221, vcc
s_mov_b64 vcc, 0xffffffff
v_cndmask_b32 v122, v212, v196, vcc
v_cndmask_b32 v123, v213, v197, vcc
v_bfi_b32 v122, v124, v27, v122
v_bfi_b32 v123, v124, v120, v123
buffer_store_dwordx2 v[122:123], v121, s[48:51], s58 offen sc1
s_setpc_b64 s[16:17]
.Lsw:
v_bfe_u32 v25, v0, 6, 3
v_lshrrev_b32 v126, 9, v0
v_lshlrev_b32 v126, 6, v126
v_lshl_add_u32 v25, v25, 11, v126
v_and_b32 v126, 48, v0
v_add_u32 v25, v25, v126
s_mov_b64 s[36:37], s[28:29]
s_mov_b32 s38, s30
s_lshl3_add_u32 s68, s69, s67
s_add_u32 s77, s76, 0x20000
s_add_u32 s72, s34, 4
s_mov_b32 s60, 128
s_mov_b32 s61, 16384
s_lshr_b32 s82, s71, 1
s_lshl2_add_u32 s82, s82, s95
s_lshl_b32 s82, s82, 10
s_add_u32 s82, s82, 32768
v_add_u32 v21, s69, v0
v_subrev_u32 v21, 0, v21
v_cmp_gt_u32 vcc, 256, v21
s_nop 1
v_cndmask_b32 v21, 0, -1, vcc
v_not_b32 v20, v21
v_lshl_or_b32 v20, v20, 31, v0
buffer_load_dwordx4 v[16:19], v20, s[48:51], s82 offen sc1
s_swappc_b64 s[6:7], s[16:17]
s_waitcnt vmcnt(15)
v_xor_b32 v16, s86, v16
v_bitop3_b32 v16, v17, s87, v16 bitop3:0xbe
v_bitop3_b32 v16, v18, s86, v16 bitop3:0xbe
v_bitop3_b32 v16, v19, s87, v16 bitop3:0xbe
v_and_b32 v16, v16, v21
v_cmp_ne_u32 vcc, 0, v16
s_nop 0
s_cbranch_vccnz .Lcf1
.Lcf1_r:
s_lshl2_add_u32 s58, s62, s57
s_lshl_b32 s58, s58, 10
s_mov_b32 s59, .Lhot_end-.Lstart
s_min_u32 s58, s58, s59
s_getpc_b64 s[18:19]
.Lcp1:
s_sub_u32 s18, s18, .Lcp1-.Lstart
s_subb_u32 s19, s19, 0
s_add_u32 s18, s18, s58
s_addc_u32 s19, s19, 0
s_and_b32 s18, s18, -16
global_load_dwordx4 v[28:31], v0, s[18:19]
s_swappc_b64 s[6:7], s[16:17]
s_waitcnt vmcnt(21)
s_cmp_eq_u32 s63, 6
s_cselect_b32 s58, s97, 0x80000000
s_cselect_b32 s84, 0x80000000, s84
buffer_store_dwordx2 v[132:133], v13, s[48:51], s58 offen sc1
v_add_u32 v21, s69, v0
v_subrev_u32 v21, 256, v21
v_cmp_gt_u32 vcc, 192, v21
s_nop 1
v_cndmask_b32 v21, 0, -1, vcc
v_not_b32 v20, v21
v_lshl_or_b32 v20, v20, 31, v0
v_mov_b32 v3, v25
s_mov_b64 s[12:13], s[28:29]
s_mov_b64 s[14:15], s[30:31]
s_mov_b32 s24, -896
s_mov_b32 s25, 14336
s_mov_b32 s26, 13440
s_swappc_b64 s[6:7], s[16:17]
.Lm0:
s_nop 7
v_bfe_u32 v26, v0, 4, 4
v_lshrrev_b32 v27, 8, v0
v_lshlrev_b32 v27, 2, v27
v_sub_u32 v26, v26, v27
v_mov_b32 v27, 0xff800000
v_cmp_gt_i32 vcc, 0, v26
v_mov_b32 v56, v27
s_nop 0
v_cndmask_b32 v40, v40, v27, vcc
v_cndmask_b32 v60, v60, v27, vcc
v_cmp_gt_i32 vcc, 1, v26
v_mov_b32 v57, v27
s_nop 0
v_cndmask_b32 v41, v41, v27, vcc
v_cndmask_b32 v61, v61, v27, vcc
v_cmp_gt_i32 vcc, 2, v26
v_mov_b32 v58, v27
s_nop 0
v_cndmask_b32 v42, v42, v27, vcc
v_cndmask_b32 v62, v62, v27, vcc
v_cmp_gt_i32 vcc, 3, v26
v_mov_b32 v59, v27
s_nop 0
v_cndmask_b32 v43, v43, v27, vcc
v_cndmask_b32 v63, v63, v27, vcc
s_branch .Lm0_r
.Lm1:
v_bfe_u32 v120, v0, 4, 4
v_lshrrev_b32 v121, 8, v0
v_lshlrev_b32 v121, 2, v121
v_sub_u32 v121, v120, v121
v_mov_b32 v122, 0xff800000
v_cndmask_b32 v121, -16, v121, s[4:5]
v_cmp_gt_i32 vcc, 0, v121
v_cndmask_b32 v180, v122, v140, s[4:5]
s_nop 0
v_cndmask_b32 v172, v132, v122, vcc
v_cndmask_b32 v188, v140, v122, vcc
v_cmp_gt_i32 vcc, 1, v121
v_cndmask_b32 v181, v122, v140, s[4:5]
s_nop 0
v_cndmask_b32 v173, v132, v122, vcc
v_cndmask_b32 v189, v140, v122, vcc
v_cmp_gt_i32 vcc, 2, v121
v_cndmask_b32 v182, v122, v140, s[4:5]
s_nop 0
v_cndmask_b32 v174, v132, v122, vcc
v_cndmask_b32 v190, v140, v122, vcc
v_cmp_gt_i32 vcc, 3, v121
v_cndmask_b32 v183, v122, v140, s[4:5]
)"
R"(
s_nop 0
v_cndmask_b32 v175, v132, v122, vcc
v_cndmask_b32 v191, v140, v122, vcc
s_nop 0
s_branch .Ldiag1
.Lhab1:
s_getpc_b64 s[16:17]
.Lhab1_g:
s_sub_u32 s16, s16, .Lhab1_g-.Lhab1_r
s_subb_u32 s17, s17, 0
s_and_b32 s58, s34, 3
s_cmp_eq_u32 s58, 1
s_cbranch_scc1 .Lbepi
s_setpc_b64 s[6:7]
.Lhd0:
s_getpc_b64 s[16:17]
.Lhd0_g:
s_sub_u32 s16, s16, .Lhd0_g-.Lhd0_r
s_subb_u32 s17, s17, 0
s_and_b32 s58, s34, 3
s_cmp_eq_u32 s58, 1
s_cbranch_scc1 .Lbepi
s_setpc_b64 s[6:7]
.Lhd1:
s_getpc_b64 s[16:17]
.Lhd1_g:
s_sub_u32 s16, s16, .Lhd1_g-.Lhd1_r
s_subb_u32 s17, s17, 0
s_and_b32 s58, s34, 3
s_cmp_eq_u32 s58, 1
s_cbranch_scc1 .Lbepi
s_setpc_b64 s[6:7]
.Lhy:
s_getpc_b64 s[16:17]
.Lhy_g:
s_sub_u32 s16, s16, .Lhy_g-.Lhy_r
s_subb_u32 s17, s17, 0
s_and_b32 s58, s34, 3
s_cmp_eq_u32 s58, 1
s_cbranch_scc1 .Lbepi
s_setpc_b64 s[6:7]
.Lhaa1:
s_getpc_b64 s[16:17]
.Lhaa1_g:
s_sub_u32 s16, s16, .Lhaa1_g-.Lhaa1_r
s_subb_u32 s17, s17, 0
s_and_b32 s58, s34, 3
s_cmp_eq_u32 s58, 1
s_cbranch_scc1 .Lbepi
s_setpc_b64 s[6:7]
.Lhaa0:
s_getpc_b64 s[16:17]
.Lhaa0_g:
s_sub_u32 s16, s16, .Lhaa0_g-.Lhaa0_r
s_subb_u32 s17, s17, 0
s_and_b32 s58, s34, 3
s_cmp_eq_u32 s58, 1
s_cbranch_scc1 .Lbepi
s_setpc_b64 s[6:7]
.Lrt0:
s_bitcmp1_b32 s75, 0
s_cselect_b32 s72, s90, s91
s_cselect_b32 s59, 0, 32
s_add_u32 s59, s59, s68
s_mul_i32 s35, s59, 7168
s_add_u32 s35, s35, 2097152
s_add_u32 s20, s35, 4096
buffer_load_dwordx4 v[196:199], v0, s[48:51], s35 offen sc1
buffer_load_dwordx4 v[204:207], v0, s[48:51], s35 offen offset:1024 sc1
buffer_load_dwordx4 v[212:215], v0, s[48:51], s35 offen offset:2048 sc1
buffer_load_dwordx4 v[220:223], v0, s[48:51], s35 offen offset:3072 sc1
buffer_load_dwordx4 v[228:231], v0, s[48:51], s20 offen sc1
buffer_load_dwordx4 v[236:239], v0, s[48:51], s20 offen offset:1024 sc1
buffer_load_dwordx4 v[244:247], v0, s[48:51], s20 offen offset:2048 sc1
s_not_b32 s21, s72
s_and_b32 s21, s21, 0x7ff
s_mul_i32 s59, s21, 2176
s_lshl_b32 s85, s68, 6
s_add_u32 s59, s59, s85
s_add_u32 s59, s59, 4194304
buffer_load_ushort v252, v7, s[48:51], s59 offen sc1
s_branch .Lrt0_r
.Lrt2:
s_bitcmp1_b32 s75, 0
s_cselect_b32 s72, s90, s91
s_cselect_b32 s59, 0, 32
s_add_u32 s59, s59, s68
s_mul_i32 s35, s59, 7168
s_add_u32 s35, s35, 2097152
s_add_u32 s20, s35, 4096
buffer_load_dwordx4 v[196:199], v0, s[48:51], s35 offen sc1
buffer_load_dwordx4 v[204:207], v0, s[48:51], s35 offen offset:1024 sc1
buffer_load_dwordx4 v[212:215], v0, s[48:51], s35 offen offset:2048 sc1
buffer_load_dwordx4 v[220:223], v0, s[48:51], s35 offen offset:3072 sc1
buffer_load_dwordx4 v[228:231], v0, s[48:51], s20 offen sc1
buffer_load_dwordx4 v[236:239], v0, s[48:51], s20 offen offset:1024 sc1
buffer_load_dwordx4 v[244:247], v0, s[48:51], s20 offen offset:2048 sc1
s_not_b32 s21, s72
s_and_b32 s21, s21, 0x7ff
s_mul_i32 s59, s21, 2176
s_lshl_b32 s85, s68, 6
s_add_u32 s59, s59, s85
s_add_u32 s59, s59, 4194304
buffer_load_ushort v252, v7, s[48:51], s59 offen sc1
s_waitcnt vmcnt(0)
.Lrt1:
s_waitcnt vmcnt(8)
s_and_b32 s59, s72, 0xffff0000
s_bitcmp1_b32 s72, 31
s_cselect_b32 s85, 0, 0x7fff0000
s_xor_b32 s59, s59, s85
v_mov_b32 v253, s59
v_mul_f32 v253, 0x3fb8aa3b, v253
v_exp_f32 v253, v253
v_lshlrev_b32 v252, 16, v252
v_add_f32 v253, 1.0, v253
v_rcp_f32 v253, v253
v_xor_b32 v196, s24, v196
v_xor_b32 v197, s25, v197
v_xor_b32 v198, s26, v198
v_xor_b32 v199, s27, v199
v_xor_b32 v204, s24, v204
v_xor_b32 v205, s25, v205
v_xor_b32 v206, s26, v206
v_xor_b32 v207, s27, v207
v_xor_b32 v212, s24, v212
v_xor_b32 v213, s25, v213
v_xor_b32 v214, s26, v214
v_xor_b32 v215, s27, v215
v_xor_b32 v220, s24, v220
v_xor_b32 v221, s25, v221
v_xor_b32 v222, s26, v222
v_xor_b32 v223, s27, v223
v_xor_b32 v228, s24, v228
v_xor_b32 v229, s25, v229
v_xor_b32 v230, s26, v230
v_xor_b32 v231, s27, v231
v_xor_b32 v236, s24, v236
v_xor_b32 v237, s25, v237
v_xor_b32 v238, s26, v238
v_xor_b32 v239, s27, v239
v_xor_b32 v244, s24, v244
v_xor_b32 v245, s25, v245
v_xor_b32 v246, s26, v246
v_xor_b32 v247, s27, v247
v_or3_b32 v254, v196, v197, v198
v_or3_b32 v254, v254, v199, v204
v_or3_b32 v254, v254, v205, v206
v_or3_b32 v254, v254, v207, v212
v_or3_b32 v254, v254, v213, v214
v_or3_b32 v254, v254, v215, v220
v_or3_b32 v254, v254, v221, v222
v_or3_b32 v254, v254, v223, v228
v_or3_b32 v254, v254, v229, v230
v_or3_b32 v254, v254, v231, v236
v_or3_b32 v254, v254, v237, v238
v_or3_b32 v254, v254, v239, v244
v_or3_b32 v254, v254, v245, v246
v_or_b32 v254, v254, v247
v_and_b32 v254, 0xff, v254
v_cmp_ne_u32 vcc, 0, v254
s_nop 0
s_cbranch_vccnz .Lyp
.Lyp_r:
v_cvt_pk_bf16_f32 v253, 0, v253
v_add_f32 v196, v196, v204
v_add_f32 v197, v197, v205
v_add_f32 v198, v198, v206
v_add_f32 v199, v199, v207
v_add_f32 v212, v212, v220
v_add_f32 v213, v213, v221
v_add_f32 v214, v214, v222
v_add_f32 v215, v215, v223
v_add_f32 v228, v228, v236
v_add_f32 v229, v229, v237
v_add_f32 v230, v230, v238
v_add_f32 v231, v231, v239
v_add_f32 v196, v196, v212
v_add_f32 v197, v197, v213
v_add_f32 v198, v198, v214
v_add_f32 v199, v199, v215
v_add_f32 v228, v228, v244
v_add_f32 v229, v229, v245
v_add_f32 v230, v230, v246
v_add_f32 v231, v231, v247
v_add_f32 v196, v196, v228
v_add_f32 v197, v197, v229
v_add_f32 v198, v198, v230
v_add_f32 v199, v199, v231
v_permlane16_swap_b32 v196, v197
s_nop 0
v_permlane16_swap_b32 v198, v199
v_add_f32 v196, v196, v197
v_add_f32 v198, v198, v199
v_lshrrev_b32 v204, 1, v6
s_lshl_b32 s59, s21, 2
v_permlane32_swap_b32 v196, v198
v_add_f32 v196, v196, v198
s_lshl_b64 vcc, 15, s59
s_lshr_b32 s59, s21, 9
v_add_f32_dpp v196, v196, v196 row_ror:8 row_mask:0xf bank_mask:0xf bound_ctrl:1
v_cvt_pk_bf16_f32 v196, 0, v196
v_mul_f32 v196, v196, v253
v_cvt_pk_bf16_f32 v196, 0, v196
v_add_f32 v196, v196, v252
v_cvt_pk_bf16_f32 v196, 0, v196
v_mul_f32 v197, 0xbfb8aa3b, v196
v_exp_f32 v197, v197
s_nop 0
v_add_f32 v197, 1.0, v197
v_rcp_f32 v197, v197
s_nop 0
v_mul_f32 v196, v196, v197
ds_bpermute_b32 v212, v204, v196 offset:0
ds_bpermute_b32 v213, v204, v196 offset:64
ds_bpermute_b32 v214, v204, v196 offset:128
ds_bpermute_b32 v215, v204, v196 offset:192
ds_bpermute_b32 v220, v204, v196 offset:4
ds_bpermute_b32 v221, v204, v196 offset:68
ds_bpermute_b32 v222, v204, v196 offset:132
ds_bpermute_b32 v223, v204, v196 offset:196
s_waitcnt lgkmcnt(0)
s_cmp_lg_u32 s59, 0
s_cbranch_scc1 .Lrt1_0
v_cndmask_b32 v64, v64, v212, vcc
v_cndmask_b32 v65, v65, v213, vcc
v_cndmask_b32 v66, v66, v214, vcc
v_cndmask_b32 v67, v67, v215, vcc
v_cndmask_b32 v68, v68, v220, vcc
v_cndmask_b32 v69, v69, v221, vcc
v_cndmask_b32 v70, v70, v222, vcc
v_cndmask_b32 v71, v71, v223, vcc
s_branch .Lrt1_e
.Lrt1_0:
s_cmp_lg_u32 s59, 1
s_cbranch_scc1 .Lrt1_1
v_cndmask_b32 v72, v72, v212, vcc
v_cndmask_b32 v73, v73, v213, vcc
v_cndmask_b32 v74, v74, v214, vcc
v_cndmask_b32 v75, v75, v215, vcc
v_cndmask_b32 v76, v76, v220, vcc
v_cndmask_b32 v77, v77, v221, vcc
v_cndmask_b32 v78, v78, v222, vcc
v_cndmask_b32 v79, v79, v223, vcc
s_branch .Lrt1_e
.Lrt1_1:
s_cmp_lg_u32 s59, 2
s_cbranch_scc1 .Lrt1_2
v_cndmask_b32 v80, v80, v212, vcc
v_cndmask_b32 v81, v81, v213, vcc
v_cndmask_b32 v82, v82, v214, vcc
v_cndmask_b32 v83, v83, v215, vcc
v_cndmask_b32 v84, v84, v220, vcc
v_cndmask_b32 v85, v85, v221, vcc
v_cndmask_b32 v86, v86, v222, vcc
v_cndmask_b32 v87, v87, v223, vcc
s_branch .Lrt1_e
.Lrt1_2:
v_cndmask_b32 v16, v16, v212, vcc
v_cndmask_b32 v17, v17, v213, vcc
v_cndmask_b32 v18, v18, v214, vcc
v_cndmask_b32 v19, v19, v215, vcc
v_cndmask_b32 v20, v20, v220, vcc
v_cndmask_b32 v21, v21, v221, vcc
v_cndmask_b32 v22, v22, v222, vcc
v_cndmask_b32 v23, v23, v223, vcc
.Lrt1_e:
s_cmp_eq_u32 s75, 3
s_mov_b32 s75, 2
s_cbranch_scc1 .Lrt2
s_branch .Lrt1_r
.Lhot_end:
.Lscan:
s_mov_b32 s85, 1048576
.Lscan_l:
s_sleep 8
buffer_load_dwordx4 v[32:35], v0, s[48:51], s75 offen sc1
buffer_load_dwordx4 v[36:39], v0, s[48:51], s75 offen offset:1024 sc1
buffer_load_dwordx4 v[40:43], v0, s[48:51], s75 offen offset:2048 sc1
buffer_load_dwordx4 v[44:47], v0, s[48:51], s75 offen offset:3072 sc1
s_waitcnt vmcnt(0)
v_xor_b32 v50, s86, v33
v_bitop3_b32 v50, v35, s87, v50 bitop3:0xbe
v_bitop3_b32 v50, v37, s86, v50 bitop3:0xbe
v_bitop3_b32 v50, v39, s87, v50 bitop3:0xbe
v_bitop3_b32 v50, v41, s86, v50 bitop3:0xbe
v_bitop3_b32 v50, v43, s87, v50 bitop3:0xbe
v_bitop3_b32 v50, v45, s86, v50 bitop3:0xbe
v_bitop3_b32 v50, v47, s87, v50 bitop3:0xbe
v_cmp_ne_u32 vcc, 0, v50
s_nop 0
s_cbranch_vccz .Lscan_r
s_sub_u32 s85, s85, 1
s_cmp_lg_u32 s85, 0
s_cbranch_scc1 .Lscan_l
s_branch .Lscan_r
.Lcf1:
s_mov_b32 s85, 1048576
.Lcf1_l:
s_sleep 4
buffer_load_dwordx4 v[16:19], v20, s[48:51], s82 offen sc1
s_waitcnt vmcnt(0)
v_xor_b32 v16, s86, v16
v_bitop3_b32 v16, v17, s87, v16 bitop3:0xbe
v_bitop3_b32 v16, v18, s86, v16 bitop3:0xbe
v_bitop3_b32 v16, v19, s87, v16 bitop3:0xbe
v_and_b32 v16, v16, v21
v_cmp_ne_u32 vcc, 0, v16
s_nop 0
s_cbranch_vccz .Lcf1_r
s_sub_u32 s85, s85, 1
s_cmp_lg_u32 s85, 0
s_cbranch_scc1 .Lcf1_l
s_branch .Lcf1_r
.Lkvf:
s_mov_b32 s85, 1048576
.Lkvf_l:
s_sleep 4
buffer_load_dwordx2 v[20:21], v1, s[48:51], s83 offen sc1
s_waitcnt vmcnt(0)
v_xor_b32 v20, s86, v20
v_bitop3_b32 v20, v21, s87, v20 bitop3:0xbe
v_cmp_ne_u32 vcc, 0, v20
s_nop 0
s_cbranch_vccz .Lkvf_r
s_sub_u32 s85, s85, 1
s_cmp_lg_u32 s85, 0
s_cbranch_scc1 .Lkvf_l
s_branch .Lkvf_r
.Lscale4:
v_accvgpr_read_b32 v28, a64
v_accvgpr_read_b32 v29, a65
v_accvgpr_read_b32 v30, a66
v_accvgpr_read_b32 v31, a67
v_mul_f32 v28, v28, v126
v_mul_f32 v29, v29, v126
v_mul_f32 v30, v30, v126
v_mul_f32 v31, v31, v126
v_accvgpr_write_b32 a64, v28
v_accvgpr_write_b32 a65, v29
v_accvgpr_write_b32 a66, v30
v_accvgpr_write_b32 a67, v31
v_accvgpr_read_b32 v28, a68
v_accvgpr_read_b32 v29, a69
v_accvgpr_read_b32 v30, a70
v_accvgpr_read_b32 v31, a71
v_mul_f32 v28, v28, v127
v_mul_f32 v29, v29, v127
v_mul_f32 v30, v30, v127
v_mul_f32 v31, v31, v127
v_accvgpr_write_b32 a68, v28
v_accvgpr_write_b32 a69, v29
v_accvgpr_write_b32 a70, v30
v_accvgpr_write_b32 a71, v31
v_accvgpr_read_b32 v28, a72
v_accvgpr_read_b32 v29, a73
v_accvgpr_read_b32 v30, a74
v_accvgpr_read_b32 v31, a75
v_mul_f32 v28, v28, v126
v_mul_f32 v29, v29, v126
v_mul_f32 v30, v30, v126
v_mul_f32 v31, v31, v126
v_accvgpr_write_b32 a72, v28
v_accvgpr_write_b32 a73, v29
v_accvgpr_write_b32 a74, v30
v_accvgpr_write_b32 a75, v31
v_accvgpr_read_b32 v28, a76
v_accvgpr_read_b32 v29, a77
v_accvgpr_read_b32 v30, a78
v_accvgpr_read_b32 v31, a79
v_mul_f32 v28, v28, v127
v_mul_f32 v29, v29, v127
v_mul_f32 v30, v30, v127
v_mul_f32 v31, v31, v127
v_accvgpr_write_b32 a76, v28
v_accvgpr_write_b32 a77, v29
v_accvgpr_write_b32 a78, v30
v_accvgpr_write_b32 a79, v31
v_accvgpr_read_b32 v28, a80
v_accvgpr_read_b32 v29, a81
v_accvgpr_read_b32 v30, a82
v_accvgpr_read_b32 v31, a83
v_mul_f32 v28, v28, v126
v_mul_f32 v29, v29, v126
v_mul_f32 v30, v30, v126
v_mul_f32 v31, v31, v126
v_accvgpr_write_b32 a80, v28
v_accvgpr_write_b32 a81, v29
v_accvgpr_write_b32 a82, v30
v_accvgpr_write_b32 a83, v31
v_accvgpr_read_b32 v28, a84
v_accvgpr_read_b32 v29, a85
v_accvgpr_read_b32 v30, a86
v_accvgpr_read_b32 v31, a87
v_mul_f32 v28, v28, v127
v_mul_f32 v29, v29, v127
v_mul_f32 v30, v30, v127
v_mul_f32 v31, v31, v127
v_accvgpr_write_b32 a84, v28
v_accvgpr_write_b32 a85, v29
v_accvgpr_write_b32 a86, v30
v_accvgpr_write_b32 a87, v31
v_accvgpr_read_b32 v28, a88
v_accvgpr_read_b32 v29, a89
v_accvgpr_read_b32 v30, a90
v_accvgpr_read_b32 v31, a91
v_mul_f32 v28, v28, v126
v_mul_f32 v29, v29, v126
v_mul_f32 v30, v30, v126
v_mul_f32 v31, v31, v126
v_accvgpr_write_b32 a88, v28
v_accvgpr_write_b32 a89, v29
v_accvgpr_write_b32 a90, v30
v_accvgpr_write_b32 a91, v31
v_accvgpr_read_b32 v28, a92
v_accvgpr_read_b32 v29, a93
v_accvgpr_read_b32 v30, a94
v_accvgpr_read_b32 v31, a95
v_mul_f32 v28, v28, v127
v_mul_f32 v29, v29, v127
v_mul_f32 v30, v30, v127
v_mul_f32 v31, v31, v127
v_accvgpr_write_b32 a92, v28
v_accvgpr_write_b32 a93, v29
v_accvgpr_write_b32 a94, v30
v_accvgpr_write_b32 a95, v31
v_accvgpr_read_b32 v28, a96
v_accvgpr_read_b32 v29, a97
v_accvgpr_read_b32 v30, a98
v_accvgpr_read_b32 v31, a99
v_mul_f32 v28, v28, v126
v_mul_f32 v29, v29, v126
v_mul_f32 v30, v30, v126
)"
R"(
v_mul_f32 v31, v31, v126
v_accvgpr_write_b32 a96, v28
v_accvgpr_write_b32 a97, v29
v_accvgpr_write_b32 a98, v30
v_accvgpr_write_b32 a99, v31
v_accvgpr_read_b32 v28, a100
v_accvgpr_read_b32 v29, a101
v_accvgpr_read_b32 v30, a102
v_accvgpr_read_b32 v31, a103
v_mul_f32 v28, v28, v127
v_mul_f32 v29, v29, v127
v_mul_f32 v30, v30, v127
v_mul_f32 v31, v31, v127
v_accvgpr_write_b32 a100, v28
v_accvgpr_write_b32 a101, v29
v_accvgpr_write_b32 a102, v30
v_accvgpr_write_b32 a103, v31
v_accvgpr_read_b32 v28, a104
v_accvgpr_read_b32 v29, a105
v_accvgpr_read_b32 v30, a106
v_accvgpr_read_b32 v31, a107
v_mul_f32 v28, v28, v126
v_mul_f32 v29, v29, v126
v_mul_f32 v30, v30, v126
v_mul_f32 v31, v31, v126
v_accvgpr_write_b32 a104, v28
v_accvgpr_write_b32 a105, v29
v_accvgpr_write_b32 a106, v30
v_accvgpr_write_b32 a107, v31
v_accvgpr_read_b32 v28, a108
v_accvgpr_read_b32 v29, a109
v_accvgpr_read_b32 v30, a110
v_accvgpr_read_b32 v31, a111
v_mul_f32 v28, v28, v127
v_mul_f32 v29, v29, v127
v_mul_f32 v30, v30, v127
v_mul_f32 v31, v31, v127
v_accvgpr_write_b32 a108, v28
v_accvgpr_write_b32 a109, v29
v_accvgpr_write_b32 a110, v30
v_accvgpr_write_b32 a111, v31
v_accvgpr_read_b32 v28, a112
v_accvgpr_read_b32 v29, a113
v_accvgpr_read_b32 v30, a114
v_accvgpr_read_b32 v31, a115
v_mul_f32 v28, v28, v126
v_mul_f32 v29, v29, v126
v_mul_f32 v30, v30, v126
v_mul_f32 v31, v31, v126
v_accvgpr_write_b32 a112, v28
v_accvgpr_write_b32 a113, v29
v_accvgpr_write_b32 a114, v30
v_accvgpr_write_b32 a115, v31
v_accvgpr_read_b32 v28, a116
v_accvgpr_read_b32 v29, a117
v_accvgpr_read_b32 v30, a118
v_accvgpr_read_b32 v31, a119
v_mul_f32 v28, v28, v127
v_mul_f32 v29, v29, v127
v_mul_f32 v30, v30, v127
v_mul_f32 v31, v31, v127
v_accvgpr_write_b32 a116, v28
v_accvgpr_write_b32 a117, v29
v_accvgpr_write_b32 a118, v30
v_accvgpr_write_b32 a119, v31
v_accvgpr_read_b32 v28, a120
v_accvgpr_read_b32 v29, a121
v_accvgpr_read_b32 v30, a122
v_accvgpr_read_b32 v31, a123
v_mul_f32 v28, v28, v126
v_mul_f32 v29, v29, v126
v_mul_f32 v30, v30, v126
v_mul_f32 v31, v31, v126
v_accvgpr_write_b32 a120, v28
v_accvgpr_write_b32 a121, v29
v_accvgpr_write_b32 a122, v30
v_accvgpr_write_b32 a123, v31
v_accvgpr_read_b32 v28, a124
v_accvgpr_read_b32 v29, a125
v_accvgpr_read_b32 v30, a126
v_accvgpr_read_b32 v31, a127
v_mul_f32 v28, v28, v127
v_mul_f32 v29, v29, v127
v_mul_f32 v30, v30, v127
v_mul_f32 v31, v31, v127
v_accvgpr_write_b32 a124, v28
v_accvgpr_write_b32 a125, v29
v_accvgpr_write_b32 a126, v30
v_accvgpr_write_b32 a127, v31
.Lscale2:
v_accvgpr_read_b32 v28, a0
v_accvgpr_read_b32 v29, a1
v_accvgpr_read_b32 v30, a2
v_accvgpr_read_b32 v31, a3
v_mul_f32 v28, v28, v124
v_mul_f32 v29, v29, v124
v_mul_f32 v30, v30, v124
v_mul_f32 v31, v31, v124
v_accvgpr_write_b32 a0, v28
v_accvgpr_write_b32 a1, v29
v_accvgpr_write_b32 a2, v30
v_accvgpr_write_b32 a3, v31
v_accvgpr_read_b32 v28, a4
v_accvgpr_read_b32 v29, a5
v_accvgpr_read_b32 v30, a6
v_accvgpr_read_b32 v31, a7
v_mul_f32 v28, v28, v125
v_mul_f32 v29, v29, v125
v_mul_f32 v30, v30, v125
v_mul_f32 v31, v31, v125
v_accvgpr_write_b32 a4, v28
v_accvgpr_write_b32 a5, v29
v_accvgpr_write_b32 a6, v30
v_accvgpr_write_b32 a7, v31
v_accvgpr_read_b32 v28, a8
v_accvgpr_read_b32 v29, a9
v_accvgpr_read_b32 v30, a10
v_accvgpr_read_b32 v31, a11
v_mul_f32 v28, v28, v124
v_mul_f32 v29, v29, v124
v_mul_f32 v30, v30, v124
v_mul_f32 v31, v31, v124
v_accvgpr_write_b32 a8, v28
v_accvgpr_write_b32 a9, v29
v_accvgpr_write_b32 a10, v30
v_accvgpr_write_b32 a11, v31
v_accvgpr_read_b32 v28, a12
v_accvgpr_read_b32 v29, a13
v_accvgpr_read_b32 v30, a14
v_accvgpr_read_b32 v31, a15
v_mul_f32 v28, v28, v125
v_mul_f32 v29, v29, v125
v_mul_f32 v30, v30, v125
v_mul_f32 v31, v31, v125
v_accvgpr_write_b32 a12, v28
v_accvgpr_write_b32 a13, v29
v_accvgpr_write_b32 a14, v30
v_accvgpr_write_b32 a15, v31
v_accvgpr_read_b32 v28, a16
v_accvgpr_read_b32 v29, a17
v_accvgpr_read_b32 v30, a18
v_accvgpr_read_b32 v31, a19
v_mul_f32 v28, v28, v124
v_mul_f32 v29, v29, v124
v_mul_f32 v30, v30, v124
v_mul_f32 v31, v31, v124
v_accvgpr_write_b32 a16, v28
v_accvgpr_write_b32 a17, v29
v_accvgpr_write_b32 a18, v30
v_accvgpr_write_b32 a19, v31
v_accvgpr_read_b32 v28, a20
v_accvgpr_read_b32 v29, a21
v_accvgpr_read_b32 v30, a22
v_accvgpr_read_b32 v31, a23
v_mul_f32 v28, v28, v125
v_mul_f32 v29, v29, v125
v_mul_f32 v30, v30, v125
v_mul_f32 v31, v31, v125
v_accvgpr_write_b32 a20, v28
v_accvgpr_write_b32 a21, v29
v_accvgpr_write_b32 a22, v30
v_accvgpr_write_b32 a23, v31
v_accvgpr_read_b32 v28, a24
v_accvgpr_read_b32 v29, a25
v_accvgpr_read_b32 v30, a26
v_accvgpr_read_b32 v31, a27
v_mul_f32 v28, v28, v124
v_mul_f32 v29, v29, v124
v_mul_f32 v30, v30, v124
v_mul_f32 v31, v31, v124
v_accvgpr_write_b32 a24, v28
v_accvgpr_write_b32 a25, v29
v_accvgpr_write_b32 a26, v30
v_accvgpr_write_b32 a27, v31
v_accvgpr_read_b32 v28, a28
v_accvgpr_read_b32 v29, a29
v_accvgpr_read_b32 v30, a30
v_accvgpr_read_b32 v31, a31
v_mul_f32 v28, v28, v125
v_mul_f32 v29, v29, v125
v_mul_f32 v30, v30, v125
v_mul_f32 v31, v31, v125
v_accvgpr_write_b32 a28, v28
v_accvgpr_write_b32 a29, v29
v_accvgpr_write_b32 a30, v30
v_accvgpr_write_b32 a31, v31
v_accvgpr_read_b32 v28, a32
v_accvgpr_read_b32 v29, a33
v_accvgpr_read_b32 v30, a34
v_accvgpr_read_b32 v31, a35
v_mul_f32 v28, v28, v124
v_mul_f32 v29, v29, v124
v_mul_f32 v30, v30, v124
v_mul_f32 v31, v31, v124
v_accvgpr_write_b32 a32, v28
v_accvgpr_write_b32 a33, v29
v_accvgpr_write_b32 a34, v30
v_accvgpr_write_b32 a35, v31
v_accvgpr_read_b32 v28, a36
v_accvgpr_read_b32 v29, a37
v_accvgpr_read_b32 v30, a38
v_accvgpr_read_b32 v31, a39
v_mul_f32 v28, v28, v125
v_mul_f32 v29, v29, v125
v_mul_f32 v30, v30, v125
v_mul_f32 v31, v31, v125
v_accvgpr_write_b32 a36, v28
v_accvgpr_write_b32 a37, v29
v_accvgpr_write_b32 a38, v30
v_accvgpr_write_b32 a39, v31
v_accvgpr_read_b32 v28, a40
v_accvgpr_read_b32 v29, a41
v_accvgpr_read_b32 v30, a42
v_accvgpr_read_b32 v31, a43
v_mul_f32 v28, v28, v124
v_mul_f32 v29, v29, v124
v_mul_f32 v30, v30, v124
v_mul_f32 v31, v31, v124
v_accvgpr_write_b32 a40, v28
v_accvgpr_write_b32 a41, v29
v_accvgpr_write_b32 a42, v30
v_accvgpr_write_b32 a43, v31
v_accvgpr_read_b32 v28, a44
v_accvgpr_read_b32 v29, a45
v_accvgpr_read_b32 v30, a46
v_accvgpr_read_b32 v31, a47
v_mul_f32 v28, v28, v125
v_mul_f32 v29, v29, v125
v_mul_f32 v30, v30, v125
v_mul_f32 v31, v31, v125
v_accvgpr_write_b32 a44, v28
v_accvgpr_write_b32 a45, v29
v_accvgpr_write_b32 a46, v30
v_accvgpr_write_b32 a47, v31
v_accvgpr_read_b32 v28, a48
v_accvgpr_read_b32 v29, a49
v_accvgpr_read_b32 v30, a50
v_accvgpr_read_b32 v31, a51
v_mul_f32 v28, v28, v124
v_mul_f32 v29, v29, v124
v_mul_f32 v30, v30, v124
v_mul_f32 v31, v31, v124
v_accvgpr_write_b32 a48, v28
v_accvgpr_write_b32 a49, v29
v_accvgpr_write_b32 a50, v30
v_accvgpr_write_b32 a51, v31
v_accvgpr_read_b32 v28, a52
v_accvgpr_read_b32 v29, a53
v_accvgpr_read_b32 v30, a54
v_accvgpr_read_b32 v31, a55
v_mul_f32 v28, v28, v125
v_mul_f32 v29, v29, v125
v_mul_f32 v30, v30, v125
v_mul_f32 v31, v31, v125
v_accvgpr_write_b32 a52, v28
v_accvgpr_write_b32 a53, v29
v_accvgpr_write_b32 a54, v30
v_accvgpr_write_b32 a55, v31
v_accvgpr_read_b32 v28, a56
v_accvgpr_read_b32 v29, a57
v_accvgpr_read_b32 v30, a58
v_accvgpr_read_b32 v31, a59
v_mul_f32 v28, v28, v124
v_mul_f32 v29, v29, v124
v_mul_f32 v30, v30, v124
v_mul_f32 v31, v31, v124
v_accvgpr_write_b32 a56, v28
v_accvgpr_write_b32 a57, v29
v_accvgpr_write_b32 a58, v30
v_accvgpr_write_b32 a59, v31
v_accvgpr_read_b32 v28, a60
v_accvgpr_read_b32 v29, a61
v_accvgpr_read_b32 v30, a62
v_accvgpr_read_b32 v31, a63
v_mul_f32 v28, v28, v125
v_mul_f32 v29, v29, v125
v_mul_f32 v30, v30, v125
v_mul_f32 v31, v31, v125
v_accvgpr_write_b32 a60, v28
v_accvgpr_write_b32 a61, v29
v_accvgpr_write_b32 a62, v30
v_accvgpr_write_b32 a63, v31
s_setpc_b64 s[16:17]
.Lab0:
s_waitcnt vmcnt(0)
s_nop 7
v_max3_f32 v120, v64, v65, v66
v_max3_f32 v120, v120, v67, v80
v_max3_f32 v120, v120, v81, v82
v_max_f32 v120, v120, v83
v_mov_b32 v26, v120
s_nop 1
v_permlane16_swap_b32 v120, v26
v_max_f32 v120, v120, v26
v_mov_b32 v26, v120
s_nop 1
v_permlane32_swap_b32 v120, v26
v_max_f32 v120, v120, v26
v_max_f32 v120, 0, v120
v_sub_f32 v26, 0, v120
v_exp_f32 v124, v26
s_nop 0
v_mul_f32 v116, v116, v124
v_max3_f32 v121, v68, v69, v70
v_max3_f32 v121, v121, v71, v84
v_max3_f32 v121, v121, v85, v86
v_max_f32 v121, v121, v87
v_mov_b32 v26, v121
s_nop 1
v_permlane16_swap_b32 v121, v26
v_max_f32 v121, v121, v26
v_mov_b32 v26, v121
s_nop 1
v_permlane32_swap_b32 v121, v26
v_max_f32 v121, v121, v26
v_max_f32 v121, 0, v121
v_sub_f32 v26, 0, v121
v_exp_f32 v125, v26
s_nop 0
v_mul_f32 v117, v117, v125
v_max3_f32 v122, v72, v73, v74
v_max3_f32 v122, v122, v75, v88
v_max3_f32 v122, v122, v89, v90
v_max_f32 v122, v122, v91
v_mov_b32 v26, v122
s_nop 1
v_permlane16_swap_b32 v122, v26
v_max_f32 v122, v122, v26
v_mov_b32 v26, v122
s_nop 1
v_permlane32_swap_b32 v122, v26
v_max_f32 v122, v122, v26
v_max_f32 v122, 0, v122
v_sub_f32 v26, 0, v122
v_exp_f32 v126, v26
s_nop 0
v_mul_f32 v118, v118, v126
v_max3_f32 v123, v76, v77, v78
v_max3_f32 v123, v123, v79, v92
v_max3_f32 v123, v123, v93, v94
v_max_f32 v123, v123, v95
v_mov_b32 v26, v123
s_nop 1
v_permlane16_swap_b32 v123, v26
v_max_f32 v123, v123, v26
v_mov_b32 v26, v123
s_nop 1
v_permlane32_swap_b32 v123, v26
v_max_f32 v123, v123, v26
v_max_f32 v123, 0, v123
v_sub_f32 v26, 0, v123
v_exp_f32 v127, v26
s_nop 0
v_mul_f32 v119, v119, v127
v_sub_f32 v28, v64, v120
v_sub_f32 v29, v65, v120
v_exp_f32 v28, v28
v_exp_f32 v29, v29
s_nop 0
v_cvt_pk_bf16_f32 v228, v28, v29
v_sub_f32 v28, v66, v120
v_sub_f32 v29, v67, v120
v_exp_f32 v28, v28
v_exp_f32 v29, v29
s_nop 0
v_cvt_pk_bf16_f32 v229, v28, v29
v_sub_f32 v32, v32, v120
v_sub_f32 v33, v33, v120
v_sub_f32 v34, v34, v120
v_sub_f32 v35, v35, v120
v_sub_f32 v28, v68, v121
v_sub_f32 v29, v69, v121
v_exp_f32 v28, v28
v_exp_f32 v29, v29
s_nop 0
v_cvt_pk_bf16_f32 v236, v28, v29
v_sub_f32 v28, v70, v121
v_sub_f32 v29, v71, v121
v_exp_f32 v28, v28
v_exp_f32 v29, v29
s_nop 0
v_cvt_pk_bf16_f32 v237, v28, v29
v_sub_f32 v36, v36, v121
v_sub_f32 v37, v37, v121
v_sub_f32 v38, v38, v121
v_sub_f32 v39, v39, v121
v_sub_f32 v28, v72, v122
v_sub_f32 v29, v73, v122
v_exp_f32 v28, v28
v_exp_f32 v29, v29
s_nop 0
v_cvt_pk_bf16_f32 v244, v28, v29
v_sub_f32 v28, v74, v122
v_sub_f32 v29, v75, v122
v_exp_f32 v28, v28
v_exp_f32 v29, v29
s_nop 0
v_cvt_pk_bf16_f32 v245, v28, v29
v_sub_f32 v40, v40, v122
v_sub_f32 v41, v41, v122
v_sub_f32 v42, v42, v122
v_sub_f32 v43, v43, v122
v_sub_f32 v28, v76, v123
v_sub_f32 v29, v77, v123
v_exp_f32 v28, v28
v_exp_f32 v29, v29
s_nop 0
v_cvt_pk_bf16_f32 v252, v28, v29
v_sub_f32 v28, v78, v123
v_sub_f32 v29, v79, v123
v_exp_f32 v28, v28
v_exp_f32 v29, v29
s_nop 0
v_cvt_pk_bf16_f32 v253, v28, v29
v_sub_f32 v44, v44, v123
v_sub_f32 v45, v45, v123
v_sub_f32 v46, v46, v123
v_sub_f32 v47, v47, v123
v_sub_f32 v28, v80, v120
v_sub_f32 v29, v81, v120
v_exp_f32 v28, v28
v_exp_f32 v29, v29
s_nop 0
v_cvt_pk_bf16_f32 v230, v28, v29
v_sub_f32 v28, v82, v120
v_sub_f32 v29, v83, v120
v_exp_f32 v28, v28
v_exp_f32 v29, v29
s_nop 0
v_cvt_pk_bf16_f32 v231, v28, v29
v_sub_f32 v48, v48, v120
v_sub_f32 v49, v49, v120
v_sub_f32 v50, v50, v120
v_sub_f32 v51, v51, v120
v_sub_f32 v28, v84, v121
v_sub_f32 v29, v85, v121
v_exp_f32 v28, v28
v_exp_f32 v29, v29
s_nop 0
v_cvt_pk_bf16_f32 v238, v28, v29
v_sub_f32 v28, v86, v121
v_sub_f32 v29, v87, v121
v_exp_f32 v28, v28
v_exp_f32 v29, v29
s_nop 0
v_cvt_pk_bf16_f32 v239, v28, v29
v_sub_f32 v52, v52, v121
v_sub_f32 v53, v53, v121
v_sub_f32 v54, v54, v121
v_sub_f32 v55, v55, v121
v_sub_f32 v28, v88, v122
v_sub_f32 v29, v89, v122
v_exp_f32 v28, v28
v_exp_f32 v29, v29
s_nop 0
v_cvt_pk_bf16_f32 v246, v28, v29
v_sub_f32 v28, v90, v122
v_sub_f32 v29, v91, v122
v_exp_f32 v28, v28
v_exp_f32 v29, v29
s_nop 0
v_cvt_pk_bf16_f32 v247, v28, v29
v_sub_f32 v56, v56, v122
v_sub_f32 v57, v57, v122
v_sub_f32 v58, v58, v122
v_sub_f32 v59, v59, v122
v_sub_f32 v28, v92, v123
v_sub_f32 v29, v93, v123
v_exp_f32 v28, v28
v_exp_f32 v29, v29
s_nop 0
v_cvt_pk_bf16_f32 v254, v28, v29
)"
R"(
v_sub_f32 v28, v94, v123
v_sub_f32 v29, v95, v123
v_exp_f32 v28, v28
v_exp_f32 v29, v29
s_nop 0
v_cvt_pk_bf16_f32 v255, v28, v29
v_sub_f32 v60, v60, v123
v_sub_f32 v61, v61, v123
v_sub_f32 v62, v62, v123
v_sub_f32 v63, v63, v123
v_sub_f32 v4, v4, v120
v_sub_f32 v5, v5, v120
v_sub_f32 v6, v6, v120
v_sub_f32 v7, v7, v120
v_sub_f32 v8, v8, v121
v_sub_f32 v9, v9, v121
v_sub_f32 v10, v10, v121
v_sub_f32 v11, v11, v121
v_sub_f32 v132, v132, v122
v_sub_f32 v133, v133, v122
v_sub_f32 v134, v134, v122
v_sub_f32 v135, v135, v122
v_sub_f32 v140, v140, v123
v_sub_f32 v141, v141, v123
v_sub_f32 v142, v142, v123
v_sub_f32 v143, v143, v123
s_getpc_b64 s[16:17]
.Lab0_g:
s_add_u32 s16, s16, .Lab0_b-.Lab0_g
s_addc_u32 s17, s17, 0
s_branch .Lscale4
.Lab0_b:
Cvvv 112,96,228,116
Mvvv 112,100,236
Mvvv 112,104,244
Mvvv 112,108,252
v_exp_f32 v120, v32
v_exp_f32 v121, v33
v_exp_f32 v122, v34
v_exp_f32 v123, v35
v_exp_f32 v124, v36
v_cvt_pk_bf16_f32 v196, v120, v121
v_cvt_pk_bf16_f32 v197, v122, v123
v_exp_f32 v125, v37
v_exp_f32 v126, v38
v_cvt_pk_bf16_f32 v204, v124, v125
v_exp_f32 v127, v39
v_exp_f32 v120, v40
v_cvt_pk_bf16_f32 v205, v126, v127
v_exp_f32 v121, v41
v_exp_f32 v122, v42
v_cvt_pk_bf16_f32 v212, v120, v121
v_exp_f32 v123, v43
v_exp_f32 v124, v44
s_nop 7
s_branch .Lab0_r
.Lab1:
s_waitcnt vmcnt(0)
s_nop 7
v_max3_f32 v120, v32, v33, v34
v_max3_f32 v120, v120, v35, v48
v_max3_f32 v120, v120, v49, v50
v_max_f32 v120, v120, v51
v_mov_b32 v26, v120
s_nop 1
v_permlane16_swap_b32 v120, v26
v_max_f32 v120, v120, v26
v_mov_b32 v26, v120
s_nop 1
v_permlane32_swap_b32 v120, v26
v_max_f32 v120, v120, v26
v_max_f32 v120, 0, v120
v_sub_f32 v26, 0, v120
v_exp_f32 v124, v26
s_nop 0
v_mul_f32 v112, v112, v124
v_max3_f32 v121, v36, v37, v38
v_max3_f32 v121, v121, v39, v52
v_max3_f32 v121, v121, v53, v54
v_max_f32 v121, v121, v55
v_mov_b32 v26, v121
s_nop 1
v_permlane16_swap_b32 v121, v26
v_max_f32 v121, v121, v26
v_mov_b32 v26, v121
s_nop 1
v_permlane32_swap_b32 v121, v26
v_max_f32 v121, v121, v26
v_max_f32 v121, 0, v121
v_sub_f32 v26, 0, v121
v_exp_f32 v125, v26
s_nop 0
v_mul_f32 v113, v113, v125
v_max3_f32 v122, v40, v41, v42
v_max3_f32 v122, v122, v43, v56
v_max3_f32 v122, v122, v57, v58
v_max_f32 v122, v122, v59
v_mov_b32 v26, v122
s_nop 1
v_permlane16_swap_b32 v122, v26
v_max_f32 v122, v122, v26
v_mov_b32 v26, v122
s_nop 1
v_permlane32_swap_b32 v122, v26
v_max_f32 v122, v122, v26
v_max_f32 v122, 0, v122
v_sub_f32 v26, 0, v122
v_exp_f32 v126, v26
s_nop 0
v_mul_f32 v114, v114, v126
v_max3_f32 v123, v44, v45, v46
v_max3_f32 v123, v123, v47, v60
v_max3_f32 v123, v123, v61, v62
v_max_f32 v123, v123, v63
v_mov_b32 v26, v123
s_nop 1
v_permlane16_swap_b32 v123, v26
v_max_f32 v123, v123, v26
v_mov_b32 v26, v123
s_nop 1
v_permlane32_swap_b32 v123, v26
v_max_f32 v123, v123, v26
v_max_f32 v123, 0, v123
v_sub_f32 v26, 0, v123
v_exp_f32 v127, v26
s_nop 0
v_mul_f32 v115, v115, v127
v_sub_f32 v28, v32, v120
v_sub_f32 v29, v33, v120
v_exp_f32 v28, v28
v_exp_f32 v29, v29
s_nop 0
v_cvt_pk_bf16_f32 v196, v28, v29
v_sub_f32 v28, v34, v120
v_sub_f32 v29, v35, v120
v_exp_f32 v28, v28
v_exp_f32 v29, v29
s_nop 0
v_cvt_pk_bf16_f32 v197, v28, v29
v_sub_f32 v64, v64, v120
v_sub_f32 v65, v65, v120
v_sub_f32 v66, v66, v120
v_sub_f32 v67, v67, v120
v_sub_f32 v28, v36, v121
v_sub_f32 v29, v37, v121
v_exp_f32 v28, v28
v_exp_f32 v29, v29
s_nop 0
v_cvt_pk_bf16_f32 v204, v28, v29
v_sub_f32 v28, v38, v121
v_sub_f32 v29, v39, v121
v_exp_f32 v28, v28
v_exp_f32 v29, v29
s_nop 0
v_cvt_pk_bf16_f32 v205, v28, v29
v_sub_f32 v68, v68, v121
v_sub_f32 v69, v69, v121
v_sub_f32 v70, v70, v121
v_sub_f32 v71, v71, v121
v_sub_f32 v28, v40, v122
v_sub_f32 v29, v41, v122
v_exp_f32 v28, v28
v_exp_f32 v29, v29
s_nop 0
v_cvt_pk_bf16_f32 v212, v28, v29
v_sub_f32 v28, v42, v122
v_sub_f32 v29, v43, v122
v_exp_f32 v28, v28
v_exp_f32 v29, v29
s_nop 0
v_cvt_pk_bf16_f32 v213, v28, v29
v_sub_f32 v72, v72, v122
v_sub_f32 v73, v73, v122
v_sub_f32 v74, v74, v122
v_sub_f32 v75, v75, v122
v_sub_f32 v28, v44, v123
v_sub_f32 v29, v45, v123
v_exp_f32 v28, v28
v_exp_f32 v29, v29
s_nop 0
v_cvt_pk_bf16_f32 v220, v28, v29
v_sub_f32 v28, v46, v123
v_sub_f32 v29, v47, v123
v_exp_f32 v28, v28
v_exp_f32 v29, v29
s_nop 0
v_cvt_pk_bf16_f32 v221, v28, v29
v_sub_f32 v76, v76, v123
v_sub_f32 v77, v77, v123
v_sub_f32 v78, v78, v123
v_sub_f32 v79, v79, v123
v_sub_f32 v28, v48, v120
v_sub_f32 v29, v49, v120
v_exp_f32 v28, v28
v_exp_f32 v29, v29
s_nop 0
v_cvt_pk_bf16_f32 v198, v28, v29
v_sub_f32 v28, v50, v120
v_sub_f32 v29, v51, v120
v_exp_f32 v28, v28
v_exp_f32 v29, v29
s_nop 0
v_cvt_pk_bf16_f32 v199, v28, v29
v_sub_f32 v80, v80, v120
v_sub_f32 v81, v81, v120
v_sub_f32 v82, v82, v120
v_sub_f32 v83, v83, v120
v_sub_f32 v28, v52, v121
v_sub_f32 v29, v53, v121
v_exp_f32 v28, v28
v_exp_f32 v29, v29
s_nop 0
v_cvt_pk_bf16_f32 v206, v28, v29
v_sub_f32 v28, v54, v121
v_sub_f32 v29, v55, v121
v_exp_f32 v28, v28
v_exp_f32 v29, v29
s_nop 0
v_cvt_pk_bf16_f32 v207, v28, v29
v_sub_f32 v84, v84, v121
v_sub_f32 v85, v85, v121
v_sub_f32 v86, v86, v121
v_sub_f32 v87, v87, v121
v_sub_f32 v28, v56, v122
v_sub_f32 v29, v57, v122
v_exp_f32 v28, v28
v_exp_f32 v29, v29
s_nop 0
v_cvt_pk_bf16_f32 v214, v28, v29
v_sub_f32 v28, v58, v122
v_sub_f32 v29, v59, v122
v_exp_f32 v28, v28
v_exp_f32 v29, v29
s_nop 0
v_cvt_pk_bf16_f32 v215, v28, v29
v_sub_f32 v88, v88, v122
v_sub_f32 v89, v89, v122
v_sub_f32 v90, v90, v122
v_sub_f32 v91, v91, v122
v_sub_f32 v28, v60, v123
v_sub_f32 v29, v61, v123
v_exp_f32 v28, v28
v_exp_f32 v29, v29
s_nop 0
v_cvt_pk_bf16_f32 v222, v28, v29
v_sub_f32 v28, v62, v123
v_sub_f32 v29, v63, v123
v_exp_f32 v28, v28
v_exp_f32 v29, v29
s_nop 0
v_cvt_pk_bf16_f32 v223, v28, v29
v_sub_f32 v92, v92, v123
v_sub_f32 v93, v93, v123
v_sub_f32 v94, v94, v123
v_sub_f32 v95, v95, v123
v_sub_f32 v4, v4, v120
v_sub_f32 v5, v5, v120
v_sub_f32 v6, v6, v120
v_sub_f32 v7, v7, v120
v_sub_f32 v8, v8, v121
v_sub_f32 v9, v9, v121
v_sub_f32 v10, v10, v121
v_sub_f32 v11, v11, v121
v_sub_f32 v132, v132, v122
v_sub_f32 v133, v133, v122
v_sub_f32 v134, v134, v122
v_sub_f32 v135, v135, v122
v_sub_f32 v140, v140, v123
v_sub_f32 v141, v141, v123
v_sub_f32 v142, v142, v123
v_sub_f32 v143, v143, v123
s_getpc_b64 s[16:17]
.Lab1_g:
s_add_u32 s16, s16, .Lab1_b-.Lab1_g
s_addc_u32 s17, s17, 0
s_branch .Lscale4
.Lab1_b:
Cvvv 116,96,196,112
Mvvv 116,100,204
Mvvv 116,104,212
Mvvv 116,108,220
v_exp_f32 v120, v64
v_exp_f32 v121, v65
s_nop 0
v_cvt_pk_bf16_f32 v228, v120, v121
v_exp_f32 v122, v66
v_exp_f32 v123, v67
v_exp_f32 v124, v68
v_exp_f32 v125, v69
v_exp_f32 v126, v70
v_cvt_pk_bf16_f32 v229, v122, v123
v_exp_f32 v127, v71
v_exp_f32 v120, v72
v_exp_f32 v121, v73
v_cvt_pk_bf16_f32 v236, v124, v125
v_exp_f32 v122, v74
v_cvt_pk_bf16_f32 v237, v126, v127
v_exp_f32 v123, v75
v_exp_f32 v124, v76
v_cvt_pk_bf16_f32 v244, v120, v121
v_exp_f32 v125, v77
s_nop 7
s_branch .Lab1_r
.Ld0:
s_waitcnt vmcnt(0)
s_nop 7
v_max3_f32 v120, v64, v65, v66
v_max3_f32 v120, v120, v67, v80
v_max3_f32 v120, v120, v81, v82
v_max_f32 v120, v120, v83
v_mov_b32 v26, v120
s_nop 1
v_permlane16_swap_b32 v120, v26
v_max_f32 v120, v120, v26
v_mov_b32 v26, v120
s_nop 1
v_permlane32_swap_b32 v120, v26
v_max_f32 v120, v120, v26
v_max_f32 v120, 0, v120
v_sub_f32 v26, 0, v120
v_exp_f32 v124, v26
s_nop 0
v_mul_f32 v116, v116, v124
v_max3_f32 v121, v68, v69, v70
v_max3_f32 v121, v121, v71, v84
v_max3_f32 v121, v121, v85, v86
v_max_f32 v121, v121, v87
v_mov_b32 v26, v121
s_nop 1
v_permlane16_swap_b32 v121, v26
v_max_f32 v121, v121, v26
v_mov_b32 v26, v121
s_nop 1
v_permlane32_swap_b32 v121, v26
v_max_f32 v121, v121, v26
v_max_f32 v121, 0, v121
v_sub_f32 v26, 0, v121
v_exp_f32 v125, v26
s_nop 0
v_mul_f32 v117, v117, v125
v_max3_f32 v122, v72, v73, v74
v_max3_f32 v122, v122, v75, v88
v_max3_f32 v122, v122, v89, v90
v_max_f32 v122, v122, v91
v_mov_b32 v26, v122
s_nop 1
v_permlane16_swap_b32 v122, v26
v_max_f32 v122, v122, v26
v_mov_b32 v26, v122
s_nop 1
v_permlane32_swap_b32 v122, v26
v_max_f32 v122, v122, v26
v_max_f32 v122, 0, v122
v_sub_f32 v26, 0, v122
v_exp_f32 v126, v26
s_nop 0
v_mul_f32 v118, v118, v126
v_max3_f32 v123, v76, v77, v78
v_max3_f32 v123, v123, v79, v92
v_max3_f32 v123, v123, v93, v94
v_max_f32 v123, v123, v95
v_mov_b32 v26, v123
s_nop 1
v_permlane16_swap_b32 v123, v26
v_max_f32 v123, v123, v26
v_mov_b32 v26, v123
s_nop 1
v_permlane32_swap_b32 v123, v26
v_max_f32 v123, v123, v26
v_max_f32 v123, 0, v123
v_sub_f32 v26, 0, v123
v_exp_f32 v127, v26
s_nop 0
v_mul_f32 v119, v119, v127
v_sub_f32 v28, v64, v120
v_sub_f32 v29, v65, v120
v_exp_f32 v28, v28
v_exp_f32 v29, v29
s_nop 0
v_cvt_pk_bf16_f32 v228, v28, v29
v_sub_f32 v28, v66, v120
v_sub_f32 v29, v67, v120
v_exp_f32 v28, v28
v_exp_f32 v29, v29
s_nop 0
v_cvt_pk_bf16_f32 v229, v28, v29
v_sub_f32 v32, v32, v120
v_sub_f32 v33, v33, v120
v_sub_f32 v34, v34, v120
v_sub_f32 v35, v35, v120
v_sub_f32 v28, v68, v121
v_sub_f32 v29, v69, v121
v_exp_f32 v28, v28
v_exp_f32 v29, v29
s_nop 0
v_cvt_pk_bf16_f32 v236, v28, v29
v_sub_f32 v28, v70, v121
v_sub_f32 v29, v71, v121
v_exp_f32 v28, v28
v_exp_f32 v29, v29
s_nop 0
v_cvt_pk_bf16_f32 v237, v28, v29
v_sub_f32 v36, v36, v121
v_sub_f32 v37, v37, v121
v_sub_f32 v38, v38, v121
v_sub_f32 v39, v39, v121
v_sub_f32 v28, v72, v122
v_sub_f32 v29, v73, v122
v_exp_f32 v28, v28
v_exp_f32 v29, v29
s_nop 0
v_cvt_pk_bf16_f32 v244, v28, v29
v_sub_f32 v28, v74, v122
v_sub_f32 v29, v75, v122
v_exp_f32 v28, v28
v_exp_f32 v29, v29
s_nop 0
v_cvt_pk_bf16_f32 v245, v28, v29
v_sub_f32 v40, v40, v122
v_sub_f32 v41, v41, v122
v_sub_f32 v42, v42, v122
v_sub_f32 v43, v43, v122
v_sub_f32 v28, v76, v123
v_sub_f32 v29, v77, v123
v_exp_f32 v28, v28
v_exp_f32 v29, v29
s_nop 0
v_cvt_pk_bf16_f32 v252, v28, v29
v_sub_f32 v28, v78, v123
v_sub_f32 v29, v79, v123
v_exp_f32 v28, v28
v_exp_f32 v29, v29
s_nop 0
v_cvt_pk_bf16_f32 v253, v28, v29
v_sub_f32 v44, v44, v123
v_sub_f32 v45, v45, v123
v_sub_f32 v46, v46, v123
v_sub_f32 v47, v47, v123
v_sub_f32 v28, v80, v120
v_sub_f32 v29, v81, v120
v_exp_f32 v28, v28
v_exp_f32 v29, v29
s_nop 0
v_cvt_pk_bf16_f32 v230, v28, v29
v_sub_f32 v28, v82, v120
v_sub_f32 v29, v83, v120
v_exp_f32 v28, v28
v_exp_f32 v29, v29
s_nop 0
v_cvt_pk_bf16_f32 v231, v28, v29
v_sub_f32 v48, v48, v120
v_sub_f32 v49, v49, v120
v_sub_f32 v50, v50, v120
v_sub_f32 v51, v51, v120
v_sub_f32 v28, v84, v121
v_sub_f32 v29, v85, v121
v_exp_f32 v28, v28
v_exp_f32 v29, v29
s_nop 0
v_cvt_pk_bf16_f32 v238, v28, v29
v_sub_f32 v28, v86, v121
v_sub_f32 v29, v87, v121
v_exp_f32 v28, v28
v_exp_f32 v29, v29
s_nop 0
v_cvt_pk_bf16_f32 v239, v28, v29
v_sub_f32 v52, v52, v121
v_sub_f32 v53, v53, v121
v_sub_f32 v54, v54, v121
v_sub_f32 v55, v55, v121
v_sub_f32 v28, v88, v122
v_sub_f32 v29, v89, v122
v_exp_f32 v28, v28
v_exp_f32 v29, v29
s_nop 0
v_cvt_pk_bf16_f32 v246, v28, v29
v_sub_f32 v28, v90, v122
v_sub_f32 v29, v91, v122
v_exp_f32 v28, v28
v_exp_f32 v29, v29
s_nop 0
v_cvt_pk_bf16_f32 v247, v28, v29
v_sub_f32 v56, v56, v122
v_sub_f32 v57, v57, v122
v_sub_f32 v58, v58, v122
v_sub_f32 v59, v59, v122
v_sub_f32 v28, v92, v123
v_sub_f32 v29, v93, v123
v_exp_f32 v28, v28
v_exp_f32 v29, v29
s_nop 0
v_cvt_pk_bf16_f32 v254, v28, v29
v_sub_f32 v28, v94, v123
v_sub_f32 v29, v95, v123
v_exp_f32 v28, v28
v_exp_f32 v29, v29
s_nop 0
v_cvt_pk_bf16_f32 v255, v28, v29
v_sub_f32 v60, v60, v123
v_sub_f32 v61, v61, v123
v_sub_f32 v62, v62, v123
v_sub_f32 v63, v63, v123
v_sub_f32 v4, v4, v120
v_sub_f32 v5, v5, v120
v_sub_f32 v6, v6, v120
v_sub_f32 v7, v7, v120
v_sub_f32 v8, v8, v121
v_sub_f32 v9, v9, v121
v_sub_f32 v10, v10, v121
v_sub_f32 v11, v11, v121
v_sub_f32 v132, v132, v122
v_sub_f32 v133, v133, v122
v_sub_f32 v134, v134, v122
v_sub_f32 v135, v135, v122
v_sub_f32 v140, v140, v123
v_sub_f32 v141, v141, v123
v_sub_f32 v142, v142, v123
v_sub_f32 v143, v143, v123
s_getpc_b64 s[16:17]
.Ld0_g:
s_add_u32 s16, s16, .Ld0_b-.Ld0_g
s_addc_u32 s17, s17, 0
s_branch .Lscale4
.Ld0_b:
Cvvv 112,96,228,116
Mvvv 112,100,236
Mvvv 112,104,244
Mvvv 112,108,252
v_exp_f32 v120, v32
v_exp_f32 v121, v33
s_nop 0
v_cvt_pk_bf16_f32 v196, v120, v121
v_exp_f32 v122, v34
)"
R"(
v_exp_f32 v123, v35
v_exp_f32 v124, v36
v_exp_f32 v125, v37
v_exp_f32 v126, v38
v_exp_f32 v127, v39
v_exp_f32 v120, v44
v_cvt_pk_bf16_f32 v197, v122, v123
v_cvt_pk_bf16_f32 v204, v124, v125
v_exp_f32 v121, v45
v_exp_f32 v122, v46
v_exp_f32 v123, v47
v_cvt_pk_bf16_f32 v205, v126, v127
v_cvt_pk_bf16_f32 v220, v120, v121
v_exp_f32 v124, v48
s_nop 7
s_branch .Ld0_r
.Ld1:
s_waitcnt vmcnt(0)
s_nop 7
v_max3_f32 v120, v32, v33, v34
v_max3_f32 v120, v120, v35, v48
v_max3_f32 v120, v120, v49, v50
v_max_f32 v120, v120, v51
v_mov_b32 v26, v120
s_nop 1
v_permlane16_swap_b32 v120, v26
v_max_f32 v120, v120, v26
v_mov_b32 v26, v120
s_nop 1
v_permlane32_swap_b32 v120, v26
v_max_f32 v120, v120, v26
v_max_f32 v120, 0, v120
v_sub_f32 v26, 0, v120
v_exp_f32 v124, v26
s_nop 0
v_mul_f32 v112, v112, v124
v_max3_f32 v121, v36, v37, v38
v_max3_f32 v121, v121, v39, v52
v_max3_f32 v121, v121, v53, v54
v_max_f32 v121, v121, v55
v_mov_b32 v26, v121
s_nop 1
v_permlane16_swap_b32 v121, v26
v_max_f32 v121, v121, v26
v_mov_b32 v26, v121
s_nop 1
v_permlane32_swap_b32 v121, v26
v_max_f32 v121, v121, v26
v_max_f32 v121, 0, v121
v_sub_f32 v26, 0, v121
v_exp_f32 v125, v26
s_nop 0
v_mul_f32 v113, v113, v125
v_max3_f32 v122, v40, v41, v42
v_max3_f32 v122, v122, v43, v56
v_max3_f32 v122, v122, v57, v58
v_max_f32 v122, v122, v59
v_mov_b32 v26, v122
s_nop 1
v_permlane16_swap_b32 v122, v26
v_max_f32 v122, v122, v26
v_mov_b32 v26, v122
s_nop 1
v_permlane32_swap_b32 v122, v26
v_max_f32 v122, v122, v26
v_max_f32 v122, 0, v122
v_sub_f32 v26, 0, v122
v_exp_f32 v126, v26
s_nop 0
v_mul_f32 v114, v114, v126
v_max3_f32 v123, v44, v45, v46
v_max3_f32 v123, v123, v47, v60
v_max3_f32 v123, v123, v61, v62
v_max_f32 v123, v123, v63
v_mov_b32 v26, v123
s_nop 1
v_permlane16_swap_b32 v123, v26
v_max_f32 v123, v123, v26
v_mov_b32 v26, v123
s_nop 1
v_permlane32_swap_b32 v123, v26
v_max_f32 v123, v123, v26
v_max_f32 v123, 0, v123
v_sub_f32 v26, 0, v123
v_exp_f32 v127, v26
s_nop 0
v_mul_f32 v115, v115, v127
v_sub_f32 v28, v32, v120
v_sub_f32 v29, v33, v120
v_exp_f32 v28, v28
v_exp_f32 v29, v29
s_nop 0
v_cvt_pk_bf16_f32 v196, v28, v29
v_sub_f32 v28, v34, v120
v_sub_f32 v29, v35, v120
v_exp_f32 v28, v28
v_exp_f32 v29, v29
s_nop 0
v_cvt_pk_bf16_f32 v197, v28, v29
v_sub_f32 v64, v64, v120
v_sub_f32 v65, v65, v120
v_sub_f32 v66, v66, v120
v_sub_f32 v67, v67, v120
v_sub_f32 v28, v36, v121
v_sub_f32 v29, v37, v121
v_exp_f32 v28, v28
v_exp_f32 v29, v29
s_nop 0
v_cvt_pk_bf16_f32 v204, v28, v29
v_sub_f32 v28, v38, v121
v_sub_f32 v29, v39, v121
v_exp_f32 v28, v28
v_exp_f32 v29, v29
s_nop 0
v_cvt_pk_bf16_f32 v205, v28, v29
v_sub_f32 v68, v68, v121
v_sub_f32 v69, v69, v121
v_sub_f32 v70, v70, v121
v_sub_f32 v71, v71, v121
v_sub_f32 v28, v40, v122
v_sub_f32 v29, v41, v122
v_exp_f32 v28, v28
v_exp_f32 v29, v29
s_nop 0
v_cvt_pk_bf16_f32 v212, v28, v29
v_sub_f32 v28, v42, v122
v_sub_f32 v29, v43, v122
v_exp_f32 v28, v28
v_exp_f32 v29, v29
s_nop 0
v_cvt_pk_bf16_f32 v213, v28, v29
v_sub_f32 v72, v72, v122
v_sub_f32 v73, v73, v122
v_sub_f32 v74, v74, v122
v_sub_f32 v75, v75, v122
v_sub_f32 v28, v44, v123
v_sub_f32 v29, v45, v123
v_exp_f32 v28, v28
v_exp_f32 v29, v29
s_nop 0
v_cvt_pk_bf16_f32 v220, v28, v29
v_sub_f32 v28, v46, v123
v_sub_f32 v29, v47, v123
v_exp_f32 v28, v28
v_exp_f32 v29, v29
s_nop 0
v_cvt_pk_bf16_f32 v221, v28, v29
v_sub_f32 v76, v76, v123
v_sub_f32 v77, v77, v123
v_sub_f32 v78, v78, v123
v_sub_f32 v79, v79, v123
v_sub_f32 v28, v48, v120
v_sub_f32 v29, v49, v120
v_exp_f32 v28, v28
v_exp_f32 v29, v29
s_nop 0
v_cvt_pk_bf16_f32 v198, v28, v29
v_sub_f32 v28, v50, v120
v_sub_f32 v29, v51, v120
v_exp_f32 v28, v28
v_exp_f32 v29, v29
s_nop 0
v_cvt_pk_bf16_f32 v199, v28, v29
v_sub_f32 v80, v80, v120
v_sub_f32 v81, v81, v120
v_sub_f32 v82, v82, v120
v_sub_f32 v83, v83, v120
v_sub_f32 v28, v52, v121
v_sub_f32 v29, v53, v121
v_exp_f32 v28, v28
v_exp_f32 v29, v29
s_nop 0
v_cvt_pk_bf16_f32 v206, v28, v29
v_sub_f32 v28, v54, v121
v_sub_f32 v29, v55, v121
v_exp_f32 v28, v28
v_exp_f32 v29, v29
s_nop 0
v_cvt_pk_bf16_f32 v207, v28, v29
v_sub_f32 v84, v84, v121
v_sub_f32 v85, v85, v121
v_sub_f32 v86, v86, v121
v_sub_f32 v87, v87, v121
v_sub_f32 v28, v56, v122
v_sub_f32 v29, v57, v122
v_exp_f32 v28, v28
v_exp_f32 v29, v29
s_nop 0
v_cvt_pk_bf16_f32 v214, v28, v29
v_sub_f32 v28, v58, v122
v_sub_f32 v29, v59, v122
v_exp_f32 v28, v28
v_exp_f32 v29, v29
s_nop 0
v_cvt_pk_bf16_f32 v215, v28, v29
v_sub_f32 v88, v88, v122
v_sub_f32 v89, v89, v122
v_sub_f32 v90, v90, v122
v_sub_f32 v91, v91, v122
v_sub_f32 v28, v60, v123
v_sub_f32 v29, v61, v123
v_exp_f32 v28, v28
v_exp_f32 v29, v29
s_nop 0
v_cvt_pk_bf16_f32 v222, v28, v29
v_sub_f32 v28, v62, v123
v_sub_f32 v29, v63, v123
v_exp_f32 v28, v28
v_exp_f32 v29, v29
s_nop 0
v_cvt_pk_bf16_f32 v223, v28, v29
v_sub_f32 v92, v92, v123
v_sub_f32 v93, v93, v123
v_sub_f32 v94, v94, v123
v_sub_f32 v95, v95, v123
v_sub_f32 v4, v4, v120
v_sub_f32 v5, v5, v120
v_sub_f32 v6, v6, v120
v_sub_f32 v7, v7, v120
v_sub_f32 v8, v8, v121
v_sub_f32 v9, v9, v121
v_sub_f32 v10, v10, v121
v_sub_f32 v11, v11, v121
v_sub_f32 v132, v132, v122
v_sub_f32 v133, v133, v122
v_sub_f32 v134, v134, v122
v_sub_f32 v135, v135, v122
v_sub_f32 v140, v140, v123
v_sub_f32 v141, v141, v123
v_sub_f32 v142, v142, v123
v_sub_f32 v143, v143, v123
s_getpc_b64 s[16:17]
.Ld1_g:
s_add_u32 s16, s16, .Ld1_b-.Ld1_g
s_addc_u32 s17, s17, 0
s_branch .Lscale4
.Ld1_b:
Cvvv 116,96,196,112
Mvvv 116,100,204
Mvvv 116,104,212
Mvvv 116,108,220
v_exp_f32 v120, v64
v_exp_f32 v121, v65
v_exp_f32 v122, v66
v_exp_f32 v123, v67
s_nop 0
v_cvt_pk_bf16_f32 v229, v122, v123
v_exp_f32 v124, v68
v_exp_f32 v125, v69
v_exp_f32 v126, v70
v_exp_f32 v127, v71
v_cvt_pk_bf16_f32 v228, v120, v121
v_cvt_pk_bf16_f32 v237, v126, v127
v_exp_f32 v120, v72
v_exp_f32 v121, v73
v_exp_f32 v122, v74
v_cvt_pk_bf16_f32 v236, v124, v125
v_exp_f32 v123, v75
s_nop 7
s_branch .Ld1_r
.Ly:
s_waitcnt vmcnt(0)
s_nop 7
v_max3_f32 v120, v64, v65, v66
v_max3_f32 v120, v120, v67, v80
v_max3_f32 v120, v120, v81, v82
v_max_f32 v120, v120, v83
v_mov_b32 v26, v120
s_nop 1
v_permlane16_swap_b32 v120, v26
v_max_f32 v120, v120, v26
v_mov_b32 v26, v120
s_nop 1
v_permlane32_swap_b32 v120, v26
v_max_f32 v120, v120, v26
v_max_f32 v120, 0, v120
v_sub_f32 v26, 0, v120
v_exp_f32 v124, v26
s_nop 0
v_mul_f32 v116, v116, v124
v_max3_f32 v121, v68, v69, v70
v_max3_f32 v121, v121, v71, v84
v_max3_f32 v121, v121, v85, v86
v_max_f32 v121, v121, v87
v_mov_b32 v26, v121
s_nop 1
v_permlane16_swap_b32 v121, v26
v_max_f32 v121, v121, v26
v_mov_b32 v26, v121
s_nop 1
v_permlane32_swap_b32 v121, v26
v_max_f32 v121, v121, v26
v_max_f32 v121, 0, v121
v_sub_f32 v26, 0, v121
v_exp_f32 v125, v26
s_nop 0
v_mul_f32 v117, v117, v125
v_max3_f32 v122, v72, v73, v74
v_max3_f32 v122, v122, v75, v88
v_max3_f32 v122, v122, v89, v90
v_max_f32 v122, v122, v91
v_mov_b32 v26, v122
s_nop 1
v_permlane16_swap_b32 v122, v26
v_max_f32 v122, v122, v26
v_mov_b32 v26, v122
s_nop 1
v_permlane32_swap_b32 v122, v26
v_max_f32 v122, v122, v26
v_max_f32 v122, 0, v122
v_sub_f32 v26, 0, v122
v_exp_f32 v126, v26
s_nop 0
v_mul_f32 v118, v118, v126
v_max3_f32 v123, v76, v77, v78
v_max3_f32 v123, v123, v79, v92
v_max3_f32 v123, v123, v93, v94
v_max_f32 v123, v123, v95
v_mov_b32 v26, v123
s_nop 1
v_permlane16_swap_b32 v123, v26
v_max_f32 v123, v123, v26
v_mov_b32 v26, v123
s_nop 1
v_permlane32_swap_b32 v123, v26
v_max_f32 v123, v123, v26
v_max_f32 v123, 0, v123
v_sub_f32 v26, 0, v123
v_exp_f32 v127, v26
s_nop 0
v_mul_f32 v119, v119, v127
v_sub_f32 v28, v64, v120
v_sub_f32 v29, v65, v120
v_exp_f32 v28, v28
v_exp_f32 v29, v29
s_nop 0
v_cvt_pk_bf16_f32 v228, v28, v29
v_sub_f32 v28, v66, v120
v_sub_f32 v29, v67, v120
v_exp_f32 v28, v28
v_exp_f32 v29, v29
s_nop 0
v_cvt_pk_bf16_f32 v229, v28, v29
v_sub_f32 v32, v32, v120
v_sub_f32 v33, v33, v120
v_sub_f32 v34, v34, v120
v_sub_f32 v35, v35, v120
v_sub_f32 v28, v68, v121
v_sub_f32 v29, v69, v121
v_exp_f32 v28, v28
v_exp_f32 v29, v29
s_nop 0
v_cvt_pk_bf16_f32 v236, v28, v29
v_sub_f32 v28, v70, v121
v_sub_f32 v29, v71, v121
v_exp_f32 v28, v28
v_exp_f32 v29, v29
s_nop 0
v_cvt_pk_bf16_f32 v237, v28, v29
v_sub_f32 v36, v36, v121
v_sub_f32 v37, v37, v121
v_sub_f32 v38, v38, v121
v_sub_f32 v39, v39, v121
v_sub_f32 v28, v72, v122
v_sub_f32 v29, v73, v122
v_exp_f32 v28, v28
v_exp_f32 v29, v29
s_nop 0
v_cvt_pk_bf16_f32 v244, v28, v29
v_sub_f32 v28, v74, v122
v_sub_f32 v29, v75, v122
v_exp_f32 v28, v28
v_exp_f32 v29, v29
s_nop 0
v_cvt_pk_bf16_f32 v245, v28, v29
v_sub_f32 v40, v40, v120
v_sub_f32 v41, v41, v120
v_sub_f32 v42, v42, v120
v_sub_f32 v43, v43, v120
v_sub_f32 v28, v76, v123
v_sub_f32 v29, v77, v123
v_exp_f32 v28, v28
v_exp_f32 v29, v29
s_nop 0
v_cvt_pk_bf16_f32 v252, v28, v29
v_sub_f32 v28, v78, v123
v_sub_f32 v29, v79, v123
v_exp_f32 v28, v28
v_exp_f32 v29, v29
s_nop 0
v_cvt_pk_bf16_f32 v253, v28, v29
v_sub_f32 v44, v44, v121
v_sub_f32 v45, v45, v121
v_sub_f32 v46, v46, v121
v_sub_f32 v47, v47, v121
v_sub_f32 v28, v80, v120
v_sub_f32 v29, v81, v120
v_exp_f32 v28, v28
v_exp_f32 v29, v29
s_nop 0
v_cvt_pk_bf16_f32 v230, v28, v29
v_sub_f32 v28, v82, v120
v_sub_f32 v29, v83, v120
v_exp_f32 v28, v28
v_exp_f32 v29, v29
s_nop 0
v_cvt_pk_bf16_f32 v231, v28, v29
v_sub_f32 v48, v48, v120
v_sub_f32 v49, v49, v120
v_sub_f32 v50, v50, v120
v_sub_f32 v51, v51, v120
v_sub_f32 v28, v84, v121
v_sub_f32 v29, v85, v121
v_exp_f32 v28, v28
v_exp_f32 v29, v29
s_nop 0
v_cvt_pk_bf16_f32 v238, v28, v29
v_sub_f32 v28, v86, v121
v_sub_f32 v29, v87, v121
v_exp_f32 v28, v28
v_exp_f32 v29, v29
s_nop 0
v_cvt_pk_bf16_f32 v239, v28, v29
v_sub_f32 v52, v52, v121
v_sub_f32 v53, v53, v121
v_sub_f32 v54, v54, v121
v_sub_f32 v55, v55, v121
v_sub_f32 v28, v88, v122
v_sub_f32 v29, v89, v122
v_exp_f32 v28, v28
v_exp_f32 v29, v29
s_nop 0
v_cvt_pk_bf16_f32 v246, v28, v29
v_sub_f32 v28, v90, v122
v_sub_f32 v29, v91, v122
v_exp_f32 v28, v28
v_exp_f32 v29, v29
s_nop 0
v_cvt_pk_bf16_f32 v247, v28, v29
v_sub_f32 v56, v56, v120
v_sub_f32 v57, v57, v120
v_sub_f32 v58, v58, v120
v_sub_f32 v59, v59, v120
v_sub_f32 v28, v92, v123
v_sub_f32 v29, v93, v123
v_exp_f32 v28, v28
v_exp_f32 v29, v29
s_nop 0
v_cvt_pk_bf16_f32 v254, v28, v29
v_sub_f32 v28, v94, v123
v_sub_f32 v29, v95, v123
v_exp_f32 v28, v28
v_exp_f32 v29, v29
s_nop 0
v_cvt_pk_bf16_f32 v255, v28, v29
v_sub_f32 v60, v60, v121
v_sub_f32 v61, v61, v121
v_sub_f32 v62, v62, v121
v_sub_f32 v63, v63, v121
v_sub_f32 v4, v4, v120
v_sub_f32 v5, v5, v120
v_sub_f32 v6, v6, v120
v_sub_f32 v7, v7, v120
v_sub_f32 v8, v8, v121
v_sub_f32 v9, v9, v121
v_sub_f32 v10, v10, v121
v_sub_f32 v11, v11, v121
s_getpc_b64 s[16:17]
.Ly_g:
s_add_u32 s16, s16, .Ly_b-.Ly_g
s_addc_u32 s17, s17, 0
s_branch .Lscale4
.Ly_b:
Cvvv 112,96,228,116
Mvvv 112,100,236
Mvvv 112,104,244
Mvvv 112,108,252
v_exp_f32 v120, v32
v_exp_f32 v121, v33
v_exp_f32 v122, v34
v_exp_f32 v123, v35
v_exp_f32 v124, v36
v_cvt_pk_bf16_f32 v196, v120, v121
v_cvt_pk_bf16_f32 v197, v122, v123
v_exp_f32 v125, v37
v_exp_f32 v126, v38
v_exp_f32 v127, v39
v_exp_f32 v120, v40
v_cvt_pk_bf16_f32 v205, v126, v127
v_exp_f32 v121, v41
v_cvt_pk_bf16_f32 v204, v124, v125
v_cvt_pk_bf16_f32 v198, v120, v121
v_exp_f32 v122, v42
s_nop 7
s_branch .Ly_r
.Laa1:
s_waitcnt vmcnt(0)
s_nop 7
v_max3_f32 v120, v32, v33, v34
v_max3_f32 v120, v120, v35, v40
v_max3_f32 v120, v120, v41, v42
v_max3_f32 v120, v120, v43, v48
v_max3_f32 v120, v120, v49, v50
v_max3_f32 v120, v120, v51, v56
v_max3_f32 v120, v120, v57, v58
v_max_f32 v120, v120, v59
v_mov_b32 v26, v120
s_nop 1
v_permlane16_swap_b32 v120, v26
v_max_f32 v120, v120, v26
v_mov_b32 v26, v120
s_nop 1
v_permlane32_swap_b32 v120, v26
v_max_f32 v120, v120, v26
v_max_f32 v120, 0, v120
v_sub_f32 v26, 0, v120
v_exp_f32 v124, v26
s_nop 0
v_mul_f32 v112, v112, v124
v_max3_f32 v121, v36, v37, v38
v_max3_f32 v121, v121, v39, v44
v_max3_f32 v121, v121, v45, v46
v_max3_f32 v121, v121, v47, v52
v_max3_f32 v121, v121, v53, v54
v_max3_f32 v121, v121, v55, v60
v_max3_f32 v121, v121, v61, v62
v_max_f32 v121, v121, v63
v_mov_b32 v26, v121
s_nop 1
v_permlane16_swap_b32 v121, v26
v_max_f32 v121, v121, v26
v_mov_b32 v26, v121
)"
R"(
s_nop 1
v_permlane32_swap_b32 v121, v26
v_max_f32 v121, v121, v26
v_max_f32 v121, 0, v121
v_sub_f32 v26, 0, v121
v_exp_f32 v125, v26
s_nop 0
v_mul_f32 v113, v113, v125
v_sub_f32 v28, v32, v120
v_sub_f32 v29, v33, v120
v_exp_f32 v28, v28
v_exp_f32 v29, v29
s_nop 0
v_cvt_pk_bf16_f32 v196, v28, v29
v_sub_f32 v28, v34, v120
v_sub_f32 v29, v35, v120
v_exp_f32 v28, v28
v_exp_f32 v29, v29
s_nop 0
v_cvt_pk_bf16_f32 v197, v28, v29
v_sub_f32 v64, v64, v120
v_sub_f32 v65, v65, v120
v_sub_f32 v66, v66, v120
v_sub_f32 v67, v67, v120
v_sub_f32 v28, v36, v121
v_sub_f32 v29, v37, v121
v_exp_f32 v28, v28
v_exp_f32 v29, v29
s_nop 0
v_cvt_pk_bf16_f32 v204, v28, v29
v_sub_f32 v28, v38, v121
v_sub_f32 v29, v39, v121
v_exp_f32 v28, v28
v_exp_f32 v29, v29
s_nop 0
v_cvt_pk_bf16_f32 v205, v28, v29
v_sub_f32 v68, v68, v121
v_sub_f32 v69, v69, v121
v_sub_f32 v70, v70, v121
v_sub_f32 v71, v71, v121
v_sub_f32 v28, v40, v120
v_sub_f32 v29, v41, v120
v_exp_f32 v28, v28
v_exp_f32 v29, v29
s_nop 0
v_cvt_pk_bf16_f32 v198, v28, v29
v_sub_f32 v28, v42, v120
v_sub_f32 v29, v43, v120
v_exp_f32 v28, v28
v_exp_f32 v29, v29
s_nop 0
v_cvt_pk_bf16_f32 v199, v28, v29
v_sub_f32 v72, v72, v120
v_sub_f32 v73, v73, v120
v_sub_f32 v74, v74, v120
v_sub_f32 v75, v75, v120
v_sub_f32 v28, v44, v121
v_sub_f32 v29, v45, v121
v_exp_f32 v28, v28
v_exp_f32 v29, v29
s_nop 0
v_cvt_pk_bf16_f32 v206, v28, v29
v_sub_f32 v28, v46, v121
v_sub_f32 v29, v47, v121
v_exp_f32 v28, v28
v_exp_f32 v29, v29
s_nop 0
v_cvt_pk_bf16_f32 v207, v28, v29
v_sub_f32 v76, v76, v121
v_sub_f32 v77, v77, v121
v_sub_f32 v78, v78, v121
v_sub_f32 v79, v79, v121
v_sub_f32 v28, v48, v120
v_sub_f32 v29, v49, v120
v_exp_f32 v28, v28
v_exp_f32 v29, v29
s_nop 0
v_cvt_pk_bf16_f32 v212, v28, v29
v_sub_f32 v28, v50, v120
v_sub_f32 v29, v51, v120
v_exp_f32 v28, v28
v_exp_f32 v29, v29
s_nop 0
v_cvt_pk_bf16_f32 v213, v28, v29
v_sub_f32 v80, v80, v120
v_sub_f32 v81, v81, v120
v_sub_f32 v82, v82, v120
v_sub_f32 v83, v83, v120
v_sub_f32 v28, v52, v121
v_sub_f32 v29, v53, v121
v_exp_f32 v28, v28
v_exp_f32 v29, v29
s_nop 0
v_cvt_pk_bf16_f32 v220, v28, v29
v_sub_f32 v28, v54, v121
v_sub_f32 v29, v55, v121
v_exp_f32 v28, v28
v_exp_f32 v29, v29
s_nop 0
v_cvt_pk_bf16_f32 v221, v28, v29
v_sub_f32 v84, v84, v121
v_sub_f32 v85, v85, v121
v_sub_f32 v86, v86, v121
v_sub_f32 v87, v87, v121
v_sub_f32 v28, v56, v120
v_sub_f32 v29, v57, v120
v_exp_f32 v28, v28
v_exp_f32 v29, v29
s_nop 0
v_cvt_pk_bf16_f32 v214, v28, v29
v_sub_f32 v28, v58, v120
v_sub_f32 v29, v59, v120
v_exp_f32 v28, v28
v_exp_f32 v29, v29
s_nop 0
v_cvt_pk_bf16_f32 v215, v28, v29
v_sub_f32 v88, v88, v120
v_sub_f32 v89, v89, v120
v_sub_f32 v90, v90, v120
v_sub_f32 v91, v91, v120
v_sub_f32 v28, v60, v121
v_sub_f32 v29, v61, v121
v_exp_f32 v28, v28
v_exp_f32 v29, v29
s_nop 0
v_cvt_pk_bf16_f32 v222, v28, v29
v_sub_f32 v28, v62, v121
v_sub_f32 v29, v63, v121
v_exp_f32 v28, v28
v_exp_f32 v29, v29
s_nop 0
v_cvt_pk_bf16_f32 v223, v28, v29
v_sub_f32 v92, v92, v121
v_sub_f32 v93, v93, v121
v_sub_f32 v94, v94, v121
v_sub_f32 v95, v95, v121
v_sub_f32 v4, v4, v120
v_sub_f32 v5, v5, v120
v_sub_f32 v6, v6, v120
v_sub_f32 v7, v7, v120
v_sub_f32 v8, v8, v121
v_sub_f32 v9, v9, v121
v_sub_f32 v10, v10, v121
v_sub_f32 v11, v11, v121
s_getpc_b64 s[16:17]
.Laa1_g:
s_add_u32 s16, s16, .Laa1_b-.Laa1_g
s_addc_u32 s17, s17, 0
s_branch .Lscale2
.Laa1_b:
Cvvv 116,96,196,112
Mvvv 116,100,204
Mvvv 116,96,212
Mvvv 116,100,220
v_exp_f32 v120, v64
v_exp_f32 v121, v65
s_nop 0
v_cvt_pk_bf16_f32 v228, v120, v121
v_exp_f32 v122, v66
v_exp_f32 v123, v67
v_exp_f32 v124, v68
v_exp_f32 v125, v69
v_cvt_pk_bf16_f32 v229, v122, v123
v_exp_f32 v126, v70
v_exp_f32 v127, v71
v_cvt_pk_bf16_f32 v236, v124, v125
v_exp_f32 v120, v72
v_exp_f32 v121, v73
v_cvt_pk_bf16_f32 v237, v126, v127
v_exp_f32 v122, v74
v_exp_f32 v123, v75
v_cvt_pk_bf16_f32 v230, v120, v121
v_exp_f32 v124, v76
v_exp_f32 v125, v77
s_nop 7
s_branch .Laa1_r
.Laa0:
s_waitcnt vmcnt(0)
s_nop 7
v_max3_f32 v120, v64, v65, v66
v_max3_f32 v120, v120, v67, v72
v_max3_f32 v120, v120, v73, v74
v_max3_f32 v120, v120, v75, v80
v_max3_f32 v120, v120, v81, v82
v_max3_f32 v120, v120, v83, v88
v_max3_f32 v120, v120, v89, v90
v_max_f32 v120, v120, v91
v_mov_b32 v26, v120
s_nop 1
v_permlane16_swap_b32 v120, v26
v_max_f32 v120, v120, v26
v_mov_b32 v26, v120
s_nop 1
v_permlane32_swap_b32 v120, v26
v_max_f32 v120, v120, v26
v_max_f32 v120, 0, v120
v_sub_f32 v26, 0, v120
v_exp_f32 v124, v26
s_nop 0
v_mul_f32 v116, v116, v124
v_max3_f32 v121, v68, v69, v70
v_max3_f32 v121, v121, v71, v76
v_max3_f32 v121, v121, v77, v78
v_max3_f32 v121, v121, v79, v84
v_max3_f32 v121, v121, v85, v86
v_max3_f32 v121, v121, v87, v92
v_max3_f32 v121, v121, v93, v94
v_max_f32 v121, v121, v95
v_mov_b32 v26, v121
s_nop 1
v_permlane16_swap_b32 v121, v26
v_max_f32 v121, v121, v26
v_mov_b32 v26, v121
s_nop 1
v_permlane32_swap_b32 v121, v26
v_max_f32 v121, v121, v26
v_max_f32 v121, 0, v121
v_sub_f32 v26, 0, v121
v_exp_f32 v125, v26
s_nop 0
v_mul_f32 v117, v117, v125
v_sub_f32 v28, v64, v120
v_sub_f32 v29, v65, v120
v_exp_f32 v28, v28
v_exp_f32 v29, v29
s_nop 0
v_cvt_pk_bf16_f32 v228, v28, v29
v_sub_f32 v28, v66, v120
v_sub_f32 v29, v67, v120
v_exp_f32 v28, v28
v_exp_f32 v29, v29
s_nop 0
v_cvt_pk_bf16_f32 v229, v28, v29
v_sub_f32 v32, v32, v120
v_sub_f32 v33, v33, v120
v_sub_f32 v34, v34, v120
v_sub_f32 v35, v35, v120
v_sub_f32 v28, v68, v121
v_sub_f32 v29, v69, v121
v_exp_f32 v28, v28
v_exp_f32 v29, v29
s_nop 0
v_cvt_pk_bf16_f32 v236, v28, v29
v_sub_f32 v28, v70, v121
v_sub_f32 v29, v71, v121
v_exp_f32 v28, v28
v_exp_f32 v29, v29
s_nop 0
v_cvt_pk_bf16_f32 v237, v28, v29
v_sub_f32 v36, v36, v121
v_sub_f32 v37, v37, v121
v_sub_f32 v38, v38, v121
v_sub_f32 v39, v39, v121
v_sub_f32 v28, v72, v120
v_sub_f32 v29, v73, v120
v_exp_f32 v28, v28
v_exp_f32 v29, v29
s_nop 0
v_cvt_pk_bf16_f32 v230, v28, v29
v_sub_f32 v28, v74, v120
v_sub_f32 v29, v75, v120
v_exp_f32 v28, v28
v_exp_f32 v29, v29
s_nop 0
v_cvt_pk_bf16_f32 v231, v28, v29
v_sub_f32 v40, v40, v120
v_sub_f32 v41, v41, v120
v_sub_f32 v42, v42, v120
v_sub_f32 v43, v43, v120
v_sub_f32 v28, v76, v121
v_sub_f32 v29, v77, v121
v_exp_f32 v28, v28
v_exp_f32 v29, v29
s_nop 0
v_cvt_pk_bf16_f32 v238, v28, v29
v_sub_f32 v28, v78, v121
v_sub_f32 v29, v79, v121
v_exp_f32 v28, v28
v_exp_f32 v29, v29
s_nop 0
v_cvt_pk_bf16_f32 v239, v28, v29
v_sub_f32 v44, v44, v121
v_sub_f32 v45, v45, v121
v_sub_f32 v46, v46, v121
v_sub_f32 v47, v47, v121
v_sub_f32 v28, v80, v120
v_sub_f32 v29, v81, v120
v_exp_f32 v28, v28
v_exp_f32 v29, v29
s_nop 0
v_cvt_pk_bf16_f32 v244, v28, v29
v_sub_f32 v28, v82, v120
v_sub_f32 v29, v83, v120
v_exp_f32 v28, v28
v_exp_f32 v29, v29
s_nop 0
v_cvt_pk_bf16_f32 v245, v28, v29
v_sub_f32 v48, v48, v120
v_sub_f32 v49, v49, v120
v_sub_f32 v50, v50, v120
v_sub_f32 v51, v51, v120
v_sub_f32 v28, v84, v121
v_sub_f32 v29, v85, v121
v_exp_f32 v28, v28
v_exp_f32 v29, v29
s_nop 0
v_cvt_pk_bf16_f32 v252, v28, v29
v_sub_f32 v28, v86, v121
v_sub_f32 v29, v87, v121
v_exp_f32 v28, v28
v_exp_f32 v29, v29
s_nop 0
v_cvt_pk_bf16_f32 v253, v28, v29
v_sub_f32 v52, v52, v121
v_sub_f32 v53, v53, v121
v_sub_f32 v54, v54, v121
v_sub_f32 v55, v55, v121
v_sub_f32 v28, v88, v120
v_sub_f32 v29, v89, v120
v_exp_f32 v28, v28
v_exp_f32 v29, v29
s_nop 0
v_cvt_pk_bf16_f32 v246, v28, v29
v_sub_f32 v28, v90, v120
v_sub_f32 v29, v91, v120
v_exp_f32 v28, v28
v_exp_f32 v29, v29
s_nop 0
v_cvt_pk_bf16_f32 v247, v28, v29
v_sub_f32 v56, v56, v120
v_sub_f32 v57, v57, v120
v_sub_f32 v58, v58, v120
v_sub_f32 v59, v59, v120
v_sub_f32 v28, v92, v121
v_sub_f32 v29, v93, v121
v_exp_f32 v28, v28
v_exp_f32 v29, v29
s_nop 0
v_cvt_pk_bf16_f32 v254, v28, v29
v_sub_f32 v28, v94, v121
v_sub_f32 v29, v95, v121
v_exp_f32 v28, v28
v_exp_f32 v29, v29
s_nop 0
v_cvt_pk_bf16_f32 v255, v28, v29
v_sub_f32 v60, v60, v121
v_sub_f32 v61, v61, v121
v_sub_f32 v62, v62, v121
v_sub_f32 v63, v63, v121
v_sub_f32 v4, v4, v120
v_sub_f32 v5, v5, v120
v_sub_f32 v6, v6, v120
v_sub_f32 v7, v7, v120
v_sub_f32 v8, v8, v121
v_sub_f32 v9, v9, v121
v_sub_f32 v10, v10, v121
v_sub_f32 v11, v11, v121
s_getpc_b64 s[16:17]
.Laa0_g:
s_add_u32 s16, s16, .Laa0_b-.Laa0_g
s_addc_u32 s17, s17, 0
s_branch .Lscale2
.Laa0_b:
Cvvv 112,96,228,116
Mvvv 112,100,236
Mvvv 112,96,244
Mvvv 112,100,252
v_exp_f32 v120, v32
v_exp_f32 v121, v33
s_nop 0
v_cvt_pk_bf16_f32 v196, v120, v121
v_exp_f32 v122, v34
v_exp_f32 v123, v35
v_exp_f32 v124, v36
v_exp_f32 v125, v37
v_cvt_pk_bf16_f32 v197, v122, v123
v_exp_f32 v126, v38
v_exp_f32 v127, v39
v_cvt_pk_bf16_f32 v204, v124, v125
v_exp_f32 v120, v40
v_exp_f32 v121, v41
v_cvt_pk_bf16_f32 v205, v126, v127
v_exp_f32 v122, v42
v_exp_f32 v123, v43
v_cvt_pk_bf16_f32 v198, v120, v121
v_exp_f32 v124, v44
v_exp_f32 v125, v45
s_nop 7
s_branch .Laa0_r
.Ldr:
s_waitcnt vmcnt(0)
s_nop 7
v_max3_f32 v120, v32, v33, v34
v_max3_f32 v120, v120, v35, v40
v_max3_f32 v120, v120, v41, v42
v_max3_f32 v120, v120, v43, v48
v_max3_f32 v120, v120, v49, v50
v_max3_f32 v120, v120, v51, v56
v_max3_f32 v120, v120, v57, v58
v_max_f32 v120, v120, v59
v_mov_b32 v26, v120
s_nop 1
v_permlane16_swap_b32 v120, v26
v_max_f32 v120, v120, v26
v_mov_b32 v26, v120
s_nop 1
v_permlane32_swap_b32 v120, v26
v_max_f32 v120, v120, v26
v_max_f32 v120, 0, v120
v_sub_f32 v26, 0, v120
v_exp_f32 v124, v26
s_nop 0
v_mul_f32 v112, v112, v124
v_max3_f32 v121, v36, v37, v38
v_max3_f32 v121, v121, v39, v44
v_max3_f32 v121, v121, v45, v46
v_max3_f32 v121, v121, v47, v52
v_max3_f32 v121, v121, v53, v54
v_max3_f32 v121, v121, v55, v60
v_max3_f32 v121, v121, v61, v62
v_max_f32 v121, v121, v63
v_mov_b32 v26, v121
s_nop 1
v_permlane16_swap_b32 v121, v26
v_max_f32 v121, v121, v26
v_mov_b32 v26, v121
s_nop 1
v_permlane32_swap_b32 v121, v26
v_max_f32 v121, v121, v26
v_max_f32 v121, 0, v121
v_sub_f32 v26, 0, v121
v_exp_f32 v125, v26
s_nop 0
v_mul_f32 v113, v113, v125
v_sub_f32 v28, v32, v120
v_sub_f32 v29, v33, v120
v_exp_f32 v28, v28
v_exp_f32 v29, v29
s_nop 0
v_cvt_pk_bf16_f32 v196, v28, v29
v_sub_f32 v28, v34, v120
v_sub_f32 v29, v35, v120
v_exp_f32 v28, v28
v_exp_f32 v29, v29
s_nop 0
v_cvt_pk_bf16_f32 v197, v28, v29
v_sub_f32 v28, v36, v121
v_sub_f32 v29, v37, v121
v_exp_f32 v28, v28
v_exp_f32 v29, v29
s_nop 0
v_cvt_pk_bf16_f32 v204, v28, v29
v_sub_f32 v28, v38, v121
v_sub_f32 v29, v39, v121
v_exp_f32 v28, v28
v_exp_f32 v29, v29
s_nop 0
v_cvt_pk_bf16_f32 v205, v28, v29
v_sub_f32 v28, v40, v120
v_sub_f32 v29, v41, v120
v_exp_f32 v28, v28
v_exp_f32 v29, v29
s_nop 0
v_cvt_pk_bf16_f32 v198, v28, v29
v_sub_f32 v28, v42, v120
v_sub_f32 v29, v43, v120
v_exp_f32 v28, v28
v_exp_f32 v29, v29
s_nop 0
v_cvt_pk_bf16_f32 v199, v28, v29
v_sub_f32 v28, v44, v121
v_sub_f32 v29, v45, v121
v_exp_f32 v28, v28
v_exp_f32 v29, v29
s_nop 0
v_cvt_pk_bf16_f32 v206, v28, v29
v_sub_f32 v28, v46, v121
v_sub_f32 v29, v47, v121
v_exp_f32 v28, v28
v_exp_f32 v29, v29
s_nop 0
v_cvt_pk_bf16_f32 v207, v28, v29
v_sub_f32 v28, v48, v120
v_sub_f32 v29, v49, v120
v_exp_f32 v28, v28
v_exp_f32 v29, v29
s_nop 0
v_cvt_pk_bf16_f32 v212, v28, v29
v_sub_f32 v28, v50, v120
v_sub_f32 v29, v51, v120
v_exp_f32 v28, v28
v_exp_f32 v29, v29
s_nop 0
v_cvt_pk_bf16_f32 v213, v28, v29
v_sub_f32 v28, v52, v121
v_sub_f32 v29, v53, v121
v_exp_f32 v28, v28
v_exp_f32 v29, v29
s_nop 0
v_cvt_pk_bf16_f32 v220, v28, v29
v_sub_f32 v28, v54, v121
v_sub_f32 v29, v55, v121
v_exp_f32 v28, v28
v_exp_f32 v29, v29
s_nop 0
v_cvt_pk_bf16_f32 v221, v28, v29
v_sub_f32 v28, v56, v120
v_sub_f32 v29, v57, v120
v_exp_f32 v28, v28
v_exp_f32 v29, v29
s_nop 0
v_cvt_pk_bf16_f32 v214, v28, v29
v_sub_f32 v28, v58, v120
v_sub_f32 v29, v59, v120
v_exp_f32 v28, v28
v_exp_f32 v29, v29
s_nop 0
v_cvt_pk_bf16_f32 v215, v28, v29
v_sub_f32 v28, v60, v121
v_sub_f32 v29, v61, v121
v_exp_f32 v28, v28
v_exp_f32 v29, v29
s_nop 0
v_cvt_pk_bf16_f32 v222, v28, v29
v_sub_f32 v28, v62, v121
v_sub_f32 v29, v63, v121
v_exp_f32 v28, v28
v_exp_f32 v29, v29
s_nop 0
v_cvt_pk_bf16_f32 v223, v28, v29
s_getpc_b64 s[16:17]
.Ldr_g:
s_add_u32 s16, s16, .Ldr_b-.Ldr_g
s_addc_u32 s17, s17, 0
s_branch .Lscale2
.Ldr_b:
Cvvv 116,96,196,112
Mvvv 116,100,204
Mvvv 116,96,212
)"
R"(
Mvvv 116,100,220
s_nop 7
s_branch .Ldr_r
.Lcf2:
s_mov_b32 s85, 1048576
.Lcf2_l:
s_sleep 4
buffer_load_dwordx4 v[16:19], v20, s[48:51], s82 offen sc1
s_waitcnt vmcnt(0)
v_xor_b32 v16, s86, v16
v_bitop3_b32 v16, v17, s87, v16 bitop3:0xbe
v_bitop3_b32 v16, v18, s86, v16 bitop3:0xbe
v_bitop3_b32 v16, v19, s87, v16 bitop3:0xbe
v_and_b32 v16, v16, v21
v_cmp_ne_u32 vcc, 0, v16
s_nop 0
s_cbranch_vccz .Lcf2_r
s_sub_u32 s85, s85, 1
s_cmp_lg_u32 s85, 0
s_cbranch_scc1 .Lcf2_l
s_branch .Lcf2_r
.Lcf3:
s_mov_b32 s85, 1048576
.Lcf3_l:
s_sleep 4
buffer_load_dwordx4 v[16:19], v20, s[48:51], s82 offen sc1
s_waitcnt vmcnt(0)
v_xor_b32 v16, s86, v16
v_bitop3_b32 v16, v17, s87, v16 bitop3:0xbe
v_bitop3_b32 v16, v18, s86, v16 bitop3:0xbe
v_bitop3_b32 v16, v19, s87, v16 bitop3:0xbe
v_and_b32 v16, v16, v21
v_cmp_ne_u32 vcc, 0, v16
s_nop 0
s_cbranch_vccz .Lcf3_r
s_sub_u32 s85, s85, 1
s_cmp_lg_u32 s85, 0
s_cbranch_scc1 .Lcf3_l
s_branch .Lcf3_r
.Lgu:
s_mov_b32 s85, 1048576
.Lgu_l:
s_sleep 4
buffer_load_dwordx4 v[40:43], v11, s[48:51], s21 offen sc1
buffer_load_dwordx4 v[44:47], v11, s[48:51], s21 offen offset:1024 sc1
buffer_load_dwordx4 v[48:51], v11, s[48:51], s21 offen offset:2048 sc1
buffer_load_dwordx4 v[52:55], v11, s[48:51], s21 offen offset:3072 sc1
s_waitcnt vmcnt(0)
v_xor_b32 v40, s24, v40
v_xor_b32 v41, s25, v41
v_xor_b32 v42, s26, v42
v_xor_b32 v43, s27, v43
v_xor_b32 v44, s24, v44
v_xor_b32 v45, s25, v45
v_xor_b32 v46, s26, v46
v_xor_b32 v47, s27, v47
v_xor_b32 v48, s24, v48
v_xor_b32 v49, s25, v49
v_xor_b32 v50, s26, v50
v_xor_b32 v51, s27, v51
v_xor_b32 v52, s24, v52
v_xor_b32 v53, s25, v53
v_xor_b32 v54, s26, v54
v_xor_b32 v55, s27, v55
v_or3_b32 v56, v40, v41, v42
v_or3_b32 v56, v56, v43, v44
v_or3_b32 v56, v56, v45, v46
v_or3_b32 v56, v56, v47, v48
v_or3_b32 v56, v56, v49, v50
v_or3_b32 v56, v56, v51, v52
v_or3_b32 v56, v56, v53, v54
v_or_b32 v56, v56, v55
v_and_b32 v56, 0xff, v56
v_cmp_ne_u32 vcc, 0, v56
s_nop 0
s_and_b64 vcc, vcc, s[36:37]
s_cbranch_vccz .Lgu_r
s_sub_u32 s85, s85, 1
s_cmp_lg_u32 s85, 0
s_cbranch_scc1 .Lgu_l
s_branch .Lgu_r
.Lpf1:
s_mov_b32 s85, 1048576
.Lpf1_l:
s_sleep 1
buffer_load_dwordx2 v[58:59], v8, s[48:51], s33 offen sc1
s_waitcnt vmcnt(0)
v_xor_b32 v58, s86, v58
v_bitop3_b32 v58, v59, s87, v58 bitop3:0xbe
v_cmp_ne_u32 vcc, 0, v58
s_nop 0
s_and_b64 vcc, vcc, 0xff
s_cbranch_vccz .Lpf1_r
s_sub_u32 s85, s85, 1
s_cmp_lg_u32 s85, 0
s_cbranch_scc1 .Lpf1_l
s_branch .Lpf1_r
.Lyp:
s_mov_b32 s85, 1048576
.Lyp_l:
s_sleep 4
buffer_load_dwordx4 v[196:199], v0, s[48:51], s35 offen sc1
buffer_load_dwordx4 v[204:207], v0, s[48:51], s35 offen offset:1024 sc1
buffer_load_dwordx4 v[212:215], v0, s[48:51], s35 offen offset:2048 sc1
buffer_load_dwordx4 v[220:223], v0, s[48:51], s35 offen offset:3072 sc1
buffer_load_dwordx4 v[228:231], v0, s[48:51], s20 offen sc1
buffer_load_dwordx4 v[236:239], v0, s[48:51], s20 offen offset:1024 sc1
buffer_load_dwordx4 v[244:247], v0, s[48:51], s20 offen offset:2048 sc1
s_waitcnt vmcnt(0)
v_xor_b32 v196, s24, v196
v_xor_b32 v197, s25, v197
v_xor_b32 v198, s26, v198
v_xor_b32 v199, s27, v199
v_xor_b32 v204, s24, v204
v_xor_b32 v205, s25, v205
v_xor_b32 v206, s26, v206
v_xor_b32 v207, s27, v207
v_xor_b32 v212, s24, v212
v_xor_b32 v213, s25, v213
v_xor_b32 v214, s26, v214
v_xor_b32 v215, s27, v215
v_xor_b32 v220, s24, v220
v_xor_b32 v221, s25, v221
v_xor_b32 v222, s26, v222
v_xor_b32 v223, s27, v223
v_xor_b32 v228, s24, v228
v_xor_b32 v229, s25, v229
v_xor_b32 v230, s26, v230
v_xor_b32 v231, s27, v231
v_xor_b32 v236, s24, v236
v_xor_b32 v237, s25, v237
v_xor_b32 v238, s26, v238
v_xor_b32 v239, s27, v239
v_xor_b32 v244, s24, v244
v_xor_b32 v245, s25, v245
v_xor_b32 v246, s26, v246
v_xor_b32 v247, s27, v247
v_or3_b32 v254, v196, v197, v198
v_or3_b32 v254, v254, v199, v204
v_or3_b32 v254, v254, v205, v206
v_or3_b32 v254, v254, v207, v212
v_or3_b32 v254, v254, v213, v214
v_or3_b32 v254, v254, v215, v220
v_or3_b32 v254, v254, v221, v222
v_or3_b32 v254, v254, v223, v228
v_or3_b32 v254, v254, v229, v230
v_or3_b32 v254, v254, v231, v236
v_or3_b32 v254, v254, v237, v238
v_or3_b32 v254, v254, v239, v244
v_or3_b32 v254, v254, v245, v246
v_or_b32 v254, v254, v247
v_and_b32 v254, 0xff, v254
v_cmp_ne_u32 vcc, 0, v254
s_nop 0
s_cbranch_vccz .Lyp_r
s_sub_u32 s85, s85, 1
s_cmp_lg_u32 s85, 0
s_cbranch_scc1 .Lyp_l
s_branch .Lyp_r
.Lpf2:
s_mov_b32 s85, 1048576
.Lpf2_l:
s_sleep 1
buffer_load_dwordx2 v[30:31], v8, s[48:51], s33 offen sc1
s_waitcnt vmcnt(0)
v_xor_b32 v30, s86, v30
v_bitop3_b32 v30, v31, s87, v30 bitop3:0xbe
v_cmp_ne_u32 vcc, 0, v30
s_nop 0
s_and_b64 vcc, vcc, 0xf00
s_cbranch_vccz .Lpf2_r
s_sub_u32 s85, s85, 1
s_cmp_lg_u32 s85, 0
s_cbranch_scc1 .Lpf2_l
s_branch .Lpf2_r
.Lpc3:
s_mov_b32 s85, 1048576
.Lpc3_l:
s_sleep 1
buffer_load_dwordx4 v[196:199], v0, s[48:51], s4 offen sc1
buffer_load_dwordx4 v[204:207], v0, s[48:51], s4 offen offset:1024 sc1
buffer_load_dwordx4 v[212:215], v0, s[48:51], s5 offen sc1
buffer_load_dwordx4 v[220:223], v0, s[48:51], s5 offen offset:1024 sc1
buffer_load_dwordx4 v[228:231], v0, s[48:51], s6 offen sc1
buffer_load_dwordx4 v[236:239], v0, s[48:51], s6 offen offset:1024 sc1
s_waitcnt vmcnt(0)
v_xor_b32 v196, s24, v196
v_xor_b32 v197, s25, v197
v_xor_b32 v198, s26, v198
v_xor_b32 v199, s27, v199
v_xor_b32 v204, s24, v204
v_xor_b32 v205, s25, v205
v_xor_b32 v206, s26, v206
v_xor_b32 v207, s27, v207
v_xor_b32 v212, s24, v212
v_xor_b32 v213, s25, v213
v_xor_b32 v214, s26, v214
v_xor_b32 v215, s27, v215
v_xor_b32 v220, s24, v220
v_xor_b32 v221, s25, v221
v_xor_b32 v222, s26, v222
v_xor_b32 v223, s27, v223
v_xor_b32 v228, s24, v228
v_xor_b32 v229, s25, v229
v_xor_b32 v230, s26, v230
v_xor_b32 v231, s27, v231
v_xor_b32 v236, s24, v236
v_xor_b32 v237, s25, v237
v_xor_b32 v238, s26, v238
v_xor_b32 v239, s27, v239
v_or3_b32 v9, v196, v197, v198
v_or3_b32 v9, v9, v199, v204
v_or3_b32 v9, v9, v205, v206
v_or3_b32 v9, v9, v207, v212
v_or3_b32 v9, v9, v213, v214
v_or3_b32 v9, v9, v215, v220
v_or3_b32 v9, v9, v221, v222
v_or3_b32 v9, v9, v223, v228
v_or3_b32 v9, v9, v229, v230
v_or3_b32 v9, v9, v231, v236
v_or3_b32 v9, v9, v237, v238
v_or_b32 v9, v9, v239
v_and_b32 v9, 0xff, v9
v_cmp_ne_u32 vcc, 0, v9
s_nop 0
s_cbranch_vccz .Lpc3_r
s_sub_u32 s85, s85, 1
s_cmp_lg_u32 s85, 0
s_cbranch_scc1 .Lpc3_l
s_branch .Lpc3_r
s_endpgm
s_nop 0
s_nop 0
s_nop 0
s_nop 0
s_nop 0
s_nop 0
s_nop 0
s_nop 0
s_nop 0
s_nop 0
s_nop 0
s_nop 0
s_nop 0
s_nop 0
s_nop 0
s_nop 0
s_nop 0
s_nop 0
s_nop 0
s_nop 0
s_nop 0
s_nop 0
s_nop 0
s_nop 0
s_nop 0
s_nop 0
s_nop 0
s_nop 0
s_nop 0
s_nop 0
s_nop 0
s_nop 0
s_nop 0
s_nop 0
s_nop 0
s_nop 0
s_nop 0
s_nop 0
s_nop 0
s_nop 0
s_nop 0
s_nop 0
s_nop 0
s_nop 0
s_nop 0
s_nop 0
s_nop 0
s_nop 0
s_nop 0
s_nop 0
s_nop 0
s_nop 0
s_nop 0
s_nop 0
s_nop 0
s_nop 0
s_nop 0
s_nop 0
s_nop 0
s_nop 0
s_nop 0
s_nop 0
s_nop 0
s_nop 0
s_nop 0
s_nop 0
s_nop 0
s_nop 0
s_nop 0
s_nop 0
s_nop 0
s_nop 0
s_nop 0
s_nop 0
s_nop 0
s_nop 0
s_nop 0
s_nop 0
s_nop 0
s_nop 0
s_nop 0
s_nop 0
s_nop 0
s_nop 0
s_nop 0
s_nop 0
s_nop 0
s_nop 0
s_nop 0
s_nop 0
s_nop 0
s_nop 0
s_nop 0
s_nop 0
s_nop 0
s_nop 0
s_nop 0
s_nop 0
s_nop 0
s_nop 0
s_nop 0
s_nop 0
s_nop 0
s_nop 0
s_nop 0
s_nop 0
s_nop 0
s_nop 0
s_nop 0
s_nop 0
s_nop 0
s_nop 0
s_nop 0
s_nop 0
s_nop 0
s_nop 0
s_nop 0
s_nop 0
s_nop 0
s_nop 0
s_nop 0
s_nop 0
s_nop 0
s_nop 0
s_nop 0
s_nop 0
s_nop 0
s_nop 0
s_nop 0
s_nop 0
s_nop 0
s_nop 0
s_nop 0
s_nop 0
s_nop 0
s_nop 0
s_nop 0
s_nop 0
s_nop 0
s_nop 0
s_nop 0
s_nop 0
s_nop 0
s_nop 0
s_nop 0
s_nop 0
s_nop 0
s_nop 0
s_nop 0
s_nop 0
s_nop 0
s_nop 0
s_nop 0
s_nop 0
s_nop 0
s_nop 0
s_nop 0
s_nop 0
s_nop 0
s_nop 0
s_nop 0
s_nop 0
s_nop 0
s_nop 0
s_nop 0
s_nop 0
s_nop 0
s_nop 0
s_nop 0
s_nop 0
s_nop 0
s_nop 0
s_nop 0
s_nop 0
s_nop 0
s_nop 0
s_nop 0
s_nop 0
s_nop 0
s_nop 0
s_nop 0
s_nop 0
s_nop 0
s_nop 0
s_nop 0
s_nop 0
s_nop 0
s_nop 0
s_nop 0
s_nop 0
s_nop 0
s_nop 0
s_nop 0
s_nop 0
s_nop 0
s_nop 0
s_nop 0
s_nop 0
s_nop 0
s_nop 0
s_nop 0
s_nop 0
s_nop 0
s_nop 0
s_nop 0
s_nop 0
s_nop 0
s_nop 0
s_nop 0
s_nop 0
s_nop 0
s_nop 0
s_nop 0
s_nop 0
s_nop 0
s_nop 0
s_nop 0
s_nop 0
s_nop 0
s_nop 0
s_nop 0
s_nop 0
s_nop 0
s_nop 0
s_nop 0
s_nop 0
s_nop 0
s_nop 0
s_nop 0
s_nop 0
s_nop 0
s_nop 0
s_nop 0
s_nop 0
s_nop 0
s_nop 0
s_nop 0
s_nop 0
s_nop 0
s_nop 0
s_nop 0
s_nop 0
s_nop 0
s_nop 0
s_nop 0
s_nop 0
s_nop 0
s_nop 0
s_nop 0
s_nop 0
s_nop 0
s_nop 0
s_nop 0
s_nop 0
s_nop 0
s_nop 0
s_nop 0
s_nop 0
s_nop 0
s_nop 0
)"

        :: [ka] "s"(__builtin_amdgcn_kernarg_segment_ptr()), [b] "s"(blockIdx.x), [w] "s"(wave)
        : "memory", "scc", "vcc", "v0", "v1", "v2", "v3", "v4", "v5", "v6", "v7", "v8", "v9", "v10", "v11", "v12", "v13", "v14", "v15", "v16", "v17", "v18", "v19", "v20", "v21", "v22", "v23", "v24", "v25", "v26", "v27", "v28", "v29", "v30", "v31", "v32", "v33", "v34", "v35", "v36", "v37", "v38", "v39", "v40", "v41", "v42", "v43", "v44", "v45", "v46", "v47", "v48", "v49", "v50", "v51", "v52", "v53", "v54", "v55", "v56", "v57", "v58", "v59", "v60", "v61", "v62", "v63", "v64", "v65", "v66", "v67", "v68", "v69", "v70", "v71", "v72", "v73", "v74", "v75", "v76", "v77", "v78", "v79", "v80", "v81", "v82", "v83", "v84", "v85", "v86", "v87", "v88", "v89", "v90", "v91", "v92", "v93", "v94", "v95", "v96", "v97", "v98", "v99", "v100", "v101", "v102", "v103", "v104", "v105", "v106", "v107", "v108", "v109", "v110", "v111", "v112", "v113", "v114", "v115", "v116", "v117", "v118", "v119", "v120", "v121", "v122", "v123", "v124", "v125", "v126", "v127", "v128", "v129", "v130", "v131", "v132", "v133", "v134", "v135", "v136", "v137", "v138", "v139", "v140", "v141", "v142", "v143", "v144", "v145", "v146", "v147", "v148", "v149", "v150", "v151", "v152", "v153", "v154", "v155", "v156", "v157", "v158", "v159", "v160", "v161", "v162", "v163", "v164", "v165", "v166", "v167", "v168", "v169", "v170", "v171", "v172", "v173", "v174", "v175", "v176", "v177", "v178", "v179", "v180", "v181", "v182", "v183", "v184", "v185", "v186", "v187", "v188", "v189", "v190", "v191", "v192", "v193", "v194", "v195", "v196", "v197", "v198", "v199", "v200", "v201", "v202", "v203", "v204", "v205", "v206", "v207", "v208", "v209", "v210", "v211", "v212", "v213", "v214", "v215", "v216", "v217", "v218", "v219", "v220", "v221", "v222", "v223", "v224", "v225", "v226", "v227", "v228", "v229", "v230", "v231", "v232", "v233", "v234", "v235", "v236", "v237", "v238", "v239", "v240", "v241", "v242", "v243", "v244", "v245", "v246", "v247", "v248", "v249", "v250", "v251", "v252", "v253", "v254", "v255", "a0", "a1", "a2", "a3", "a4", "a5", "a6", "a7", "a8", "a9", "a10", "a11", "a12", "a13", "a14", "a15", "a16", "a17", "a18", "a19", "a20", "a21", "a22", "a23", "a24", "a25", "a26", "a27", "a28", "a29", "a30", "a31", "a32", "a33", "a34", "a35", "a36", "a37", "a38", "a39", "a40", "a41", "a42", "a43", "a44", "a45", "a46", "a47", "a48", "a49", "a50", "a51", "a52", "a53", "a54", "a55", "a56", "a57", "a58", "a59", "a60", "a61", "a62", "a63", "a64", "a65", "a66", "a67", "a68", "a69", "a70", "a71", "a72", "a73", "a74", "a75", "a76", "a77", "a78", "a79", "a80", "a81", "a82", "a83", "a84", "a85", "a86", "a87", "a88", "a89", "a90", "a91", "a92", "a93", "a94", "a95", "a96", "a97", "a98", "a99", "a100", "a101", "a102", "a103", "a104", "a105", "a106", "a107", "a108", "a109", "a110", "a111", "a112", "a113", "a114", "a115", "a116", "a117", "a118", "a119", "a120", "a121", "a122", "a123", "a124", "a125", "a126", "a127", "a128", "a129", "a130", "a131", "a132", "a133", "a134", "a135", "a136", "a137", "a138", "a139", "a140", "a141", "a142", "a143", "a144", "a145", "a146", "a147", "a148", "a149", "a150", "a151", "a152", "a153", "a154", "a155", "a156", "a157", "a158", "a159", "a160", "a161", "a162", "a163", "a164", "a165", "a166", "a167", "a168", "a169", "a170", "a171", "a172", "a173", "a174", "a175", "a176", "a177", "a178", "a179", "a180", "a181", "a182", "a183", "a184", "a185", "a186", "a187", "a188", "a189", "a190", "a191", "a192", "a193", "a194", "a195", "a196", "a197", "a198", "a199", "a200", "a201", "a202", "a203", "a204", "a205", "a206", "a207", "a208", "a209", "a210", "a211", "a212", "a213", "a214", "a215", "a216", "a217", "a218", "a219", "a220", "a221", "a222", "a223", "a224", "a225", "a226", "a227", "a228", "a229", "a230", "a231", "a232", "a233", "a234", "a235", "a236", "a237", "a238", "a239", "a240", "a241", "a242", "a243", "a244", "a245", "a246", "a247", "a248", "a249", "a250", "a251", "a252", "a253", "a254", "a255", "s4", "s5", "s6", "s7", "s8", "s9", "s10", "s11", "s12", "s13", "s14", "s15", "s16", "s17", "s18", "s19", "s20", "s21", "s22", "s23", "s24", "s25", "s26", "s27", "s28", "s29", "s30", "s31", "s32", "s33", "s34", "s35", "s36", "s37", "s38", "s39", "s40", "s41", "s42", "s43", "s44", "s45", "s46", "s47", "s48", "s49", "s50", "s51", "s52", "s53", "s54", "s55", "s56", "s57", "s58", "s59", "s60", "s61", "s62", "s63", "s64", "s65", "s66", "s67", "s68", "s69", "s70", "s71", "s72", "s73", "s74", "s75", "s76", "s77", "s78", "s79", "s80", "s81", "s82", "s83", "s84", "s85", "s86", "s87", "s88", "s89", "s90", "s91", "s92", "s93", "s94", "s95", "s96", "s97", "s98", "s99");
}
extern "C" void kernel_launch(void* const* d_in, const int* in_sizes, int n_in, void* d_out, int out_size, void* d_ws, size_t ws_size, hipStream_t stream) {
    (void)n_in; (void)ws_size;
    (void)hipFuncSetAttribute((const void*)k_all, hipFuncAttributeMaxDynamicSharedMemorySize, 163840);
#define IN(i) d_in[i], 2u * (unsigned)in_sizes[i], 0x20000u
    k_all<<<256, 256, 163840, stream>>>(IN(0), IN(1), IN(2), IN(3), IN(4), IN(5), IN(7), IN(8), IN(9), IN(10), d_ws, 109051904u, 0x20000u, d_out, 2u * (unsigned)out_size, 0x20000u);
}
```
